```python
import torch

def _softmax(x, axis=-1):
    return torch.softmax(x, dim=axis)
def _gelu(x):
    return torch.nn.functional.gelu(x, approximate="tanh")
def _silu(x):
    return torch.nn.functional.silu(x)
def _sigmoid(x):
    return torch.sigmoid(x)
def _layernorm(x, axis=-1, eps=1e-5):
    m = x.mean(dim=axis, keepdim=True)
    v = x.var(dim=axis, keepdim=True, unbiased=False)
    return (x - m) / torch.sqrt(v + eps)
def _scatter_add(tgt, idx, src, axis=0):
    return tgt.clone().index_add_(axis, idx, src)
def _segment_sum(src, seg, n):
    out = torch.zeros((n,) + src.shape[1:], dtype=src.dtype)
    return out.index_add_(0, seg, src)
def _pad(x, pw):
    flat = tuple(v for pair in reversed(pw) for v in pair)
    return torch.nn.functional.pad(x, flat)

kernel_name = 'sequential_preLN__mha_block__glu_gated_reglu__prompt'

def setup_inputs():

    _w = (lambda a: (lambda t: t.to(torch.bfloat16) if t.is_floating_point() else t)(torch.as_tensor(a)))
    def _A_setup():
        B, S, D, H = 1, 2048, 1024, 32
        return {
            "x": _w((lambda *s: torch.randn(*s, dtype=torch.bfloat16) * 0.05)(B, S, D)),
            "wqkv": _w((lambda *s: torch.randn(*s, dtype=torch.bfloat16) * 0.05)(D, 3 * D)),
            "wo": _w((lambda *s: torch.randn(*s, dtype=torch.bfloat16) * 0.05)(D, D)),
        }
    _A_in = _A_setup()

    def _B_setup():
        B, S, D, F = 1, 2048, 1024, 3584
        return {
            "x": _w((lambda *s: torch.randn(*s, dtype=torch.bfloat16) * 0.05)(B, S, D)),
            "W_gate": _w((lambda *s: torch.randn(*s, dtype=torch.bfloat16) * 0.8)(D, F) * (2.0 / D) ** 0.5),
            "W_up": _w((lambda *s: torch.randn(*s, dtype=torch.bfloat16) * 0.8)(D, F) * (2.0 / D) ** 0.5),
            "W_down": _w((lambda *s: torch.randn(*s, dtype=torch.bfloat16) * 0.8)(F, D) * (1.0 / F) ** 0.5),
        }
    _B_in = _B_setup()

    return {
        "A_x": _A_in["x"],
        "A_wqkv": _A_in["wqkv"],
        "A_wo": _A_in["wo"],
        "B_x": _B_in["x"],
        "B_W_gate": _B_in["W_gate"],
        "B_W_up": _B_in["W_up"],
        "B_W_down": _B_in["W_down"],
    }

def reference(A_x, A_wqkv, A_wo, B_x, B_W_gate, B_W_up, B_W_down):
    def _A_ref(x, wqkv, wo):
        B, S, D = x.shape
        H = 32
        Dh = D // H
        qkv = (lambda x, *d: x.permute(*d))((x @ wqkv).reshape(B, S, 3, H, Dh), 2, 0, 3, 1, 4)
        q, k, v = qkv[0], qkv[1], qkv[2]
        scores = torch.einsum("bhqd,bhkd->bhqk", q, k) * (Dh ** -0.5)
        p = _softmax(scores, axis=-1)
        o = torch.einsum("bhqk,bhkd->bhqd", p, v)
        return (lambda x, *d: x.permute(*d))(o, 0, 2, 1, 3).reshape(B, S, D) @ wo

    def _B_ref(x, W_gate, W_up, W_down):
        gate = torch.einsum("bsd,df->bsf", x, W_gate)
        up = torch.einsum("bsd,df->bsf", x, W_up)
        gate = gate * (lambda x, dt: x.to(dt))(gate > 0, gate.dtype)
        h = gate * up
        return torch.einsum("bsf,fd->bsd", h, W_down)

    x = A_x
    h = x + _A_ref(_layernorm(x, axis=-1), A_wqkv, A_wo)
    h2 = h + _B_ref(_layernorm(h, axis=-1), B_W_gate, B_W_up, B_W_down)
    return h2
```

```cpp
#include <hip/hip_runtime.h>
#include <hip/hip_bf16.h>
#include <stdint.h>
typedef unsigned short u16;
__device__ __forceinline__ float bf2f(u16 v){ return __uint_as_float(((unsigned)v)<<16); }
__device__ __forceinline__ u16 f2bf(float f){ __hip_bfloat16 b = __float2bfloat16(f); return *(u16*)&b; }
constexpr int S=2048, D=1024, H=32, DH=32, F=3584;

__global__ void k_ln(const u16* x, u16* y){
  int row=blockIdx.x; int t=threadIdx.x; __shared__ float red[256];
  float v[4]; float s=0; for(int i=0;i<4;i++){ v[i]=bf2f(x[row*D+t*4+i]); s+=v[i]; }
  red[t]=s; __syncthreads(); for(int o=128;o>0;o>>=1){ if(t<o) red[t]+=red[t+o]; __syncthreads(); }
  float m=red[0]/D; __syncthreads();
  float q=0; for(int i=0;i<4;i++){ float d=v[i]-m; q+=d*d; }
  red[t]=q; __syncthreads(); for(int o=128;o>0;o>>=1){ if(t<o) red[t]+=red[t+o]; __syncthreads(); }
  float r=rsqrtf(red[0]/D+1e-5f);
  for(int i=0;i<4;i++) y[row*D+t*4+i]=f2bf((v[i]-m)*r);
}
template<int MODE>
__global__ void k_gemm(const u16* A,const u16* B,const u16* B2,const u16* res,u16* C,int M,int N,int K){
  __shared__ float As[64][17], Bs[16][65], B2s[16][65];
  int tx=threadIdx.x&15, ty=threadIdx.x>>4; int m0=blockIdx.y*64, n0=blockIdx.x*64;
  float acc[4][4]={}, acc2[4][4]={};
  for(int k0=0;k0<K;k0+=16){
    for(int i=threadIdx.x;i<64*16;i+=256){ int r=i>>4,c=i&15; As[r][c]=bf2f(A[(size_t)(m0+r)*K+k0+c]); }
    for(int i=threadIdx.x;i<64*16;i+=256){ int r=i>>6,c=i&63; Bs[r][c]=bf2f(B[(size_t)(k0+r)*N+n0+c]); if(MODE==2) B2s[r][c]=bf2f(B2[(size_t)(k0+r)*N+n0+c]); }
    __syncthreads();
    for(int k=0;k<16;k++){
      #pragma unroll
      for(int i=0;i<4;i++){
      #pragma unroll
      for(int j=0;j<4;j++){ acc[i][j]+=As[ty*4+i][k]*Bs[k][tx*4+j]; if(MODE==2) acc2[i][j]+=As[ty*4+i][k]*B2s[k][tx*4+j]; } } }
    __syncthreads();
  }
  #pragma unroll
  for(int i=0;i<4;i++)
  #pragma unroll
  for(int j=0;j<4;j++){ size_t o=(size_t)(m0+ty*4+i)*N+n0+tx*4+j; float v=acc[i][j];
    if(MODE==1) v+=bf2f(res[o]); if(MODE==2) v=(v>0?v:0.f)*acc2[i][j]; C[o]=f2bf(v); }
}
__global__ void k_attn(const u16* qkv,u16* o){
  int h=blockIdx.y; int q=blockIdx.x*256+threadIdx.x; __shared__ float ks[64][32], vs[64][32];
  float qv[32], acc[32];
  #pragma unroll
  for(int d=0;d<32;d++){ qv[d]=bf2f(qkv[(size_t)q*3*D+h*DH+d])*0.17677669529663687f; acc[d]=0; }
  float m=-1e30f,l=0;
  for(int k0=0;k0<S;k0+=64){
    for(int i=threadIdx.x;i<64*32;i+=256){ int r=i>>5,c=i&31; ks[r][c]=bf2f(qkv[(size_t)(k0+r)*3*D+D+h*DH+c]); vs[r][c]=bf2f(qkv[(size_t)(k0+r)*3*D+2*D+h*DH+c]); }
    __syncthreads();
    for(int j=0;j<64;j++){ float s=0;
      #pragma unroll
      for(int d=0;d<32;d++) s+=qv[d]*ks[j][d];
      float mn=fmaxf(m,s); float a=__expf(m-mn), p=__expf(s-mn); l=l*a+p;
      #pragma unroll
      for(int d=0;d<32;d++) acc[d]=acc[d]*a+p*vs[j][d]; m=mn; }
    __syncthreads();
  }
  #pragma unroll
  for(int d=0;d<32;d++) o[(size_t)q*D+h*DH+d]=f2bf(acc[d]/l);
}
extern "C" void kernel_launch(void* const* d_in, const int* in_sizes, int n_in, void* d_out, int out_size, void* d_ws, size_t ws_size, hipStream_t stream){
  const u16* x=(const u16*)d_in[0]; const u16* wqkv=(const u16*)d_in[1]; const u16* wo=(const u16*)d_in[2];
  const u16* wg=(const u16*)d_in[4]; const u16* wu=(const u16*)d_in[5]; const u16* wd=(const u16*)d_in[6];
  u16* ws=(u16*)d_ws; u16* xn=ws; u16* qkv=xn+S*D; u16* o=qkv+S*3*D; u16* h=o+S*D; u16* hn=h+S*D; u16* hh=hn+S*D;
  k_ln<<<S,256,0,stream>>>(x,xn);
  k_gemm<0><<<dim3(3*D/64,S/64),256,0,stream>>>(xn,wqkv,nullptr,nullptr,qkv,S,3*D,D);
  k_attn<<<dim3(S/256,H),256,0,stream>>>(qkv,o);
  k_gemm<1><<<dim3(D/64,S/64),256,0,stream>>>(o,wo,nullptr,x,h,S,D,D);
  k_ln<<<S,256,0,stream>>>(h,hn);
  k_gemm<2><<<dim3(F/64,S/64),256,0,stream>>>(hn,wg,wu,nullptr,hh,S,F,D);
  k_gemm<1><<<dim3(D/64,S/64),256,0,stream>>>(hh,wd,nullptr,h,(u16*)d_out,S,D,F);
}
```

```cpp
#include <hip/hip_runtime.h>
#include <hip/hip_bf16.h>
#include <stdint.h>
typedef unsigned short u16;
__device__ __forceinline__ float bf2f(u16 v){ return __uint_as_float(((unsigned)v)<<16); }
__device__ __forceinline__ u16 f2bf(float f){ __hip_bfloat16 b = __float2bfloat16(f); return *(u16*)&b; }
constexpr int S=2048, D=1024, H=32, DH=32, F=3584;

__global__ void k_ln(const u16* x, u16* y){
  int row=blockIdx.x; int t=threadIdx.x; __shared__ float red[256];
  float v[4]; float s=0; for(int i=0;i<4;i++){ v[i]=bf2f(x[row*D+t*4+i]); s+=v[i]; }
  red[t]=s; __syncthreads(); for(int o=128;o>0;o>>=1){ if(t<o) red[t]+=red[t+o]; __syncthreads(); }
  float m=red[0]/D; __syncthreads();
  float q=0; for(int i=0;i<4;i++){ float d=v[i]-m; q+=d*d; }
  red[t]=q; __syncthreads(); for(int o=128;o>0;o>>=1){ if(t<o) red[t]+=red[t+o]; __syncthreads(); }
  float r=rsqrtf(red[0]/D+1e-5f);
  for(int i=0;i<4;i++) y[row*D+t*4+i]=f2bf((v[i]-m)*r);
}
template<int MODE>
__global__ void k_gemm(const u16* A,const u16* B,const u16* B2,const u16* res,u16* C,int M,int N,int K){
  __shared__ float As[64][17], Bs[16][65], B2s[16][65];
  int tx=threadIdx.x&15, ty=threadIdx.x>>4; int m0=blockIdx.y*64, n0=blockIdx.x*64;
  float acc[4][4]={}, acc2[4][4]={};
  for(int k0=0;k0<K;k0+=16){
    for(int i=threadIdx.x;i<64*16;i+=256){ int r=i>>4,c=i&15; As[r][c]=bf2f(A[(size_t)(m0+r)*K+k0+c]); }
    for(int i=threadIdx.x;i<64*16;i+=256){ int r=i>>6,c=i&63; Bs[r][c]=bf2f(B[(size_t)(k0+r)*N+n0+c]); if(MODE==2) B2s[r][c]=bf2f(B2[(size_t)(k0+r)*N+n0+c]); }
    __syncthreads();
    for(int k=0;k<16;k++){
      #pragma unroll
      for(int i=0;i<4;i++){
      #pragma unroll
      for(int j=0;j<4;j++){ acc[i][j]+=As[ty*4+i][k]*Bs[k][tx*4+j]; if(MODE==2) acc2[i][j]+=As[ty*4+i][k]*B2s[k][tx*4+j]; } } }
    __syncthreads();
  }
  #pragma unroll
  for(int i=0;i<4;i++)
  #pragma unroll
  for(int j=0;j<4;j++){ size_t o=(size_t)(m0+ty*4+i)*N+n0+tx*4+j; float v=acc[i][j];
    if(MODE==1) v+=bf2f(res[o]); if(MODE==2) v=(v>0?v:0.f)*acc2[i][j]; C[o]=f2bf(v); }
}
__global__ void k_attn(const u16* qkv,u16* o){
  int h=blockIdx.y; int q=blockIdx.x*256+threadIdx.x; __shared__ float ks[64][32], vs[64][32];
  float qv[32], acc[32];
  #pragma unroll
  for(int d=0;d<32;d++){ qv[d]=bf2f(qkv[(size_t)q*3*D+h*DH+d])*0.17677669529663687f; acc[d]=0; }
  float m=-1e30f,l=0;
  for(int k0=0;k0<S;k0+=64){
    for(int i=threadIdx.x;i<64*32;i+=256){ int r=i>>5,c=i&31; ks[r][c]=bf2f(qkv[(size_t)(k0+r)*3*D+D+h*DH+c]); vs[r][c]=bf2f(qkv[(size_t)(k0+r)*3*D+2*D+h*DH+c]); }
    __syncthreads();
    for(int j=0;j<64;j++){ float s=0;
      #pragma unroll
      for(int d=0;d<32;d++) s+=qv[d]*ks[j][d];
      float mn=fmaxf(m,s); float a=__expf(m-mn), p=__expf(s-mn); l=l*a+p;
      #pragma unroll
      for(int d=0;d<32;d++) acc[d]=acc[d]*a+p*vs[j][d]; m=mn; }
    __syncthreads();
  }
  #pragma unroll
  for(int d=0;d<32;d++) o[(size_t)q*D+h*DH+d]=f2bf(acc[d]/l);
}

template<int NT>
__global__ __launch_bounds__(256) void k_probe(const char* p, unsigned bytes_per_wg, unsigned amp){
  extern __shared__ char lds[];
  int wid=__builtin_amdgcn_readfirstlane(threadIdx.x>>6), lane=threadIdx.x&63;
  unsigned long long r0,r1;
  __syncthreads();
  asm volatile("s_memrealtime %0\n\ts_waitcnt lgkmcnt(0)":"=s"(r0)::"memory");
  unsigned n=bytes_per_wg/4096; unsigned off=blockIdx.x*bytes_per_wg+wid*1024+lane*16;
  for(unsigned i=0;i<n;i++){
    if(NT) asm volatile("s_mov_b32 m0, %2\n\ts_nop 0\n\tglobal_load_lds_dwordx4 %0, %1 nt" :: "v"(off), "s"(p), "s"(wid*1024) : "memory");
    else asm volatile("s_mov_b32 m0, %2\n\ts_nop 0\n\tglobal_load_lds_dwordx4 %0, %1" :: "v"(off), "s"(p), "s"(wid*1024) : "memory");
    off+=4096; }
  asm volatile("s_waitcnt vmcnt(0)":::"memory");
  __syncthreads();
  asm volatile("s_memrealtime %0\n\ts_waitcnt lgkmcnt(0)":"=s"(r1)::"memory");
  unsigned long long dt=(r1-r0)*amp; if(dt>300000ull) dt=300000ull;
  if(blockIdx.x==100){ while(__builtin_amdgcn_s_memrealtime()-r1<dt) __builtin_amdgcn_s_sleep(4); }
}
extern "C" void kernel_launch(void* const* d_in, const int* in_sizes, int n_in, void* d_out, int out_size, void* d_ws, size_t ws_size, hipStream_t stream){
  const u16* x=(const u16*)d_in[0]; const u16* wqkv=(const u16*)d_in[1]; const u16* wo=(const u16*)d_in[2];
  const u16* wg=(const u16*)d_in[4]; const u16* wu=(const u16*)d_in[5]; const u16* wd=(const u16*)d_in[6];
  u16* ws=(u16*)d_ws; u16* xn=ws; u16* qkv=xn+S*D; u16* o=qkv+S*3*D; u16* h=o+S*D; u16* hn=h+S*D; u16* hh=hn+S*D;
  k_probe<0><<<256,256,4096,stream>>>((const char*)d_in[6],28672,200);
  k_ln<<<S,256,0,stream>>>(x,xn);
  k_gemm<0><<<dim3(3*D/64,S/64),256,0,stream>>>(xn,wqkv,nullptr,nullptr,qkv,S,3*D,D);
  k_attn<<<dim3(S/256,H),256,0,stream>>>(qkv,o);
  k_gemm<1><<<dim3(D/64,S/64),256,0,stream>>>(o,wo,nullptr,x,h,S,D,D);
  k_ln<<<S,256,0,stream>>>(h,hn);
  k_gemm<2><<<dim3(F/64,S/64),256,0,stream>>>(hn,wg,wu,nullptr,hh,S,F,D);
  k_gemm<1><<<dim3(D/64,S/64),256,0,stream>>>(hh,wd,nullptr,h,(u16*)d_out,S,D,F);
}
```

```cpp
#include <hip/hip_runtime.h>
#include <hip/hip_bf16.h>
#include <stdint.h>
typedef unsigned short u16;
__device__ __forceinline__ float bf2f(u16 v){ return __uint_as_float(((unsigned)v)<<16); }
__device__ __forceinline__ u16 f2bf(float f){ __hip_bfloat16 b = __float2bfloat16(f); return *(u16*)&b; }
constexpr int S=2048, D=1024, H=32, DH=32, F=3584;

__global__ void k_ln(const u16* x, u16* y){
  int row=blockIdx.x; int t=threadIdx.x; __shared__ float red[256];
  float v[4]; float s=0; for(int i=0;i<4;i++){ v[i]=bf2f(x[row*D+t*4+i]); s+=v[i]; }
  red[t]=s; __syncthreads(); for(int o=128;o>0;o>>=1){ if(t<o) red[t]+=red[t+o]; __syncthreads(); }
  float m=red[0]/D; __syncthreads();
  float q=0; for(int i=0;i<4;i++){ float d=v[i]-m; q+=d*d; }
  red[t]=q; __syncthreads(); for(int o=128;o>0;o>>=1){ if(t<o) red[t]+=red[t+o]; __syncthreads(); }
  float r=rsqrtf(red[0]/D+1e-5f);
  for(int i=0;i<4;i++) y[row*D+t*4+i]=f2bf((v[i]-m)*r);
}
template<int MODE>
__global__ void k_gemm(const u16* A,const u16* B,const u16* B2,const u16* res,u16* C,int M,int N,int K){
  __shared__ float As[64][17], Bs[16][65], B2s[16][65];
  int tx=threadIdx.x&15, ty=threadIdx.x>>4; int m0=blockIdx.y*64, n0=blockIdx.x*64;
  float acc[4][4]={}, acc2[4][4]={};
  for(int k0=0;k0<K;k0+=16){
    for(int i=threadIdx.x;i<64*16;i+=256){ int r=i>>4,c=i&15; As[r][c]=bf2f(A[(size_t)(m0+r)*K+k0+c]); }
    for(int i=threadIdx.x;i<64*16;i+=256){ int r=i>>6,c=i&63; Bs[r][c]=bf2f(B[(size_t)(k0+r)*N+n0+c]); if(MODE==2) B2s[r][c]=bf2f(B2[(size_t)(k0+r)*N+n0+c]); }
    __syncthreads();
    for(int k=0;k<16;k++){
      #pragma unroll
      for(int i=0;i<4;i++){
      #pragma unroll
      for(int j=0;j<4;j++){ acc[i][j]+=As[ty*4+i][k]*Bs[k][tx*4+j]; if(MODE==2) acc2[i][j]+=As[ty*4+i][k]*B2s[k][tx*4+j]; } } }
    __syncthreads();
  }
  #pragma unroll
  for(int i=0;i<4;i++)
  #pragma unroll
  for(int j=0;j<4;j++){ size_t o=(size_t)(m0+ty*4+i)*N+n0+tx*4+j; float v=acc[i][j];
    if(MODE==1) v+=bf2f(res[o]); if(MODE==2) v=(v>0?v:0.f)*acc2[i][j]; C[o]=f2bf(v); }
}
__global__ void k_attn(const u16* qkv,u16* o){
  int h=blockIdx.y; int q=blockIdx.x*256+threadIdx.x; __shared__ float ks[64][32], vs[64][32];
  float qv[32], acc[32];
  #pragma unroll
  for(int d=0;d<32;d++){ qv[d]=bf2f(qkv[(size_t)q*3*D+h*DH+d])*0.17677669529663687f; acc[d]=0; }
  float m=-1e30f,l=0;
  for(int k0=0;k0<S;k0+=64){
    for(int i=threadIdx.x;i<64*32;i+=256){ int r=i>>5,c=i&31; ks[r][c]=bf2f(qkv[(size_t)(k0+r)*3*D+D+h*DH+c]); vs[r][c]=bf2f(qkv[(size_t)(k0+r)*3*D+2*D+h*DH+c]); }
    __syncthreads();
    for(int j=0;j<64;j++){ float s=0;
      #pragma unroll
      for(int d=0;d<32;d++) s+=qv[d]*ks[j][d];
      float mn=fmaxf(m,s); float a=__expf(m-mn), p=__expf(s-mn); l=l*a+p;
      #pragma unroll
      for(int d=0;d<32;d++) acc[d]=acc[d]*a+p*vs[j][d]; m=mn; }
    __syncthreads();
  }
  #pragma unroll
  for(int d=0;d<32;d++) o[(size_t)q*D+h*DH+d]=f2bf(acc[d]/l);
}

typedef __attribute__((ext_vector_type(8))) short bf16x8;
typedef __attribute__((ext_vector_type(16))) float f32x16;
typedef __attribute__((ext_vector_type(4))) float f32x4;
template<int MODE>
__global__ __launch_bounds__(256) void k_probe(const char* p, float* sink, int iters, unsigned amp){
  unsigned long long r0,r1;
  bf16x8 a[4],b[4];
  for(int i=0;i<4;i++){ a[i]=*(const bf16x8*)(p+(((blockIdx.x*256+threadIdx.x)&0x7fff)*8+i)*16); b[i]=*(const bf16x8*)(p+(((blockIdx.x*256+threadIdx.x)&0x7fff)*8+4+i)*16);}
  f32x16 c[16]={}; f32x4 e[16]={};
  __syncthreads();
  asm volatile("s_waitcnt vmcnt(0)\n\ts_memrealtime %0\n\ts_waitcnt lgkmcnt(0)":"=s"(r0)::"memory");
  if(MODE==0){ unsigned long long t0=__builtin_amdgcn_s_memtime(); while(__builtin_amdgcn_s_memtime()-t0<(unsigned long long)iters*512) __builtin_amdgcn_s_sleep(1); }
  if(MODE==1){ for(int it=0;it<iters;it++){
    #pragma unroll
    for(int i=0;i<4;i++)
    #pragma unroll
    for(int j=0;j<4;j++) c[i*4+j]=__builtin_amdgcn_mfma_f32_32x32x16_bf16(a[i],b[j],c[i*4+j],0,0,0); } }
  if(MODE==2){ for(int it=0;it<iters*2;it++){
    #pragma unroll
    for(int i=0;i<4;i++)
    #pragma unroll
    for(int j=0;j<4;j++) e[i*4+j]=__builtin_amdgcn_mfma_f32_16x16x32_bf16(a[i],b[j],e[i*4+j],0,0,0); } }
  float s=0; for(int i=0;i<16;i++){ for(int j=0;j<16;j++) s+=c[i][j]; for(int j=0;j<4;j++) s+=e[i][j]; }
  if(s==1.2345f) *sink=s;
  __syncthreads();
  asm volatile("s_memrealtime %0\n\ts_waitcnt lgkmcnt(0)":"=s"(r1)::"memory");
  unsigned long long dt=(r1-r0)*amp; if(dt>600000ull) dt=600000ull;
  if(blockIdx.x==100){ while(__builtin_amdgcn_s_memrealtime()-r1<dt) __builtin_amdgcn_s_sleep(4); }
}
extern "C" void kernel_launch(void* const* d_in, const int* in_sizes, int n_in, void* d_out, int out_size, void* d_ws, size_t ws_size, hipStream_t stream){
  const u16* x=(const u16*)d_in[0]; const u16* wqkv=(const u16*)d_in[1]; const u16* wo=(const u16*)d_in[2];
  const u16* wg=(const u16*)d_in[4]; const u16* wu=(const u16*)d_in[5]; const u16* wd=(const u16*)d_in[6];
  u16* ws=(u16*)d_ws; u16* xn=ws; u16* qkv=xn+S*D; u16* o=qkv+S*3*D; u16* h=o+S*D; u16* hn=h+S*D; u16* hh=hn+S*D;
  k_probe<0><<<256,256,0,stream>>>((const char*)d_in[4],(float*)d_ws,200,0);
  k_ln<<<S,256,0,stream>>>(x,xn);
  k_gemm<0><<<dim3(3*D/64,S/64),256,0,stream>>>(xn,wqkv,nullptr,nullptr,qkv,S,3*D,D);
  k_attn<<<dim3(S/256,H),256,0,stream>>>(qkv,o);
  k_gemm<1><<<dim3(D/64,S/64),256,0,stream>>>(o,wo,nullptr,x,h,S,D,D);
  k_ln<<<S,256,0,stream>>>(h,hn);
  k_gemm<2><<<dim3(F/64,S/64),256,0,stream>>>(hn,wg,wu,nullptr,hh,S,F,D);
  k_gemm<1><<<dim3(D/64,S/64),256,0,stream>>>(hh,wd,nullptr,h,(u16*)d_out,S,D,F);
}
```

```cpp
#include <hip/hip_runtime.h>
#include <stdint.h>
typedef unsigned short u16;
typedef unsigned u32x2 __attribute__((ext_vector_type(2)));
typedef unsigned u32x4 __attribute__((ext_vector_type(4)));
typedef unsigned u32x8 __attribute__((ext_vector_type(8)));
typedef unsigned u32x16 __attribute__((ext_vector_type(16)));
typedef unsigned u32x32 __attribute__((ext_vector_type(32)));
typedef float f32x2 __attribute__((ext_vector_type(2)));
typedef float f32x4 __attribute__((ext_vector_type(4)));
typedef float f32x8 __attribute__((ext_vector_type(8)));
typedef float f32x16 __attribute__((ext_vector_type(16)));
typedef float f32x32 __attribute__((ext_vector_type(32)));
typedef __bf16 bf16x2 __attribute__((ext_vector_type(2)));
typedef __bf16 bf16x8 __attribute__((ext_vector_type(8)));
#define DI __device__ __forceinline__
constexpr int S_ = 2048, D_ = 1024, F_ = 3584;
constexpr size_t WS_Q = 4u << 20, WS_KV = 8u << 20, WS_O = 16u << 20, WS_H1 = 20u << 20, WS_HH = 24u << 20, WS_EX = 44u << 20, WS_SM = 40u << 20, WS_WG = 48u << 20, WS_WD = 62u << 20, WS_END = 70u << 20;
constexpr unsigned SM_XM = 0, SM_WB = 196608, SM_MR = 40960, SM_M2 = 65536, SM_FL = 262144, FL_SZ = 131072;
constexpr size_t WS_XM = WS_SM + SM_XM, WS_WB = WS_SM + SM_WB, WS_MR = WS_SM + SM_MR, WS_M2 = WS_SM + SM_M2, WS_EP = WS_SM + 32768;
DI unsigned cvtpk(float lo, float hi) { f32x2 v = {lo, hi}; return __builtin_bit_cast(unsigned, __builtin_convertvector(v, bf16x2)); }
DI float bflo(unsigned w) { return __uint_as_float(w << 16); }
DI float bfhi(unsigned w) { return __uint_as_float(w & 0xffff0000u); }
DI u32x4 mksrd(const void* p, unsigned bytes) { unsigned long long a = (unsigned long long)p; u32x4 r; r[0] = (unsigned)a; r[1] = (unsigned)(a >> 32) & 0xffff; r[2] = bytes; r[3] = 0x20000; return r; }
DI void st16_dev(void* p, u32x4 v) { asm volatile("global_store_dwordx4 %0, %1, off sc1\n\ts_nop 1" :: "v"(p), "v"(v) : "memory"); }
DI void st4_dev(void* p, float v) { asm volatile("global_store_dword %0, %1, off sc1" :: "v"(p), "v"(v) : "memory"); }
DI unsigned lane_id() { unsigned l; asm volatile("v_mbcnt_lo_u32_b32 %0, -1, 0\n\tv_mbcnt_hi_u32_b32 %0, -1, %0" : "=v"(l)); return l; }
DI float relu(float x) { float r; asm("v_max_f32 %0, 0, %1" : "=v"(r) : "v"(x)); return r; }
DI float xsum16(float v) { auto r = __builtin_amdgcn_permlane16_swap(__float_as_uint(v), __float_as_uint(v), false, false); return __uint_as_float(r[0]) + __uint_as_float(r[1]); }
DI float xsum32(float v) { auto r = __builtin_amdgcn_permlane32_swap(__float_as_uint(v), __float_as_uint(v), false, false); return __uint_as_float(r[0]) + __uint_as_float(r[1]); }
template <int C> DI float dpp(float v) { return __int_as_float(__builtin_amdgcn_update_dpp(0, __float_as_int(v), C, 0xf, 0xf, true)); }
#define STAMP(k, i)
#define PS_DECL
#define STAMP0
#define PS_OUT
#define PS_ST(k)
#define STAMP_END(k)

#define CODE_ADDR(A, B, N, a_, o_) unsigned long long a_; unsigned o_; { unsigned n_; \
    asm volatile("s_getpc_b64 s[60:61]\n\ts_add_u32 s60, s60, " #A "@rel32@lo+4\n\ts_addc_u32 s61, s61, " #A "@rel32@hi+12\n\ts_mov_b32 %1, " #B "-" #A : "={s[60:61]}"(a_), "=s"(n_) :: "scc"); \
    o_ = min((((blockIdx.x >> 3) * 4 + wid) * (N) + min(lane_id(), (unsigned)(N) - 1)) * 128, n_ - 4); }
#define CODE_PF(A, B, N, LDSOFF) do { if (1) { CODE_ADDR(A, B, N, a_, o_) \
    asm volatile("s_mov_b32 m0, %2\n\ts_nop 0\n\tglobal_load_lds_dword %0, %1" :: "v"(o_), "s"(a_), "s"(LDSOFF) : "memory"); } } while (0)
typedef __attribute__((address_space(3))) float* lds_f;
typedef __attribute__((address_space(3))) f32x4* lds_f4;
typedef __attribute__((address_space(3))) f32x2* lds_f2;
DI void att_slow(const char* qp, const char* kvp, unsigned l2, f32x16& o, float& l) {
  const bf16x8 q0 = *(const bf16x8*)(qp + l2 * 16), q1 = *(const bf16x8*)(qp + 1024 + l2 * 16);
  float m = -INFINITY; l = 0.f; o = f32x16{};
#pragma unroll 1
  for (int b = 0; b < 64; b++) {
    const char* p = kvp + b * 4096 + l2 * 16;
    const bf16x8 k0 = *(const bf16x8*)p, k1 = *(const bf16x8*)(p + 1024), v0 = *(const bf16x8*)(p + 2048), v1 = *(const bf16x8*)(p + 3072);
    f32x16 s = __builtin_amdgcn_mfma_f32_32x32x16_bf16(k0, q0, f32x16{}, 0, 0, 0);
    s = __builtin_amdgcn_mfma_f32_32x32x16_bf16(k1, q1, s, 0, 0, 0);
    float mx = s[0];
#pragma unroll
    for (int i = 1; i < 16; i++) mx = fmaxf(mx, s[i]);
    mx = fmaxf(mx, __shfl_xor(mx, 32));
    const float mn = fmaxf(m, mx), al = __builtin_amdgcn_exp2f(m - mn);
    m = mn;
    u32x4 p0, p1; float ps = 0.f;
#pragma unroll
    for (int i = 0; i < 4; i++) {
      p0[i] = cvtpk(__builtin_amdgcn_exp2f(s[2 * i] - mn), __builtin_amdgcn_exp2f(s[2 * i + 1] - mn));
      p1[i] = cvtpk(__builtin_amdgcn_exp2f(s[8 + 2 * i] - mn), __builtin_amdgcn_exp2f(s[9 + 2 * i] - mn));
      ps += (bflo(p0[i]) + bfhi(p0[i])) + (bflo(p1[i]) + bfhi(p1[i]));
    }
    l = l * al + ps; o *= al;
    o = __builtin_amdgcn_mfma_f32_32x32x16_bf16(v0, __builtin_bit_cast(bf16x8, p0), o, 0, 0, 0);
    o = __builtin_amdgcn_mfma_f32_32x32x16_bf16(v1, __builtin_bit_cast(bf16x8, p1), o, 0, 0, 0);
  }
  l += __shfl_xor(l, 32);
}
extern "C" __global__ __launch_bounds__(256, 1) void k_main(char* __restrict__ ws, const u16* __restrict__ x, const u16* __restrict__ wqkv, const u16* __restrict__ wo, const u16* __restrict__ wg, const u16* __restrict__ wu, const u16* __restrict__ wd, u16* __restrict__ out) {
  STAMP0
  const int wid = __builtin_amdgcn_readfirstlane(threadIdx.x >> 6);
  const int bid = blockIdx.x;
  const int xcd = bid & 7, rr = bid >> 3;
  CODE_PF(k_main, ktext_end, 6, 81664);
  __builtin_amdgcn_sched_barrier(0);
  const int gw = bid * 4 + wid, gx = 1 ? xcd * 128 + rr * 4 + wid : gw;
  {
    const unsigned l = threadIdx.x & 63, lc = min(l, 47u), lr = (lc * 43) >> 9, j = lc - lr * 12, p = j >> 2;
    const u32x2 a0 = {gx * 4096 + l * 16, 1 ? (((rr * 4 + wid) * 8 + (xcd & 4) + lr) * 48 + p * 16 + (((xcd & 3) * 4 + (j & 3) + p * 5) & 15)) * 128 : (gw * 48 + lc) * 128};
    asm volatile(
  "s_add_u32 m0, s30, 40960\n\t"
  "s_nop 0\n\t"
  "global_load_lds_dwordx4 v0, s[20:21] offset:0\n\t"
  "global_load_lds_dwordx4 v0, s[20:21] offset:1024\n\t"
  "global_load_lds_dwordx4 v0, s[20:21] offset:2048\n\t"
  "global_load_lds_dwordx4 v0, s[20:21] offset:3072\n\t"
  "s_mov_b32 m0, 81664\n\t"
  "s_nop 0\n\t"
  "global_load_lds_dword v1, s[24:25]\n\t"
     :: "{s[20:21]}"(x), "{s[24:25]}"(wqkv), "{s30}"(wid * 4096), "{v[0:1]}"(a0) : "memory", "scc"); }
  const u32x4 sws = mksrd(ws, WS_END);
  unsigned long long tag;
  const __amdgpu_buffer_rsrc_t rws = __builtin_amdgcn_make_buffer_rsrc(ws, 0, WS_END, 0x20000);
  const int om = (xcd >> 1) * 4 + (rr >> 3), on = (xcd & 1) + (rr & 7) * 2, kp = on & 1, kh = wid & 1, tf0 = om * 8 + (wid >> 1) * 4, tg = om * 2 + (wid >> 1);
  const int head = xcd * 4 + (rr >> 3), qblk = (rr & 7) * 8 + wid * 2;
  const int gm = (xcd >> 2) * 4 + (rr >> 3), gn = (xcd & 3) * 8 + (rr & 7), odd = gn & 1;
  constexpr unsigned FL = WS_SM + SM_FL, OOB = 0x7fffff00u, FS = 32, LV = 8, GS = 1024, QS_ = 128;
  u32x16 fv, wv; u32x8 fw; u32x2 wb2, wc, wx, we; unsigned ov, mv, ev, qv, pv, kv, bv, wm8, gp, dp; u32x8 tv; u32x16 rv; u32x4 rw;
  const unsigned u = rr * 4 + wid, nat = u - ((u * 147) >> 10) * 7 == 3;
  CODE_ADDR(kattb_begin, ktext_end, 5, ca, co)
  {
    const unsigned lane = lane_id(), l16 = lane * 16, lw = min(lane & 31, 27u), r = lane & 15, tk = tf0 + kh;
    ev = WS_EX + ((om * 16 + (on ^ 1)) * 4 + wid) * 4096 + l16;
    fv[0] = WS_H1 + (gm * 16 + wid * 4) * 1024 + l16;
    fv[1] = WS_WG + gn * 458752 + wid * 7168 + l16; fv[2] = fv[1] + 4096; fv[3] = l16; fv[4] = l16 + 57344;
    fv[5] = fv[0] + (unsigned)(WS_HH - WS_H1);
    fv[6] = WS_HH + (gm * 16 + wid * 4 + ((lane >> 4) & 1)) * 1024 + (odd * 2 + (lane >> 5)) * 256 + r * 16;
    fv[7] = co;
    fv[8] = lane == 0 ? FL + FL_SZ + tg * GS + (kh * 16 + on) * FS : OOB;
    fv[9] = FL + FL_SZ + (gm * 4 + wid) * GS + (lane & 31) * FS;
    fv[10] = lane == 0 ? FL + 2 * FL_SZ + (gm * 4 + wid) * GS + gn * FS : OOB;
    fv[11] = FL + 2 * FL_SZ + tg * GS + (kp * 16 + r) * FS; fv[12] = FL + 2 * FL_SZ + tg * GS + (lane & 31) * FS;
    fv[13] = WS_HH + tk * 1024 + l16;
    fv[14] = WS_WD + (on >> 1) * 917504 + (kp * 4 + wid) * 1024 + l16; fv[15] = (fv[14] ^ 4096) - 1024;
    fw[0] = 0; fw[1] = 0; fw[2] = ev; fw[3] = ev ^ 16384;
    fw[4] = WS_M2 + tk * 1024 + (lane >> 4) * 256 + r * 4; fw[5] = 98304 + l16; fw[6] = ((tk * 16 + r) * D_ + on * 64 + ((lane >> 4) & 1) * 16 + (lane >> 5) * 8) * 2; fw[7] = 32768 + l16;
    mv = lane < 16 ? WS_M2 + tk * 1024 + on * 64 + lane * 4 : OOB;
    wv[0] = (lane | wid) == 0 ? FL + 4 * FL_SZ + (((xcd & 3) * 4 + (rr & 3)) * 16 + ((xcd >> 2) * 8 + (rr >> 2))) * QS_ : OOB;
    kv = (lane | (wid ^ 3)) == 0 ? FL + 4 * FL_SZ + 32768 + bid * 8 : OOB;
    bv = lane == 0 ? WS_WB + ((gw & ~31) + ((gw >> 2) & 3) * 8 + ((gw >> 4) & 1) * 4 + (gw & 3)) * 2 : OOB;
    wv[1] = (wid * 32 + (lane >> 3)) * 2048 + on * 128 + ((lane & 7) ^ ((lane >> 3) & 6)) * 16;
    wv[2] = WS_WB + wid * 1024 + l16;
    wv[3] = lane == 0 ? FL + (qblk >> 1) * GS + ((head & 1) * 16 + (head >> 1)) * FS : OOB;
    wv[4] = FL + tg * GS + (kh * 16 + r) * FS;
    wv[5] = ((tk * 16 + r) * D_ + on * 64 + ((lane >> 4) & 1) * 16 + (lane >> 5) * 8) * 2; wv[6] = wv[5] + 32 * D_ * 2;
    wv[7] = WS_XM + (tk * 16 + r) * 16;
#pragma unroll
    for (int j = 0; j < 4; j++) { wv[8 + j] = (lane >> 2) * 128 + ((j ^ ((lane >> 3) & 3)) * 32) + (lane & 3) * 8; wv[12 + j] = 0; }
    ov = WS_O + ((head * 128 + qblk * 2 + ((lane >> 4) & 1)) * 64 + (lane >> 5) * 16 + r) * 16;
    wb2[0] = (lane | wid) == 0 ? FL + 3 * FL_SZ + (xcd * 32 + rr) * 64 : OOB;
    gp = FL + 3 * FL_SZ + ((gn >> 2) * 32 + lw) * 64; dp = FL + 3 * FL_SZ + ((on >> 1) * 32 + lw) * 64 + 8; wb2[1] = FL + 4 * FL_SZ + 32768 + (wid * 64 + lane) * 8;
    pv = FL + 4 * FL_SZ + (lane < 32 ? (((head >> 1) - (lane < 16 ? 1 : 2) * 5) & 15) * 16 + r : (head >> 1) * 16 + (rr & 7) * 2 + (lane & 1)) * QS_;
    wc[0] = 32768 + kh * 64 + (lane & 1) * 2048 + (lane >> 4) * 16; wc[1] = WS_H1 + on * 262144 + tk * 1024 + l16;
    wx[0] = WS_O + tk * 1024 + l16; wx[1] = wx[0] ^ 1024;
    we[0] = 36864 + wid * 5120 + l16; we[1] = 36864 + (wid ^ 1) * 5120 + l16;
    qv = WS_Q + (head * 64 + qblk) * 2048 + l16;
    asm volatile("" : "+a"(fv), "+a"(fw), "+a"(mv), "+{v219}"(ov), "+{v[220:221]}"(wb2), "+{v[222:223]}"(wc),
                      "+{v[224:239]}"(wv), "+{v244}"(qv), "+{v245}"(pv), "+{v[246:247]}"(wx), "+{v[248:249]}"(we), "+{v251}"(co), "+{v250}"(bv), "+{v29}"(kv), "+{v30}"(gp), "+a"(dp));
  }
  {
    const int lane = threadIdx.x & 63;
    const int bm = (xcd >> 2) * 8 + (rr >> 2), bn = (xcd & 3) * 4 + (rr & 3), wm = wid >> 1, wn = wid & 1, sg = wm ^ wn;
    const int jq = bn, jk = (bn + 5) & 15, jv = (bn + 2 * 5) & 15;
    const u32x4 sx = mksrd((const char*)x - 4096, S_ * D_ * 2 + 4096), sw = mksrd((const char*)wqkv - 4096, D_ * 3 * D_ * 2 + 4096);
    u32x16 av; u32x8 aw;
#pragma unroll
    for (int j = 0; j < 4; j++) av[j] = (bm * 128 + wid * 32 + j * 8 + (lane >> 3)) * 2048 + (((lane & 7) ^ ((j & 1) * 4 + (lane >> 4))) * 16) + 4096 - j * 1024;
    const unsigned wb = ((wid & 1) * 16 + (lane >> 5) * 8 + (wid >> 1) * 4 + ((lane >> 3) & 3)) * 6144 + ((lane & 7) ^ ((lane >> 3) & 6)) * 16 + 4096;
    av[4] = wb + jq * 128; av[5] = wb + jk * 128 + 1024; av[6] = wb + jv * 128 + 2048;
    av[7] = av[4] + 32 * 6144; av[8] = av[5] + 32 * 6144; av[9] = av[6] + 32 * 6144;
#pragma unroll
    for (int j = 0; j < 2; j++) av[10 + j] = (wm * 64 + (lane & 15)) * 128 + ((((j ^ sg) * 4 + (lane >> 4)) ^ ((lane >> 1) & 7)) * 16);
#pragma unroll
    for (int j = 0; j < 4; j++) av[12 + j] = 16384 + ((j >> 1) ^ sg) * 12288 + (lane >> 5) * 3072 + ((lane >> 2) & 7) * 128 + (((wn * 2 + (j & 1)) ^ ((lane >> 3) & 3)) * 32) + (lane & 3) * 8;
    aw[0] = (lane & 31) == 0 ? WS_XM + (gx * 2 + (lane >> 5)) * 16 : 0x7fffff00u; aw[1] = WS_XM + (bm * 128 + wm * 64 + lane) * 16;
    aw[2] = ((lane & 14) == ((lane >> 4) & 2)) ? 0x3f803f80u : 0u; aw[3] = (lane & 1) ? 0xEEEEEEEEu : 0x44444444u;
    aw[4] = 81920 + wid * 3072 + lane * 16; aw[5] = 40960 + wid * 4096 + (lane >> 5) * 2048 + (lane & 31) * 16; aw[6] = gw * 2048 + lane * 16;
    aw[7] = 0;
    const u32x4 so = mksrd(wo, D_ * D_ * 2);
    const unsigned m0x = wid * 4096, m0w = 16384 + wid * 3072;
    f32x32 cq, ck, cv; u32x4 gr;
    asm volatile(
  "s_load_dwordx2 s[36:37], s[46:47], 0x0\n\t"
  "s_mov_b32 s28, 0\n\t"
  "s_mov_b32 s29, 0\n\t"
  "s_waitcnt vmcnt(1)\n\t"
  "s_add_u32 m0, s44, 81920\n\t"
  "s_nop 0\n\t"
  "buffer_load_dwordx4 v4, s[24:27], s29 offen offset:0 lds\n\t"
  "buffer_load_dwordx4 v5, s[24:27], s29 offen offset:1024 lds\n\t"
  "buffer_load_dwordx4 v6, s[24:27], s29 offen offset:2048 lds\n\t"
  "s_add_u32 m0, s44, 94208\n\t"
  "s_nop 0\n\t"
  "buffer_load_dwordx4 v7, s[24:27], s29 offen offset:0 lds\n\t"
  "buffer_load_dwordx4 v8, s[24:27], s29 offen offset:1024 lds\n\t"
  "buffer_load_dwordx4 v9, s[24:27], s29 offen offset:2048 lds\n\t"
  "s_add_u32 s29, s29, 393216\n\t"
  "s_add_u32 m0, s30, 81920\n\t"
  "s_nop 0\n\t"
  "buffer_load_dwordx4 v0, s[20:23], s28 offen offset:0 lds\n\t"
  "buffer_load_dwordx4 v1, s[20:23], s28 offen offset:1024 lds\n\t"
  "buffer_load_dwordx4 v2, s[20:23], s28 offen offset:2048 lds\n\t"
  "buffer_load_dwordx4 v3, s[20:23], s28 offen offset:3072 lds\n\t"
  "s_add_u32 s28, s28, 128\n\t"
  "s_add_u32 m0, s44, 122880\n\t"
  "s_nop 0\n\t"
  "buffer_load_dwordx4 v4, s[24:27], s29 offen offset:0 lds\n\t"
  "buffer_load_dwordx4 v5, s[24:27], s29 offen offset:1024 lds\n\t"
  "buffer_load_dwordx4 v6, s[24:27], s29 offen offset:2048 lds\n\t"
  "s_add_u32 m0, s44, 135168\n\t"
  "s_nop 0\n\t"
  "buffer_load_dwordx4 v7, s[24:27], s29 offen offset:0 lds\n\t"
  "buffer_load_dwordx4 v8, s[24:27], s29 offen offset:1024 lds\n\t"
  "buffer_load_dwordx4 v9, s[24:27], s29 offen offset:2048 lds\n\t"
  "s_add_u32 s29, s29, 393216\n\t"
  "v_add_u32 v200, 40960, v10\n\t"
  "v_add_u32 v201, 40960, v11\n\t"
  "v_add_u32 v202, 40960, v12\n\t"
  "v_add_u32 v203, 40960, v13\n\t"
  "v_add_u32 v204, 40960, v14\n\t"
  "v_add_u32 v205, 40960, v15\n\t"
  "v_add_u32 v206, 81920, v10\n\t"
  "v_add_u32 v207, 81920, v11\n\t"
  "v_add_u32 v208, 81920, v12\n\t"
  "v_add_u32 v209, 81920, v13\n\t"
  "v_add_u32 v210, 81920, v14\n\t"
  "v_add_u32 v211, 81920, v15\n\t"
  "v_add_u32 v212, 122880, v10\n\t"
  "v_add_u32 v213, 122880, v11\n\t"
  "v_add_u32 v214, 122880, v12\n\t"
  "v_add_u32 v215, 122880, v13\n\t"
  "v_add_u32 v216, 122880, v14\n\t"
  "v_add_u32 v217, 122880, v15\n\t"
  "v_accvgpr_write_b32 a60, v18\n\t"
  "v_accvgpr_write_b32 a61, v18\n\t"
  "v_accvgpr_write_b32 a62, v18\n\t"
  "v_accvgpr_write_b32 a63, v18\n\t"
  "v_mov_b64 v[128:129], 0\n\t"
  "v_mov_b64 v[130:131], 0\n\t"
  "v_mov_b64 v[132:133], 0\n\t"
  "v_mov_b64 v[134:135], 0\n\t"
  "v_mov_b64 v[136:137], 0\n\t"
  "v_mov_b64 v[138:139], 0\n\t"
  "v_mov_b64 v[192:193], 0\n\t"
  "v_mov_b64 v[194:195], 0\n\t"
  "s_waitcnt vmcnt(17)\n\t"
  "ds_read_b128 v[32:35], v21 offset:0\n\t"
  "ds_read_b128 v[36:39], v21 offset:512\n\t"
  "ds_read_b128 v[40:43], v21 offset:1024\n\t"
  "ds_read_b128 v[44:47], v21 offset:1536\n\t"
  "s_waitcnt lgkmcnt(0)\n\t"
  "s_mul_i32 s34, s36, 0x7F4A7C15\n\t"
  "s_mul_hi_u32 s35, s36, 0x7F4A7C15\n\t"
  "s_mul_i32 s37, s37, 0x7F4A7C15\n\t"
  "s_mul_i32 s36, s36, 0x9E3779B9\n\t"
  "s_add_u32 s35, s35, s37\n\t"
  "s_add_u32 s35, s35, s36\n\t"
  "s_add_u32 s34, s34, 0xD192ED03\n\t"
  "s_addc_u32 s35, s35, 0xD1B54A32\n\t"
  "v_lshlrev_b32 v64, 16, v32\n\t"
  "v_and_b32 v65, 0xffff0000, v32\n\t"
  "v_lshlrev_b32 v66, 16, v33\n\t"
  "v_and_b32 v67, 0xffff0000, v33\n\t"
  "v_lshlrev_b32 v68, 16, v34\n\t"
  "v_and_b32 v69, 0xffff0000, v34\n\t"
  "v_lshlrev_b32 v70, 16, v35\n\t"
  "v_and_b32 v71, 0xffff0000, v35\n\t"
  "v_lshlrev_b32 v72, 16, v36\n\t"
  "v_and_b32 v73, 0xffff0000, v36\n\t"
  "v_lshlrev_b32 v74, 16, v37\n\t"
  "v_and_b32 v75, 0xffff0000, v37\n\t"
  "v_lshlrev_b32 v76, 16, v38\n\t"
  "v_and_b32 v77, 0xffff0000, v38\n\t"
  "v_lshlrev_b32 v78, 16, v39\n\t"
  "v_and_b32 v79, 0xffff0000, v39\n\t"
  "v_lshlrev_b32 v80, 16, v40\n\t"
  "v_and_b32 v81, 0xffff0000, v40\n\t"
  "v_lshlrev_b32 v82, 16, v41\n\t"
  "v_and_b32 v83, 0xffff0000, v41\n\t"
  "v_lshlrev_b32 v84, 16, v42\n\t"
  "v_and_b32 v85, 0xffff0000, v42\n\t"
  "v_lshlrev_b32 v86, 16, v43\n\t"
  "v_and_b32 v87, 0xffff0000, v43\n\t"
  "v_lshlrev_b32 v88, 16, v44\n\t"
  "v_and_b32 v89, 0xffff0000, v44\n\t"
  "v_lshlrev_b32 v90, 16, v45\n\t"
  "v_and_b32 v91, 0xffff0000, v45\n\t"
  "v_lshlrev_b32 v92, 16, v46\n\t"
  "v_and_b32 v93, 0xffff0000, v46\n\t"
  "v_lshlrev_b32 v94, 16, v47\n\t"
  "v_and_b32 v95, 0xffff0000, v47\n\t"
  "v_pk_add_f32 v[32:33], v[64:65], v[80:81]\n\t"
  "v_pk_add_f32 v[34:35], v[66:67], v[82:83]\n\t"
  "v_pk_add_f32 v[36:37], v[68:69], v[84:85]\n\t"
  "v_pk_add_f32 v[38:39], v[70:71], v[86:87]\n\t"
  "v_pk_add_f32 v[40:41], v[72:73], v[88:89]\n\t"
  "v_pk_add_f32 v[42:43], v[74:75], v[90:91]\n\t"
  "v_pk_add_f32 v[44:45], v[76:77], v[92:93]\n\t"
  "v_pk_add_f32 v[46:47], v[78:79], v[94:95]\n\t"
  "v_pk_add_f32 v[32:33], v[32:33], v[40:41]\n\t"
  "v_pk_add_f32 v[34:35], v[34:35], v[42:43]\n\t"
  "v_pk_add_f32 v[36:37], v[36:37], v[44:45]\n\t"
  "v_pk_add_f32 v[38:39], v[38:39], v[46:47]\n\t"
  "v_pk_add_f32 v[32:33], v[32:33], v[36:37]\n\t"
  "v_pk_add_f32 v[34:35], v[34:35], v[38:39]\n\t"
  "v_pk_add_f32 v[32:33], v[32:33], v[34:35]\n\t"
  "v_add_f32 v32, v32, v33\n\t"
  "s_nop 1\n\t"
  "v_add_f32_dpp v32, v32, v32 quad_perm:[1,0,3,2] row_mask:0xf bank_mask:0xf\n\t"
  "s_nop 1\n\t"
  "v_add_f32_dpp v32, v32, v32 quad_perm:[2,3,0,1] row_mask:0xf bank_mask:0xf\n\t"
  "s_nop 1\n\t"
  "v_add_f32_dpp v32, v32, v32 row_half_mirror row_mask:0xf bank_mask:0xf\n\t"
  "s_nop 1\n\t"
  "v_add_f32_dpp v32, v32, v32 row_mirror row_mask:0xf bank_mask:0xf\n\t"
  "v_mov_b32 v33, v32\n\t"
  "s_nop 1\n\t"
  "v_permlane16_swap_b32 v32, v33\n\t"
  "s_nop 1\n\t"
  "v_add_f32 v32, v32, v33\n\t"
  "v_mul_f32 v140, 0x3a800000, v32\n\t"
  "v_pk_add_f32 v[64:65], v[64:65], v[140:141] op_sel_hi:[1,0] neg_lo:[0,1] neg_hi:[0,1]\n\t"
  "v_pk_add_f32 v[66:67], v[66:67], v[140:141] op_sel_hi:[1,0] neg_lo:[0,1] neg_hi:[0,1]\n\t"
  "v_pk_add_f32 v[68:69], v[68:69], v[140:141] op_sel_hi:[1,0] neg_lo:[0,1] neg_hi:[0,1]\n\t"
  "v_pk_add_f32 v[70:71], v[70:71], v[140:141] op_sel_hi:[1,0] neg_lo:[0,1] neg_hi:[0,1]\n\t"
  "v_pk_add_f32 v[72:73], v[72:73], v[140:141] op_sel_hi:[1,0] neg_lo:[0,1] neg_hi:[0,1]\n\t"
  "v_pk_add_f32 v[74:75], v[74:75], v[140:141] op_sel_hi:[1,0] neg_lo:[0,1] neg_hi:[0,1]\n\t"
  "v_pk_add_f32 v[76:77], v[76:77], v[140:141] op_sel_hi:[1,0] neg_lo:[0,1] neg_hi:[0,1]\n\t"
  "v_pk_add_f32 v[78:79], v[78:79], v[140:141] op_sel_hi:[1,0] neg_lo:[0,1] neg_hi:[0,1]\n\t"
  "v_pk_add_f32 v[80:81], v[80:81], v[140:141] op_sel_hi:[1,0] neg_lo:[0,1] neg_hi:[0,1]\n\t"
  "v_pk_add_f32 v[82:83], v[82:83], v[140:141] op_sel_hi:[1,0] neg_lo:[0,1] neg_hi:[0,1]\n\t"
  "v_pk_add_f32 v[84:85], v[84:85], v[140:141] op_sel_hi:[1,0] neg_lo:[0,1] neg_hi:[0,1]\n\t"
  "v_pk_add_f32 v[86:87], v[86:87], v[140:141] op_sel_hi:[1,0] neg_lo:[0,1] neg_hi:[0,1]\n\t"
  "v_pk_add_f32 v[88:89], v[88:89], v[140:141] op_sel_hi:[1,0] neg_lo:[0,1] neg_hi:[0,1]\n\t"
  "v_pk_add_f32 v[90:91], v[90:91], v[140:141] op_sel_hi:[1,0] neg_lo:[0,1] neg_hi:[0,1]\n\t"
  "v_pk_add_f32 v[92:93], v[92:93], v[140:141] op_sel_hi:[1,0] neg_lo:[0,1] neg_hi:[0,1]\n\t"
  "v_pk_add_f32 v[94:95], v[94:95], v[140:141] op_sel_hi:[1,0] neg_lo:[0,1] neg_hi:[0,1]\n\t"
  "v_pk_mul_f32 v[32:33], v[64:65], v[64:65]\n\t"
  "v_pk_mul_f32 v[34:35], v[66:67], v[66:67]\n\t"
  "v_pk_fma_f32 v[32:33], v[68:69], v[68:69], v[32:33]\n\t"
  "v_pk_fma_f32 v[34:35], v[70:71], v[70:71], v[34:35]\n\t"
  "v_pk_fma_f32 v[32:33], v[72:73], v[72:73], v[32:33]\n\t"
  "v_pk_fma_f32 v[34:35], v[74:75], v[74:75], v[34:35]\n\t"
  "v_pk_fma_f32 v[32:33], v[76:77], v[76:77], v[32:33]\n\t"
  "v_pk_fma_f32 v[34:35], v[78:79], v[78:79], v[34:35]\n\t"
  "v_pk_fma_f32 v[32:33], v[80:81], v[80:81], v[32:33]\n\t"
  "v_pk_fma_f32 v[34:35], v[82:83], v[82:83], v[34:35]\n\t"
  "v_pk_fma_f32 v[32:33], v[84:85], v[84:85], v[32:33]\n\t"
  "v_pk_fma_f32 v[34:35], v[86:87], v[86:87], v[34:35]\n\t"
  "v_pk_fma_f32 v[32:33], v[88:89], v[88:89], v[32:33]\n\t"
  "v_pk_fma_f32 v[34:35], v[90:91], v[90:91], v[34:35]\n\t"
  "v_pk_fma_f32 v[32:33], v[92:93], v[92:93], v[32:33]\n\t"
  "v_pk_fma_f32 v[34:35], v[94:95], v[94:95], v[34:35]\n\t"
  "v_pk_add_f32 v[32:33], v[32:33], v[34:35]\n\t"
  "v_add_f32 v32, v32, v33\n\t"
  "s_nop 1\n\t"
  "v_add_f32_dpp v32, v32, v32 quad_perm:[1,0,3,2] row_mask:0xf bank_mask:0xf\n\t"
  "s_nop 1\n\t"
  "v_add_f32_dpp v32, v32, v32 quad_perm:[2,3,0,1] row_mask:0xf bank_mask:0xf\n\t"
  "s_nop 1\n\t"
  "v_add_f32_dpp v32, v32, v32 row_half_mirror row_mask:0xf bank_mask:0xf\n\t"
  "s_nop 1\n\t"
  "v_add_f32_dpp v32, v32, v32 row_mirror row_mask:0xf bank_mask:0xf\n\t"
  "v_mov_b32 v33, v32\n\t"
  "s_nop 1\n\t"
  "v_permlane16_swap_b32 v32, v33\n\t"
  "s_nop 1\n\t"
  "v_add_f32 v32, v32, v33\n\t"
  "v_mov_b32 v142, 0x3727c5ac\n\t"
  "v_fmac_f32 v142, 0x3a800000, v32\n\t"
  "v_mov_b32 v141, s34\n\t"
  "v_rsq_f32 v142, v142\n\t"
  "v_mov_b32 v143, s34\n\t"
  "buffer_store_dwordx4 v[140:143], v16, s[52:55], 0 offen sc1\n\t"
  "s_add_u32 m0, s30, 122880\n\t"
  "s_nop 0\n\t"
  "buffer_load_dwordx4 v0, s[20:23], s28 offen offset:0 lds\n\t"
  "buffer_load_dwordx4 v1, s[20:23], s28 offen offset:1024 lds\n\t"
  "buffer_load_dwordx4 v2, s[20:23], s28 offen offset:2048 lds\n\t"
  "buffer_load_dwordx4 v3, s[20:23], s28 offen offset:3072 lds\n\t"
  "s_add_u32 s28, s28, 128\n\t"
  "s_add_u32 m0, s44, 0\n\t"
  "s_nop 0\n\t"
  "buffer_load_dwordx4 v4, s[24:27], s29 offen offset:0 lds\n\t"
  "buffer_load_dwordx4 v5, s[24:27], s29 offen offset:1024 lds\n\t"
  "buffer_load_dwordx4 v6, s[24:27], s29 offen offset:2048 lds\n\t"
  "s_add_u32 m0, s44, 12288\n\t"
  "s_nop 0\n\t"
  "buffer_load_dwordx4 v7, s[24:27], s29 offen offset:0 lds\n\t"
  "buffer_load_dwordx4 v8, s[24:27], s29 offen offset:1024 lds\n\t"
  "s_waitcnt vmcnt(16)\n\t"
  "s_barrier\n\t"
  "ds_read_b128 a[0:3], v206 offset:0\n\t"
  "ds_read_b128 a[4:7], v206 offset:2048\n\t"
  "ds_read_b128 a[8:11], v206 offset:4096\n\t"
  "ds_read_b128 a[12:15], v206 offset:6144\n\t"
  "ds_read_b64_tr_b16 a[32:33], v208 offset:0\n\t"
  "ds_read_b64_tr_b16 a[34:35], v208 offset:6144\n\t"
  "ds_read_b64_tr_b16 a[36:37], v209 offset:0\n\t"
  "ds_read_b64_tr_b16 a[38:39], v209 offset:6144\n\t"
  "ds_read_b64_tr_b16 a[40:41], v208 offset:1024\n\t"
  "ds_read_b64_tr_b16 a[42:43], v208 offset:7168\n\t"
  "s_waitcnt lgkmcnt(4)\n\t"
  "v_mfma_f32_16x16x32_bf16 v[32:35], a[32:35], a[0:3], 0\n\t"
  "ds_read_b64_tr_b16 a[44:45], v209 offset:1024\n\t"
  "v_mfma_f32_16x16x32_bf16 v[36:39], a[32:35], a[4:7], 0\n\t"
  "ds_read_b64_tr_b16 a[46:47], v209 offset:7168\n\t"
  "v_mfma_f32_16x16x32_bf16 v[40:43], a[32:35], a[8:11], 0\n\t"
  "v_mfma_f32_16x16x32_bf16 v[44:47], a[32:35], a[12:15], 0\n\t"
  "s_waitcnt lgkmcnt(4)\n\t"
  "v_mfma_f32_16x16x32_bf16 v[60:63], a[36:39], a[12:15], 0\n\t"
  "ds_read_b64_tr_b16 a[48:49], v208 offset:2048\n\t"
  "v_mfma_f32_16x16x32_bf16 v[56:59], a[36:39], a[8:11], 0\n\t"
  "ds_read_b64_tr_b16 a[50:51], v208 offset:8192\n\t"
  "v_mfma_f32_16x16x32_bf16 v[52:55], a[36:39], a[4:7], 0\n\t"
  "buffer_load_dwordx4 v9, s[24:27], s29 offen offset:2048 lds\n\t"
  "v_mfma_f32_16x16x32_bf16 v[48:51], a[36:39], a[0:3], 0\n\t"
  "ds_read_b128 a[16:19], v207 offset:0\n\t"
  "v_smfmac_f32_16x16x64_bf16 v[128:131], a[60:63], a[32:39], v19\n\t"
  "s_add_u32 s29, s29, 393216\n\t"
  "s_waitcnt lgkmcnt(5)\n\t"
  "v_mfma_f32_16x16x32_bf16 v[64:67], a[40:43], a[0:3], 0\n\t"
  "ds_read_b64_tr_b16 a[52:53], v209 offset:2048\n\t"
  "v_mfma_f32_16x16x32_bf16 v[68:71], a[40:43], a[4:7], 0\n\t"
  "ds_read_b64_tr_b16 a[54:55], v209 offset:8192\n\t"
  "s_add_u32 m0, s30, 0\n\t"
  "v_mfma_f32_16x16x32_bf16 v[72:75], a[40:43], a[8:11], 0\n\t"
  "buffer_load_dwordx4 v0, s[20:23], s28 offen offset:0 lds\n\t"
  "v_mfma_f32_16x16x32_bf16 v[76:79], a[40:43], a[12:15], 0\n\t"
  "ds_read_b128 a[20:23], v207 offset:2048\n\t"
  "s_waitcnt lgkmcnt(6)\n\t"
  "v_mfma_f32_16x16x32_bf16 v[92:95], a[44:47], a[12:15], 0\n\t"
  "ds_read_b64_tr_b16 a[32:33], v210 offset:0\n\t"
  "v_mfma_f32_16x16x32_bf16 v[88:91], a[44:47], a[8:11], 0\n\t"
  "ds_read_b64_tr_b16 a[34:35], v210 offset:6144\n\t"
  "v_mfma_f32_16x16x32_bf16 v[84:87], a[44:47], a[4:7], 0\n\t"
  "buffer_load_dwordx4 v1, s[20:23], s28 offen offset:1024 lds\n\t"
  "v_mfma_f32_16x16x32_bf16 v[80:83], a[44:47], a[0:3], 0\n\t"
  "ds_read_b128 a[24:27], v207 offset:4096\n\t"
  "v_smfmac_f32_16x16x64_bf16 v[132:135], a[60:63], a[40:47], v19\n\t"
  "s_waitcnt lgkmcnt(7)\n\t"
  "v_mfma_f32_16x16x32_bf16 v[96:99], a[0:3], a[48:51], 0\n\t"
  "ds_read_b64_tr_b16 a[36:37], v211 offset:0\n\t"
  "v_mfma_f32_16x16x32_bf16 v[100:103], a[4:7], a[48:51], 0\n\t"
  "ds_read_b64_tr_b16 a[38:39], v211 offset:6144\n\t"
  "v_mfma_f32_16x16x32_bf16 v[104:107], a[8:11], a[48:51], 0\n\t"
  "buffer_load_dwordx4 v2, s[20:23], s28 offen offset:2048 lds\n\t"
  "v_mfma_f32_16x16x32_bf16 v[108:111], a[12:15], a[48:51], 0\n\t"
  "ds_read_b128 a[28:31], v207 offset:6144\n\t"
  "s_waitcnt lgkmcnt(7)\n\t"
  "v_mfma_f32_16x16x32_bf16 v[124:127], a[12:15], a[52:55], 0\n\t"
  "ds_read_b64_tr_b16 a[40:41], v210 offset:1024\n\t"
  "v_mfma_f32_16x16x32_bf16 v[120:123], a[8:11], a[52:55], 0\n\t"
  "ds_read_b64_tr_b16 a[42:43], v210 offset:7168\n\t"
  "v_mfma_f32_16x16x32_bf16 v[116:119], a[4:7], a[52:55], 0\n\t"
  "buffer_load_dwordx4 v3, s[20:23], s28 offen offset:3072 lds\n\t"
  "v_mfma_f32_16x16x32_bf16 v[112:115], a[0:3], a[52:55], 0\n\t"
  "v_smfmac_f32_16x16x64_bf16 v[136:139], a[60:63], a[48:55], v19\n\t"
  "s_add_u32 s28, s28, 128\n\t"
  "s_waitcnt lgkmcnt(2)\n\t"
  "v_mfma_f32_16x16x32_bf16 v[32:35], a[32:35], a[16:19], v[32:35]\n\t"
  "ds_read_b64_tr_b16 a[44:45], v211 offset:1024\n\t"
  "v_mfma_f32_16x16x32_bf16 v[36:39], a[32:35], a[20:23], v[36:39]\n\t"
  "ds_read_b64_tr_b16 a[46:47], v211 offset:7168\n\t"
  "s_waitcnt vmcnt(10)\n\t"
  "v_mfma_f32_16x16x32_bf16 v[40:43], a[32:35], a[24:27], v[40:43]\n\t"
  "s_barrier\n\t"
  "v_mfma_f32_16x16x32_bf16 v[44:47], a[32:35], a[28:31], v[44:47]\n\t"
  "v_mfma_f32_16x16x32_bf16 v[60:63], a[36:39], a[28:31], v[60:63]\n\t"
  "ds_read_b64_tr_b16 a[48:49], v210 offset:2048\n\t"
  "v_mfma_f32_16x16x32_bf16 v[56:59], a[36:39], a[24:27], v[56:59]\n\t"
  "ds_read_b64_tr_b16 a[50:51], v210 offset:8192\n\t"
  "s_add_u32 m0, s44, 40960\n\t"
  "v_mfma_f32_16x16x32_bf16 v[52:55], a[36:39], a[20:23], v[52:55]\n\t"
  "buffer_load_dwordx4 v4, s[24:27], s29 offen offset:0 lds\n\t"
  "v_mfma_f32_16x16x32_bf16 v[48:51], a[36:39], a[16:19], v[48:51]\n\t"
  "ds_read_b128 a[0:3], v212 offset:0\n\t"
  "s_waitcnt lgkmcnt(5)\n\t"
  "v_mfma_f32_16x16x32_bf16 v[64:67], a[40:43], a[16:19], v[64:67]\n\t"
  "ds_read_b64_tr_b16 a[52:53], v211 offset:2048\n\t"
  "v_mfma_f32_16x16x32_bf16 v[68:71], a[40:43], a[20:23], v[68:71]\n\t"
  "ds_read_b64_tr_b16 a[54:55], v211 offset:8192\n\t"
  "v_mfma_f32_16x16x32_bf16 v[72:75], a[40:43], a[24:27], v[72:75]\n\t"
  "buffer_load_dwordx4 v5, s[24:27], s29 offen offset:1024 lds\n\t"
  "v_mfma_f32_16x16x32_bf16 v[76:79], a[40:43], a[28:31], v[76:79]\n\t"
  "ds_read_b128 a[4:7], v212 offset:2048\n\t"
  "s_waitcnt lgkmcnt(6)\n\t"
  "v_mfma_f32_16x16x32_bf16 v[92:95], a[44:47], a[28:31], v[92:95]\n\t"
  "ds_read_b64_tr_b16 a[32:33], v214 offset:0\n\t"
  "v_mfma_f32_16x16x32_bf16 v[88:91], a[44:47], a[24:27], v[88:91]\n\t"
  "ds_read_b64_tr_b16 a[34:35], v214 offset:6144\n\t"
  "v_mfma_f32_16x16x32_bf16 v[84:87], a[44:47], a[20:23], v[84:87]\n\t"
  "buffer_load_dwordx4 v6, s[24:27], s29 offen offset:2048 lds\n\t"
  "v_mfma_f32_16x16x32_bf16 v[80:83], a[44:47], a[16:19], v[80:83]\n\t"
  "ds_read_b128 a[8:11], v212 offset:4096\n\t"
  "s_waitcnt lgkmcnt(7)\n\t"
  "v_mfma_f32_16x16x32_bf16 v[96:99], a[16:19], a[48:51], v[96:99]\n\t"
  "ds_read_b64_tr_b16 a[36:37], v215 offset:0\n\t"
  "v_mfma_f32_16x16x32_bf16 v[100:103], a[20:23], a[48:51], v[100:103]\n\t"
  "ds_read_b64_tr_b16 a[38:39], v215 offset:6144\n\t"
  "s_add_u32 m0, s44, 53248\n\t"
  "v_mfma_f32_16x16x32_bf16 v[104:107], a[24:27], a[48:51], v[104:107]\n\t"
  "buffer_load_dwordx4 v7, s[24:27], s29 offen offset:0 lds\n\t"
  "v_mfma_f32_16x16x32_bf16 v[108:111], a[28:31], a[48:51], v[108:111]\n\t"
  "ds_read_b128 a[12:15], v212 offset:6144\n\t"
  "s_waitcnt lgkmcnt(7)\n\t"
  "v_mfma_f32_16x16x32_bf16 v[124:127], a[28:31], a[52:55], v[124:127]\n\t"
  "ds_read_b64_tr_b16 a[40:41], v214 offset:1024\n\t"
  "v_mfma_f32_16x16x32_bf16 v[120:123], a[24:27], a[52:55], v[120:123]\n\t"
  "ds_read_b64_tr_b16 a[42:43], v214 offset:7168\n\t"
  "v_mfma_f32_16x16x32_bf16 v[116:119], a[20:23], a[52:55], v[116:119]\n\t"
  "buffer_load_dwordx4 v8, s[24:27], s29 offen offset:1024 lds\n\t"
  "v_mfma_f32_16x16x32_bf16 v[112:115], a[16:19], a[52:55], v[112:115]\n\t"
  "s_mov_b32 s31, 3\n\t"
  "LQKV_%=:\n\t"
  "s_waitcnt lgkmcnt(2)\n\t"
  "v_mfma_f32_16x16x32_bf16 v[32:35], a[32:35], a[0:3], v[32:35]\n\t"
  "ds_read_b64_tr_b16 a[44:45], v215 offset:1024\n\t"
  "v_mfma_f32_16x16x32_bf16 v[36:39], a[32:35], a[4:7], v[36:39]\n\t"
  "ds_read_b64_tr_b16 a[46:47], v215 offset:7168\n\t"
  "v_mfma_f32_16x16x32_bf16 v[40:43], a[32:35], a[8:11], v[40:43]\n\t"
  "v_mfma_f32_16x16x32_bf16 v[44:47], a[32:35], a[12:15], v[44:47]\n\t"
  "v_mfma_f32_16x16x32_bf16 v[60:63], a[36:39], a[12:15], v[60:63]\n\t"
  "ds_read_b64_tr_b16 a[48:49], v214 offset:2048\n\t"
  "v_mfma_f32_16x16x32_bf16 v[56:59], a[36:39], a[8:11], v[56:59]\n\t"
  "ds_read_b64_tr_b16 a[50:51], v214 offset:8192\n\t"
  "v_mfma_f32_16x16x32_bf16 v[52:55], a[36:39], a[4:7], v[52:55]\n\t"
  "buffer_load_dwordx4 v9, s[24:27], s29 offen offset:2048 lds\n\t"
  "v_mfma_f32_16x16x32_bf16 v[48:51], a[36:39], a[0:3], v[48:51]\n\t"
  "ds_read_b128 a[16:19], v213 offset:0\n\t"
  "v_smfmac_f32_16x16x64_bf16 v[128:131], a[60:63], a[32:39], v19\n\t"
  "s_add_u32 s29, s29, 393216\n\t"
  "s_waitcnt lgkmcnt(5)\n\t"
  "v_mfma_f32_16x16x32_bf16 v[64:67], a[40:43], a[0:3], v[64:67]\n\t"
  "ds_read_b64_tr_b16 a[52:53], v215 offset:2048\n\t"
  "v_mfma_f32_16x16x32_bf16 v[68:71], a[40:43], a[4:7], v[68:71]\n\t"
  "ds_read_b64_tr_b16 a[54:55], v215 offset:8192\n\t"
  "s_add_u32 m0, s30, 40960\n\t"
  "v_mfma_f32_16x16x32_bf16 v[72:75], a[40:43], a[8:11], v[72:75]\n\t"
  "buffer_load_dwordx4 v0, s[20:23], s28 offen offset:0 lds\n\t"
  "v_mfma_f32_16x16x32_bf16 v[76:79], a[40:43], a[12:15], v[76:79]\n\t"
  "ds_read_b128 a[20:23], v213 offset:2048\n\t"
  "s_waitcnt lgkmcnt(6)\n\t"
  "v_mfma_f32_16x16x32_bf16 v[92:95], a[44:47], a[12:15], v[92:95]\n\t"
  "ds_read_b64_tr_b16 a[32:33], v216 offset:0\n\t"
  "v_mfma_f32_16x16x32_bf16 v[88:91], a[44:47], a[8:11], v[88:91]\n\t"
  "ds_read_b64_tr_b16 a[34:35], v216 offset:6144\n\t"
  "v_mfma_f32_16x16x32_bf16 v[84:87], a[44:47], a[4:7], v[84:87]\n\t"
  "buffer_load_dwordx4 v1, s[20:23], s28 offen offset:1024 lds\n\t"
  "v_mfma_f32_16x16x32_bf16 v[80:83], a[44:47], a[0:3], v[80:83]\n\t"
  "ds_read_b128 a[24:27], v213 offset:4096\n\t"
  "v_smfmac_f32_16x16x64_bf16 v[132:135], a[60:63], a[40:47], v19\n\t"
  "s_waitcnt lgkmcnt(7)\n\t"
  "v_mfma_f32_16x16x32_bf16 v[96:99], a[0:3], a[48:51], v[96:99]\n\t"
  "ds_read_b64_tr_b16 a[36:37], v217 offset:0\n\t"
  "v_mfma_f32_16x16x32_bf16 v[100:103], a[4:7], a[48:51], v[100:103]\n\t"
  "ds_read_b64_tr_b16 a[38:39], v217 offset:6144\n\t"
  "v_mfma_f32_16x16x32_bf16 v[104:107], a[8:11], a[48:51], v[104:107]\n\t"
  "buffer_load_dwordx4 v2, s[20:23], s28 offen offset:2048 lds\n\t"
  "v_mfma_f32_16x16x32_bf16 v[108:111], a[12:15], a[48:51], v[108:111]\n\t"
  "ds_read_b128 a[28:31], v213 offset:6144\n\t"
  "s_waitcnt lgkmcnt(7)\n\t"
  "v_mfma_f32_16x16x32_bf16 v[124:127], a[12:15], a[52:55], v[124:127]\n\t"
  "ds_read_b64_tr_b16 a[40:41], v216 offset:1024\n\t"
  "v_mfma_f32_16x16x32_bf16 v[120:123], a[8:11], a[52:55], v[120:123]\n\t"
  "ds_read_b64_tr_b16 a[42:43], v216 offset:7168\n\t"
  "v_mfma_f32_16x16x32_bf16 v[116:119], a[4:7], a[52:55], v[116:119]\n\t"
  "buffer_load_dwordx4 v3, s[20:23], s28 offen offset:3072 lds\n\t"
  "v_mfma_f32_16x16x32_bf16 v[112:115], a[0:3], a[52:55], v[112:115]\n\t"
  "v_smfmac_f32_16x16x64_bf16 v[136:139], a[60:63], a[48:55], v19\n\t"
  "s_add_u32 s28, s28, 128\n\t"
  "s_waitcnt lgkmcnt(2)\n\t"
  "v_mfma_f32_16x16x32_bf16 v[32:35], a[32:35], a[16:19], v[32:35]\n\t"
  "ds_read_b64_tr_b16 a[44:45], v217 offset:1024\n\t"
  "v_mfma_f32_16x16x32_bf16 v[36:39], a[32:35], a[20:23], v[36:39]\n\t"
  "ds_read_b64_tr_b16 a[46:47], v217 offset:7168\n\t"
  "s_waitcnt vmcnt(10)\n\t"
  "v_mfma_f32_16x16x32_bf16 v[40:43], a[32:35], a[24:27], v[40:43]\n\t"
  "s_barrier\n\t"
  "v_mfma_f32_16x16x32_bf16 v[44:47], a[32:35], a[28:31], v[44:47]\n\t"
  "v_mfma_f32_16x16x32_bf16 v[60:63], a[36:39], a[28:31], v[60:63]\n\t"
  "ds_read_b64_tr_b16 a[48:49], v216 offset:2048\n\t"
  "v_mfma_f32_16x16x32_bf16 v[56:59], a[36:39], a[24:27], v[56:59]\n\t"
  "ds_read_b64_tr_b16 a[50:51], v216 offset:8192\n\t"
  "s_add_u32 m0, s44, 81920\n\t"
  "v_mfma_f32_16x16x32_bf16 v[52:55], a[36:39], a[20:23], v[52:55]\n\t"
  "buffer_load_dwordx4 v4, s[24:27], s29 offen offset:0 lds\n\t"
  "v_mfma_f32_16x16x32_bf16 v[48:51], a[36:39], a[16:19], v[48:51]\n\t"
  "ds_read_b128 a[0:3], v10 offset:0\n\t"
  "s_waitcnt lgkmcnt(5)\n\t"
  "v_mfma_f32_16x16x32_bf16 v[64:67], a[40:43], a[16:19], v[64:67]\n\t"
  "ds_read_b64_tr_b16 a[52:53], v217 offset:2048\n\t"
  "v_mfma_f32_16x16x32_bf16 v[68:71], a[40:43], a[20:23], v[68:71]\n\t"
  "ds_read_b64_tr_b16 a[54:55], v217 offset:8192\n\t"
  "v_mfma_f32_16x16x32_bf16 v[72:75], a[40:43], a[24:27], v[72:75]\n\t"
  "buffer_load_dwordx4 v5, s[24:27], s29 offen offset:1024 lds\n\t"
  "v_mfma_f32_16x16x32_bf16 v[76:79], a[40:43], a[28:31], v[76:79]\n\t"
  "ds_read_b128 a[4:7], v10 offset:2048\n\t"
  "s_waitcnt lgkmcnt(6)\n\t"
  "v_mfma_f32_16x16x32_bf16 v[92:95], a[44:47], a[28:31], v[92:95]\n\t"
  "ds_read_b64_tr_b16 a[32:33], v12 offset:0\n\t"
  "v_mfma_f32_16x16x32_bf16 v[88:91], a[44:47], a[24:27], v[88:91]\n\t"
  "ds_read_b64_tr_b16 a[34:35], v12 offset:6144\n\t"
  "v_mfma_f32_16x16x32_bf16 v[84:87], a[44:47], a[20:23], v[84:87]\n\t"
  "buffer_load_dwordx4 v6, s[24:27], s29 offen offset:2048 lds\n\t"
  "v_mfma_f32_16x16x32_bf16 v[80:83], a[44:47], a[16:19], v[80:83]\n\t"
  "ds_read_b128 a[8:11], v10 offset:4096\n\t"
  "s_waitcnt lgkmcnt(7)\n\t"
  "v_mfma_f32_16x16x32_bf16 v[96:99], a[16:19], a[48:51], v[96:99]\n\t"
  "ds_read_b64_tr_b16 a[36:37], v13 offset:0\n\t"
  "v_mfma_f32_16x16x32_bf16 v[100:103], a[20:23], a[48:51], v[100:103]\n\t"
  "ds_read_b64_tr_b16 a[38:39], v13 offset:6144\n\t"
  "s_add_u32 m0, s44, 94208\n\t"
  "v_mfma_f32_16x16x32_bf16 v[104:107], a[24:27], a[48:51], v[104:107]\n\t"
  "buffer_load_dwordx4 v7, s[24:27], s29 offen offset:0 lds\n\t"
  "v_mfma_f32_16x16x32_bf16 v[108:111], a[28:31], a[48:51], v[108:111]\n\t"
  "ds_read_b128 a[12:15], v10 offset:6144\n\t"
  "s_waitcnt lgkmcnt(7)\n\t"
  "v_mfma_f32_16x16x32_bf16 v[124:127], a[28:31], a[52:55], v[124:127]\n\t"
  "ds_read_b64_tr_b16 a[40:41], v12 offset:1024\n\t"
  "v_mfma_f32_16x16x32_bf16 v[120:123], a[24:27], a[52:55], v[120:123]\n\t"
  "ds_read_b64_tr_b16 a[42:43], v12 offset:7168\n\t"
  "v_mfma_f32_16x16x32_bf16 v[116:119], a[20:23], a[52:55], v[116:119]\n\t"
  "buffer_load_dwordx4 v8, s[24:27], s29 offen offset:1024 lds\n\t"
  "v_mfma_f32_16x16x32_bf16 v[112:115], a[16:19], a[52:55], v[112:115]\n\t"
  "s_waitcnt lgkmcnt(2)\n\t"
  "v_mfma_f32_16x16x32_bf16 v[32:35], a[32:35], a[0:3], v[32:35]\n\t"
  "ds_read_b64_tr_b16 a[44:45], v13 offset:1024\n\t"
  "v_mfma_f32_16x16x32_bf16 v[36:39], a[32:35], a[4:7], v[36:39]\n\t"
  "ds_read_b64_tr_b16 a[46:47], v13 offset:7168\n\t"
  "v_mfma_f32_16x16x32_bf16 v[40:43], a[32:35], a[8:11], v[40:43]\n\t"
  "v_mfma_f32_16x16x32_bf16 v[44:47], a[32:35], a[12:15], v[44:47]\n\t"
  "v_mfma_f32_16x16x32_bf16 v[60:63], a[36:39], a[12:15], v[60:63]\n\t"
  "ds_read_b64_tr_b16 a[48:49], v12 offset:2048\n\t"
  "v_mfma_f32_16x16x32_bf16 v[56:59], a[36:39], a[8:11], v[56:59]\n\t"
  "ds_read_b64_tr_b16 a[50:51], v12 offset:8192\n\t"
  "v_mfma_f32_16x16x32_bf16 v[52:55], a[36:39], a[4:7], v[52:55]\n\t"
  "buffer_load_dwordx4 v9, s[24:27], s29 offen offset:2048 lds\n\t"
  "v_mfma_f32_16x16x32_bf16 v[48:51], a[36:39], a[0:3], v[48:51]\n\t"
  "ds_read_b128 a[16:19], v11 offset:0\n\t"
  "v_smfmac_f32_16x16x64_bf16 v[128:131], a[60:63], a[32:39], v19\n\t"
  "s_add_u32 s29, s29, 393216\n\t"
  "s_waitcnt lgkmcnt(5)\n\t"
  "v_mfma_f32_16x16x32_bf16 v[64:67], a[40:43], a[0:3], v[64:67]\n\t"
  "ds_read_b64_tr_b16 a[52:53], v13 offset:2048\n\t"
  "v_mfma_f32_16x16x32_bf16 v[68:71], a[40:43], a[4:7], v[68:71]\n\t"
  "ds_read_b64_tr_b16 a[54:55], v13 offset:8192\n\t"
  "s_add_u32 m0, s30, 81920\n\t"
  "v_mfma_f32_16x16x32_bf16 v[72:75], a[40:43], a[8:11], v[72:75]\n\t"
  "buffer_load_dwordx4 v0, s[20:23], s28 offen offset:0 lds\n\t"
  "v_mfma_f32_16x16x32_bf16 v[76:79], a[40:43], a[12:15], v[76:79]\n\t"
  "ds_read_b128 a[20:23], v11 offset:2048\n\t"
  "s_waitcnt lgkmcnt(6)\n\t"
  "v_mfma_f32_16x16x32_bf16 v[92:95], a[44:47], a[12:15], v[92:95]\n\t"
  "ds_read_b64_tr_b16 a[32:33], v14 offset:0\n\t"
  "v_mfma_f32_16x16x32_bf16 v[88:91], a[44:47], a[8:11], v[88:91]\n\t"
  "ds_read_b64_tr_b16 a[34:35], v14 offset:6144\n\t"
  "v_mfma_f32_16x16x32_bf16 v[84:87], a[44:47], a[4:7], v[84:87]\n\t"
  "buffer_load_dwordx4 v1, s[20:23], s28 offen offset:1024 lds\n\t"
  "v_mfma_f32_16x16x32_bf16 v[80:83], a[44:47], a[0:3], v[80:83]\n\t"
  "ds_read_b128 a[24:27], v11 offset:4096\n\t"
  "v_smfmac_f32_16x16x64_bf16 v[132:135], a[60:63], a[40:47], v19\n\t"
  "s_waitcnt lgkmcnt(7)\n\t"
  "v_mfma_f32_16x16x32_bf16 v[96:99], a[0:3], a[48:51], v[96:99]\n\t"
  "ds_read_b64_tr_b16 a[36:37], v15 offset:0\n\t"
  "v_mfma_f32_16x16x32_bf16 v[100:103], a[4:7], a[48:51], v[100:103]\n\t"
  "ds_read_b64_tr_b16 a[38:39], v15 offset:6144\n\t"
  "v_mfma_f32_16x16x32_bf16 v[104:107], a[8:11], a[48:51], v[104:107]\n\t"
  "buffer_load_dwordx4 v2, s[20:23], s28 offen offset:2048 lds\n\t"
  "v_mfma_f32_16x16x32_bf16 v[108:111], a[12:15], a[48:51], v[108:111]\n\t"
  "ds_read_b128 a[28:31], v11 offset:6144\n\t"
  "s_waitcnt lgkmcnt(7)\n\t"
  "v_mfma_f32_16x16x32_bf16 v[124:127], a[12:15], a[52:55], v[124:127]\n\t"
  "ds_read_b64_tr_b16 a[40:41], v14 offset:1024\n\t"
  "v_mfma_f32_16x16x32_bf16 v[120:123], a[8:11], a[52:55], v[120:123]\n\t"
  "ds_read_b64_tr_b16 a[42:43], v14 offset:7168\n\t"
  "v_mfma_f32_16x16x32_bf16 v[116:119], a[4:7], a[52:55], v[116:119]\n\t"
  "buffer_load_dwordx4 v3, s[20:23], s28 offen offset:3072 lds\n\t"
  "v_mfma_f32_16x16x32_bf16 v[112:115], a[0:3], a[52:55], v[112:115]\n\t"
  "v_smfmac_f32_16x16x64_bf16 v[136:139], a[60:63], a[48:55], v19\n\t"
  "s_add_u32 s28, s28, 128\n\t"
  "s_waitcnt lgkmcnt(2)\n\t"
  "v_mfma_f32_16x16x32_bf16 v[32:35], a[32:35], a[16:19], v[32:35]\n\t"
  "ds_read_b64_tr_b16 a[44:45], v15 offset:1024\n\t"
  "v_mfma_f32_16x16x32_bf16 v[36:39], a[32:35], a[20:23], v[36:39]\n\t"
  "ds_read_b64_tr_b16 a[46:47], v15 offset:7168\n\t"
  "s_waitcnt vmcnt(10)\n\t"
  "v_mfma_f32_16x16x32_bf16 v[40:43], a[32:35], a[24:27], v[40:43]\n\t"
  "s_barrier\n\t"
  "v_mfma_f32_16x16x32_bf16 v[44:47], a[32:35], a[28:31], v[44:47]\n\t"
  "v_mfma_f32_16x16x32_bf16 v[60:63], a[36:39], a[28:31], v[60:63]\n\t"
  "ds_read_b64_tr_b16 a[48:49], v14 offset:2048\n\t"
  "v_mfma_f32_16x16x32_bf16 v[56:59], a[36:39], a[24:27], v[56:59]\n\t"
  "ds_read_b64_tr_b16 a[50:51], v14 offset:8192\n\t"
  "s_add_u32 m0, s44, 122880\n\t"
  "v_mfma_f32_16x16x32_bf16 v[52:55], a[36:39], a[20:23], v[52:55]\n\t"
  "buffer_load_dwordx4 v4, s[24:27], s29 offen offset:0 lds\n\t"
  "v_mfma_f32_16x16x32_bf16 v[48:51], a[36:39], a[16:19], v[48:51]\n\t"
  "ds_read_b128 a[0:3], v200 offset:0\n\t"
  "s_waitcnt lgkmcnt(5)\n\t"
  "v_mfma_f32_16x16x32_bf16 v[64:67], a[40:43], a[16:19], v[64:67]\n\t"
  "ds_read_b64_tr_b16 a[52:53], v15 offset:2048\n\t"
  "v_mfma_f32_16x16x32_bf16 v[68:71], a[40:43], a[20:23], v[68:71]\n\t"
  "ds_read_b64_tr_b16 a[54:55], v15 offset:8192\n\t"
  "v_mfma_f32_16x16x32_bf16 v[72:75], a[40:43], a[24:27], v[72:75]\n\t"
  "buffer_load_dwordx4 v5, s[24:27], s29 offen offset:1024 lds\n\t"
  "v_mfma_f32_16x16x32_bf16 v[76:79], a[40:43], a[28:31], v[76:79]\n\t"
  "ds_read_b128 a[4:7], v200 offset:2048\n\t"
  "s_waitcnt lgkmcnt(6)\n\t"
  "v_mfma_f32_16x16x32_bf16 v[92:95], a[44:47], a[28:31], v[92:95]\n\t"
  "ds_read_b64_tr_b16 a[32:33], v202 offset:0\n\t"
  "v_mfma_f32_16x16x32_bf16 v[88:91], a[44:47], a[24:27], v[88:91]\n\t"
  "ds_read_b64_tr_b16 a[34:35], v202 offset:6144\n\t"
  "v_mfma_f32_16x16x32_bf16 v[84:87], a[44:47], a[20:23], v[84:87]\n\t"
  "buffer_load_dwordx4 v6, s[24:27], s29 offen offset:2048 lds\n\t"
  "v_mfma_f32_16x16x32_bf16 v[80:83], a[44:47], a[16:19], v[80:83]\n\t"
  "ds_read_b128 a[8:11], v200 offset:4096\n\t"
  "s_waitcnt lgkmcnt(7)\n\t"
  "v_mfma_f32_16x16x32_bf16 v[96:99], a[16:19], a[48:51], v[96:99]\n\t"
  "ds_read_b64_tr_b16 a[36:37], v203 offset:0\n\t"
  "v_mfma_f32_16x16x32_bf16 v[100:103], a[20:23], a[48:51], v[100:103]\n\t"
  "ds_read_b64_tr_b16 a[38:39], v203 offset:6144\n\t"
  "s_add_u32 m0, s44, 135168\n\t"
  "v_mfma_f32_16x16x32_bf16 v[104:107], a[24:27], a[48:51], v[104:107]\n\t"
  "buffer_load_dwordx4 v7, s[24:27], s29 offen offset:0 lds\n\t"
  "v_mfma_f32_16x16x32_bf16 v[108:111], a[28:31], a[48:51], v[108:111]\n\t"
  "ds_read_b128 a[12:15], v200 offset:6144\n\t"
  "s_waitcnt lgkmcnt(7)\n\t"
  "v_mfma_f32_16x16x32_bf16 v[124:127], a[28:31], a[52:55], v[124:127]\n\t"
  "ds_read_b64_tr_b16 a[40:41], v202 offset:1024\n\t"
  "v_mfma_f32_16x16x32_bf16 v[120:123], a[24:27], a[52:55], v[120:123]\n\t"
  "ds_read_b64_tr_b16 a[42:43], v202 offset:7168\n\t"
  "v_mfma_f32_16x16x32_bf16 v[116:119], a[20:23], a[52:55], v[116:119]\n\t"
  "buffer_load_dwordx4 v8, s[24:27], s29 offen offset:1024 lds\n\t"
  "v_mfma_f32_16x16x32_bf16 v[112:115], a[16:19], a[52:55], v[112:115]\n\t"
  "s_waitcnt lgkmcnt(2)\n\t"
  "v_mfma_f32_16x16x32_bf16 v[32:35], a[32:35], a[0:3], v[32:35]\n\t"
  "ds_read_b64_tr_b16 a[44:45], v203 offset:1024\n\t"
  "v_mfma_f32_16x16x32_bf16 v[36:39], a[32:35], a[4:7], v[36:39]\n\t"
  "ds_read_b64_tr_b16 a[46:47], v203 offset:7168\n\t"
  "v_mfma_f32_16x16x32_bf16 v[40:43], a[32:35], a[8:11], v[40:43]\n\t"
  "v_mfma_f32_16x16x32_bf16 v[44:47], a[32:35], a[12:15], v[44:47]\n\t"
  "v_mfma_f32_16x16x32_bf16 v[60:63], a[36:39], a[12:15], v[60:63]\n\t"
  "ds_read_b64_tr_b16 a[48:49], v202 offset:2048\n\t"
  "v_mfma_f32_16x16x32_bf16 v[56:59], a[36:39], a[8:11], v[56:59]\n\t"
  "ds_read_b64_tr_b16 a[50:51], v202 offset:8192\n\t"
  "v_mfma_f32_16x16x32_bf16 v[52:55], a[36:39], a[4:7], v[52:55]\n\t"
  "buffer_load_dwordx4 v9, s[24:27], s29 offen offset:2048 lds\n\t"
  "v_mfma_f32_16x16x32_bf16 v[48:51], a[36:39], a[0:3], v[48:51]\n\t"
  "ds_read_b128 a[16:19], v201 offset:0\n\t"
  "v_smfmac_f32_16x16x64_bf16 v[128:131], a[60:63], a[32:39], v19\n\t"
  "s_add_u32 s29, s29, 393216\n\t"
  "s_waitcnt lgkmcnt(5)\n\t"
  "v_mfma_f32_16x16x32_bf16 v[64:67], a[40:43], a[0:3], v[64:67]\n\t"
  "ds_read_b64_tr_b16 a[52:53], v203 offset:2048\n\t"
  "v_mfma_f32_16x16x32_bf16 v[68:71], a[40:43], a[4:7], v[68:71]\n\t"
  "ds_read_b64_tr_b16 a[54:55], v203 offset:8192\n\t"
  "s_add_u32 m0, s30, 122880\n\t"
  "v_mfma_f32_16x16x32_bf16 v[72:75], a[40:43], a[8:11], v[72:75]\n\t"
  "buffer_load_dwordx4 v0, s[20:23], s28 offen offset:0 lds\n\t"
  "v_mfma_f32_16x16x32_bf16 v[76:79], a[40:43], a[12:15], v[76:79]\n\t"
  "ds_read_b128 a[20:23], v201 offset:2048\n\t"
  "s_waitcnt lgkmcnt(6)\n\t"
  "v_mfma_f32_16x16x32_bf16 v[92:95], a[44:47], a[12:15], v[92:95]\n\t"
  "ds_read_b64_tr_b16 a[32:33], v204 offset:0\n\t"
  "v_mfma_f32_16x16x32_bf16 v[88:91], a[44:47], a[8:11], v[88:91]\n\t"
  "ds_read_b64_tr_b16 a[34:35], v204 offset:6144\n\t"
  "v_mfma_f32_16x16x32_bf16 v[84:87], a[44:47], a[4:7], v[84:87]\n\t"
  "buffer_load_dwordx4 v1, s[20:23], s28 offen offset:1024 lds\n\t"
  "v_mfma_f32_16x16x32_bf16 v[80:83], a[44:47], a[0:3], v[80:83]\n\t"
  "ds_read_b128 a[24:27], v201 offset:4096\n\t"
  "v_smfmac_f32_16x16x64_bf16 v[132:135], a[60:63], a[40:47], v19\n\t"
  "s_waitcnt lgkmcnt(7)\n\t"
  "v_mfma_f32_16x16x32_bf16 v[96:99], a[0:3], a[48:51], v[96:99]\n\t"
  "ds_read_b64_tr_b16 a[36:37], v205 offset:0\n\t"
  "v_mfma_f32_16x16x32_bf16 v[100:103], a[4:7], a[48:51], v[100:103]\n\t"
  "ds_read_b64_tr_b16 a[38:39], v205 offset:6144\n\t"
  "v_mfma_f32_16x16x32_bf16 v[104:107], a[8:11], a[48:51], v[104:107]\n\t"
  "buffer_load_dwordx4 v2, s[20:23], s28 offen offset:2048 lds\n\t"
  "v_mfma_f32_16x16x32_bf16 v[108:111], a[12:15], a[48:51], v[108:111]\n\t"
  "ds_read_b128 a[28:31], v201 offset:6144\n\t"
  "s_waitcnt lgkmcnt(7)\n\t"
  "v_mfma_f32_16x16x32_bf16 v[124:127], a[12:15], a[52:55], v[124:127]\n\t"
  "ds_read_b64_tr_b16 a[40:41], v204 offset:1024\n\t"
  "v_mfma_f32_16x16x32_bf16 v[120:123], a[8:11], a[52:55], v[120:123]\n\t"
  "ds_read_b64_tr_b16 a[42:43], v204 offset:7168\n\t"
  "v_mfma_f32_16x16x32_bf16 v[116:119], a[4:7], a[52:55], v[116:119]\n\t"
  "buffer_load_dwordx4 v3, s[20:23], s28 offen offset:3072 lds\n\t"
  "v_mfma_f32_16x16x32_bf16 v[112:115], a[0:3], a[52:55], v[112:115]\n\t"
  "v_smfmac_f32_16x16x64_bf16 v[136:139], a[60:63], a[48:55], v19\n\t"
  "s_add_u32 s28, s28, 128\n\t"
  "s_waitcnt lgkmcnt(2)\n\t"
  "v_mfma_f32_16x16x32_bf16 v[32:35], a[32:35], a[16:19], v[32:35]\n\t"
  "ds_read_b64_tr_b16 a[44:45], v205 offset:1024\n\t"
  "v_mfma_f32_16x16x32_bf16 v[36:39], a[32:35], a[20:23], v[36:39]\n\t"
  "ds_read_b64_tr_b16 a[46:47], v205 offset:7168\n\t"
  "s_waitcnt vmcnt(10)\n\t"
  "v_mfma_f32_16x16x32_bf16 v[40:43], a[32:35], a[24:27], v[40:43]\n\t"
  "s_barrier\n\t"
  "v_mfma_f32_16x16x32_bf16 v[44:47], a[32:35], a[28:31], v[44:47]\n\t"
  "v_mfma_f32_16x16x32_bf16 v[60:63], a[36:39], a[28:31], v[60:63]\n\t"
  "ds_read_b64_tr_b16 a[48:49], v204 offset:2048\n\t"
  "v_mfma_f32_16x16x32_bf16 v[56:59], a[36:39], a[24:27], v[56:59]\n\t"
  "ds_read_b64_tr_b16 a[50:51], v204 offset:8192\n\t"
  "s_add_u32 m0, s44, 0\n\t"
  "v_mfma_f32_16x16x32_bf16 v[52:55], a[36:39], a[20:23], v[52:55]\n\t"
  "buffer_load_dwordx4 v4, s[24:27], s29 offen offset:0 lds\n\t"
  "v_mfma_f32_16x16x32_bf16 v[48:51], a[36:39], a[16:19], v[48:51]\n\t"
  "ds_read_b128 a[0:3], v206 offset:0\n\t"
  "s_waitcnt lgkmcnt(5)\n\t"
  "v_mfma_f32_16x16x32_bf16 v[64:67], a[40:43], a[16:19], v[64:67]\n\t"
  "ds_read_b64_tr_b16 a[52:53], v205 offset:2048\n\t"
  "v_mfma_f32_16x16x32_bf16 v[68:71], a[40:43], a[20:23], v[68:71]\n\t"
  "ds_read_b64_tr_b16 a[54:55], v205 offset:8192\n\t"
  "v_mfma_f32_16x16x32_bf16 v[72:75], a[40:43], a[24:27], v[72:75]\n\t"
  "buffer_load_dwordx4 v5, s[24:27], s29 offen offset:1024 lds\n\t"
  "v_mfma_f32_16x16x32_bf16 v[76:79], a[40:43], a[28:31], v[76:79]\n\t"
  "ds_read_b128 a[4:7], v206 offset:2048\n\t"
  "s_waitcnt lgkmcnt(6)\n\t"
  "v_mfma_f32_16x16x32_bf16 v[92:95], a[44:47], a[28:31], v[92:95]\n\t"
  "ds_read_b64_tr_b16 a[32:33], v208 offset:0\n\t"
  "v_mfma_f32_16x16x32_bf16 v[88:91], a[44:47], a[24:27], v[88:91]\n\t"
  "ds_read_b64_tr_b16 a[34:35], v208 offset:6144\n\t"
  "v_mfma_f32_16x16x32_bf16 v[84:87], a[44:47], a[20:23], v[84:87]\n\t"
  "buffer_load_dwordx4 v6, s[24:27], s29 offen offset:2048 lds\n\t"
  "v_mfma_f32_16x16x32_bf16 v[80:83], a[44:47], a[16:19], v[80:83]\n\t"
  "ds_read_b128 a[8:11], v206 offset:4096\n\t"
  "s_waitcnt lgkmcnt(7)\n\t"
  "v_mfma_f32_16x16x32_bf16 v[96:99], a[16:19], a[48:51], v[96:99]\n\t"
  "ds_read_b64_tr_b16 a[36:37], v209 offset:0\n\t"
  "v_mfma_f32_16x16x32_bf16 v[100:103], a[20:23], a[48:51], v[100:103]\n\t"
  "ds_read_b64_tr_b16 a[38:39], v209 offset:6144\n\t"
  "s_add_u32 m0, s44, 12288\n\t"
  "v_mfma_f32_16x16x32_bf16 v[104:107], a[24:27], a[48:51], v[104:107]\n\t"
  "buffer_load_dwordx4 v7, s[24:27], s29 offen offset:0 lds\n\t"
  "v_mfma_f32_16x16x32_bf16 v[108:111], a[28:31], a[48:51], v[108:111]\n\t"
  "ds_read_b128 a[12:15], v206 offset:6144\n\t"
  "s_waitcnt lgkmcnt(7)\n\t"
  "v_mfma_f32_16x16x32_bf16 v[124:127], a[28:31], a[52:55], v[124:127]\n\t"
  "ds_read_b64_tr_b16 a[40:41], v208 offset:1024\n\t"
  "v_mfma_f32_16x16x32_bf16 v[120:123], a[24:27], a[52:55], v[120:123]\n\t"
  "ds_read_b64_tr_b16 a[42:43], v208 offset:7168\n\t"
  "v_mfma_f32_16x16x32_bf16 v[116:119], a[20:23], a[52:55], v[116:119]\n\t"
  "buffer_load_dwordx4 v8, s[24:27], s29 offen offset:1024 lds\n\t"
  "v_mfma_f32_16x16x32_bf16 v[112:115], a[16:19], a[52:55], v[112:115]\n\t"
  "s_waitcnt lgkmcnt(2)\n\t"
  "v_mfma_f32_16x16x32_bf16 v[32:35], a[32:35], a[0:3], v[32:35]\n\t"
  "ds_read_b64_tr_b16 a[44:45], v209 offset:1024\n\t"
  "v_mfma_f32_16x16x32_bf16 v[36:39], a[32:35], a[4:7], v[36:39]\n\t"
  "ds_read_b64_tr_b16 a[46:47], v209 offset:7168\n\t"
  "v_mfma_f32_16x16x32_bf16 v[40:43], a[32:35], a[8:11], v[40:43]\n\t"
  "v_mfma_f32_16x16x32_bf16 v[44:47], a[32:35], a[12:15], v[44:47]\n\t"
  "v_mfma_f32_16x16x32_bf16 v[60:63], a[36:39], a[12:15], v[60:63]\n\t"
  "ds_read_b64_tr_b16 a[48:49], v208 offset:2048\n\t"
  "v_mfma_f32_16x16x32_bf16 v[56:59], a[36:39], a[8:11], v[56:59]\n\t"
  "ds_read_b64_tr_b16 a[50:51], v208 offset:8192\n\t"
  "v_mfma_f32_16x16x32_bf16 v[52:55], a[36:39], a[4:7], v[52:55]\n\t"
  "buffer_load_dwordx4 v9, s[24:27], s29 offen offset:2048 lds\n\t"
  "v_mfma_f32_16x16x32_bf16 v[48:51], a[36:39], a[0:3], v[48:51]\n\t"
  "ds_read_b128 a[16:19], v207 offset:0\n\t"
  "v_smfmac_f32_16x16x64_bf16 v[128:131], a[60:63], a[32:39], v19\n\t"
  "s_add_u32 s29, s29, 393216\n\t"
  "s_waitcnt lgkmcnt(5)\n\t"
  "v_mfma_f32_16x16x32_bf16 v[64:67], a[40:43], a[0:3], v[64:67]\n\t"
  "ds_read_b64_tr_b16 a[52:53], v209 offset:2048\n\t"
  "v_mfma_f32_16x16x32_bf16 v[68:71], a[40:43], a[4:7], v[68:71]\n\t"
  "ds_read_b64_tr_b16 a[54:55], v209 offset:8192\n\t"
  "s_add_u32 m0, s30, 0\n\t"
  "v_mfma_f32_16x16x32_bf16 v[72:75], a[40:43], a[8:11], v[72:75]\n\t"
  "buffer_load_dwordx4 v0, s[20:23], s28 offen offset:0 lds\n\t"
  "v_mfma_f32_16x16x32_bf16 v[76:79], a[40:43], a[12:15], v[76:79]\n\t"
  "ds_read_b128 a[20:23], v207 offset:2048\n\t"
  "s_waitcnt lgkmcnt(6)\n\t"
  "v_mfma_f32_16x16x32_bf16 v[92:95], a[44:47], a[12:15], v[92:95]\n\t"
  "ds_read_b64_tr_b16 a[32:33], v210 offset:0\n\t"
  "v_mfma_f32_16x16x32_bf16 v[88:91], a[44:47], a[8:11], v[88:91]\n\t"
  "ds_read_b64_tr_b16 a[34:35], v210 offset:6144\n\t"
  "v_mfma_f32_16x16x32_bf16 v[84:87], a[44:47], a[4:7], v[84:87]\n\t"
  "buffer_load_dwordx4 v1, s[20:23], s28 offen offset:1024 lds\n\t"
  "v_mfma_f32_16x16x32_bf16 v[80:83], a[44:47], a[0:3], v[80:83]\n\t"
  "ds_read_b128 a[24:27], v207 offset:4096\n\t"
  "v_smfmac_f32_16x16x64_bf16 v[132:135], a[60:63], a[40:47], v19\n\t"
  "s_waitcnt lgkmcnt(7)\n\t"
  "v_mfma_f32_16x16x32_bf16 v[96:99], a[0:3], a[48:51], v[96:99]\n\t"
  "ds_read_b64_tr_b16 a[36:37], v211 offset:0\n\t"
  "v_mfma_f32_16x16x32_bf16 v[100:103], a[4:7], a[48:51], v[100:103]\n\t"
  "ds_read_b64_tr_b16 a[38:39], v211 offset:6144\n\t"
  "v_mfma_f32_16x16x32_bf16 v[104:107], a[8:11], a[48:51], v[104:107]\n\t"
  "buffer_load_dwordx4 v2, s[20:23], s28 offen offset:2048 lds\n\t"
  "v_mfma_f32_16x16x32_bf16 v[108:111], a[12:15], a[48:51], v[108:111]\n\t"
  "ds_read_b128 a[28:31], v207 offset:6144\n\t"
  "s_waitcnt lgkmcnt(7)\n\t"
  "v_mfma_f32_16x16x32_bf16 v[124:127], a[12:15], a[52:55], v[124:127]\n\t"
  "ds_read_b64_tr_b16 a[40:41], v210 offset:1024\n\t"
  "v_mfma_f32_16x16x32_bf16 v[120:123], a[8:11], a[52:55], v[120:123]\n\t"
  "ds_read_b64_tr_b16 a[42:43], v210 offset:7168\n\t"
  "v_mfma_f32_16x16x32_bf16 v[116:119], a[4:7], a[52:55], v[116:119]\n\t"
  "buffer_load_dwordx4 v3, s[20:23], s28 offen offset:3072 lds\n\t"
  "v_mfma_f32_16x16x32_bf16 v[112:115], a[0:3], a[52:55], v[112:115]\n\t"
  "v_smfmac_f32_16x16x64_bf16 v[136:139], a[60:63], a[48:55], v19\n\t"
  "s_add_u32 s28, s28, 128\n\t"
  "s_waitcnt lgkmcnt(2)\n\t"
  "v_mfma_f32_16x16x32_bf16 v[32:35], a[32:35], a[16:19], v[32:35]\n\t"
  "ds_read_b64_tr_b16 a[44:45], v211 offset:1024\n\t"
  "v_mfma_f32_16x16x32_bf16 v[36:39], a[32:35], a[20:23], v[36:39]\n\t"
  "ds_read_b64_tr_b16 a[46:47], v211 offset:7168\n\t"
  "s_waitcnt vmcnt(10)\n\t"
  "v_mfma_f32_16x16x32_bf16 v[40:43], a[32:35], a[24:27], v[40:43]\n\t"
  "s_barrier\n\t"
  "v_mfma_f32_16x16x32_bf16 v[44:47], a[32:35], a[28:31], v[44:47]\n\t"
  "v_mfma_f32_16x16x32_bf16 v[60:63], a[36:39], a[28:31], v[60:63]\n\t"
  "ds_read_b64_tr_b16 a[48:49], v210 offset:2048\n\t"
  "v_mfma_f32_16x16x32_bf16 v[56:59], a[36:39], a[24:27], v[56:59]\n\t"
  "ds_read_b64_tr_b16 a[50:51], v210 offset:8192\n\t"
  "s_add_u32 m0, s44, 40960\n\t"
  "v_mfma_f32_16x16x32_bf16 v[52:55], a[36:39], a[20:23], v[52:55]\n\t"
  "buffer_load_dwordx4 v4, s[24:27], s29 offen offset:0 lds\n\t"
  "v_mfma_f32_16x16x32_bf16 v[48:51], a[36:39], a[16:19], v[48:51]\n\t"
  "ds_read_b128 a[0:3], v212 offset:0\n\t"
  "s_waitcnt lgkmcnt(5)\n\t"
  "v_mfma_f32_16x16x32_bf16 v[64:67], a[40:43], a[16:19], v[64:67]\n\t"
  "ds_read_b64_tr_b16 a[52:53], v211 offset:2048\n\t"
  "v_mfma_f32_16x16x32_bf16 v[68:71], a[40:43], a[20:23], v[68:71]\n\t"
  "ds_read_b64_tr_b16 a[54:55], v211 offset:8192\n\t"
  "v_mfma_f32_16x16x32_bf16 v[72:75], a[40:43], a[24:27], v[72:75]\n\t"
  "buffer_load_dwordx4 v5, s[24:27], s29 offen offset:1024 lds\n\t"
  "v_mfma_f32_16x16x32_bf16 v[76:79], a[40:43], a[28:31], v[76:79]\n\t"
  "ds_read_b128 a[4:7], v212 offset:2048\n\t"
  "s_waitcnt lgkmcnt(6)\n\t"
  "v_mfma_f32_16x16x32_bf16 v[92:95], a[44:47], a[28:31], v[92:95]\n\t"
  "ds_read_b64_tr_b16 a[32:33], v214 offset:0\n\t"
  "v_mfma_f32_16x16x32_bf16 v[88:91], a[44:47], a[24:27], v[88:91]\n\t"
  "ds_read_b64_tr_b16 a[34:35], v214 offset:6144\n\t"
  "v_mfma_f32_16x16x32_bf16 v[84:87], a[44:47], a[20:23], v[84:87]\n\t"
  "buffer_load_dwordx4 v6, s[24:27], s29 offen offset:2048 lds\n\t"
  "v_mfma_f32_16x16x32_bf16 v[80:83], a[44:47], a[16:19], v[80:83]\n\t"
  "ds_read_b128 a[8:11], v212 offset:4096\n\t"
  "s_waitcnt lgkmcnt(7)\n\t"
  "v_mfma_f32_16x16x32_bf16 v[96:99], a[16:19], a[48:51], v[96:99]\n\t"
  "ds_read_b64_tr_b16 a[36:37], v215 offset:0\n\t"
  "v_mfma_f32_16x16x32_bf16 v[100:103], a[20:23], a[48:51], v[100:103]\n\t"
  "ds_read_b64_tr_b16 a[38:39], v215 offset:6144\n\t"
  "s_add_u32 m0, s44, 53248\n\t"
  "v_mfma_f32_16x16x32_bf16 v[104:107], a[24:27], a[48:51], v[104:107]\n\t"
  "buffer_load_dwordx4 v7, s[24:27], s29 offen offset:0 lds\n\t"
  "v_mfma_f32_16x16x32_bf16 v[108:111], a[28:31], a[48:51], v[108:111]\n\t"
  "ds_read_b128 a[12:15], v212 offset:6144\n\t"
  "s_waitcnt lgkmcnt(7)\n\t"
  "v_mfma_f32_16x16x32_bf16 v[124:127], a[28:31], a[52:55], v[124:127]\n\t"
  "ds_read_b64_tr_b16 a[40:41], v214 offset:1024\n\t"
  "v_mfma_f32_16x16x32_bf16 v[120:123], a[24:27], a[52:55], v[120:123]\n\t"
  "ds_read_b64_tr_b16 a[42:43], v214 offset:7168\n\t"
  "v_mfma_f32_16x16x32_bf16 v[116:119], a[20:23], a[52:55], v[116:119]\n\t"
  "buffer_load_dwordx4 v8, s[24:27], s29 offen offset:1024 lds\n\t"
  "s_sub_u32 s31, s31, 1\n\t"
  "s_cmp_lg_u32 s31, 0\n\t"
  "v_mfma_f32_16x16x32_bf16 v[112:115], a[16:19], a[52:55], v[112:115]\n\t"
  "s_cbranch_scc1 LQKV_%=\n\t"
  "s_waitcnt lgkmcnt(2)\n\t"
  "v_mfma_f32_16x16x32_bf16 v[32:35], a[32:35], a[0:3], v[32:35]\n\t"
  "ds_read_b64_tr_b16 a[44:45], v215 offset:1024\n\t"
  "v_mfma_f32_16x16x32_bf16 v[36:39], a[32:35], a[4:7], v[36:39]\n\t"
  "ds_read_b64_tr_b16 a[46:47], v215 offset:7168\n\t"
  "v_mfma_f32_16x16x32_bf16 v[40:43], a[32:35], a[8:11], v[40:43]\n\t"
  "v_mfma_f32_16x16x32_bf16 v[44:47], a[32:35], a[12:15], v[44:47]\n\t"
  "v_mfma_f32_16x16x32_bf16 v[60:63], a[36:39], a[12:15], v[60:63]\n\t"
  "ds_read_b64_tr_b16 a[48:49], v214 offset:2048\n\t"
  "v_mfma_f32_16x16x32_bf16 v[56:59], a[36:39], a[8:11], v[56:59]\n\t"
  "ds_read_b64_tr_b16 a[50:51], v214 offset:8192\n\t"
  "v_mfma_f32_16x16x32_bf16 v[52:55], a[36:39], a[4:7], v[52:55]\n\t"
  "buffer_load_dwordx4 v9, s[24:27], s29 offen offset:2048 lds\n\t"
  "v_mfma_f32_16x16x32_bf16 v[48:51], a[36:39], a[0:3], v[48:51]\n\t"
  "ds_read_b128 a[16:19], v213 offset:0\n\t"
  "v_smfmac_f32_16x16x64_bf16 v[128:131], a[60:63], a[32:39], v19\n\t"
  "s_waitcnt lgkmcnt(5)\n\t"
  "v_mfma_f32_16x16x32_bf16 v[64:67], a[40:43], a[0:3], v[64:67]\n\t"
  "ds_read_b64_tr_b16 a[52:53], v215 offset:2048\n\t"
  "v_mfma_f32_16x16x32_bf16 v[68:71], a[40:43], a[4:7], v[68:71]\n\t"
  "ds_read_b64_tr_b16 a[54:55], v215 offset:8192\n\t"
  "s_add_u32 m0, s30, 40960\n\t"
  "v_mfma_f32_16x16x32_bf16 v[72:75], a[40:43], a[8:11], v[72:75]\n\t"
  "buffer_load_dwordx4 v0, s[20:23], s28 offen offset:0 lds\n\t"
  "v_mfma_f32_16x16x32_bf16 v[76:79], a[40:43], a[12:15], v[76:79]\n\t"
  "ds_read_b128 a[20:23], v213 offset:2048\n\t"
  "s_waitcnt lgkmcnt(6)\n\t"
  "v_mfma_f32_16x16x32_bf16 v[92:95], a[44:47], a[12:15], v[92:95]\n\t"
  "ds_read_b64_tr_b16 a[32:33], v216 offset:0\n\t"
  "v_mfma_f32_16x16x32_bf16 v[88:91], a[44:47], a[8:11], v[88:91]\n\t"
  "ds_read_b64_tr_b16 a[34:35], v216 offset:6144\n\t"
  "v_mfma_f32_16x16x32_bf16 v[84:87], a[44:47], a[4:7], v[84:87]\n\t"
  "buffer_load_dwordx4 v1, s[20:23], s28 offen offset:1024 lds\n\t"
  "v_mfma_f32_16x16x32_bf16 v[80:83], a[44:47], a[0:3], v[80:83]\n\t"
  "ds_read_b128 a[24:27], v213 offset:4096\n\t"
  "v_smfmac_f32_16x16x64_bf16 v[132:135], a[60:63], a[40:47], v19\n\t"
  "s_waitcnt lgkmcnt(7)\n\t"
  "v_mfma_f32_16x16x32_bf16 v[96:99], a[0:3], a[48:51], v[96:99]\n\t"
  "ds_read_b64_tr_b16 a[36:37], v217 offset:0\n\t"
  "v_mfma_f32_16x16x32_bf16 v[100:103], a[4:7], a[48:51], v[100:103]\n\t"
  "ds_read_b64_tr_b16 a[38:39], v217 offset:6144\n\t"
  "v_mfma_f32_16x16x32_bf16 v[104:107], a[8:11], a[48:51], v[104:107]\n\t"
  "buffer_load_dwordx4 v2, s[20:23], s28 offen offset:2048 lds\n\t"
  "v_mfma_f32_16x16x32_bf16 v[108:111], a[12:15], a[48:51], v[108:111]\n\t"
  "ds_read_b128 a[28:31], v213 offset:6144\n\t"
  "s_waitcnt lgkmcnt(7)\n\t"
  "v_mfma_f32_16x16x32_bf16 v[124:127], a[12:15], a[52:55], v[124:127]\n\t"
  "ds_read_b64_tr_b16 a[40:41], v216 offset:1024\n\t"
  "v_mfma_f32_16x16x32_bf16 v[120:123], a[8:11], a[52:55], v[120:123]\n\t"
  "ds_read_b64_tr_b16 a[42:43], v216 offset:7168\n\t"
  "v_mfma_f32_16x16x32_bf16 v[116:119], a[4:7], a[52:55], v[116:119]\n\t"
  "buffer_load_dwordx4 v3, s[20:23], s28 offen offset:3072 lds\n\t"
  "v_mfma_f32_16x16x32_bf16 v[112:115], a[0:3], a[52:55], v[112:115]\n\t"
  "v_smfmac_f32_16x16x64_bf16 v[136:139], a[60:63], a[48:55], v19\n\t"
  "s_waitcnt lgkmcnt(2)\n\t"
  "v_mfma_f32_16x16x32_bf16 v[32:35], a[32:35], a[16:19], v[32:35]\n\t"
  "ds_read_b64_tr_b16 a[44:45], v217 offset:1024\n\t"
  "v_mfma_f32_16x16x32_bf16 v[36:39], a[32:35], a[20:23], v[36:39]\n\t"
  "ds_read_b64_tr_b16 a[46:47], v217 offset:7168\n\t"
  "s_waitcnt vmcnt(10)\n\t"
  "v_mfma_f32_16x16x32_bf16 v[40:43], a[32:35], a[24:27], v[40:43]\n\t"
  "s_barrier\n\t"
  "v_mfma_f32_16x16x32_bf16 v[44:47], a[32:35], a[28:31], v[44:47]\n\t"
  "v_mfma_f32_16x16x32_bf16 v[60:63], a[36:39], a[28:31], v[60:63]\n\t"
  "ds_read_b64_tr_b16 a[48:49], v216 offset:2048\n\t"
  "v_mfma_f32_16x16x32_bf16 v[56:59], a[36:39], a[24:27], v[56:59]\n\t"
  "ds_read_b64_tr_b16 a[50:51], v216 offset:8192\n\t"
  "v_mfma_f32_16x16x32_bf16 v[52:55], a[36:39], a[20:23], v[52:55]\n\t"
  "v_mfma_f32_16x16x32_bf16 v[48:51], a[36:39], a[16:19], v[48:51]\n\t"
  "ds_read_b128 a[0:3], v10 offset:0\n\t"
  "s_waitcnt lgkmcnt(5)\n\t"
  "v_mfma_f32_16x16x32_bf16 v[64:67], a[40:43], a[16:19], v[64:67]\n\t"
  "ds_read_b64_tr_b16 a[52:53], v217 offset:2048\n\t"
  "v_mfma_f32_16x16x32_bf16 v[68:71], a[40:43], a[20:23], v[68:71]\n\t"
  "ds_read_b64_tr_b16 a[54:55], v217 offset:8192\n\t"
  "v_mfma_f32_16x16x32_bf16 v[72:75], a[40:43], a[24:27], v[72:75]\n\t"
  "buffer_load_dwordx4 v[140:143], v17, s[52:55], 0 offen sc1\n\t"
  "v_mfma_f32_16x16x32_bf16 v[76:79], a[40:43], a[28:31], v[76:79]\n\t"
  "ds_read_b128 a[4:7], v10 offset:2048\n\t"
  "s_waitcnt lgkmcnt(6)\n\t"
  "v_mfma_f32_16x16x32_bf16 v[92:95], a[44:47], a[28:31], v[92:95]\n\t"
  "ds_read_b64_tr_b16 a[32:33], v12 offset:0\n\t"
  "v_mfma_f32_16x16x32_bf16 v[88:91], a[44:47], a[24:27], v[88:91]\n\t"
  "ds_read_b64_tr_b16 a[34:35], v12 offset:6144\n\t"
  "v_mfma_f32_16x16x32_bf16 v[84:87], a[44:47], a[20:23], v[84:87]\n\t"
  "buffer_load_dwordx4 a[64:67], v22, s[48:51], 0 offen offset:0\n\t"
  "v_mfma_f32_16x16x32_bf16 v[80:83], a[44:47], a[16:19], v[80:83]\n\t"
  "ds_read_b128 a[8:11], v10 offset:4096\n\t"
  "s_waitcnt lgkmcnt(7)\n\t"
  "v_mfma_f32_16x16x32_bf16 v[96:99], a[16:19], a[48:51], v[96:99]\n\t"
  "ds_read_b64_tr_b16 a[36:37], v13 offset:0\n\t"
  "v_mfma_f32_16x16x32_bf16 v[100:103], a[20:23], a[48:51], v[100:103]\n\t"
  "ds_read_b64_tr_b16 a[38:39], v13 offset:6144\n\t"
  "v_mfma_f32_16x16x32_bf16 v[104:107], a[24:27], a[48:51], v[104:107]\n\t"
  "buffer_load_dwordx4 a[68:71], v22, s[48:51], 0 offen offset:1024\n\t"
  "v_mfma_f32_16x16x32_bf16 v[108:111], a[28:31], a[48:51], v[108:111]\n\t"
  "ds_read_b128 a[12:15], v10 offset:6144\n\t"
  "s_waitcnt lgkmcnt(7)\n\t"
  "v_mfma_f32_16x16x32_bf16 v[124:127], a[28:31], a[52:55], v[124:127]\n\t"
  "ds_read_b64_tr_b16 a[40:41], v12 offset:1024\n\t"
  "v_mfma_f32_16x16x32_bf16 v[120:123], a[24:27], a[52:55], v[120:123]\n\t"
  "ds_read_b64_tr_b16 a[42:43], v12 offset:7168\n\t"
  "v_mfma_f32_16x16x32_bf16 v[116:119], a[20:23], a[52:55], v[116:119]\n\t"
  "v_mfma_f32_16x16x32_bf16 v[112:115], a[16:19], a[52:55], v[112:115]\n\t"
  "s_waitcnt lgkmcnt(2)\n\t"
  "v_mfma_f32_16x16x32_bf16 v[32:35], a[32:35], a[0:3], v[32:35]\n\t"
  "ds_read_b64_tr_b16 a[44:45], v13 offset:1024\n\t"
  "v_mfma_f32_16x16x32_bf16 v[36:39], a[32:35], a[4:7], v[36:39]\n\t"
  "ds_read_b64_tr_b16 a[46:47], v13 offset:7168\n\t"
  "v_mfma_f32_16x16x32_bf16 v[40:43], a[32:35], a[8:11], v[40:43]\n\t"
  "v_mfma_f32_16x16x32_bf16 v[44:47], a[32:35], a[12:15], v[44:47]\n\t"
  "v_mfma_f32_16x16x32_bf16 v[60:63], a[36:39], a[12:15], v[60:63]\n\t"
  "ds_read_b64_tr_b16 a[48:49], v12 offset:2048\n\t"
  "v_mfma_f32_16x16x32_bf16 v[56:59], a[36:39], a[8:11], v[56:59]\n\t"
  "ds_read_b64_tr_b16 a[50:51], v12 offset:8192\n\t"
  "v_mfma_f32_16x16x32_bf16 v[52:55], a[36:39], a[4:7], v[52:55]\n\t"
  "v_mfma_f32_16x16x32_bf16 v[48:51], a[36:39], a[0:3], v[48:51]\n\t"
  "ds_read_b128 a[16:19], v11 offset:0\n\t"
  "v_smfmac_f32_16x16x64_bf16 v[128:131], a[60:63], a[32:39], v19\n\t"
  "s_waitcnt lgkmcnt(5)\n\t"
  "v_mfma_f32_16x16x32_bf16 v[64:67], a[40:43], a[0:3], v[64:67]\n\t"
  "ds_read_b64_tr_b16 a[52:53], v13 offset:2048\n\t"
  "v_mfma_f32_16x16x32_bf16 v[68:71], a[40:43], a[4:7], v[68:71]\n\t"
  "ds_read_b64_tr_b16 a[54:55], v13 offset:8192\n\t"
  "v_mfma_f32_16x16x32_bf16 v[72:75], a[40:43], a[8:11], v[72:75]\n\t"
  "v_mfma_f32_16x16x32_bf16 v[76:79], a[40:43], a[12:15], v[76:79]\n\t"
  "ds_read_b128 a[20:23], v11 offset:2048\n\t"
  "s_waitcnt lgkmcnt(6)\n\t"
  "v_mfma_f32_16x16x32_bf16 v[92:95], a[44:47], a[12:15], v[92:95]\n\t"
  "ds_read_b64_tr_b16 a[32:33], v14 offset:0\n\t"
  "v_mfma_f32_16x16x32_bf16 v[88:91], a[44:47], a[8:11], v[88:91]\n\t"
  "ds_read_b64_tr_b16 a[34:35], v14 offset:6144\n\t"
  "v_mfma_f32_16x16x32_bf16 v[84:87], a[44:47], a[4:7], v[84:87]\n\t"
  "v_mfma_f32_16x16x32_bf16 v[80:83], a[44:47], a[0:3], v[80:83]\n\t"
  "ds_read_b128 a[24:27], v11 offset:4096\n\t"
  "v_smfmac_f32_16x16x64_bf16 v[132:135], a[60:63], a[40:47], v19\n\t"
  "s_waitcnt lgkmcnt(7)\n\t"
  "v_mfma_f32_16x16x32_bf16 v[96:99], a[0:3], a[48:51], v[96:99]\n\t"
  "ds_read_b64_tr_b16 a[36:37], v15 offset:0\n\t"
  "v_mfma_f32_16x16x32_bf16 v[100:103], a[4:7], a[48:51], v[100:103]\n\t"
  "ds_read_b64_tr_b16 a[38:39], v15 offset:6144\n\t"
  "v_mfma_f32_16x16x32_bf16 v[104:107], a[8:11], a[48:51], v[104:107]\n\t"
  "v_mfma_f32_16x16x32_bf16 v[108:111], a[12:15], a[48:51], v[108:111]\n\t"
  "ds_read_b128 a[28:31], v11 offset:6144\n\t"
  "s_waitcnt lgkmcnt(7)\n\t"
  "v_mfma_f32_16x16x32_bf16 v[124:127], a[12:15], a[52:55], v[124:127]\n\t"
  "ds_read_b64_tr_b16 a[40:41], v14 offset:1024\n\t"
  "v_mfma_f32_16x16x32_bf16 v[120:123], a[8:11], a[52:55], v[120:123]\n\t"
  "ds_read_b64_tr_b16 a[42:43], v14 offset:7168\n\t"
  "v_mfma_f32_16x16x32_bf16 v[116:119], a[4:7], a[52:55], v[116:119]\n\t"
  "v_mfma_f32_16x16x32_bf16 v[112:115], a[0:3], a[52:55], v[112:115]\n\t"
  "v_smfmac_f32_16x16x64_bf16 v[136:139], a[60:63], a[48:55], v19\n\t"
  "s_waitcnt lgkmcnt(2)\n\t"
  "v_mfma_f32_16x16x32_bf16 v[32:35], a[32:35], a[16:19], v[32:35]\n\t"
  "ds_read_b64_tr_b16 a[44:45], v15 offset:1024\n\t"
  "v_mfma_f32_16x16x32_bf16 v[36:39], a[32:35], a[20:23], v[36:39]\n\t"
  "ds_read_b64_tr_b16 a[46:47], v15 offset:7168\n\t"
  "s_waitcnt vmcnt(3)\n\t"
  "v_mfma_f32_16x16x32_bf16 v[40:43], a[32:35], a[24:27], v[40:43]\n\t"
  "s_barrier\n\t"
  "v_mfma_f32_16x16x32_bf16 v[44:47], a[32:35], a[28:31], v[44:47]\n\t"
  "v_mfma_f32_16x16x32_bf16 v[60:63], a[36:39], a[28:31], v[60:63]\n\t"
  "ds_read_b64_tr_b16 a[48:49], v14 offset:2048\n\t"
  "v_mfma_f32_16x16x32_bf16 v[56:59], a[36:39], a[24:27], v[56:59]\n\t"
  "ds_read_b64_tr_b16 a[50:51], v14 offset:8192\n\t"
  "v_mfma_f32_16x16x32_bf16 v[52:55], a[36:39], a[20:23], v[52:55]\n\t"
  "v_mfma_f32_16x16x32_bf16 v[48:51], a[36:39], a[16:19], v[48:51]\n\t"
  "ds_read_b128 a[0:3], v200 offset:0\n\t"
  "s_waitcnt lgkmcnt(5)\n\t"
  "v_mfma_f32_16x16x32_bf16 v[64:67], a[40:43], a[16:19], v[64:67]\n\t"
  "ds_read_b64_tr_b16 a[52:53], v15 offset:2048\n\t"
  "v_mfma_f32_16x16x32_bf16 v[68:71], a[40:43], a[20:23], v[68:71]\n\t"
  "ds_read_b64_tr_b16 a[54:55], v15 offset:8192\n\t"
  "v_mfma_f32_16x16x32_bf16 v[72:75], a[40:43], a[24:27], v[72:75]\n\t"
  "v_mfma_f32_16x16x32_bf16 v[76:79], a[40:43], a[28:31], v[76:79]\n\t"
  "ds_read_b128 a[4:7], v200 offset:2048\n\t"
  "s_waitcnt lgkmcnt(6)\n\t"
  "v_mfma_f32_16x16x32_bf16 v[92:95], a[44:47], a[28:31], v[92:95]\n\t"
  "ds_read_b64_tr_b16 a[32:33], v202 offset:0\n\t"
  "v_mfma_f32_16x16x32_bf16 v[88:91], a[44:47], a[24:27], v[88:91]\n\t"
  "ds_read_b64_tr_b16 a[34:35], v202 offset:6144\n\t"
  "v_mfma_f32_16x16x32_bf16 v[84:87], a[44:47], a[20:23], v[84:87]\n\t"
  "v_mfma_f32_16x16x32_bf16 v[80:83], a[44:47], a[16:19], v[80:83]\n\t"
  "ds_read_b128 a[8:11], v200 offset:4096\n\t"
  "s_waitcnt lgkmcnt(7)\n\t"
  "v_mfma_f32_16x16x32_bf16 v[96:99], a[16:19], a[48:51], v[96:99]\n\t"
  "ds_read_b64_tr_b16 a[36:37], v203 offset:0\n\t"
  "v_mfma_f32_16x16x32_bf16 v[100:103], a[20:23], a[48:51], v[100:103]\n\t"
  "ds_read_b64_tr_b16 a[38:39], v203 offset:6144\n\t"
  "v_mfma_f32_16x16x32_bf16 v[104:107], a[24:27], a[48:51], v[104:107]\n\t"
  "v_mfma_f32_16x16x32_bf16 v[108:111], a[28:31], a[48:51], v[108:111]\n\t"
  "ds_read_b128 a[12:15], v200 offset:6144\n\t"
  "s_waitcnt lgkmcnt(7)\n\t"
  "v_mfma_f32_16x16x32_bf16 v[124:127], a[28:31], a[52:55], v[124:127]\n\t"
  "ds_read_b64_tr_b16 a[40:41], v202 offset:1024\n\t"
  "v_mfma_f32_16x16x32_bf16 v[120:123], a[24:27], a[52:55], v[120:123]\n\t"
  "ds_read_b64_tr_b16 a[42:43], v202 offset:7168\n\t"
  "v_mfma_f32_16x16x32_bf16 v[116:119], a[20:23], a[52:55], v[116:119]\n\t"
  "v_mfma_f32_16x16x32_bf16 v[112:115], a[16:19], a[52:55], v[112:115]\n\t"
  "s_waitcnt lgkmcnt(2)\n\t"
  "v_mfma_f32_16x16x32_bf16 v[32:35], a[32:35], a[0:3], v[32:35]\n\t"
  "ds_read_b64_tr_b16 a[44:45], v203 offset:1024\n\t"
  "s_waitcnt vmcnt(0)\n\t"
  "v_mfma_f32_16x16x32_bf16 v[36:39], a[32:35], a[4:7], v[36:39]\n\t"
  "ds_read_b64_tr_b16 a[46:47], v203 offset:7168\n\t"
  "v_smfmac_f32_16x16x64_bf16 v[192:195], a[60:63], a[64:71], v19\n\t"
  "v_mfma_f32_16x16x32_bf16 v[40:43], a[32:35], a[8:11], v[40:43]\n\t"
  "v_mfma_f32_16x16x32_bf16 v[44:47], a[32:35], a[12:15], v[44:47]\n\t"
  "v_mfma_f32_16x16x32_bf16 v[60:63], a[36:39], a[12:15], v[60:63]\n\t"
  "ds_read_b64_tr_b16 a[48:49], v202 offset:2048\n\t"
  "v_mfma_f32_16x16x32_bf16 v[56:59], a[36:39], a[8:11], v[56:59]\n\t"
  "ds_read_b64_tr_b16 a[50:51], v202 offset:8192\n\t"
  "v_mfma_f32_16x16x32_bf16 v[52:55], a[36:39], a[4:7], v[52:55]\n\t"
  "v_mfma_f32_16x16x32_bf16 v[48:51], a[36:39], a[0:3], v[48:51]\n\t"
  "ds_read_b128 a[16:19], v201 offset:0\n\t"
  "v_smfmac_f32_16x16x64_bf16 v[128:131], a[60:63], a[32:39], v19\n\t"
  "s_waitcnt lgkmcnt(5)\n\t"
  "v_mfma_f32_16x16x32_bf16 v[64:67], a[40:43], a[0:3], v[64:67]\n\t"
  "ds_read_b64_tr_b16 a[52:53], v203 offset:2048\n\t"
  "v_mfma_f32_16x16x32_bf16 v[68:71], a[40:43], a[4:7], v[68:71]\n\t"
  "ds_read_b64_tr_b16 a[54:55], v203 offset:8192\n\t"
  "v_mfma_f32_16x16x32_bf16 v[72:75], a[40:43], a[8:11], v[72:75]\n\t"
  "v_mfma_f32_16x16x32_bf16 v[76:79], a[40:43], a[12:15], v[76:79]\n\t"
  "ds_read_b128 a[20:23], v201 offset:2048\n\t"
  "s_waitcnt lgkmcnt(6)\n\t"
  "v_mfma_f32_16x16x32_bf16 v[92:95], a[44:47], a[12:15], v[92:95]\n\t"
  "ds_read_b64_tr_b16 a[32:33], v204 offset:0\n\t"
  "v_add_f32 v192, v192, v193\n\t"
  "v_mfma_f32_16x16x32_bf16 v[88:91], a[44:47], a[8:11], v[88:91]\n\t"
  "ds_read_b64_tr_b16 a[34:35], v204 offset:6144\n\t"
  "v_mfma_f32_16x16x32_bf16 v[84:87], a[44:47], a[4:7], v[84:87]\n\t"
  "v_add_f32 v194, v194, v195\n\t"
  "v_mfma_f32_16x16x32_bf16 v[80:83], a[44:47], a[0:3], v[80:83]\n\t"
  "ds_read_b128 a[24:27], v201 offset:4096\n\t"
  "v_smfmac_f32_16x16x64_bf16 v[132:135], a[60:63], a[40:47], v19\n\t"
  "s_waitcnt lgkmcnt(7)\n\t"
  "v_add_f32 v192, v192, v194\n\t"
  "v_mfma_f32_16x16x32_bf16 v[96:99], a[0:3], a[48:51], v[96:99]\n\t"
  "ds_read_b64_tr_b16 a[36:37], v205 offset:0\n\t"
  "v_mfma_f32_16x16x32_bf16 v[100:103], a[4:7], a[48:51], v[100:103]\n\t"
  "ds_read_b64_tr_b16 a[38:39], v205 offset:6144\n\t"
  "v_mfma_f32_16x16x32_bf16 v[104:107], a[8:11], a[48:51], v[104:107]\n\t"
  "v_mfma_f32_16x16x32_bf16 v[108:111], a[12:15], a[48:51], v[108:111]\n\t"
  "ds_read_b128 a[28:31], v201 offset:6144\n\t"
  "s_waitcnt lgkmcnt(7)\n\t"
  "v_add_f32_dpp v192, v192, v192 quad_perm:[1,0,3,2] row_mask:0xf bank_mask:0xf\n\t"
  "v_mfma_f32_16x16x32_bf16 v[124:127], a[12:15], a[52:55], v[124:127]\n\t"
  "ds_read_b64_tr_b16 a[40:41], v204 offset:1024\n\t"
  "v_mfma_f32_16x16x32_bf16 v[120:123], a[8:11], a[52:55], v[120:123]\n\t"
  "ds_read_b64_tr_b16 a[42:43], v204 offset:7168\n\t"
  "v_mfma_f32_16x16x32_bf16 v[116:119], a[4:7], a[52:55], v[116:119]\n\t"
  "v_mfma_f32_16x16x32_bf16 v[112:115], a[0:3], a[52:55], v[112:115]\n\t"
  "v_add_f32_dpp v192, v192, v192 quad_perm:[2,3,0,1] row_mask:0xf bank_mask:0xf\n\t"
  "v_smfmac_f32_16x16x64_bf16 v[136:139], a[60:63], a[48:55], v19\n\t"
  "s_waitcnt lgkmcnt(2)\n\t"
  "v_mfma_f32_16x16x32_bf16 v[32:35], a[32:35], a[16:19], v[32:35]\n\t"
  "ds_read_b64_tr_b16 a[44:45], v205 offset:1024\n\t"
  "v_mfma_f32_16x16x32_bf16 v[36:39], a[32:35], a[20:23], v[36:39]\n\t"
  "ds_read_b64_tr_b16 a[46:47], v205 offset:7168\n\t"
  "v_mfma_f32_16x16x32_bf16 v[40:43], a[32:35], a[24:27], v[40:43]\n\t"
  "v_add_f32_dpp v192, v192, v192 row_half_mirror row_mask:0xf bank_mask:0xf\n\t"
  "v_mfma_f32_16x16x32_bf16 v[44:47], a[32:35], a[28:31], v[44:47]\n\t"
  "v_mfma_f32_16x16x32_bf16 v[60:63], a[36:39], a[28:31], v[60:63]\n\t"
  "ds_read_b64_tr_b16 a[48:49], v204 offset:2048\n\t"
  "v_mfma_f32_16x16x32_bf16 v[56:59], a[36:39], a[24:27], v[56:59]\n\t"
  "ds_read_b64_tr_b16 a[50:51], v204 offset:8192\n\t"
  "v_mfma_f32_16x16x32_bf16 v[52:55], a[36:39], a[20:23], v[52:55]\n\t"
  "ds_write_b128 v20, v[128:131] offset:0\n\t"
  "v_add_f32_dpp v192, v192, v192 row_mirror row_mask:0xf bank_mask:0xf\n\t"
  "v_mfma_f32_16x16x32_bf16 v[48:51], a[36:39], a[16:19], v[48:51]\n\t"
  "ds_write_b128 v20, v[132:135] offset:1024\n\t"
  "s_waitcnt lgkmcnt(6)\n\t"
  "v_mfma_f32_16x16x32_bf16 v[64:67], a[40:43], a[16:19], v[64:67]\n\t"
  "ds_read_b64_tr_b16 a[52:53], v205 offset:2048\n\t"
  "v_mfma_f32_16x16x32_bf16 v[68:71], a[40:43], a[20:23], v[68:71]\n\t"
  "ds_read_b64_tr_b16 a[54:55], v205 offset:8192\n\t"
  "v_mfma_f32_16x16x32_bf16 v[72:75], a[40:43], a[24:27], v[72:75]\n\t"
  "ds_write_b128 v20, v[136:139] offset:2048\n\t"
  "v_mul_f32 v192, 0x3a800000, v192\n\t"
  "v_mfma_f32_16x16x32_bf16 v[76:79], a[40:43], a[28:31], v[76:79]\n\t"
  "s_waitcnt lgkmcnt(7)\n\t"
  "v_mfma_f32_16x16x32_bf16 v[92:95], a[44:47], a[28:31], v[92:95]\n\t"
  "v_cvt_pk_bf16_f32 v193, v192, v192\n\t"
  "v_mfma_f32_16x16x32_bf16 v[88:91], a[44:47], a[24:27], v[88:91]\n\t"
  "v_mfma_f32_16x16x32_bf16 v[84:87], a[44:47], a[20:23], v[84:87]\n\t"
  "v_lshlrev_b32 v194, 16, v193\n\t"
  "v_mfma_f32_16x16x32_bf16 v[80:83], a[44:47], a[16:19], v[80:83]\n\t"
  "s_waitcnt lgkmcnt(5)\n\t"
  "v_mfma_f32_16x16x32_bf16 v[96:99], a[16:19], a[48:51], v[96:99]\n\t"
  "v_sub_f32 v194, v192, v194\n\t"
  "v_mfma_f32_16x16x32_bf16 v[100:103], a[20:23], a[48:51], v[100:103]\n\t"
  "v_mfma_f32_16x16x32_bf16 v[104:107], a[24:27], a[48:51], v[104:107]\n\t"
  "v_cvt_pk_bf16_f32 v218, v192, v194\n\t"
  "v_mfma_f32_16x16x32_bf16 v[108:111], a[28:31], a[48:51], v[108:111]\n\t"
  "s_waitcnt lgkmcnt(1)\n\t"
  "v_mfma_f32_16x16x32_bf16 v[124:127], a[28:31], a[52:55], v[124:127]\n\t"
  "v_mfma_f32_16x16x32_bf16 v[120:123], a[24:27], a[52:55], v[120:123]\n\t"
  "v_mfma_f32_16x16x32_bf16 v[116:119], a[20:23], a[52:55], v[116:119]\n\t"
  "v_mfma_f32_16x16x32_bf16 v[112:115], a[16:19], a[52:55], v[112:115]\n\t"
  "s_waitcnt lgkmcnt(0)\n\t"
  "s_barrier\n\t"
  "s_nop 7\n\t"
     : "={v[32:63]}"(cq), "={v[64:95]}"(ck), "={v[96:127]}"(cv), "={v[140:143]}"(gr), "={v218}"(wm8), "={s[34:35]}"(tag)
     : "{s[20:23]}"(sx), "{s[24:27]}"(sw), "{s30}"(m0x), "{s44}"(m0w), "{v[0:15]}"(av), "{s[48:51]}"(so), "{s[52:55]}"(sws), "{v[16:23]}"(aw), "{s[46:47]}"(ws + WS_EP)
     : "memory", "scc", "s28", "s29", "s31", "s36", "s37", "v128","v129","v130","v131","v132","v133","v134","v135","v136","v137","v138","v139","v144","v145","v146","v147","v192","v193","v194","v195","v196","v197","v198","v199","v200","v201","v202","v203","v204","v205","v206","v207","v208","v209","v210","v211","v212","v213","v214","v215","v216","v217","a0","a1","a2","a3","a4","a5","a6","a7","a8","a9","a10","a11","a12","a13","a14","a15","a16","a17","a18","a19","a20","a21","a22","a23","a24","a25","a26","a27","a28","a29","a30","a31","a32","a33","a34","a35","a36","a37","a38","a39","a40","a41","a42","a43","a44","a45","a46","a47","a48","a49","a50","a51","a52","a53","a54","a55","a56","a57","a58","a59","a60","a61","a62","a63","a64","a65","a66","a67","a68","a69","a70","a71");
    const unsigned l2 = lane_id(), r = l2 & 15, g = l2 >> 4;
#pragma unroll 1
    for (unsigned spin = 0; spin < (1u << 17) && __any(gr[1] != (unsigned)tag || gr[3] != (unsigned)tag); spin++)
      asm volatile("buffer_load_dwordx4 %0, %1, %2, 0 offen sc1\n\ts_waitcnt vmcnt(0)" : "=&v"(gr) : "v"(aw[1]), "s"(sws) : "memory");
    lds_f tb = (lds_f)(size_t)(81920 + wid * 3072), tc = (lds_f)(size_t)(81920 + (wid ^ 2) * 3072), tf = (lds_f)(size_t)(94208 + wid * 1024);
    { const int co = (g < 3 ? g : 0) * 256 + r * 4;
      const f32x4 c = *(lds_f4)(tb + co) + *(lds_f4)(tc + co);
      const float rs = __uint_as_float(gr[2]);
      tf[l2] = rs; tf[64 + l2] = -__uint_as_float(gr[0]) * rs;
      tf[128 + g * 32 + r] = c[0] + c[1]; tf[128 + g * 32 + 16 + r] = c[2] + c[3]; }
    f32x4 cs[4]; float csv[2];
#pragma unroll
    for (int n = 0; n < 4; n++) cs[n] = *(lds_f4)(tf + 128 + n * 16 + g * 4);
#pragma unroll
    for (int n = 0; n < 2; n++) csv[n] = tf[192 + n * 16 + r];
    constexpr float QS = 0.17677669529663687f * 1.4426950408889634f;
#pragma unroll
    for (int e = 0; e < 2; e++) {
      const int blk = bm * 4 + wm * 2 + e;
      const unsigned qp = WS_Q + ((jq * 2 + wn) * 64 + blk) * 2048 + g * 512 + r * 16;
      const unsigned kp = WS_KV + ((jk * 2 + wn) * 64 + blk) * 4096 + g * 512 + ((r >> 3) * 16 + (r & 7)) * 16;
      const unsigned vp = WS_KV + ((jv * 2 + wn) * 64 + blk) * 4096 + 2048 + g * 512 + ((r >> 3) * 16 + (r & 7)) * 16;
      const f32x4 ra = *(lds_f4)(tf + e * 32 + g * 4), rb = *(lds_f4)(tf + e * 32 + 16 + g * 4), ma = *(lds_f4)(tf + 64 + e * 32 + g * 4), mb = *(lds_f4)(tf + 64 + e * 32 + 16 + g * 4);
#pragma unroll
      for (int m = 0; m < 2; m++) {
        const int i0 = (2 * e + m) * 4, i1 = i0 + 16;
        const float rs = tf[(2 * e + m) * 16 + r], nm = tf[64 + (2 * e + m) * 16 + r];
        f32x4 a = (f32x4){ck[i0], ck[i0 + 1], ck[i0 + 2], ck[i0 + 3]} * rs + cs[2] * nm, b = (f32x4){ck[i1], ck[i1 + 1], ck[i1 + 2], ck[i1 + 3]} * rs + cs[3] * nm;
        __builtin_amdgcn_raw_buffer_store_b128((u32x4){cvtpk(a[0], a[1]), cvtpk(a[2], a[3]), cvtpk(b[0], b[1]), cvtpk(b[2], b[3])}, rws, kp + m * 128, 0, 16);
        const int j0 = m * 16 + e * 8, j1 = j0 + 4;
        a = (f32x4){cv[j0], cv[j0 + 1], cv[j0 + 2], cv[j0 + 3]} * ra + ma * csv[m]; b = (f32x4){cv[j1], cv[j1 + 1], cv[j1 + 2], cv[j1 + 3]} * rb + mb * csv[m];
        __builtin_amdgcn_raw_buffer_store_b128((u32x4){cvtpk(a[0], a[1]), cvtpk(a[2], a[3]), cvtpk(b[0], b[1]), cvtpk(b[2], b[3])}, rws, vp + m * 128, 0, 16);
        const float rq = rs * QS, nq = nm * QS;
        a = (f32x4){cq[i0], cq[i0 + 1], cq[i0 + 2], cq[i0 + 3]} * rq + cs[0] * nq; b = (f32x4){cq[i1], cq[i1 + 1], cq[i1 + 2], cq[i1 + 3]} * rq + cs[1] * nq;
        __builtin_amdgcn_raw_buffer_store_b128((u32x4){cvtpk(a[0], a[1]), cvtpk(a[2], a[3]), cvtpk(b[0], b[1]), cvtpk(b[2], b[3])}, rws, qp + m * 256, 0, 16);
      }
    }
  __builtin_amdgcn_sched_barrier(0);
  }
  CODE_PF(katt_begin, kfd_begin, 1, 81664);
  { const unsigned lane = lane_id(), l16 = lane * 16;
    tv[0] = l16; tv[1] = WS_KV + head * 262144 + wid * 4096 + l16; tv[2] = wid * 4096 + l16;
    tv[3] = (lane & 1) ? 0xEEEEEEEEu : 0x44444444u;
    const unsigned one = (lane & 2) ? 0u : 0x3f803f80u;
    tv[4] = tv[5] = (lane & 4) ? 0u : one; tv[6] = tv[7] = (lane & 4) ? one : 0u;
    const unsigned idle = rr < 28 ? 0u : OOB, swz = ((lane & 7) ^ ((lane >> 3) & 6)) * 16;
    const unsigned gsrc = ((lane >> 3) * (F_ * 2) + swz) | idle, dsrc = ((nat ? (lane >> 5) * 8 + ((lane >> 3) & 3) : (lane >> 3)) * 2048 + swz) | idle, li = l16 | idle;
#pragma unroll
    for (int b = 0; b < 2; b++) {
      const unsigned be = 2 * u + b, kb = (be * 293) >> 11, c = be - kb * 7;
      rv[b] = gsrc + kb * (32 * F_ * 2) + (xcd * 7 + c) * 128;
      const unsigned tb = (unsigned)((c < 3 ? 0x5A928718A418820ull : c < 6 ? 0xCE3D6AD2728C1ACull : 0xEF37Aull) >> ((c < 3 ? c : c < 6 ? c - 3 : 0) * 20));
      const unsigned gd = WS_WG + (xcd * 128 + kb) * 14336, dd = WS_WD + (xcd * 112 + u) * 8192 + b * 4096 - 2048;
#pragma unroll
      for (int j = 0; j < 4; j++) { rv[2 + b * 4 + j] = li + (gd + ((tb >> (5 * j + 3)) & 3) * 458752 + ((tb >> (5 * j)) & 7) * 2048);
        if (b == 0) rv[12 + j] = li + (dd + j * 1024); else rw[j] = li + (dd + j * 1024); }
      rv[10 + b] = dsrc + (u * 65536 + (xcd * 2 + b) * 128);
    }
  }
  fw[0] = (unsigned)tag; fw[1] = (unsigned)(tag >> 32);
  unsigned bad, late, so8 = kh * 131072;
  f32x16 ia, ib; f32x2 xm; u32x4 b1, b2;
  u32x2 so = {(unsigned)((gn >> 1) * 7 + odd * 4) * 131072, (unsigned)((gn >> 1) * 7 + 3) * 131072};
  u32x4 sd = {(unsigned)kp * 56 * 131072, (unsigned)kp * 56 * 8192, 98304u + wid * 2048, 131072u};
  u32x2 sj = {0u - (52 + 56 * kp) * 131072, 0u - (52 + 56 * kp) * 8192}; unsigned m7 = wid * 7168;
  asm volatile("" : "+{v[0:15]}"(fv), "+{v[16:23]}"(fw), "+{v27}"(mv), "+s"(so), "+s"(sd), "+s"(sj), "+s"(m7));
  {
    u32x4 sw = mksrd(wg, D_ * F_ * 2);
    u32x8 sp = {(unsigned)(size_t)wu, (unsigned)((size_t)wu >> 32), (unsigned)(size_t)wd, (unsigned)((size_t)wd >> 32), (unsigned)(size_t)wo, (unsigned)((size_t)wo >> 32), (unsigned)(size_t)x, (unsigned)((size_t)x >> 32)};
    u32x8 sq = {56320u, 112640u, 168960u, nat ? 31744u : 15360u, nat ? 6144u : 30720u, nat ? 37888u : 46080u, 65536u + wid * 8192, (unsigned)wid * 1024};
    asm volatile(
  "katt_begin:\n\t"
  "v_mov_b32 v240, s34\n\t"
  "v_mov_b32 v241, s35\n\t"
  "v_accvgpr_write_b32 a16, -1\n\t"
  "v_accvgpr_write_b32 a17, -1\n\t"
  "v_accvgpr_write_b32 a18, -1\n\t"
  "v_accvgpr_write_b32 a19, -1\n\t"
  "v_add_u32 v236, s54, v232\n\t"
  "v_add_u32 v237, s54, v233\n\t"
  "v_add_u32 v238, s54, v234\n\t"
  "v_add_u32 v239, s54, v235\n\t"
  "v_accvgpr_write_b32 a60, 0\n\t"
  "v_accvgpr_write_b32 a61, 0\n\t"
  "v_accvgpr_write_b32 a62, 0\n\t"
  "v_accvgpr_write_b32 a63, 0\n\t"
  "v_mov_b64 v[120:121], 0\n\t"
  "v_mov_b64 v[122:123], 0\n\t"
  "v_mov_b64 v[124:125], 0\n\t"
  "v_mov_b64 v[126:127], 0\n\t"
  "v_mov_b64 v[128:129], 0\n\t"
  "v_mov_b64 v[130:131], 0\n\t"
  "v_mov_b64 v[132:133], 0\n\t"
  "v_mov_b64 v[134:135], 0\n\t"
  "v_mov_b64 v[136:137], 0\n\t"
  "v_mov_b64 v[138:139], 0\n\t"
  "v_mov_b64 v[140:141], 0\n\t"
  "v_mov_b64 v[142:143], 0\n\t"
  "v_mov_b64 v[144:145], 0\n\t"
  "v_mov_b64 v[146:147], 0\n\t"
  "v_mov_b64 v[148:149], 0\n\t"
  "v_mov_b64 v[150:151], 0\n\t"
  "v_mov_b64 v[152:153], 0\n\t"
  "v_mov_b64 v[154:155], 0\n\t"
  "v_mov_b64 v[156:157], 0\n\t"
  "v_mov_b64 v[158:159], 0\n\t"
  "v_mov_b64 v[160:161], 0\n\t"
  "v_mov_b64 v[162:163], 0\n\t"
  "v_mov_b64 v[164:165], 0\n\t"
  "v_mov_b64 v[166:167], 0\n\t"
  "s_waitcnt vmcnt(0) lgkmcnt(0)\n\t"
  "s_barrier\n\t"
  "buffer_store_dwordx2 v[240:241], v224, s[20:23], 0 offen offset:0 sc1\n\t"
  "global_load_dwordx4 v[96:99], v229, s[46:47]\n\t"
  "global_load_dwordx4 v[100:103], v229, s[46:47] offset:64\n\t"
  "global_load_dwordx4 v[104:107], v230, s[46:47]\n\t"
  "global_load_dwordx4 v[108:111], v230, s[46:47] offset:64\n\t"
  "s_cmp_lg_u32 s55, 1024\n\t"
  "s_cbranch_scc1 LP1b_%=\n\t"
  "s_sleep 18\n\t"
  "s_mov_b32 s72, 0x20000\n\t"
  "s_not_b32 s68, s34\n\t"
  "s_mov_b32 s73, 64\n\t"
  "LP1a_%=:\n\t"
  "v_not_b32 v242, s34\n\t"
  "buffer_load_dwordx2 v[242:243], v245, s[20:23], 0 offen offset:0 sc1\n\t"
  "s_mov_b32 s73, 64\n\t"
  "LP1w_%=:\n\t"
  "s_sleep 1\n\t"
  "LP1c_%=:\n\t"
  "v_cmp_eq_u32 vcc, s68, v242\n\t"
  "s_nop 1\n\t"
  "s_cmp_eq_u64 vcc, 0\n\t"
  "v_cmp_ne_u64 vcc, s[34:35], v[242:243]\n\t"
  "s_cbranch_vccz LP1b_%=\n\t"
  "s_cbranch_scc1 LP1r_%=\n\t"
  "s_sub_u32 s73, s73, 1\n\t"
  "s_cbranch_scc0 LP1w_%=\n\t"
  "s_waitcnt vmcnt(0)\n\t"
  "LP1r_%=:\n\t"
  "s_sleep 4\n\t"
  "s_sub_u32 s72, s72, 1\n\t"
  "s_cbranch_scc0 LP1a_%=\n\t"
  "LP1b_%=:\n\t"
  "s_barrier\n\t"
  "v_mov_b32 v245, 0x7b4097ce\n\t"
  "buffer_load_dwordx2 v[112:113], v231, s[20:23], 0 offen offset:0 sc1\n\t"
  "buffer_load_dwordx2 v[114:115], v231, s[20:23], 0 offen offset:512 sc1\n\t"
  "s_mov_b32 m0, s30\n\t"
  "s_mov_b32 s28, 0\n\t"
  "buffer_load_dwordx4 v169, s[20:23], s28 offen offset:0 sc1 lds\n\t"
  "buffer_load_dwordx4 v169, s[20:23], s28 offen offset:1024 sc1 lds\n\t"
  "buffer_load_dwordx4 v169, s[20:23], s28 offen offset:2048 sc1 lds\n\t"
  "buffer_load_dwordx4 v169, s[20:23], s28 offen offset:3072 sc1 lds\n\t"
  "s_add_u32 m0, s30, 49152\n\t"
  "s_add_u32 s28, s28, 16384\n\t"
  "buffer_load_dwordx4 v244, s[20:23], 0 offen offset:0 sc1 lds\n\t"
  "buffer_load_dwordx4 v244, s[20:23], 0 offen offset:1024 sc1 lds\n\t"
  "buffer_load_dwordx4 v244, s[20:23], 0 offen offset:2048 sc1 lds\n\t"
  "buffer_load_dwordx4 v244, s[20:23], 0 offen offset:3072 sc1 lds\n\t"
  "s_mov_b32 s29, 0\n\t"
  "s_add_u32 m0, s30, 16384\n\t"
  "s_mov_b32 s31, 3\n\t"
  "buffer_load_dwordx4 v169, s[20:23], s28 offen offset:0 sc1 lds\n\t"
  "buffer_load_dwordx4 v169, s[20:23], s28 offen offset:1024 sc1 lds\n\t"
  "buffer_load_dwordx4 v169, s[20:23], s28 offen offset:2048 sc1 lds\n\t"
  "buffer_load_dwordx4 v169, s[20:23], s28 offen offset:3072 sc1 lds\n\t"
  "s_add_u32 s28, s28, 16384\n\t"
  "s_mov_b32 s36, 0x40000000\n\t"
  "s_mov_b32 s72, 0x20000\n\t"
  "s_waitcnt vmcnt(12)\n\t"
  "LMa_%=:\n\t"
  "v_xor_b32 v216, s34, v113\n\t"
  "v_xor_b32 v217, s34, v115\n\t"
  "v_or_b32 v216, v216, v217\n\t"
  "v_cmp_ne_u32 vcc, 0, v216\n\t"
  "s_cbranch_vccz LMb_%=\n\t"
  "buffer_load_dwordx2 v[112:113], v231, s[20:23], 0 offen offset:0 sc1\n\t"
  "buffer_load_dwordx2 v[114:115], v231, s[20:23], 0 offen offset:512 sc1\n\t"
  "s_waitcnt vmcnt(0)\n\t"
  "s_sub_u32 s72, s72, 1\n\t"
  "s_cbranch_scc0 LMa_%=\n\t"
  "LMb_%=:\n\t"
  "v_mov_b32 v216, v112\n\t"
  "v_mov_b32 v217, v114\n\t"
  "v_permlane16_swap_b32 v96, v98\n\t"
  "v_permlane16_swap_b32 v97, v99\n\t"
  "v_permlane16_swap_b32 v100, v102\n\t"
  "v_permlane16_swap_b32 v101, v103\n\t"
  "v_lshlrev_b32 v32, 16, v96\n\t"
  "v_and_b32 v33, 0xffff0000, v96\n\t"
  "v_lshlrev_b32 v34, 16, v97\n\t"
  "v_and_b32 v35, 0xffff0000, v97\n\t"
  "v_pk_add_f32 v[32:33], v[32:33], v[216:217] op_sel_hi:[1,0] neg_lo:[0,1] neg_hi:[0,1]\n\t"
  "v_pk_add_f32 v[34:35], v[34:35], v[216:217] op_sel_hi:[1,0] neg_lo:[0,1] neg_hi:[0,1]\n\t"
  "v_lshlrev_b32 v36, 16, v98\n\t"
  "v_and_b32 v37, 0xffff0000, v98\n\t"
  "v_lshlrev_b32 v38, 16, v99\n\t"
  "v_and_b32 v39, 0xffff0000, v99\n\t"
  "v_pk_add_f32 v[36:37], v[36:37], v[216:217] op_sel_hi:[1,0] neg_lo:[0,1] neg_hi:[0,1]\n\t"
  "v_pk_add_f32 v[38:39], v[38:39], v[216:217] op_sel_hi:[1,0] neg_lo:[0,1] neg_hi:[0,1]\n\t"
  "v_lshlrev_b32 v40, 16, v100\n\t"
  "v_and_b32 v41, 0xffff0000, v100\n\t"
  "v_lshlrev_b32 v42, 16, v101\n\t"
  "v_and_b32 v43, 0xffff0000, v101\n\t"
  "v_pk_add_f32 v[40:41], v[40:41], v[216:217] op_sel_hi:[1,0] neg_lo:[0,1] neg_hi:[0,1]\n\t"
  "v_pk_add_f32 v[42:43], v[42:43], v[216:217] op_sel_hi:[1,0] neg_lo:[0,1] neg_hi:[0,1]\n\t"
  "v_lshlrev_b32 v44, 16, v102\n\t"
  "v_and_b32 v45, 0xffff0000, v102\n\t"
  "v_lshlrev_b32 v46, 16, v103\n\t"
  "v_and_b32 v47, 0xffff0000, v103\n\t"
  "v_pk_add_f32 v[44:45], v[44:45], v[216:217] op_sel_hi:[1,0] neg_lo:[0,1] neg_hi:[0,1]\n\t"
  "v_pk_add_f32 v[46:47], v[46:47], v[216:217] op_sel_hi:[1,0] neg_lo:[0,1] neg_hi:[0,1]\n\t"
  "v_permlane16_swap_b32 v104, v106\n\t"
  "v_permlane16_swap_b32 v105, v107\n\t"
  "v_permlane16_swap_b32 v108, v110\n\t"
  "v_permlane16_swap_b32 v109, v111\n\t"
  "v_lshlrev_b32 v200, 16, v104\n\t"
  "v_and_b32 v201, 0xffff0000, v104\n\t"
  "v_lshlrev_b32 v202, 16, v105\n\t"
  "v_and_b32 v203, 0xffff0000, v105\n\t"
  "v_pk_add_f32 v[200:201], v[200:201], v[216:217] op_sel:[0,1] op_sel_hi:[1,1] neg_lo:[0,1] neg_hi:[0,1]\n\t"
  "v_pk_add_f32 v[202:203], v[202:203], v[216:217] op_sel:[0,1] op_sel_hi:[1,1] neg_lo:[0,1] neg_hi:[0,1]\n\t"
  "v_lshlrev_b32 v204, 16, v106\n\t"
  "v_and_b32 v205, 0xffff0000, v106\n\t"
  "v_lshlrev_b32 v206, 16, v107\n\t"
  "v_and_b32 v207, 0xffff0000, v107\n\t"
  "v_pk_add_f32 v[204:205], v[204:205], v[216:217] op_sel:[0,1] op_sel_hi:[1,1] neg_lo:[0,1] neg_hi:[0,1]\n\t"
  "v_pk_add_f32 v[206:207], v[206:207], v[216:217] op_sel:[0,1] op_sel_hi:[1,1] neg_lo:[0,1] neg_hi:[0,1]\n\t"
  "v_lshlrev_b32 v208, 16, v108\n\t"
  "v_and_b32 v209, 0xffff0000, v108\n\t"
  "v_lshlrev_b32 v210, 16, v109\n\t"
  "v_and_b32 v211, 0xffff0000, v109\n\t"
  "v_pk_add_f32 v[208:209], v[208:209], v[216:217] op_sel:[0,1] op_sel_hi:[1,1] neg_lo:[0,1] neg_hi:[0,1]\n\t"
  "v_pk_add_f32 v[210:211], v[210:211], v[216:217] op_sel:[0,1] op_sel_hi:[1,1] neg_lo:[0,1] neg_hi:[0,1]\n\t"
  "v_lshlrev_b32 v212, 16, v110\n\t"
  "v_and_b32 v213, 0xffff0000, v110\n\t"
  "v_lshlrev_b32 v214, 16, v111\n\t"
  "v_and_b32 v215, 0xffff0000, v111\n\t"
  "v_pk_add_f32 v[212:213], v[212:213], v[216:217] op_sel:[0,1] op_sel_hi:[1,1] neg_lo:[0,1] neg_hi:[0,1]\n\t"
  "v_pk_add_f32 v[214:215], v[214:215], v[216:217] op_sel:[0,1] op_sel_hi:[1,1] neg_lo:[0,1] neg_hi:[0,1]\n\t"
  "v_add_u32 v229, 15360, v225\n\t"
  "v_add_u32 v230, 30720, v225\n\t"
  "v_add_u32 v231, 46080, v225\n\t"
  "s_add_u32 s46, s44, 0x80000\n\t"
  "s_addc_u32 s47, s45, 0\n\t"
  "s_add_u32 s39, s30, 98304\n\t"
  "s_lshl_b32 s37, s55, 2\n\t"
  "s_and_b32 s37, s37, 0x1000\n\t"
  "v_add_u32 v232, s37, v232\n\t"
  "v_add_u32 v233, s37, v233\n\t"
  "v_add_u32 v234, s37, v234\n\t"
  "v_add_u32 v235, s37, v235\n\t"
  "v_add_u32 v196, 0x10000, v232\n\t"
  "v_add_u32 v197, 0x10000, v233\n\t"
  "v_add_u32 v198, 0x10000, v234\n\t"
  "v_add_u32 v199, 0x10000, v235\n\t"
  "v_add_u32 v252, 0x20000, v232\n\t"
  "v_add_u32 v253, 0x20000, v233\n\t"
  "v_add_u32 v254, 0x20000, v234\n\t"
  "v_add_u32 v255, 0x20000, v235\n\t"
  "s_waitcnt vmcnt(4)\n\t"
  "s_barrier\n\t"
  "ds_read_b128 a[32:35], v168 offset:0\n\t"
  "ds_read_b128 a[64:67], v170 offset:49152\n\t"
  "ds_read_b128 a[40:43], v168 offset:1024\n\t"
  "ds_read_b128 a[68:71], v170 offset:50176\n\t"
  "ds_read_b128 a[72:75], v170 offset:51200\n\t"
  "ds_read_b128 a[76:79], v170 offset:52224\n\t"
  "ds_read_b128 a[36:39], v168 offset:4096\n\t"
  "ds_read_b128 a[44:47], v168 offset:5120\n\t"
  "ds_read_b128 a[48:51], v168 offset:2048\n\t"
  "ds_read_b128 a[56:59], v168 offset:3072\n\t"
  "ds_read_b128 a[52:55], v168 offset:6144\n\t"
  "s_waitcnt lgkmcnt(9)\n\t"
  "v_mfma_f32_32x32x16_bf16 v[80:95], a[32:35], a[64:67], 0\n\t"
  "s_waitcnt lgkmcnt(7)\n\t"
  "v_mfma_f32_32x32x16_bf16 v[80:95], a[40:43], a[68:71], v[80:95]\n\t"
  "s_waitcnt lgkmcnt(5)\n\t"
  "v_mfma_f32_32x32x16_bf16 v[96:111], a[32:35], a[72:75], 0\n\t"
  "v_mfma_f32_32x32x16_bf16 v[96:111], a[40:43], a[76:79], v[96:111]\n\t"
  "s_add_u32 m0, s30, 32768\n\t"
  "s_nop 0\n\t"
  "buffer_load_dwordx4 v169, s[20:23], s28 offen offset:0 sc1 lds\n\t"
  "buffer_load_dwordx4 v169, s[20:23], s28 offen offset:1024 sc1 lds\n\t"
  "buffer_store_short v218, v250, s[20:23], 0 offen sc1\n\t"
  "buffer_store_short_d16_hi v218, v250, s[20:23], 0 offen offset:2048 sc1\n\t"
  "s_nop 4\n\t"
  "v_max3_f32 v48, v80, v81, v82\n\t"
  "v_max3_f32 v49, v83, v84, v85\n\t"
  "v_max3_f32 v48, v48, v86, v87\n\t"
  "v_max3_f32 v49, v49, v88, v89\n\t"
  "v_max3_f32 v48, v48, v90, v91\n\t"
  "v_max3_f32 v49, v49, v92, v93\n\t"
  "v_max3_f32 v48, v48, v94, v95\n\t"
  "v_max_f32 v48, v48, v49\n\t"
  "v_mov_b32 v49, v48\n\t"
  "s_nop 1\n\t"
  "v_permlane32_swap_b32 v48, v49\n\t"
  "s_nop 1\n\t"
  "v_min_f32_e64 v48, -v48, -v49\n\t"
  "v_mov_b32 v49, v48\n\t"
  "v_mov_b64 v[50:51], v[48:49]\n\t"
  "v_mov_b64 v[52:53], v[48:49]\n\t"
  "v_mov_b64 v[54:55], v[48:49]\n\t"
  "v_mov_b64 v[56:57], v[48:49]\n\t"
  "v_mov_b64 v[58:59], v[48:49]\n\t"
  "v_mov_b64 v[60:61], v[48:49]\n\t"
  "v_mov_b64 v[62:63], v[48:49]\n\t"
  "v_max3_f32 v64, v96, v97, v98\n\t"
  "v_max3_f32 v65, v99, v100, v101\n\t"
  "v_max3_f32 v64, v64, v102, v103\n\t"
  "v_max3_f32 v65, v65, v104, v105\n\t"
  "v_max3_f32 v64, v64, v106, v107\n\t"
  "v_max3_f32 v65, v65, v108, v109\n\t"
  "v_max3_f32 v64, v64, v110, v111\n\t"
  "v_max_f32 v64, v64, v65\n\t"
  "v_mov_b32 v65, v64\n\t"
  "s_nop 1\n\t"
  "v_permlane32_swap_b32 v64, v65\n\t"
  "s_nop 1\n\t"
  "v_min_f32_e64 v64, -v64, -v65\n\t"
  "v_mov_b32 v65, v64\n\t"
  "v_mov_b64 v[66:67], v[64:65]\n\t"
  "v_mov_b64 v[68:69], v[64:65]\n\t"
  "v_mov_b64 v[70:71], v[64:65]\n\t"
  "v_mov_b64 v[72:73], v[64:65]\n\t"
  "v_mov_b64 v[74:75], v[64:65]\n\t"
  "v_mov_b64 v[76:77], v[64:65]\n\t"
  "v_mov_b64 v[78:79], v[64:65]\n\t"
  "v_pk_add_f32 v[80:81], v[80:81], v[48:49]\n\t"
  "v_pk_add_f32 v[82:83], v[82:83], v[48:49]\n\t"
  "v_pk_add_f32 v[84:85], v[84:85], v[48:49]\n\t"
  "v_pk_add_f32 v[86:87], v[86:87], v[48:49]\n\t"
  "v_pk_add_f32 v[88:89], v[88:89], v[48:49]\n\t"
  "v_pk_add_f32 v[90:91], v[90:91], v[48:49]\n\t"
  "v_pk_add_f32 v[92:93], v[92:93], v[48:49]\n\t"
  "v_pk_add_f32 v[94:95], v[94:95], v[48:49]\n\t"
  "s_waitcnt vmcnt(4) lgkmcnt(0)\n\t"
  "LATT_%=:\n\t"
  "v_mfma_f32_32x32x16_bf16 v[96:111], a[32:35], a[72:75], v[64:79]\n\t"
  "ds_read_b128 a[32:35], v168 offset:8192\n\t"
  "v_exp_f32 v80, v80\n\t"
  "v_exp_f32 v81, v81\n\t"
  "v_exp_f32 v82, v82\n\t"
  "v_exp_f32 v83, v83\n\t"
  "v_mfma_f32_32x32x16_bf16 v[144:159], a[60:63], v[124:127], v[144:159]\n\t"
  "s_add_u32 m0, s30, 32768\n\t"
  "v_exp_f32 v84, v84\n\t"
  "v_exp_f32 v85, v85\n\t"
  "v_cvt_pk_bf16_f32 v112, v80, v81\n\t"
  "v_exp_f32 v86, v86\n\t"
  "v_cvt_pk_bf16_f32 v113, v82, v83\n\t"
  "v_mfma_f32_32x32x16_bf16 v[96:111], a[40:43], a[76:79], v[96:111]\n\t"
  "ds_read_b128 a[40:43], v168 offset:9216\n\t"
  "v_exp_f32 v87, v87\n\t"
  "v_exp_f32 v88, v88\n\t"
  "v_cvt_pk_bf16_f32 v114, v84, v85\n\t"
  "v_exp_f32 v89, v89\n\t"
  "v_cvt_pk_bf16_f32 v115, v86, v87\n\t"
  "v_smfmac_f32_16x16x64_bf16 v[164:167], v[172:175], v[120:127], v171\n\t"
  "buffer_load_dwordx4 v169, s[20:23], s28 offen offset:2048 sc1 lds\n\t"
  "v_exp_f32 v90, v90\n\t"
  "v_exp_f32 v91, v91\n\t"
  "v_cvt_pk_bf16_f32 v116, v88, v89\n\t"
  "v_exp_f32 v92, v92\n\t"
  "v_mfma_f32_32x32x16_bf16 v[128:143], a[48:51], v[112:115], v[128:143]\n\t"
  "s_waitcnt lgkmcnt(2)\n\t"
  "v_exp_f32 v93, v93\n\t"
  "v_exp_f32 v94, v94\n\t"
  "v_cvt_pk_bf16_f32 v117, v90, v91\n\t"
  "v_exp_f32 v95, v95\n\t"
  "v_cvt_pk_bf16_f32 v118, v92, v93\n\t"
  "v_cvt_pk_bf16_f32 v119, v94, v95\n\t"
  "v_mfma_f32_32x32x16_bf16 v[80:95], a[36:39], a[64:67], v[48:63]\n\t"
  "s_add_u32 m0, s54, 0\n\t"
  "v_exp_f32 v96, v96\n\t"
  "v_exp_f32 v97, v97\n\t"
  "v_exp_f32 v98, v98\n\t"
  "v_exp_f32 v99, v99\n\t"
  "v_mfma_f32_32x32x16_bf16 v[128:143], a[56:59], v[116:119], v[128:143]\n\t"
  "v_exp_f32 v100, v100\n\t"
  "v_exp_f32 v101, v101\n\t"
  "v_cvt_pk_bf16_f32 v120, v96, v97\n\t"
  "v_exp_f32 v102, v102\n\t"
  "v_cvt_pk_bf16_f32 v121, v98, v99\n\t"
  "v_mfma_f32_32x32x16_bf16 v[80:95], a[44:47], a[68:71], v[80:95]\n\t"
  "ds_read_b128 a[60:63], v168 offset:7168\n\t"
  "v_exp_f32 v103, v103\n\t"
  "v_exp_f32 v104, v104\n\t"
  "v_cvt_pk_bf16_f32 v122, v100, v101\n\t"
  "v_exp_f32 v105, v105\n\t"
  "v_cvt_pk_bf16_f32 v123, v102, v103\n\t"
  "v_smfmac_f32_16x16x64_bf16 v[160:163], v[172:175], v[112:119], v171\n\t"
  "buffer_load_dwordx4 v176, s[24:27], 0 offen offset:0 lds\n\t"
  "v_exp_f32 v106, v106\n\t"
  "v_exp_f32 v107, v107\n\t"
  "v_cvt_pk_bf16_f32 v124, v104, v105\n\t"
  "v_exp_f32 v108, v108\n\t"
  "v_mfma_f32_32x32x16_bf16 v[144:159], a[48:51], v[120:123], v[144:159]\n\t"
  "ds_read_b128 a[48:51], v168 offset:10240\n\t"
  "v_exp_f32 v109, v109\n\t"
  "v_exp_f32 v110, v110\n\t"
  "v_cvt_pk_bf16_f32 v125, v106, v107\n\t"
  "v_exp_f32 v111, v111\n\t"
  "v_cvt_pk_bf16_f32 v126, v108, v109\n\t"
  "v_cvt_pk_bf16_f32 v127, v110, v111\n\t"
  "v_mfma_f32_32x32x16_bf16 v[96:111], a[36:39], a[72:75], v[64:79]\n\t"
  "ds_read_b128 a[36:39], v168 offset:12288\n\t"
  "v_exp_f32 v80, v80\n\t"
  "v_exp_f32 v81, v81\n\t"
  "v_exp_f32 v82, v82\n\t"
  "v_exp_f32 v83, v83\n\t"
  "v_mfma_f32_32x32x16_bf16 v[144:159], a[56:59], v[124:127], v[144:159]\n\t"
  "s_add_u32 m0, s30, 32768\n\t"
  "v_exp_f32 v84, v84\n\t"
  "v_exp_f32 v85, v85\n\t"
  "v_cvt_pk_bf16_f32 v112, v80, v81\n\t"
  "v_exp_f32 v86, v86\n\t"
  "v_cvt_pk_bf16_f32 v113, v82, v83\n\t"
  "v_mfma_f32_32x32x16_bf16 v[96:111], a[44:47], a[76:79], v[96:111]\n\t"
  "ds_read_b128 a[44:47], v168 offset:13312\n\t"
  "v_exp_f32 v87, v87\n\t"
  "v_exp_f32 v88, v88\n\t"
  "v_cvt_pk_bf16_f32 v114, v84, v85\n\t"
  "v_exp_f32 v89, v89\n\t"
  "v_cvt_pk_bf16_f32 v115, v86, v87\n\t"
  "v_smfmac_f32_16x16x64_bf16 v[164:167], v[172:175], v[120:127], v171\n\t"
  "buffer_load_dwordx4 v169, s[20:23], s28 offen offset:3072 sc1 lds\n\t"
  "v_exp_f32 v90, v90\n\t"
  "v_exp_f32 v91, v91\n\t"
  "v_cvt_pk_bf16_f32 v116, v88, v89\n\t"
  "v_exp_f32 v92, v92\n\t"
  "v_mfma_f32_32x32x16_bf16 v[128:143], a[52:55], v[112:115], v[128:143]\n\t"
  "s_waitcnt lgkmcnt(2)\n\t"
  "s_add_u32 s28, s28, 16384\n\t"
  "v_exp_f32 v93, v93\n\t"
  "v_exp_f32 v94, v94\n\t"
  "v_cvt_pk_bf16_f32 v117, v90, v91\n\t"
  "v_exp_f32 v95, v95\n\t"
  "v_cvt_pk_bf16_f32 v118, v92, v93\n\t"
  "v_cvt_pk_bf16_f32 v119, v94, v95\n\t"
  "v_mfma_f32_32x32x16_bf16 v[80:95], a[32:35], a[64:67], v[48:63]\n\t"
  "s_add_u32 m0, s54, 0\n\t"
  "v_exp_f32 v96, v96\n\t"
  "v_exp_f32 v97, v97\n\t"
  "v_exp_f32 v98, v98\n\t"
  "v_exp_f32 v99, v99\n\t"
  "v_mfma_f32_32x32x16_bf16 v[128:143], a[60:63], v[116:119], v[128:143]\n\t"
  "s_waitcnt vmcnt(10)\n\t"
  "v_exp_f32 v100, v100\n\t"
  "v_exp_f32 v101, v101\n\t"
  "v_cvt_pk_bf16_f32 v120, v96, v97\n\t"
  "v_exp_f32 v102, v102\n\t"
  "v_cvt_pk_bf16_f32 v121, v98, v99\n\t"
  "v_mfma_f32_32x32x16_bf16 v[80:95], a[40:43], a[68:71], v[80:95]\n\t"
  "s_barrier\n\t"
  "v_exp_f32 v103, v103\n\t"
  "v_exp_f32 v104, v104\n\t"
  "v_cvt_pk_bf16_f32 v122, v100, v101\n\t"
  "v_exp_f32 v105, v105\n\t"
  "v_cvt_pk_bf16_f32 v123, v102, v103\n\t"
  "v_smfmac_f32_16x16x64_bf16 v[160:163], v[172:175], v[112:119], v171\n\t"
  "buffer_load_dwordx4 v176, s[24:27], s48 offen offset:1024 lds\n\t"
  "ds_read_b128 a[56:59], v168 offset:11264\n\t"
  "v_exp_f32 v106, v106\n\t"
  "v_exp_f32 v107, v107\n\t"
  "v_cvt_pk_bf16_f32 v124, v104, v105\n\t"
  "v_exp_f32 v108, v108\n\t"
  "v_mfma_f32_32x32x16_bf16 v[144:159], a[52:55], v[120:123], v[144:159]\n\t"
  "ds_read_b128 a[52:55], v168 offset:14336\n\t"
  "v_exp_f32 v109, v109\n\t"
  "v_exp_f32 v110, v110\n\t"
  "v_cvt_pk_bf16_f32 v125, v106, v107\n\t"
  "v_exp_f32 v111, v111\n\t"
  "v_cvt_pk_bf16_f32 v126, v108, v109\n\t"
  "v_cvt_pk_bf16_f32 v127, v110, v111\n\t"
  "v_mfma_f32_32x32x16_bf16 v[96:111], a[32:35], a[72:75], v[64:79]\n\t"
  "ds_read_b128 a[32:35], v168 offset:16384\n\t"
  "v_exp_f32 v80, v80\n\t"
  "v_exp_f32 v81, v81\n\t"
  "v_exp_f32 v82, v82\n\t"
  "v_exp_f32 v83, v83\n\t"
  "v_mfma_f32_32x32x16_bf16 v[144:159], a[60:63], v[124:127], v[144:159]\n\t"
  "s_add_u32 m0, s30, 49152\n\t"
  "ds_read_b64_tr_b16 a[0:1], v236 offset:4096\n\t"
  "ds_read_b64_tr_b16 a[2:3], v236 offset:6144\n\t"
  "v_exp_f32 v84, v84\n\t"
  "v_exp_f32 v85, v85\n\t"
  "v_cvt_pk_bf16_f32 v112, v80, v81\n\t"
  "v_exp_f32 v86, v86\n\t"
  "v_cvt_pk_bf16_f32 v113, v82, v83\n\t"
  "v_mfma_f32_32x32x16_bf16 v[96:111], a[40:43], a[76:79], v[96:111]\n\t"
  "ds_read_b128 a[40:43], v168 offset:17408\n\t"
  "v_exp_f32 v87, v87\n\t"
  "v_exp_f32 v88, v88\n\t"
  "v_cvt_pk_bf16_f32 v114, v84, v85\n\t"
  "v_exp_f32 v89, v89\n\t"
  "v_cvt_pk_bf16_f32 v115, v86, v87\n\t"
  "v_smfmac_f32_16x16x64_bf16 v[164:167], v[172:175], v[120:127], v171\n\t"
  "buffer_load_dwordx4 v169, s[20:23], s28 offen offset:0 sc1 lds\n\t"
  "v_exp_f32 v90, v90\n\t"
  "v_exp_f32 v91, v91\n\t"
  "v_cvt_pk_bf16_f32 v116, v88, v89\n\t"
  "v_exp_f32 v92, v92\n\t"
  "v_mfma_f32_32x32x16_bf16 v[128:143], a[48:51], v[112:115], v[128:143]\n\t"
  "s_waitcnt lgkmcnt(4)\n\t"
  "v_exp_f32 v93, v93\n\t"
  "v_exp_f32 v94, v94\n\t"
  "v_cvt_pk_bf16_f32 v117, v90, v91\n\t"
  "v_exp_f32 v95, v95\n\t"
  "v_cvt_pk_bf16_f32 v118, v92, v93\n\t"
  "v_cvt_pk_bf16_f32 v119, v94, v95\n\t"
  "v_mfma_f32_32x32x16_bf16 v[80:95], a[36:39], a[64:67], v[48:63]\n\t"
  "s_add_u32 m0, s54, 0\n\t"
  "v_exp_f32 v96, v96\n\t"
  "v_exp_f32 v97, v97\n\t"
  "v_exp_f32 v98, v98\n\t"
  "v_exp_f32 v99, v99\n\t"
  "v_mfma_f32_32x32x16_bf16 v[128:143], a[56:59], v[116:119], v[128:143]\n\t"
  "ds_read_b64_tr_b16 a[4:5], v237 offset:4096\n\t"
  "ds_read_b64_tr_b16 a[6:7], v237 offset:6144\n\t"
  "v_exp_f32 v100, v100\n\t"
  "v_exp_f32 v101, v101\n\t"
  "v_cvt_pk_bf16_f32 v120, v96, v97\n\t"
  "v_exp_f32 v102, v102\n\t"
  "v_cvt_pk_bf16_f32 v121, v98, v99\n\t"
  "v_mfma_f32_32x32x16_bf16 v[80:95], a[44:47], a[68:71], v[80:95]\n\t"
  "ds_read_b128 a[60:63], v168 offset:15360\n\t"
  "v_exp_f32 v103, v103\n\t"
  "v_exp_f32 v104, v104\n\t"
  "v_cvt_pk_bf16_f32 v122, v100, v101\n\t"
  "v_exp_f32 v105, v105\n\t"
  "v_cvt_pk_bf16_f32 v123, v102, v103\n\t"
  "v_smfmac_f32_16x16x64_bf16 v[160:163], v[172:175], v[112:119], v171\n\t"
  "buffer_load_dwordx4 v176, s[24:27], s49 offen offset:2048 lds\n\t"
  "v_exp_f32 v106, v106\n\t"
  "v_exp_f32 v107, v107\n\t"
  "v_cvt_pk_bf16_f32 v124, v104, v105\n\t"
  "v_exp_f32 v108, v108\n\t"
  "v_mfma_f32_32x32x16_bf16 v[144:159], a[48:51], v[120:123], v[144:159]\n\t"
  "ds_read_b128 a[48:51], v168 offset:18432\n\t"
  "v_exp_f32 v109, v109\n\t"
  "v_exp_f32 v110, v110\n\t"
  "v_cvt_pk_bf16_f32 v125, v106, v107\n\t"
  "v_exp_f32 v111, v111\n\t"
  "v_cvt_pk_bf16_f32 v126, v108, v109\n\t"
  "v_cvt_pk_bf16_f32 v127, v110, v111\n\t"
  "v_mfma_f32_32x32x16_bf16 v[96:111], a[36:39], a[72:75], v[64:79]\n\t"
  "ds_read_b128 a[36:39], v168 offset:20480\n\t"
  "v_exp_f32 v80, v80\n\t"
  "v_exp_f32 v81, v81\n\t"
  "v_exp_f32 v82, v82\n\t"
  "v_exp_f32 v83, v83\n\t"
  "v_mfma_f32_32x32x16_bf16 v[144:159], a[56:59], v[124:127], v[144:159]\n\t"
  "s_add_u32 m0, s30, 49152\n\t"
  "ds_read_b64_tr_b16 a[8:9], v238 offset:4096\n\t"
  "ds_read_b64_tr_b16 a[10:11], v238 offset:6144\n\t"
  "v_exp_f32 v84, v84\n\t"
  "v_exp_f32 v85, v85\n\t"
  "v_cvt_pk_bf16_f32 v112, v80, v81\n\t"
  "v_exp_f32 v86, v86\n\t"
  "v_cvt_pk_bf16_f32 v113, v82, v83\n\t"
  "v_mfma_f32_32x32x16_bf16 v[96:111], a[44:47], a[76:79], v[96:111]\n\t"
  "ds_read_b128 a[44:47], v168 offset:21504\n\t"
  "v_exp_f32 v87, v87\n\t"
  "v_exp_f32 v88, v88\n\t"
  "v_cvt_pk_bf16_f32 v114, v84, v85\n\t"
  "v_exp_f32 v89, v89\n\t"
  "v_cvt_pk_bf16_f32 v115, v86, v87\n\t"
  "v_smfmac_f32_16x16x64_bf16 v[164:167], v[172:175], v[120:127], v171\n\t"
  "buffer_load_dwordx4 v169, s[20:23], s28 offen offset:1024 sc1 lds\n\t"
  "v_exp_f32 v90, v90\n\t"
  "v_exp_f32 v91, v91\n\t"
  "v_cvt_pk_bf16_f32 v116, v88, v89\n\t"
  "v_exp_f32 v92, v92\n\t"
  "v_mfma_f32_32x32x16_bf16 v[128:143], a[52:55], v[112:115], v[128:143]\n\t"
  "s_waitcnt lgkmcnt(4)\n\t"
  "v_exp_f32 v93, v93\n\t"
  "v_exp_f32 v94, v94\n\t"
  "v_cvt_pk_bf16_f32 v117, v90, v91\n\t"
  "v_exp_f32 v95, v95\n\t"
  "v_cvt_pk_bf16_f32 v118, v92, v93\n\t"
  "v_cvt_pk_bf16_f32 v119, v94, v95\n\t"
  "v_mfma_f32_32x32x16_bf16 v[80:95], a[32:35], a[64:67], v[48:63]\n\t"
  "s_add_u32 m0, s54, 0\n\t"
  "v_exp_f32 v96, v96\n\t"
  "v_exp_f32 v97, v97\n\t"
  "v_exp_f32 v98, v98\n\t"
  "v_exp_f32 v99, v99\n\t"
  "v_mfma_f32_32x32x16_bf16 v[128:143], a[60:63], v[116:119], v[128:143]\n\t"
  "ds_read_b64_tr_b16 a[12:13], v239 offset:4096\n\t"
  "ds_read_b64_tr_b16 a[14:15], v239 offset:6144\n\t"
  "v_exp_f32 v100, v100\n\t"
  "v_exp_f32 v101, v101\n\t"
  "v_cvt_pk_bf16_f32 v120, v96, v97\n\t"
  "v_exp_f32 v102, v102\n\t"
  "v_cvt_pk_bf16_f32 v121, v98, v99\n\t"
  "v_mfma_f32_32x32x16_bf16 v[80:95], a[40:43], a[68:71], v[80:95]\n\t"
  "ds_read_b128 a[56:59], v168 offset:19456\n\t"
  "v_exp_f32 v103, v103\n\t"
  "v_exp_f32 v104, v104\n\t"
  "v_cvt_pk_bf16_f32 v122, v100, v101\n\t"
  "v_exp_f32 v105, v105\n\t"
  "v_cvt_pk_bf16_f32 v123, v102, v103\n\t"
  "v_smfmac_f32_16x16x64_bf16 v[160:163], v[172:175], v[112:119], v171\n\t"
  "buffer_load_dwordx4 v176, s[24:27], s50 offen offset:3072 lds\n\t"
  "v_exp_f32 v106, v106\n\t"
  "v_exp_f32 v107, v107\n\t"
  "v_cvt_pk_bf16_f32 v124, v104, v105\n\t"
  "v_exp_f32 v108, v108\n\t"
  "v_mfma_f32_32x32x16_bf16 v[144:159], a[52:55], v[120:123], v[144:159]\n\t"
  "ds_read_b128 a[52:55], v168 offset:22528\n\t"
  "v_exp_f32 v109, v109\n\t"
  "v_exp_f32 v110, v110\n\t"
  "v_cvt_pk_bf16_f32 v125, v106, v107\n\t"
  "v_exp_f32 v111, v111\n\t"
  "v_cvt_pk_bf16_f32 v126, v108, v109\n\t"
  "v_cvt_pk_bf16_f32 v127, v110, v111\n\t"
  "v_mfma_f32_32x32x16_bf16 v[96:111], a[32:35], a[72:75], v[64:79]\n\t"
  "ds_read_b128 a[32:35], v168 offset:24576\n\t"
  "v_exp_f32 v80, v80\n\t"
  "v_exp_f32 v81, v81\n\t"
  "v_exp_f32 v82, v82\n\t"
  "v_exp_f32 v83, v83\n\t"
  "v_mfma_f32_32x32x16_bf16 v[144:159], a[60:63], v[124:127], v[144:159]\n\t"
  "s_add_u32 m0, s30, 49152\n\t"
  "v_exp_f32 v84, v84\n\t"
  "v_exp_f32 v85, v85\n\t"
  "v_cvt_pk_bf16_f32 v112, v80, v81\n\t"
  "v_exp_f32 v86, v86\n\t"
  "v_cvt_pk_bf16_f32 v113, v82, v83\n\t"
  "v_mfma_f32_32x32x16_bf16 v[96:111], a[40:43], a[76:79], v[96:111]\n\t"
  "ds_read_b128 a[40:43], v168 offset:25600\n\t"
  "v_exp_f32 v87, v87\n\t"
  "v_exp_f32 v88, v88\n\t"
  "v_cvt_pk_bf16_f32 v114, v84, v85\n\t"
  "v_exp_f32 v89, v89\n\t"
  "v_cvt_pk_bf16_f32 v115, v86, v87\n\t"
  "v_smfmac_f32_16x16x64_bf16 v[164:167], v[172:175], v[120:127], v171\n\t"
  "buffer_load_dwordx4 v169, s[20:23], s28 offen offset:2048 sc1 lds\n\t"
  "v_exp_f32 v90, v90\n\t"
  "v_exp_f32 v91, v91\n\t"
  "v_cvt_pk_bf16_f32 v116, v88, v89\n\t"
  "v_exp_f32 v92, v92\n\t"
  "v_mfma_f32_32x32x16_bf16 v[128:143], a[48:51], v[112:115], v[128:143]\n\t"
  "s_waitcnt lgkmcnt(2)\n\t"
  "v_exp_f32 v93, v93\n\t"
  "v_exp_f32 v94, v94\n\t"
  "v_cvt_pk_bf16_f32 v117, v90, v91\n\t"
  "v_exp_f32 v95, v95\n\t"
  "v_cvt_pk_bf16_f32 v118, v92, v93\n\t"
  "v_cvt_pk_bf16_f32 v119, v94, v95\n\t"
  "v_mfma_f32_32x32x16_bf16 v[80:95], a[36:39], a[64:67], v[48:63]\n\t"
  "ds_read_b128 a[60:63], v168 offset:23552\n\t"
  "v_exp_f32 v96, v96\n\t"
  "v_exp_f32 v97, v97\n\t"
  "v_exp_f32 v98, v98\n\t"
  "v_exp_f32 v99, v99\n\t"
  "v_mfma_f32_32x32x16_bf16 v[128:143], a[56:59], v[116:119], v[128:143]\n\t"
  "v_exp_f32 v100, v100\n\t"
  "v_exp_f32 v101, v101\n\t"
  "v_cvt_pk_bf16_f32 v120, v96, v97\n\t"
  "v_exp_f32 v102, v102\n\t"
  "v_cvt_pk_bf16_f32 v121, v98, v99\n\t"
  "v_mfma_f32_32x32x16_bf16 v[80:95], a[44:47], a[68:71], v[80:95]\n\t"
  "v_exp_f32 v103, v103\n\t"
  "v_exp_f32 v104, v104\n\t"
  "v_cvt_pk_bf16_f32 v122, v100, v101\n\t"
  "v_exp_f32 v105, v105\n\t"
  "v_cvt_pk_bf16_f32 v123, v102, v103\n\t"
  "v_smfmac_f32_16x16x64_bf16 v[160:163], v[172:175], v[112:119], v171\n\t"
  "buffer_store_dwordx4 a[0:3], v182, s[20:23], s36 offen sc1\n\t"
  "v_exp_f32 v106, v106\n\t"
  "v_exp_f32 v107, v107\n\t"
  "v_cvt_pk_bf16_f32 v124, v104, v105\n\t"
  "v_exp_f32 v108, v108\n\t"
  "v_mfma_f32_32x32x16_bf16 v[144:159], a[48:51], v[120:123], v[144:159]\n\t"
  "ds_read_b128 a[48:51], v168 offset:26624\n\t"
  "v_exp_f32 v109, v109\n\t"
  "v_exp_f32 v110, v110\n\t"
  "v_cvt_pk_bf16_f32 v125, v106, v107\n\t"
  "v_exp_f32 v111, v111\n\t"
  "v_cvt_pk_bf16_f32 v126, v108, v109\n\t"
  "v_cvt_pk_bf16_f32 v127, v110, v111\n\t"
  "v_mfma_f32_32x32x16_bf16 v[96:111], a[36:39], a[72:75], v[64:79]\n\t"
  "ds_read_b128 a[36:39], v168 offset:28672\n\t"
  "v_exp_f32 v80, v80\n\t"
  "v_exp_f32 v81, v81\n\t"
  "v_exp_f32 v82, v82\n\t"
  "v_exp_f32 v83, v83\n\t"
  "v_mfma_f32_32x32x16_bf16 v[144:159], a[56:59], v[124:127], v[144:159]\n\t"
  "s_add_u32 m0, s30, 49152\n\t"
  "v_exp_f32 v84, v84\n\t"
  "v_exp_f32 v85, v85\n\t"
  "v_cvt_pk_bf16_f32 v112, v80, v81\n\t"
  "v_exp_f32 v86, v86\n\t"
  "v_cvt_pk_bf16_f32 v113, v82, v83\n\t"
  "v_mfma_f32_32x32x16_bf16 v[96:111], a[44:47], a[76:79], v[96:111]\n\t"
  "ds_read_b128 a[44:47], v168 offset:29696\n\t"
  "v_exp_f32 v87, v87\n\t"
  "v_exp_f32 v88, v88\n\t"
  "v_cvt_pk_bf16_f32 v114, v84, v85\n\t"
  "v_exp_f32 v89, v89\n\t"
  "v_cvt_pk_bf16_f32 v115, v86, v87\n\t"
  "v_smfmac_f32_16x16x64_bf16 v[164:167], v[172:175], v[120:127], v171\n\t"
  "buffer_load_dwordx4 v169, s[20:23], s28 offen offset:3072 sc1 lds\n\t"
  "v_exp_f32 v90, v90\n\t"
  "v_exp_f32 v91, v91\n\t"
  "v_cvt_pk_bf16_f32 v116, v88, v89\n\t"
  "v_exp_f32 v92, v92\n\t"
  "v_mfma_f32_32x32x16_bf16 v[128:143], a[52:55], v[112:115], v[128:143]\n\t"
  "s_waitcnt lgkmcnt(2)\n\t"
  "s_add_u32 s28, s28, 16384\n\t"
  "v_exp_f32 v93, v93\n\t"
  "v_exp_f32 v94, v94\n\t"
  "v_cvt_pk_bf16_f32 v117, v90, v91\n\t"
  "v_exp_f32 v95, v95\n\t"
  "v_cvt_pk_bf16_f32 v118, v92, v93\n\t"
  "v_cvt_pk_bf16_f32 v119, v94, v95\n\t"
  "v_mfma_f32_32x32x16_bf16 v[80:95], a[32:35], a[64:67], v[48:63]\n\t"
  "ds_read_b128 a[56:59], v168 offset:27648\n\t"
  "v_exp_f32 v96, v96\n\t"
  "v_exp_f32 v97, v97\n\t"
  "v_exp_f32 v98, v98\n\t"
  "v_exp_f32 v99, v99\n\t"
  "v_mfma_f32_32x32x16_bf16 v[128:143], a[60:63], v[116:119], v[128:143]\n\t"
  "s_waitcnt vmcnt(8)\n\t"
  "v_exp_f32 v100, v100\n\t"
  "v_exp_f32 v101, v101\n\t"
  "v_cvt_pk_bf16_f32 v120, v96, v97\n\t"
  "v_exp_f32 v102, v102\n\t"
  "v_cvt_pk_bf16_f32 v121, v98, v99\n\t"
  "v_mfma_f32_32x32x16_bf16 v[80:95], a[40:43], a[68:71], v[80:95]\n\t"
  "s_barrier\n\t"
  "v_exp_f32 v103, v103\n\t"
  "v_exp_f32 v104, v104\n\t"
  "v_cvt_pk_bf16_f32 v122, v100, v101\n\t"
  "v_exp_f32 v105, v105\n\t"
  "v_cvt_pk_bf16_f32 v123, v102, v103\n\t"
  "v_smfmac_f32_16x16x64_bf16 v[160:163], v[172:175], v[112:119], v171\n\t"
  "buffer_store_dwordx4 a[4:7], v183, s[20:23], s36 offen sc1\n\t"
  "v_exp_f32 v106, v106\n\t"
  "v_exp_f32 v107, v107\n\t"
  "v_cvt_pk_bf16_f32 v124, v104, v105\n\t"
  "v_exp_f32 v108, v108\n\t"
  "v_mfma_f32_32x32x16_bf16 v[144:159], a[52:55], v[120:123], v[144:159]\n\t"
  "ds_read_b128 a[52:55], v168 offset:30720\n\t"
  "v_exp_f32 v109, v109\n\t"
  "v_exp_f32 v110, v110\n\t"
  "v_cvt_pk_bf16_f32 v125, v106, v107\n\t"
  "v_exp_f32 v111, v111\n\t"
  "v_cvt_pk_bf16_f32 v126, v108, v109\n\t"
  "v_cvt_pk_bf16_f32 v127, v110, v111\n\t"
  "v_mfma_f32_32x32x16_bf16 v[96:111], a[32:35], a[72:75], v[64:79]\n\t"
  "ds_read_b128 a[32:35], v168 offset:32768\n\t"
  "v_exp_f32 v80, v80\n\t"
  "v_exp_f32 v81, v81\n\t"
  "v_exp_f32 v82, v82\n\t"
  "v_exp_f32 v83, v83\n\t"
  "v_mfma_f32_32x32x16_bf16 v[144:159], a[60:63], v[124:127], v[144:159]\n\t"
  "s_add_u32 m0, s30, 0\n\t"
  "v_exp_f32 v84, v84\n\t"
  "v_exp_f32 v85, v85\n\t"
  "v_cvt_pk_bf16_f32 v112, v80, v81\n\t"
  "v_exp_f32 v86, v86\n\t"
  "v_cvt_pk_bf16_f32 v113, v82, v83\n\t"
  "v_mfma_f32_32x32x16_bf16 v[96:111], a[40:43], a[76:79], v[96:111]\n\t"
  "ds_read_b128 a[40:43], v168 offset:33792\n\t"
  "v_exp_f32 v87, v87\n\t"
  "v_exp_f32 v88, v88\n\t"
  "v_cvt_pk_bf16_f32 v114, v84, v85\n\t"
  "v_exp_f32 v89, v89\n\t"
  "v_cvt_pk_bf16_f32 v115, v86, v87\n\t"
  "v_smfmac_f32_16x16x64_bf16 v[164:167], v[172:175], v[120:127], v171\n\t"
  "buffer_load_dwordx4 v169, s[20:23], s28 offen offset:0 sc1 lds\n\t"
  "v_exp_f32 v90, v90\n\t"
  "v_exp_f32 v91, v91\n\t"
  "v_cvt_pk_bf16_f32 v116, v88, v89\n\t"
  "v_exp_f32 v92, v92\n\t"
  "v_mfma_f32_32x32x16_bf16 v[128:143], a[48:51], v[112:115], v[128:143]\n\t"
  "s_waitcnt lgkmcnt(2)\n\t"
  "v_exp_f32 v93, v93\n\t"
  "v_exp_f32 v94, v94\n\t"
  "v_cvt_pk_bf16_f32 v117, v90, v91\n\t"
  "v_exp_f32 v95, v95\n\t"
  "v_cvt_pk_bf16_f32 v118, v92, v93\n\t"
  "v_cvt_pk_bf16_f32 v119, v94, v95\n\t"
  "v_mfma_f32_32x32x16_bf16 v[80:95], a[36:39], a[64:67], v[48:63]\n\t"
  "s_mov_b32 m0, s39\n\t"
  "v_exp_f32 v96, v96\n\t"
  "v_exp_f32 v97, v97\n\t"
  "v_exp_f32 v98, v98\n\t"
  "v_exp_f32 v99, v99\n\t"
  "v_mfma_f32_32x32x16_bf16 v[128:143], a[56:59], v[116:119], v[128:143]\n\t"
  "global_load_lds_dwordx4 v225, s[46:47] offset:0\n\t"
  "v_exp_f32 v100, v100\n\t"
  "v_exp_f32 v101, v101\n\t"
  "v_cvt_pk_bf16_f32 v120, v96, v97\n\t"
  "v_exp_f32 v102, v102\n\t"
  "v_cvt_pk_bf16_f32 v121, v98, v99\n\t"
  "v_mfma_f32_32x32x16_bf16 v[80:95], a[44:47], a[68:71], v[80:95]\n\t"
  "ds_read_b128 a[60:63], v168 offset:31744\n\t"
  "v_exp_f32 v103, v103\n\t"
  "v_exp_f32 v104, v104\n\t"
  "v_cvt_pk_bf16_f32 v122, v100, v101\n\t"
  "v_exp_f32 v105, v105\n\t"
  "v_cvt_pk_bf16_f32 v123, v102, v103\n\t"
  "v_smfmac_f32_16x16x64_bf16 v[160:163], v[172:175], v[112:119], v171\n\t"
  "buffer_store_dwordx4 a[8:11], v184, s[20:23], s36 offen sc1\n\t"
  "v_exp_f32 v106, v106\n\t"
  "v_exp_f32 v107, v107\n\t"
  "v_cvt_pk_bf16_f32 v124, v104, v105\n\t"
  "v_exp_f32 v108, v108\n\t"
  "v_mfma_f32_32x32x16_bf16 v[144:159], a[48:51], v[120:123], v[144:159]\n\t"
  "ds_read_b128 a[48:51], v168 offset:34816\n\t"
  "v_exp_f32 v109, v109\n\t"
  "v_exp_f32 v110, v110\n\t"
  "v_cvt_pk_bf16_f32 v125, v106, v107\n\t"
  "v_exp_f32 v111, v111\n\t"
  "v_cvt_pk_bf16_f32 v126, v108, v109\n\t"
  "v_cvt_pk_bf16_f32 v127, v110, v111\n\t"
  "v_mfma_f32_32x32x16_bf16 v[96:111], a[36:39], a[72:75], v[64:79]\n\t"
  "ds_read_b128 a[36:39], v168 offset:36864\n\t"
  "v_exp_f32 v80, v80\n\t"
  "v_exp_f32 v81, v81\n\t"
  "v_exp_f32 v82, v82\n\t"
  "v_exp_f32 v83, v83\n\t"
  "v_mfma_f32_32x32x16_bf16 v[144:159], a[56:59], v[124:127], v[144:159]\n\t"
  "s_add_u32 m0, s30, 0\n\t"
  "v_exp_f32 v84, v84\n\t"
  "v_exp_f32 v85, v85\n\t"
  "v_cvt_pk_bf16_f32 v112, v80, v81\n\t"
  "v_exp_f32 v86, v86\n\t"
  "v_cvt_pk_bf16_f32 v113, v82, v83\n\t"
  "v_mfma_f32_32x32x16_bf16 v[96:111], a[44:47], a[76:79], v[96:111]\n\t"
  "ds_read_b128 a[44:47], v168 offset:37888\n\t"
  "v_exp_f32 v87, v87\n\t"
  "v_exp_f32 v88, v88\n\t"
  "v_cvt_pk_bf16_f32 v114, v84, v85\n\t"
  "v_exp_f32 v89, v89\n\t"
  "v_cvt_pk_bf16_f32 v115, v86, v87\n\t"
  "v_smfmac_f32_16x16x64_bf16 v[164:167], v[172:175], v[120:127], v171\n\t"
  "buffer_load_dwordx4 v169, s[20:23], s28 offen offset:1024 sc1 lds\n\t"
  "v_exp_f32 v90, v90\n\t"
  "v_exp_f32 v91, v91\n\t"
  "v_cvt_pk_bf16_f32 v116, v88, v89\n\t"
  "v_exp_f32 v92, v92\n\t"
  "v_mfma_f32_32x32x16_bf16 v[128:143], a[52:55], v[112:115], v[128:143]\n\t"
  "s_waitcnt lgkmcnt(2)\n\t"
  "v_exp_f32 v93, v93\n\t"
  "v_exp_f32 v94, v94\n\t"
  "v_cvt_pk_bf16_f32 v117, v90, v91\n\t"
  "v_exp_f32 v95, v95\n\t"
  "v_cvt_pk_bf16_f32 v118, v92, v93\n\t"
  "v_cvt_pk_bf16_f32 v119, v94, v95\n\t"
  "v_mfma_f32_32x32x16_bf16 v[80:95], a[32:35], a[64:67], v[48:63]\n\t"
  "s_mov_b32 m0, s39\n\t"
  "v_exp_f32 v96, v96\n\t"
  "v_exp_f32 v97, v97\n\t"
  "v_exp_f32 v98, v98\n\t"
  "v_exp_f32 v99, v99\n\t"
  "v_mfma_f32_32x32x16_bf16 v[128:143], a[60:63], v[116:119], v[128:143]\n\t"
  "global_load_lds_dwordx4 v229, s[46:47] offset:1024\n\t"
  "v_exp_f32 v100, v100\n\t"
  "v_exp_f32 v101, v101\n\t"
  "v_cvt_pk_bf16_f32 v120, v96, v97\n\t"
  "v_exp_f32 v102, v102\n\t"
  "v_cvt_pk_bf16_f32 v121, v98, v99\n\t"
  "v_mfma_f32_32x32x16_bf16 v[80:95], a[40:43], a[68:71], v[80:95]\n\t"
  "ds_read_b128 a[56:59], v168 offset:35840\n\t"
  "v_exp_f32 v103, v103\n\t"
  "v_exp_f32 v104, v104\n\t"
  "v_cvt_pk_bf16_f32 v122, v100, v101\n\t"
  "v_exp_f32 v105, v105\n\t"
  "v_cvt_pk_bf16_f32 v123, v102, v103\n\t"
  "v_smfmac_f32_16x16x64_bf16 v[160:163], v[172:175], v[112:119], v171\n\t"
  "buffer_store_dwordx4 a[12:15], v185, s[20:23], s36 offen sc1\n\t"
  "v_exp_f32 v106, v106\n\t"
  "v_exp_f32 v107, v107\n\t"
  "v_cvt_pk_bf16_f32 v124, v104, v105\n\t"
  "v_exp_f32 v108, v108\n\t"
  "v_mfma_f32_32x32x16_bf16 v[144:159], a[52:55], v[120:123], v[144:159]\n\t"
  "ds_read_b128 a[52:55], v168 offset:38912\n\t"
  "v_exp_f32 v109, v109\n\t"
  "v_exp_f32 v110, v110\n\t"
  "v_cvt_pk_bf16_f32 v125, v106, v107\n\t"
  "v_exp_f32 v111, v111\n\t"
  "v_cvt_pk_bf16_f32 v126, v108, v109\n\t"
  "v_cvt_pk_bf16_f32 v127, v110, v111\n\t"
  "v_mfma_f32_32x32x16_bf16 v[96:111], a[32:35], a[72:75], v[64:79]\n\t"
  "ds_read_b128 a[32:35], v168 offset:40960\n\t"
  "v_exp_f32 v80, v80\n\t"
  "v_exp_f32 v81, v81\n\t"
  "v_exp_f32 v82, v82\n\t"
  "v_exp_f32 v83, v83\n\t"
  "v_mfma_f32_32x32x16_bf16 v[144:159], a[60:63], v[124:127], v[144:159]\n\t"
  "s_add_u32 m0, s30, 0\n\t"
  "v_exp_f32 v84, v84\n\t"
  "v_exp_f32 v85, v85\n\t"
  "v_cvt_pk_bf16_f32 v112, v80, v81\n\t"
  "v_exp_f32 v86, v86\n\t"
  "v_cvt_pk_bf16_f32 v113, v82, v83\n\t"
  "v_mfma_f32_32x32x16_bf16 v[96:111], a[40:43], a[76:79], v[96:111]\n\t"
  "ds_read_b128 a[40:43], v168 offset:41984\n\t"
  "v_exp_f32 v87, v87\n\t"
  "v_exp_f32 v88, v88\n\t"
  "v_cvt_pk_bf16_f32 v114, v84, v85\n\t"
  "v_exp_f32 v89, v89\n\t"
  "v_cvt_pk_bf16_f32 v115, v86, v87\n\t"
  "v_smfmac_f32_16x16x64_bf16 v[164:167], v[172:175], v[120:127], v171\n\t"
  "buffer_load_dwordx4 v169, s[20:23], s28 offen offset:2048 sc1 lds\n\t"
  "v_exp_f32 v90, v90\n\t"
  "v_exp_f32 v91, v91\n\t"
  "v_cvt_pk_bf16_f32 v116, v88, v89\n\t"
  "v_exp_f32 v92, v92\n\t"
  "v_mfma_f32_32x32x16_bf16 v[128:143], a[48:51], v[112:115], v[128:143]\n\t"
  "s_waitcnt lgkmcnt(2)\n\t"
  "v_exp_f32 v93, v93\n\t"
  "v_exp_f32 v94, v94\n\t"
  "v_cvt_pk_bf16_f32 v117, v90, v91\n\t"
  "v_exp_f32 v95, v95\n\t"
  "v_cvt_pk_bf16_f32 v118, v92, v93\n\t"
  "v_cvt_pk_bf16_f32 v119, v94, v95\n\t"
  "v_mfma_f32_32x32x16_bf16 v[80:95], a[36:39], a[64:67], v[48:63]\n\t"
  "s_add_u32 m0, s54, 4096\n\t"
  "v_exp_f32 v96, v96\n\t"
  "v_exp_f32 v97, v97\n\t"
  "v_exp_f32 v98, v98\n\t"
  "v_exp_f32 v99, v99\n\t"
  "v_mfma_f32_32x32x16_bf16 v[128:143], a[56:59], v[116:119], v[128:143]\n\t"
  "v_exp_f32 v100, v100\n\t"
  "v_exp_f32 v101, v101\n\t"
  "v_cvt_pk_bf16_f32 v120, v96, v97\n\t"
  "v_exp_f32 v102, v102\n\t"
  "v_cvt_pk_bf16_f32 v121, v98, v99\n\t"
  "v_mfma_f32_32x32x16_bf16 v[80:95], a[44:47], a[68:71], v[80:95]\n\t"
  "ds_read_b128 a[60:63], v168 offset:39936\n\t"
  "v_exp_f32 v103, v103\n\t"
  "v_exp_f32 v104, v104\n\t"
  "v_cvt_pk_bf16_f32 v122, v100, v101\n\t"
  "v_exp_f32 v105, v105\n\t"
  "v_cvt_pk_bf16_f32 v123, v102, v103\n\t"
  "v_smfmac_f32_16x16x64_bf16 v[160:163], v[172:175], v[112:119], v171\n\t"
  "buffer_load_dwordx4 v177, s[24:27], 0 offen offset:0 lds\n\t"
  "v_exp_f32 v106, v106\n\t"
  "v_exp_f32 v107, v107\n\t"
  "v_cvt_pk_bf16_f32 v124, v104, v105\n\t"
  "v_exp_f32 v108, v108\n\t"
  "v_mfma_f32_32x32x16_bf16 v[144:159], a[48:51], v[120:123], v[144:159]\n\t"
  "ds_read_b128 a[48:51], v168 offset:43008\n\t"
  "v_exp_f32 v109, v109\n\t"
  "v_exp_f32 v110, v110\n\t"
  "v_cvt_pk_bf16_f32 v125, v106, v107\n\t"
  "v_exp_f32 v111, v111\n\t"
  "v_cvt_pk_bf16_f32 v126, v108, v109\n\t"
  "v_cvt_pk_bf16_f32 v127, v110, v111\n\t"
  "v_mfma_f32_32x32x16_bf16 v[96:111], a[36:39], a[72:75], v[64:79]\n\t"
  "ds_read_b128 a[36:39], v168 offset:45056\n\t"
  "v_exp_f32 v80, v80\n\t"
  "v_exp_f32 v81, v81\n\t"
  "v_exp_f32 v82, v82\n\t"
  "v_exp_f32 v83, v83\n\t"
  "v_mfma_f32_32x32x16_bf16 v[144:159], a[56:59], v[124:127], v[144:159]\n\t"
  "s_add_u32 m0, s30, 0\n\t"
  "v_exp_f32 v84, v84\n\t"
  "v_exp_f32 v85, v85\n\t"
  "v_cvt_pk_bf16_f32 v112, v80, v81\n\t"
  "v_exp_f32 v86, v86\n\t"
  "v_cvt_pk_bf16_f32 v113, v82, v83\n\t"
  "v_mfma_f32_32x32x16_bf16 v[96:111], a[44:47], a[76:79], v[96:111]\n\t"
  "ds_read_b128 a[44:47], v168 offset:46080\n\t"
  "v_exp_f32 v87, v87\n\t"
  "v_exp_f32 v88, v88\n\t"
  "v_cvt_pk_bf16_f32 v114, v84, v85\n\t"
  "v_exp_f32 v89, v89\n\t"
  "v_cvt_pk_bf16_f32 v115, v86, v87\n\t"
  "v_smfmac_f32_16x16x64_bf16 v[164:167], v[172:175], v[120:127], v171\n\t"
  "buffer_load_dwordx4 v169, s[20:23], s28 offen offset:3072 sc1 lds\n\t"
  "v_exp_f32 v90, v90\n\t"
  "v_exp_f32 v91, v91\n\t"
  "v_cvt_pk_bf16_f32 v116, v88, v89\n\t"
  "v_exp_f32 v92, v92\n\t"
  "v_mfma_f32_32x32x16_bf16 v[128:143], a[52:55], v[112:115], v[128:143]\n\t"
  "s_waitcnt lgkmcnt(2)\n\t"
  "s_add_u32 s28, s28, 16384\n\t"
  "v_exp_f32 v93, v93\n\t"
  "v_exp_f32 v94, v94\n\t"
  "v_cvt_pk_bf16_f32 v117, v90, v91\n\t"
  "v_exp_f32 v95, v95\n\t"
  "v_cvt_pk_bf16_f32 v118, v92, v93\n\t"
  "v_cvt_pk_bf16_f32 v119, v94, v95\n\t"
  "v_mfma_f32_32x32x16_bf16 v[80:95], a[32:35], a[64:67], v[48:63]\n\t"
  "s_add_u32 m0, s54, 4096\n\t"
  "v_exp_f32 v96, v96\n\t"
  "v_exp_f32 v97, v97\n\t"
  "v_exp_f32 v98, v98\n\t"
  "v_exp_f32 v99, v99\n\t"
  "v_mfma_f32_32x32x16_bf16 v[128:143], a[60:63], v[116:119], v[128:143]\n\t"
  "s_waitcnt vmcnt(10)\n\t"
  "v_exp_f32 v100, v100\n\t"
  "v_exp_f32 v101, v101\n\t"
  "v_cvt_pk_bf16_f32 v120, v96, v97\n\t"
  "v_exp_f32 v102, v102\n\t"
  "v_cvt_pk_bf16_f32 v121, v98, v99\n\t"
  "v_mfma_f32_32x32x16_bf16 v[80:95], a[40:43], a[68:71], v[80:95]\n\t"
  "s_barrier\n\t"
  "v_exp_f32 v103, v103\n\t"
  "v_exp_f32 v104, v104\n\t"
  "v_cvt_pk_bf16_f32 v122, v100, v101\n\t"
  "v_exp_f32 v105, v105\n\t"
  "v_cvt_pk_bf16_f32 v123, v102, v103\n\t"
  "v_smfmac_f32_16x16x64_bf16 v[160:163], v[172:175], v[112:119], v171\n\t"
  "buffer_load_dwordx4 v177, s[24:27], s48 offen offset:1024 lds\n\t"
  "ds_read_b128 a[56:59], v168 offset:44032\n\t"
  "v_exp_f32 v106, v106\n\t"
  "v_exp_f32 v107, v107\n\t"
  "v_cvt_pk_bf16_f32 v124, v104, v105\n\t"
  "v_exp_f32 v108, v108\n\t"
  "v_mfma_f32_32x32x16_bf16 v[144:159], a[52:55], v[120:123], v[144:159]\n\t"
  "ds_read_b128 a[52:55], v168 offset:47104\n\t"
  "v_exp_f32 v109, v109\n\t"
  "v_exp_f32 v110, v110\n\t"
  "v_cvt_pk_bf16_f32 v125, v106, v107\n\t"
  "v_exp_f32 v111, v111\n\t"
  "v_cvt_pk_bf16_f32 v126, v108, v109\n\t"
  "v_cvt_pk_bf16_f32 v127, v110, v111\n\t"
  "v_mfma_f32_32x32x16_bf16 v[96:111], a[32:35], a[72:75], v[64:79]\n\t"
  "ds_read_b128 a[32:35], v168 offset:49152\n\t"
  "v_exp_f32 v80, v80\n\t"
  "v_exp_f32 v81, v81\n\t"
  "v_exp_f32 v82, v82\n\t"
  "v_exp_f32 v83, v83\n\t"
  "v_mfma_f32_32x32x16_bf16 v[144:159], a[60:63], v[124:127], v[144:159]\n\t"
  "s_add_u32 m0, s30, 16384\n\t"
  "ds_read_b64_tr_b16 a[0:1], v236 offset:0\n\t"
  "ds_read_b64_tr_b16 a[2:3], v236 offset:2048\n\t"
  "v_exp_f32 v84, v84\n\t"
  "v_exp_f32 v85, v85\n\t"
  "v_cvt_pk_bf16_f32 v112, v80, v81\n\t"
  "v_exp_f32 v86, v86\n\t"
  "v_cvt_pk_bf16_f32 v113, v82, v83\n\t"
  "v_mfma_f32_32x32x16_bf16 v[96:111], a[40:43], a[76:79], v[96:111]\n\t"
  "ds_read_b128 a[40:43], v168 offset:50176\n\t"
  "v_exp_f32 v87, v87\n\t"
  "v_exp_f32 v88, v88\n\t"
  "v_cvt_pk_bf16_f32 v114, v84, v85\n\t"
  "v_exp_f32 v89, v89\n\t"
  "v_cvt_pk_bf16_f32 v115, v86, v87\n\t"
  "v_smfmac_f32_16x16x64_bf16 v[164:167], v[172:175], v[120:127], v171\n\t"
  "buffer_load_dwordx4 v169, s[20:23], s28 offen offset:0 sc1 lds\n\t"
  "v_exp_f32 v90, v90\n\t"
  "v_exp_f32 v91, v91\n\t"
  "v_cvt_pk_bf16_f32 v116, v88, v89\n\t"
  "v_exp_f32 v92, v92\n\t"
  "v_mfma_f32_32x32x16_bf16 v[128:143], a[48:51], v[112:115], v[128:143]\n\t"
  "s_waitcnt lgkmcnt(4)\n\t"
  "v_exp_f32 v93, v93\n\t"
  "v_exp_f32 v94, v94\n\t"
  "v_cvt_pk_bf16_f32 v117, v90, v91\n\t"
  "v_exp_f32 v95, v95\n\t"
  "v_cvt_pk_bf16_f32 v118, v92, v93\n\t"
  "v_cvt_pk_bf16_f32 v119, v94, v95\n\t"
  "v_mfma_f32_32x32x16_bf16 v[80:95], a[36:39], a[64:67], v[48:63]\n\t"
  "s_add_u32 m0, s54, 4096\n\t"
  "v_exp_f32 v96, v96\n\t"
  "v_exp_f32 v97, v97\n\t"
  "v_exp_f32 v98, v98\n\t"
  "v_exp_f32 v99, v99\n\t"
  "v_mfma_f32_32x32x16_bf16 v[128:143], a[56:59], v[116:119], v[128:143]\n\t"
  "ds_read_b64_tr_b16 a[4:5], v237 offset:0\n\t"
  "ds_read_b64_tr_b16 a[6:7], v237 offset:2048\n\t"
  "v_exp_f32 v100, v100\n\t"
  "v_exp_f32 v101, v101\n\t"
  "v_cvt_pk_bf16_f32 v120, v96, v97\n\t"
  "v_exp_f32 v102, v102\n\t"
  "v_cvt_pk_bf16_f32 v121, v98, v99\n\t"
  "v_mfma_f32_32x32x16_bf16 v[80:95], a[44:47], a[68:71], v[80:95]\n\t"
  "ds_read_b128 a[60:63], v168 offset:48128\n\t"
  "v_exp_f32 v103, v103\n\t"
  "v_exp_f32 v104, v104\n\t"
  "v_cvt_pk_bf16_f32 v122, v100, v101\n\t"
  "v_exp_f32 v105, v105\n\t"
  "v_cvt_pk_bf16_f32 v123, v102, v103\n\t"
  "v_smfmac_f32_16x16x64_bf16 v[160:163], v[172:175], v[112:119], v171\n\t"
  "buffer_load_dwordx4 v177, s[24:27], s49 offen offset:2048 lds\n\t"
  "v_exp_f32 v106, v106\n\t"
  "v_exp_f32 v107, v107\n\t"
  "v_cvt_pk_bf16_f32 v124, v104, v105\n\t"
  "v_exp_f32 v108, v108\n\t"
  "v_mfma_f32_32x32x16_bf16 v[144:159], a[48:51], v[120:123], v[144:159]\n\t"
  "ds_read_b128 a[48:51], v168 offset:51200\n\t"
  "v_exp_f32 v109, v109\n\t"
  "v_exp_f32 v110, v110\n\t"
  "v_cvt_pk_bf16_f32 v125, v106, v107\n\t"
  "v_exp_f32 v111, v111\n\t"
  "v_cvt_pk_bf16_f32 v126, v108, v109\n\t"
  "v_cvt_pk_bf16_f32 v127, v110, v111\n\t"
  "v_mfma_f32_32x32x16_bf16 v[96:111], a[36:39], a[72:75], v[64:79]\n\t"
  "ds_read_b128 a[36:39], v168 offset:53248\n\t"
  "v_exp_f32 v80, v80\n\t"
  "v_exp_f32 v81, v81\n\t"
  "v_exp_f32 v82, v82\n\t"
  "v_exp_f32 v83, v83\n\t"
  "v_mfma_f32_32x32x16_bf16 v[144:159], a[56:59], v[124:127], v[144:159]\n\t"
  "s_add_u32 m0, s30, 16384\n\t"
  "ds_read_b64_tr_b16 a[8:9], v238 offset:0\n\t"
  "ds_read_b64_tr_b16 a[10:11], v238 offset:2048\n\t"
  "v_exp_f32 v84, v84\n\t"
  "v_exp_f32 v85, v85\n\t"
  "v_cvt_pk_bf16_f32 v112, v80, v81\n\t"
  "v_exp_f32 v86, v86\n\t"
  "v_cvt_pk_bf16_f32 v113, v82, v83\n\t"
  "v_mfma_f32_32x32x16_bf16 v[96:111], a[44:47], a[76:79], v[96:111]\n\t"
  "ds_read_b128 a[44:47], v168 offset:54272\n\t"
  "v_exp_f32 v87, v87\n\t"
  "v_exp_f32 v88, v88\n\t"
  "v_cvt_pk_bf16_f32 v114, v84, v85\n\t"
  "v_exp_f32 v89, v89\n\t"
  "v_cvt_pk_bf16_f32 v115, v86, v87\n\t"
  "v_smfmac_f32_16x16x64_bf16 v[164:167], v[172:175], v[120:127], v171\n\t"
  "buffer_load_dwordx4 v169, s[20:23], s28 offen offset:1024 sc1 lds\n\t"
  "v_exp_f32 v90, v90\n\t"
  "v_exp_f32 v91, v91\n\t"
  "v_cvt_pk_bf16_f32 v116, v88, v89\n\t"
  "v_exp_f32 v92, v92\n\t"
  "v_mfma_f32_32x32x16_bf16 v[128:143], a[52:55], v[112:115], v[128:143]\n\t"
  "s_waitcnt lgkmcnt(4)\n\t"
  "v_exp_f32 v93, v93\n\t"
  "v_exp_f32 v94, v94\n\t"
  "v_cvt_pk_bf16_f32 v117, v90, v91\n\t"
  "v_exp_f32 v95, v95\n\t"
  "v_cvt_pk_bf16_f32 v118, v92, v93\n\t"
  "v_cvt_pk_bf16_f32 v119, v94, v95\n\t"
  "v_mfma_f32_32x32x16_bf16 v[80:95], a[32:35], a[64:67], v[48:63]\n\t"
  "s_add_u32 m0, s54, 4096\n\t"
  "v_exp_f32 v96, v96\n\t"
  "v_exp_f32 v97, v97\n\t"
  "v_exp_f32 v98, v98\n\t"
  "v_exp_f32 v99, v99\n\t"
  "v_mfma_f32_32x32x16_bf16 v[128:143], a[60:63], v[116:119], v[128:143]\n\t"
  "ds_read_b64_tr_b16 a[12:13], v239 offset:0\n\t"
  "ds_read_b64_tr_b16 a[14:15], v239 offset:2048\n\t"
  "v_exp_f32 v100, v100\n\t"
  "v_exp_f32 v101, v101\n\t"
  "v_cvt_pk_bf16_f32 v120, v96, v97\n\t"
  "v_exp_f32 v102, v102\n\t"
  "v_cvt_pk_bf16_f32 v121, v98, v99\n\t"
  "v_mfma_f32_32x32x16_bf16 v[80:95], a[40:43], a[68:71], v[80:95]\n\t"
  "ds_read_b128 a[56:59], v168 offset:52224\n\t"
  "v_exp_f32 v103, v103\n\t"
  "v_exp_f32 v104, v104\n\t"
  "v_cvt_pk_bf16_f32 v122, v100, v101\n\t"
  "v_exp_f32 v105, v105\n\t"
  "v_cvt_pk_bf16_f32 v123, v102, v103\n\t"
  "v_smfmac_f32_16x16x64_bf16 v[160:163], v[172:175], v[112:119], v171\n\t"
  "buffer_load_dwordx4 v177, s[24:27], s50 offen offset:3072 lds\n\t"
  "v_exp_f32 v106, v106\n\t"
  "v_exp_f32 v107, v107\n\t"
  "v_cvt_pk_bf16_f32 v124, v104, v105\n\t"
  "v_exp_f32 v108, v108\n\t"
  "v_mfma_f32_32x32x16_bf16 v[144:159], a[52:55], v[120:123], v[144:159]\n\t"
  "ds_read_b128 a[52:55], v168 offset:55296\n\t"
  "v_exp_f32 v109, v109\n\t"
  "v_exp_f32 v110, v110\n\t"
  "v_cvt_pk_bf16_f32 v125, v106, v107\n\t"
  "v_exp_f32 v111, v111\n\t"
  "v_cvt_pk_bf16_f32 v126, v108, v109\n\t"
  "v_cvt_pk_bf16_f32 v127, v110, v111\n\t"
  "v_mfma_f32_32x32x16_bf16 v[96:111], a[32:35], a[72:75], v[64:79]\n\t"
  "ds_read_b128 a[32:35], v168 offset:57344\n\t"
  "v_exp_f32 v80, v80\n\t"
  "v_exp_f32 v81, v81\n\t"
  "v_exp_f32 v82, v82\n\t"
  "v_exp_f32 v83, v83\n\t"
  "v_mfma_f32_32x32x16_bf16 v[144:159], a[60:63], v[124:127], v[144:159]\n\t"
  "s_add_u32 m0, s30, 16384\n\t"
  "v_exp_f32 v84, v84\n\t"
  "v_exp_f32 v85, v85\n\t"
  "v_cvt_pk_bf16_f32 v112, v80, v81\n\t"
  "v_exp_f32 v86, v86\n\t"
  "v_cvt_pk_bf16_f32 v113, v82, v83\n\t"
  "v_mfma_f32_32x32x16_bf16 v[96:111], a[40:43], a[76:79], v[96:111]\n\t"
  "ds_read_b128 a[40:43], v168 offset:58368\n\t"
  "v_exp_f32 v87, v87\n\t"
  "v_exp_f32 v88, v88\n\t"
  "v_cvt_pk_bf16_f32 v114, v84, v85\n\t"
  "v_exp_f32 v89, v89\n\t"
  "v_cvt_pk_bf16_f32 v115, v86, v87\n\t"
  "v_smfmac_f32_16x16x64_bf16 v[164:167], v[172:175], v[120:127], v171\n\t"
  "buffer_load_dwordx4 v169, s[20:23], s28 offen offset:2048 sc1 lds\n\t"
  "v_exp_f32 v90, v90\n\t"
  "v_exp_f32 v91, v91\n\t"
  "v_cvt_pk_bf16_f32 v116, v88, v89\n\t"
  "v_exp_f32 v92, v92\n\t"
  "v_mfma_f32_32x32x16_bf16 v[128:143], a[48:51], v[112:115], v[128:143]\n\t"
  "s_waitcnt lgkmcnt(2)\n\t"
  "v_exp_f32 v93, v93\n\t"
  "v_exp_f32 v94, v94\n\t"
  "v_cvt_pk_bf16_f32 v117, v90, v91\n\t"
  "v_exp_f32 v95, v95\n\t"
  "v_cvt_pk_bf16_f32 v118, v92, v93\n\t"
  "v_cvt_pk_bf16_f32 v119, v94, v95\n\t"
  "v_mfma_f32_32x32x16_bf16 v[80:95], a[36:39], a[64:67], v[48:63]\n\t"
  "ds_read_b128 a[60:63], v168 offset:56320\n\t"
  "v_exp_f32 v96, v96\n\t"
  "v_exp_f32 v97, v97\n\t"
  "v_exp_f32 v98, v98\n\t"
  "v_exp_f32 v99, v99\n\t"
  "v_mfma_f32_32x32x16_bf16 v[128:143], a[56:59], v[116:119], v[128:143]\n\t"
  "s_mov_b64 s[24:25], s[40:41]\n\t"
  "v_exp_f32 v100, v100\n\t"
  "v_exp_f32 v101, v101\n\t"
  "v_cvt_pk_bf16_f32 v120, v96, v97\n\t"
  "v_exp_f32 v102, v102\n\t"
  "v_cvt_pk_bf16_f32 v121, v98, v99\n\t"
  "v_mfma_f32_32x32x16_bf16 v[80:95], a[44:47], a[68:71], v[80:95]\n\t"
  "s_sub_u32 s31, s31, 1\n\t"
  "v_exp_f32 v103, v103\n\t"
  "v_exp_f32 v104, v104\n\t"
  "v_cvt_pk_bf16_f32 v122, v100, v101\n\t"
  "v_exp_f32 v105, v105\n\t"
  "v_cvt_pk_bf16_f32 v123, v102, v103\n\t"
  "v_smfmac_f32_16x16x64_bf16 v[160:163], v[172:175], v[112:119], v171\n\t"
  "buffer_store_dwordx4 a[0:3], v178, s[20:23], s29 offen sc1\n\t"
  "v_exp_f32 v106, v106\n\t"
  "v_exp_f32 v107, v107\n\t"
  "v_cvt_pk_bf16_f32 v124, v104, v105\n\t"
  "v_exp_f32 v108, v108\n\t"
  "v_mfma_f32_32x32x16_bf16 v[144:159], a[48:51], v[120:123], v[144:159]\n\t"
  "ds_read_b128 a[48:51], v168 offset:59392\n\t"
  "v_exp_f32 v109, v109\n\t"
  "v_exp_f32 v110, v110\n\t"
  "v_cvt_pk_bf16_f32 v125, v106, v107\n\t"
  "v_exp_f32 v111, v111\n\t"
  "v_cvt_pk_bf16_f32 v126, v108, v109\n\t"
  "v_cvt_pk_bf16_f32 v127, v110, v111\n\t"
  "v_mfma_f32_32x32x16_bf16 v[96:111], a[36:39], a[72:75], v[64:79]\n\t"
  "ds_read_b128 a[36:39], v168 offset:61440\n\t"
  "v_exp_f32 v80, v80\n\t"
  "v_exp_f32 v81, v81\n\t"
  "v_exp_f32 v82, v82\n\t"
  "v_exp_f32 v83, v83\n\t"
  "v_mfma_f32_32x32x16_bf16 v[144:159], a[56:59], v[124:127], v[144:159]\n\t"
  "s_add_u32 m0, s30, 16384\n\t"
  "s_mov_b64 s[40:41], s[42:43]\n\t"
  "v_exp_f32 v84, v84\n\t"
  "v_exp_f32 v85, v85\n\t"
  "v_cvt_pk_bf16_f32 v112, v80, v81\n\t"
  "v_exp_f32 v86, v86\n\t"
  "v_cvt_pk_bf16_f32 v113, v82, v83\n\t"
  "v_mfma_f32_32x32x16_bf16 v[96:111], a[44:47], a[76:79], v[96:111]\n\t"
  "ds_read_b128 a[44:47], v168 offset:62464\n\t"
  "v_exp_f32 v87, v87\n\t"
  "v_exp_f32 v88, v88\n\t"
  "v_cvt_pk_bf16_f32 v114, v84, v85\n\t"
  "v_exp_f32 v89, v89\n\t"
  "v_cvt_pk_bf16_f32 v115, v86, v87\n\t"
  "v_smfmac_f32_16x16x64_bf16 v[164:167], v[172:175], v[120:127], v171\n\t"
  "buffer_load_dwordx4 v169, s[20:23], s28 offen offset:3072 sc1 lds\n\t"
  "v_exp_f32 v90, v90\n\t"
  "v_exp_f32 v91, v91\n\t"
  "v_cvt_pk_bf16_f32 v116, v88, v89\n\t"
  "v_exp_f32 v92, v92\n\t"
  "v_mfma_f32_32x32x16_bf16 v[128:143], a[52:55], v[112:115], v[128:143]\n\t"
  "s_waitcnt lgkmcnt(2)\n\t"
  "s_add_u32 s28, s28, 16384\n\t"
  "v_exp_f32 v93, v93\n\t"
  "v_exp_f32 v94, v94\n\t"
  "v_cvt_pk_bf16_f32 v117, v90, v91\n\t"
  "v_exp_f32 v95, v95\n\t"
  "v_cvt_pk_bf16_f32 v118, v92, v93\n\t"
  "v_cvt_pk_bf16_f32 v119, v94, v95\n\t"
  "v_mfma_f32_32x32x16_bf16 v[80:95], a[32:35], a[64:67], v[48:63]\n\t"
  "ds_read_b128 a[56:59], v168 offset:60416\n\t"
  "v_exp_f32 v96, v96\n\t"
  "v_exp_f32 v97, v97\n\t"
  "v_exp_f32 v98, v98\n\t"
  "v_exp_f32 v99, v99\n\t"
  "v_mfma_f32_32x32x16_bf16 v[128:143], a[60:63], v[116:119], v[128:143]\n\t"
  "s_waitcnt vmcnt(8)\n\t"
  "v_exp_f32 v100, v100\n\t"
  "v_exp_f32 v101, v101\n\t"
  "v_cvt_pk_bf16_f32 v120, v96, v97\n\t"
  "v_exp_f32 v102, v102\n\t"
  "v_cvt_pk_bf16_f32 v121, v98, v99\n\t"
  "v_mfma_f32_32x32x16_bf16 v[80:95], a[40:43], a[68:71], v[80:95]\n\t"
  "s_barrier\n\t"
  "v_exp_f32 v103, v103\n\t"
  "v_exp_f32 v104, v104\n\t"
  "v_cvt_pk_bf16_f32 v122, v100, v101\n\t"
  "v_exp_f32 v105, v105\n\t"
  "v_cvt_pk_bf16_f32 v123, v102, v103\n\t"
  "v_smfmac_f32_16x16x64_bf16 v[160:163], v[172:175], v[112:119], v171\n\t"
  "buffer_store_dwordx4 a[4:7], v179, s[20:23], s29 offen sc1\n\t"
  "v_exp_f32 v106, v106\n\t"
  "v_exp_f32 v107, v107\n\t"
  "v_cvt_pk_bf16_f32 v124, v104, v105\n\t"
  "v_exp_f32 v108, v108\n\t"
  "v_mfma_f32_32x32x16_bf16 v[144:159], a[52:55], v[120:123], v[144:159]\n\t"
  "ds_read_b128 a[52:55], v168 offset:63488\n\t"
  "v_exp_f32 v109, v109\n\t"
  "v_exp_f32 v110, v110\n\t"
  "v_cvt_pk_bf16_f32 v125, v106, v107\n\t"
  "v_exp_f32 v111, v111\n\t"
  "v_cvt_pk_bf16_f32 v126, v108, v109\n\t"
  "v_cvt_pk_bf16_f32 v127, v110, v111\n\t"
  "v_mfma_f32_32x32x16_bf16 v[96:111], a[32:35], a[72:75], v[64:79]\n\t"
  "ds_read_b128 a[32:35], v168 offset:0\n\t"
  "v_exp_f32 v80, v80\n\t"
  "v_exp_f32 v81, v81\n\t"
  "v_exp_f32 v82, v82\n\t"
  "v_exp_f32 v83, v83\n\t"
  "v_mfma_f32_32x32x16_bf16 v[144:159], a[60:63], v[124:127], v[144:159]\n\t"
  "s_add_u32 m0, s30, 32768\n\t"
  "v_exp_f32 v84, v84\n\t"
  "v_exp_f32 v85, v85\n\t"
  "v_cvt_pk_bf16_f32 v112, v80, v81\n\t"
  "v_exp_f32 v86, v86\n\t"
  "v_cvt_pk_bf16_f32 v113, v82, v83\n\t"
  "v_mfma_f32_32x32x16_bf16 v[96:111], a[40:43], a[76:79], v[96:111]\n\t"
  "ds_read_b128 a[40:43], v168 offset:1024\n\t"
  "v_exp_f32 v87, v87\n\t"
  "v_exp_f32 v88, v88\n\t"
  "v_cvt_pk_bf16_f32 v114, v84, v85\n\t"
  "v_exp_f32 v89, v89\n\t"
  "v_cvt_pk_bf16_f32 v115, v86, v87\n\t"
  "v_smfmac_f32_16x16x64_bf16 v[164:167], v[172:175], v[120:127], v171\n\t"
  "buffer_load_dwordx4 v169, s[20:23], s28 offen offset:0 sc1 lds\n\t"
  "v_exp_f32 v90, v90\n\t"
  "v_exp_f32 v91, v91\n\t"
  "v_cvt_pk_bf16_f32 v116, v88, v89\n\t"
  "v_exp_f32 v92, v92\n\t"
  "v_mfma_f32_32x32x16_bf16 v[128:143], a[48:51], v[112:115], v[128:143]\n\t"
  "s_waitcnt lgkmcnt(2)\n\t"
  "v_exp_f32 v93, v93\n\t"
  "v_exp_f32 v94, v94\n\t"
  "v_cvt_pk_bf16_f32 v117, v90, v91\n\t"
  "v_exp_f32 v95, v95\n\t"
  "v_cvt_pk_bf16_f32 v118, v92, v93\n\t"
  "v_cvt_pk_bf16_f32 v119, v94, v95\n\t"
  "v_mfma_f32_32x32x16_bf16 v[80:95], a[36:39], a[64:67], v[48:63]\n\t"
  "s_mov_b32 m0, s39\n\t"
  "v_exp_f32 v96, v96\n\t"
  "v_exp_f32 v97, v97\n\t"
  "v_exp_f32 v98, v98\n\t"
  "v_exp_f32 v99, v99\n\t"
  "v_mfma_f32_32x32x16_bf16 v[128:143], a[56:59], v[116:119], v[128:143]\n\t"
  "global_load_lds_dwordx4 v230, s[46:47] offset:2048\n\t"
  "s_mov_b64 s[42:43], s[44:45]\n\t"
  "s_mov_b32 s36, s29\n\t"
  "v_exp_f32 v100, v100\n\t"
  "v_exp_f32 v101, v101\n\t"
  "v_cvt_pk_bf16_f32 v120, v96, v97\n\t"
  "v_exp_f32 v102, v102\n\t"
  "v_cvt_pk_bf16_f32 v121, v98, v99\n\t"
  "v_mfma_f32_32x32x16_bf16 v[80:95], a[44:47], a[68:71], v[80:95]\n\t"
  "ds_read_b128 a[60:63], v168 offset:64512\n\t"
  "v_exp_f32 v103, v103\n\t"
  "v_exp_f32 v104, v104\n\t"
  "v_cvt_pk_bf16_f32 v122, v100, v101\n\t"
  "v_exp_f32 v105, v105\n\t"
  "v_cvt_pk_bf16_f32 v123, v102, v103\n\t"
  "v_smfmac_f32_16x16x64_bf16 v[160:163], v[172:175], v[112:119], v171\n\t"
  "buffer_store_dwordx4 a[8:11], v180, s[20:23], s29 offen sc1\n\t"
  "v_exp_f32 v106, v106\n\t"
  "v_exp_f32 v107, v107\n\t"
  "v_cvt_pk_bf16_f32 v124, v104, v105\n\t"
  "v_exp_f32 v108, v108\n\t"
  "v_mfma_f32_32x32x16_bf16 v[144:159], a[48:51], v[120:123], v[144:159]\n\t"
  "ds_read_b128 a[48:51], v168 offset:2048\n\t"
  "v_exp_f32 v109, v109\n\t"
  "v_exp_f32 v110, v110\n\t"
  "v_cvt_pk_bf16_f32 v125, v106, v107\n\t"
  "v_exp_f32 v111, v111\n\t"
  "v_cvt_pk_bf16_f32 v126, v108, v109\n\t"
  "v_cvt_pk_bf16_f32 v127, v110, v111\n\t"
  "v_mfma_f32_32x32x16_bf16 v[96:111], a[36:39], a[72:75], v[64:79]\n\t"
  "ds_read_b128 a[36:39], v168 offset:4096\n\t"
  "v_exp_f32 v80, v80\n\t"
  "v_exp_f32 v81, v81\n\t"
  "v_exp_f32 v82, v82\n\t"
  "v_exp_f32 v83, v83\n\t"
  "v_mfma_f32_32x32x16_bf16 v[144:159], a[56:59], v[124:127], v[144:159]\n\t"
  "s_add_u32 m0, s30, 32768\n\t"
  "v_exp_f32 v84, v84\n\t"
  "v_exp_f32 v85, v85\n\t"
  "v_cvt_pk_bf16_f32 v112, v80, v81\n\t"
  "v_exp_f32 v86, v86\n\t"
  "v_cvt_pk_bf16_f32 v113, v82, v83\n\t"
  "v_mfma_f32_32x32x16_bf16 v[96:111], a[44:47], a[76:79], v[96:111]\n\t"
  "ds_read_b128 a[44:47], v168 offset:5120\n\t"
  "v_exp_f32 v87, v87\n\t"
  "v_exp_f32 v88, v88\n\t"
  "v_cvt_pk_bf16_f32 v114, v84, v85\n\t"
  "v_exp_f32 v89, v89\n\t"
  "v_cvt_pk_bf16_f32 v115, v86, v87\n\t"
  "v_smfmac_f32_16x16x64_bf16 v[164:167], v[172:175], v[120:127], v171\n\t"
  "buffer_load_dwordx4 v169, s[20:23], s28 offen offset:1024 sc1 lds\n\t"
  "v_exp_f32 v90, v90\n\t"
  "v_exp_f32 v91, v91\n\t"
  "v_cvt_pk_bf16_f32 v116, v88, v89\n\t"
  "v_exp_f32 v92, v92\n\t"
  "v_mfma_f32_32x32x16_bf16 v[128:143], a[52:55], v[112:115], v[128:143]\n\t"
  "s_waitcnt lgkmcnt(2)\n\t"
  "v_exp_f32 v93, v93\n\t"
  "v_exp_f32 v94, v94\n\t"
  "v_cvt_pk_bf16_f32 v117, v90, v91\n\t"
  "v_exp_f32 v95, v95\n\t"
  "v_cvt_pk_bf16_f32 v118, v92, v93\n\t"
  "v_cvt_pk_bf16_f32 v119, v94, v95\n\t"
  "v_mfma_f32_32x32x16_bf16 v[80:95], a[32:35], a[64:67], v[48:63]\n\t"
  "s_mov_b32 m0, s39\n\t"
  "v_exp_f32 v96, v96\n\t"
  "v_exp_f32 v97, v97\n\t"
  "v_exp_f32 v98, v98\n\t"
  "v_exp_f32 v99, v99\n\t"
  "v_mfma_f32_32x32x16_bf16 v[128:143], a[60:63], v[116:119], v[128:143]\n\t"
  "global_load_lds_dwordx4 v231, s[46:47] offset:3072\n\t"
  "v_exp_f32 v100, v100\n\t"
  "v_exp_f32 v101, v101\n\t"
  "v_cvt_pk_bf16_f32 v120, v96, v97\n\t"
  "v_exp_f32 v102, v102\n\t"
  "v_cvt_pk_bf16_f32 v121, v98, v99\n\t"
  "v_mfma_f32_32x32x16_bf16 v[80:95], a[40:43], a[68:71], v[80:95]\n\t"
  "s_add_u32 s46, s46, 0x40000\n\t"
  "s_addc_u32 s47, s47, 0\n\t"
  "v_exp_f32 v103, v103\n\t"
  "v_exp_f32 v104, v104\n\t"
  "v_cvt_pk_bf16_f32 v122, v100, v101\n\t"
  "v_exp_f32 v105, v105\n\t"
  "v_cvt_pk_bf16_f32 v123, v102, v103\n\t"
  "v_smfmac_f32_16x16x64_bf16 v[160:163], v[172:175], v[112:119], v171\n\t"
  "buffer_store_dwordx4 a[12:15], v181, s[20:23], s29 offen sc1\n\t"
  "ds_read_b128 a[56:59], v168 offset:3072\n\t"
  "v_exp_f32 v106, v106\n\t"
  "v_exp_f32 v107, v107\n\t"
  "v_cvt_pk_bf16_f32 v124, v104, v105\n\t"
  "v_exp_f32 v108, v108\n\t"
  "v_mfma_f32_32x32x16_bf16 v[144:159], a[52:55], v[120:123], v[144:159]\n\t"
  "ds_read_b128 a[52:55], v168 offset:6144\n\t"
  "s_add_u32 s39, s39, 16384\n\t"
  "s_add_u32 s29, s29, 1024\n\t"
  "v_exp_f32 v109, v109\n\t"
  "v_exp_f32 v110, v110\n\t"
  "v_cvt_pk_bf16_f32 v125, v106, v107\n\t"
  "v_exp_f32 v111, v111\n\t"
  "v_cvt_pk_bf16_f32 v126, v108, v109\n\t"
  "v_cvt_pk_bf16_f32 v127, v110, v111\n\t"
  "s_cmp_lg_u32 s31, 2\n\t"
  "s_cbranch_scc1 LATTc_%=\n\t"
  "buffer_store_dwordx2 v[240:241], v29, s[20:23], 0 offen offset:0 sc1\n\t"
  "buffer_store_dwordx4 a[16:19], v18, s[20:23], 0 offen offset:0 sc1\n\t"
  "buffer_store_dwordx4 a[16:19], v18, s[20:23], 0 offen offset:1024 sc1\n\t"
  "s_branch LATT_%=\n\t"
  "LATTc_%=:\n\t"
  "s_cmp_eq_u32 s31, 0\n\t"
  "s_cbranch_scc1 LATTd_%=\n\t"
  "buffer_load_dwordx2 v[242:243], v221, s[20:23], 0 offen offset:0 sc1\n\t"
  "global_load_dword v224, v251, s[74:75]\n\t"
  "buffer_store_dwordx4 a[16:19], v18, s[20:23], 0 offen offset:2048 sc1\n\t"
  "buffer_store_dwordx4 a[16:19], v18, s[20:23], 0 offen offset:3072 sc1\n\t"
  "v_mov_b64 v[176:177], v[186:187]\n\t"
  "v_mov_b64 v[178:179], v[188:189]\n\t"
  "v_mov_b64 v[180:181], v[190:191]\n\t"
  "s_mov_b32 s48, s51\n\t"
  "s_mov_b32 s49, s52\n\t"
  "s_mov_b32 s50, s53\n\t"
  "s_branch LATT_%=\n\t"
  "LATTd_%=:\n\t"
  "kattb_begin:\n\t"
  "buffer_store_dwordx2 v[240:241], v220, s[20:23], 0 offen offset:0 sc1\n\t"
  "s_mov_b32 s72, 0x20000\n\t"
  "s_not_b32 s68, s34\n\t"
  "s_mov_b32 s73, 64\n\t"
  "s_branch LPWc_%=\n\t"
  "LPWa_%=:\n\t"
  "v_not_b32 v242, s34\n\t"
  "buffer_load_dwordx2 v[242:243], v221, s[20:23], 0 offen offset:0 sc1\n\t"
  "s_mov_b32 s73, 64\n\t"
  "LPWw_%=:\n\t"
  "s_sleep 1\n\t"
  "LPWc_%=:\n\t"
  "v_cmp_eq_u32 vcc, s68, v242\n\t"
  "s_nop 1\n\t"
  "s_cmp_eq_u64 vcc, 0\n\t"
  "v_cmp_ne_u64 vcc, s[34:35], v[242:243]\n\t"
  "s_cbranch_vccz LPWb_%=\n\t"
  "s_cbranch_scc1 LPWr_%=\n\t"
  "s_sub_u32 s73, s73, 1\n\t"
  "s_cbranch_scc0 LPWw_%=\n\t"
  "s_waitcnt vmcnt(0)\n\t"
  "LPWr_%=:\n\t"
  "s_sleep 2\n\t"
  "s_sub_u32 s72, s72, 1\n\t"
  "s_cbranch_scc0 LPWa_%=\n\t"
  "LPWb_%=:\n\t"
  "ds_read_b128 a[32:35], v168 offset:8192\n\t"
  "ds_read_b128 a[40:43], v168 offset:9216\n\t"
  "s_waitcnt lgkmcnt(4)\n\t"
  "s_add_u32 m0, s30, 32768\n\t"
  "v_mfma_f32_32x32x16_bf16 v[96:111], a[36:39], a[64:67], v[48:63]\n\t"
  "ds_read_b128 a[36:39], v168 offset:12288\n\t"
  "buffer_load_dwordx4 v169, s[20:23], s28 offen offset:2048 sc1 lds\n\t"
  "v_exp_f32 v80, v80\n\t"
  "v_exp_f32 v81, v81\n\t"
  "v_exp_f32 v82, v82\n\t"
  "v_exp_f32 v83, v83\n\t"
  "v_mfma_f32_32x32x16_bf16 v[144:159], a[60:63], v[124:127], v[144:159]\n\t"
  "s_waitcnt vmcnt(9)\n\t"
  "ds_read_b128 a[60:63], v168 offset:7168\n\t"
  "buffer_load_dwordx4 v169, s[20:23], s28 offen offset:3072 sc1 lds\n\t"
  "s_add_u32 s28, s28, 16384\n\t"
  "v_exp_f32 v84, v84\n\t"
  "v_exp_f32 v85, v85\n\t"
  "v_cvt_pk_bf16_f32 v112, v80, v81\n\t"
  "v_exp_f32 v86, v86\n\t"
  "v_cvt_pk_bf16_f32 v113, v82, v83\n\t"
  "v_mfma_f32_32x32x16_bf16 v[96:111], a[44:47], a[68:71], v[96:111]\n\t"
  "s_barrier\n\t"
  "ds_read_b128 a[44:47], v168 offset:13312\n\t"
  "s_add_u32 m0, s30, 49152\n\t"
  "v_exp_f32 v87, v87\n\t"
  "v_exp_f32 v88, v88\n\t"
  "v_cvt_pk_bf16_f32 v114, v84, v85\n\t"
  "v_exp_f32 v89, v89\n\t"
  "v_cvt_pk_bf16_f32 v115, v86, v87\n\t"
  "v_smfmac_f32_16x16x64_bf16 v[164:167], v[172:175], v[120:127], v171\n\t"
  "buffer_load_dwordx4 v169, s[20:23], s28 offen offset:0 sc1 lds\n\t"
  "v_exp_f32 v90, v90\n\t"
  "v_exp_f32 v91, v91\n\t"
  "v_cvt_pk_bf16_f32 v116, v88, v89\n\t"
  "v_exp_f32 v92, v92\n\t"
  "v_mfma_f32_32x32x16_bf16 v[128:143], a[48:51], v[112:115], v[128:143]\n\t"
  "s_waitcnt lgkmcnt(3)\n\t"
  "ds_read_b128 a[48:51], v168 offset:10240\n\t"
  "v_exp_f32 v93, v93\n\t"
  "v_exp_f32 v94, v94\n\t"
  "v_cvt_pk_bf16_f32 v117, v90, v91\n\t"
  "v_exp_f32 v95, v95\n\t"
  "v_cvt_pk_bf16_f32 v118, v92, v93\n\t"
  "v_cvt_pk_bf16_f32 v119, v94, v95\n\t"
  "v_mfma_f32_32x32x16_bf16 v[80:95], a[32:35], a[64:67], v[48:63]\n\t"
  "ds_read_b128 a[32:35], v168 offset:16384\n\t"
  "v_exp_f32 v96, v96\n\t"
  "v_exp_f32 v97, v97\n\t"
  "v_exp_f32 v98, v98\n\t"
  "v_exp_f32 v99, v99\n\t"
  "v_mfma_f32_32x32x16_bf16 v[128:143], a[56:59], v[116:119], v[128:143]\n\t"
  "ds_read_b128 a[56:59], v168 offset:11264\n\t"
  "ds_read_b64_tr_b16 a[0:1], v236 offset:4096\n\t"
  "v_exp_f32 v100, v100\n\t"
  "v_exp_f32 v101, v101\n\t"
  "v_cvt_pk_bf16_f32 v120, v96, v97\n\t"
  "v_exp_f32 v102, v102\n\t"
  "v_cvt_pk_bf16_f32 v121, v98, v99\n\t"
  "v_mfma_f32_32x32x16_bf16 v[80:95], a[40:43], a[68:71], v[80:95]\n\t"
  "ds_read_b128 a[40:43], v168 offset:17408\n\t"
  "ds_read_b64_tr_b16 a[2:3], v236 offset:6144\n\t"
  "v_exp_f32 v103, v103\n\t"
  "v_exp_f32 v104, v104\n\t"
  "v_cvt_pk_bf16_f32 v122, v100, v101\n\t"
  "v_exp_f32 v105, v105\n\t"
  "v_cvt_pk_bf16_f32 v123, v102, v103\n\t"
  "v_smfmac_f32_16x16x64_bf16 v[160:163], v[172:175], v[112:119], v171\n\t"
  "buffer_load_dwordx4 v169, s[20:23], s28 offen offset:1024 sc1 lds\n\t"
  "v_exp_f32 v106, v106\n\t"
  "v_exp_f32 v107, v107\n\t"
  "v_cvt_pk_bf16_f32 v124, v104, v105\n\t"
  "v_exp_f32 v108, v108\n\t"
  "v_mfma_f32_32x32x16_bf16 v[128:143], a[52:55], v[120:123], v[128:143]\n\t"
  "s_waitcnt lgkmcnt(5)\n\t"
  "ds_read_b128 a[52:55], v168 offset:14336\n\t"
  "v_exp_f32 v109, v109\n\t"
  "v_exp_f32 v110, v110\n\t"
  "v_cvt_pk_bf16_f32 v125, v106, v107\n\t"
  "v_exp_f32 v111, v111\n\t"
  "v_cvt_pk_bf16_f32 v126, v108, v109\n\t"
  "v_cvt_pk_bf16_f32 v127, v110, v111\n\t"
  "v_mfma_f32_32x32x16_bf16 v[96:111], a[36:39], a[64:67], v[48:63]\n\t"
  "ds_read_b128 a[36:39], v168 offset:20480\n\t"
  "v_exp_f32 v80, v80\n\t"
  "v_exp_f32 v81, v81\n\t"
  "v_exp_f32 v82, v82\n\t"
  "v_exp_f32 v83, v83\n\t"
  "v_mfma_f32_32x32x16_bf16 v[128:143], a[60:63], v[124:127], v[128:143]\n\t"
  "ds_read_b128 a[60:63], v168 offset:15360\n\t"
  "ds_read_b64_tr_b16 a[4:5], v237 offset:4096\n\t"
  "v_exp_f32 v84, v84\n\t"
  "v_exp_f32 v85, v85\n\t"
  "v_cvt_pk_bf16_f32 v112, v80, v81\n\t"
  "v_exp_f32 v86, v86\n\t"
  "v_cvt_pk_bf16_f32 v113, v82, v83\n\t"
  "v_mfma_f32_32x32x16_bf16 v[96:111], a[44:47], a[68:71], v[96:111]\n\t"
  "ds_read_b128 a[44:47], v168 offset:21504\n\t"
  "ds_read_b64_tr_b16 a[6:7], v237 offset:6144\n\t"
  "v_exp_f32 v87, v87\n\t"
  "v_exp_f32 v88, v88\n\t"
  "v_cvt_pk_bf16_f32 v114, v84, v85\n\t"
  "v_exp_f32 v89, v89\n\t"
  "v_cvt_pk_bf16_f32 v115, v86, v87\n\t"
  "v_smfmac_f32_16x16x64_bf16 v[160:163], v[172:175], v[120:127], v171\n\t"
  "buffer_load_dwordx4 v169, s[20:23], s28 offen offset:2048 sc1 lds\n\t"
  "v_exp_f32 v90, v90\n\t"
  "v_exp_f32 v91, v91\n\t"
  "v_cvt_pk_bf16_f32 v116, v88, v89\n\t"
  "v_exp_f32 v92, v92\n\t"
  "v_mfma_f32_32x32x16_bf16 v[128:143], a[48:51], v[112:115], v[128:143]\n\t"
  "s_waitcnt lgkmcnt(5)\n\t"
  "ds_read_b128 a[48:51], v168 offset:18432\n\t"
  "v_exp_f32 v93, v93\n\t"
  "v_exp_f32 v94, v94\n\t"
  "v_cvt_pk_bf16_f32 v117, v90, v91\n\t"
  "v_exp_f32 v95, v95\n\t"
  "v_cvt_pk_bf16_f32 v118, v92, v93\n\t"
  "v_cvt_pk_bf16_f32 v119, v94, v95\n\t"
  "v_mfma_f32_32x32x16_bf16 v[80:95], a[32:35], a[64:67], v[48:63]\n\t"
  "ds_read_b128 a[32:35], v168 offset:24576\n\t"
  "v_exp_f32 v96, v96\n\t"
  "v_exp_f32 v97, v97\n\t"
  "v_exp_f32 v98, v98\n\t"
  "v_exp_f32 v99, v99\n\t"
  "v_mfma_f32_32x32x16_bf16 v[128:143], a[56:59], v[116:119], v[128:143]\n\t"
  "ds_read_b128 a[56:59], v168 offset:19456\n\t"
  "ds_read_b64_tr_b16 a[8:9], v238 offset:4096\n\t"
  "v_exp_f32 v100, v100\n\t"
  "v_exp_f32 v101, v101\n\t"
  "v_cvt_pk_bf16_f32 v120, v96, v97\n\t"
  "v_exp_f32 v102, v102\n\t"
  "v_cvt_pk_bf16_f32 v121, v98, v99\n\t"
  "v_mfma_f32_32x32x16_bf16 v[80:95], a[40:43], a[68:71], v[80:95]\n\t"
  "ds_read_b128 a[40:43], v168 offset:25600\n\t"
  "ds_read_b64_tr_b16 a[10:11], v238 offset:6144\n\t"
  "v_exp_f32 v103, v103\n\t"
  "v_exp_f32 v104, v104\n\t"
  "v_cvt_pk_bf16_f32 v122, v100, v101\n\t"
  "v_exp_f32 v105, v105\n\t"
  "v_cvt_pk_bf16_f32 v123, v102, v103\n\t"
  "v_smfmac_f32_16x16x64_bf16 v[160:163], v[172:175], v[112:119], v171\n\t"
  "buffer_load_dwordx4 v169, s[20:23], s28 offen offset:3072 sc1 lds\n\t"
  "v_exp_f32 v106, v106\n\t"
  "v_exp_f32 v107, v107\n\t"
  "v_cvt_pk_bf16_f32 v124, v104, v105\n\t"
  "v_exp_f32 v108, v108\n\t"
  "v_mfma_f32_32x32x16_bf16 v[128:143], a[52:55], v[120:123], v[128:143]\n\t"
  "s_waitcnt lgkmcnt(5)\n\t"
  "ds_read_b128 a[52:55], v168 offset:22528\n\t"
  "v_exp_f32 v109, v109\n\t"
  "v_exp_f32 v110, v110\n\t"
  "v_cvt_pk_bf16_f32 v125, v106, v107\n\t"
  "v_exp_f32 v111, v111\n\t"
  "v_cvt_pk_bf16_f32 v126, v108, v109\n\t"
  "v_cvt_pk_bf16_f32 v127, v110, v111\n\t"
  "v_mfma_f32_32x32x16_bf16 v[96:111], a[36:39], a[64:67], v[48:63]\n\t"
  "ds_read_b128 a[36:39], v168 offset:28672\n\t"
  "s_mov_b32 m0, s39\n\t"
  "v_exp_f32 v80, v80\n\t"
  "v_exp_f32 v81, v81\n\t"
  "v_exp_f32 v82, v82\n\t"
  "v_exp_f32 v83, v83\n\t"
  "v_mfma_f32_32x32x16_bf16 v[128:143], a[60:63], v[124:127], v[128:143]\n\t"
  "s_waitcnt vmcnt(4)\n\t"
  "ds_read_b128 a[60:63], v168 offset:23552\n\t"
  "ds_read_b64_tr_b16 a[12:13], v239 offset:4096\n\t"
  "v_exp_f32 v84, v84\n\t"
  "v_exp_f32 v85, v85\n\t"
  "v_cvt_pk_bf16_f32 v112, v80, v81\n\t"
  "v_exp_f32 v86, v86\n\t"
  "v_cvt_pk_bf16_f32 v113, v82, v83\n\t"
  "v_mfma_f32_32x32x16_bf16 v[96:111], a[44:47], a[68:71], v[96:111]\n\t"
  "s_barrier\n\t"
  "ds_read_b128 a[44:47], v168 offset:29696\n\t"
  "ds_read_b64_tr_b16 a[14:15], v239 offset:6144\n\t"
  "global_load_lds_dwordx4 v225, s[46:47] offset:0\n\t"
  "v_exp_f32 v87, v87\n\t"
  "v_exp_f32 v88, v88\n\t"
  "v_cvt_pk_bf16_f32 v114, v84, v85\n\t"
  "v_exp_f32 v89, v89\n\t"
  "v_cvt_pk_bf16_f32 v115, v86, v87\n\t"
  "v_smfmac_f32_16x16x64_bf16 v[160:163], v[172:175], v[120:127], v171\n\t"
  "buffer_store_dwordx4 a[0:3], v192, s[20:23], s36 offen sc1\n\t"
  "v_exp_f32 v90, v90\n\t"
  "v_exp_f32 v91, v91\n\t"
  "v_cvt_pk_bf16_f32 v116, v88, v89\n\t"
  "v_exp_f32 v92, v92\n\t"
  "v_mfma_f32_32x32x16_bf16 v[128:143], a[48:51], v[112:115], v[128:143]\n\t"
  "s_waitcnt lgkmcnt(5)\n\t"
  "ds_read_b128 a[48:51], v168 offset:26624\n\t"
  "v_exp_f32 v93, v93\n\t"
  "v_exp_f32 v94, v94\n\t"
  "v_cvt_pk_bf16_f32 v117, v90, v91\n\t"
  "v_exp_f32 v95, v95\n\t"
  "v_cvt_pk_bf16_f32 v118, v92, v93\n\t"
  "v_cvt_pk_bf16_f32 v119, v94, v95\n\t"
  "v_mfma_f32_32x32x16_bf16 v[80:95], a[32:35], a[64:67], v[48:63]\n\t"
  "ds_read_b128 a[32:35], v168 offset:32768\n\t"
  "v_exp_f32 v96, v96\n\t"
  "v_exp_f32 v97, v97\n\t"
  "v_exp_f32 v98, v98\n\t"
  "v_exp_f32 v99, v99\n\t"
  "v_mfma_f32_32x32x16_bf16 v[128:143], a[56:59], v[116:119], v[128:143]\n\t"
  "ds_read_b128 a[56:59], v168 offset:27648\n\t"
  "v_exp_f32 v100, v100\n\t"
  "v_exp_f32 v101, v101\n\t"
  "v_cvt_pk_bf16_f32 v120, v96, v97\n\t"
  "v_exp_f32 v102, v102\n\t"
  "v_cvt_pk_bf16_f32 v121, v98, v99\n\t"
  "v_mfma_f32_32x32x16_bf16 v[80:95], a[40:43], a[68:71], v[80:95]\n\t"
  "ds_read_b128 a[40:43], v168 offset:33792\n\t"
  "global_load_lds_dwordx4 v229, s[46:47] offset:1024\n\t"
  "v_exp_f32 v103, v103\n\t"
  "v_exp_f32 v104, v104\n\t"
  "v_cvt_pk_bf16_f32 v122, v100, v101\n\t"
  "v_exp_f32 v105, v105\n\t"
  "v_cvt_pk_bf16_f32 v123, v102, v103\n\t"
  "v_smfmac_f32_16x16x64_bf16 v[160:163], v[172:175], v[112:119], v171\n\t"
  "buffer_store_dwordx4 a[4:7], v193, s[20:23], s36 offen sc1\n\t"
  "v_exp_f32 v106, v106\n\t"
  "v_exp_f32 v107, v107\n\t"
  "v_cvt_pk_bf16_f32 v124, v104, v105\n\t"
  "v_exp_f32 v108, v108\n\t"
  "v_mfma_f32_32x32x16_bf16 v[128:143], a[52:55], v[120:123], v[128:143]\n\t"
  "s_waitcnt lgkmcnt(3)\n\t"
  "ds_read_b128 a[52:55], v168 offset:30720\n\t"
  "v_exp_f32 v109, v109\n\t"
  "v_exp_f32 v110, v110\n\t"
  "v_cvt_pk_bf16_f32 v125, v106, v107\n\t"
  "v_exp_f32 v111, v111\n\t"
  "v_cvt_pk_bf16_f32 v126, v108, v109\n\t"
  "v_cvt_pk_bf16_f32 v127, v110, v111\n\t"
  "v_mfma_f32_32x32x16_bf16 v[96:111], a[36:39], a[64:67], v[48:63]\n\t"
  "ds_read_b128 a[36:39], v168 offset:36864\n\t"
  "v_exp_f32 v80, v80\n\t"
  "v_exp_f32 v81, v81\n\t"
  "v_exp_f32 v82, v82\n\t"
  "v_exp_f32 v83, v83\n\t"
  "v_mfma_f32_32x32x16_bf16 v[128:143], a[60:63], v[124:127], v[128:143]\n\t"
  "ds_read_b128 a[60:63], v168 offset:31744\n\t"
  "v_exp_f32 v84, v84\n\t"
  "v_exp_f32 v85, v85\n\t"
  "v_cvt_pk_bf16_f32 v112, v80, v81\n\t"
  "v_exp_f32 v86, v86\n\t"
  "v_cvt_pk_bf16_f32 v113, v82, v83\n\t"
  "v_mfma_f32_32x32x16_bf16 v[96:111], a[44:47], a[68:71], v[96:111]\n\t"
  "ds_read_b128 a[44:47], v168 offset:37888\n\t"
  "global_load_lds_dwordx4 v230, s[46:47] offset:2048\n\t"
  "v_exp_f32 v87, v87\n\t"
  "v_exp_f32 v88, v88\n\t"
  "v_cvt_pk_bf16_f32 v114, v84, v85\n\t"
  "v_exp_f32 v89, v89\n\t"
  "v_cvt_pk_bf16_f32 v115, v86, v87\n\t"
  "v_smfmac_f32_16x16x64_bf16 v[160:163], v[172:175], v[120:127], v171\n\t"
  "buffer_store_dwordx4 a[8:11], v194, s[20:23], s36 offen sc1\n\t"
  "v_exp_f32 v90, v90\n\t"
  "v_exp_f32 v91, v91\n\t"
  "v_cvt_pk_bf16_f32 v116, v88, v89\n\t"
  "v_exp_f32 v92, v92\n\t"
  "v_mfma_f32_32x32x16_bf16 v[128:143], a[48:51], v[112:115], v[128:143]\n\t"
  "s_waitcnt lgkmcnt(3)\n\t"
  "ds_read_b128 a[48:51], v168 offset:34816\n\t"
  "v_exp_f32 v93, v93\n\t"
  "v_exp_f32 v94, v94\n\t"
  "v_cvt_pk_bf16_f32 v117, v90, v91\n\t"
  "v_exp_f32 v95, v95\n\t"
  "v_cvt_pk_bf16_f32 v118, v92, v93\n\t"
  "v_cvt_pk_bf16_f32 v119, v94, v95\n\t"
  "v_mfma_f32_32x32x16_bf16 v[80:95], a[32:35], a[64:67], v[48:63]\n\t"
  "ds_read_b128 a[32:35], v168 offset:40960\n\t"
  "v_exp_f32 v96, v96\n\t"
  "v_exp_f32 v97, v97\n\t"
  "v_exp_f32 v98, v98\n\t"
  "v_exp_f32 v99, v99\n\t"
  "v_mfma_f32_32x32x16_bf16 v[128:143], a[56:59], v[116:119], v[128:143]\n\t"
  "ds_read_b128 a[56:59], v168 offset:35840\n\t"
  "v_exp_f32 v100, v100\n\t"
  "v_exp_f32 v101, v101\n\t"
  "v_cvt_pk_bf16_f32 v120, v96, v97\n\t"
  "v_exp_f32 v102, v102\n\t"
  "v_cvt_pk_bf16_f32 v121, v98, v99\n\t"
  "v_mfma_f32_32x32x16_bf16 v[80:95], a[40:43], a[68:71], v[80:95]\n\t"
  "ds_read_b128 a[40:43], v168 offset:41984\n\t"
  "global_load_lds_dwordx4 v231, s[46:47] offset:3072\n\t"
  "v_exp_f32 v103, v103\n\t"
  "v_exp_f32 v104, v104\n\t"
  "v_cvt_pk_bf16_f32 v122, v100, v101\n\t"
  "v_exp_f32 v105, v105\n\t"
  "v_cvt_pk_bf16_f32 v123, v102, v103\n\t"
  "v_smfmac_f32_16x16x64_bf16 v[160:163], v[172:175], v[112:119], v171\n\t"
  "buffer_store_dwordx4 a[12:15], v195, s[20:23], s36 offen sc1\n\t"
  "v_exp_f32 v106, v106\n\t"
  "v_exp_f32 v107, v107\n\t"
  "v_cvt_pk_bf16_f32 v124, v104, v105\n\t"
  "v_exp_f32 v108, v108\n\t"
  "v_mfma_f32_32x32x16_bf16 v[128:143], a[52:55], v[120:123], v[128:143]\n\t"
  "s_waitcnt lgkmcnt(3)\n\t"
  "ds_read_b128 a[52:55], v168 offset:38912\n\t"
  "v_exp_f32 v109, v109\n\t"
  "v_exp_f32 v110, v110\n\t"
  "v_cvt_pk_bf16_f32 v125, v106, v107\n\t"
  "v_exp_f32 v111, v111\n\t"
  "v_cvt_pk_bf16_f32 v126, v108, v109\n\t"
  "v_cvt_pk_bf16_f32 v127, v110, v111\n\t"
  "v_mfma_f32_32x32x16_bf16 v[96:111], a[36:39], a[64:67], v[48:63]\n\t"
  "ds_read_b128 a[36:39], v168 offset:45056\n\t"
  "v_exp_f32 v80, v80\n\t"
  "v_exp_f32 v81, v81\n\t"
  "v_exp_f32 v82, v82\n\t"
  "v_exp_f32 v83, v83\n\t"
  "v_mfma_f32_32x32x16_bf16 v[128:143], a[60:63], v[124:127], v[128:143]\n\t"
  "s_waitcnt vmcnt(8)\n\t"
  "ds_read_b128 a[60:63], v168 offset:39936\n\t"
  "v_exp_f32 v84, v84\n\t"
  "v_exp_f32 v85, v85\n\t"
  "v_cvt_pk_bf16_f32 v112, v80, v81\n\t"
  "v_exp_f32 v86, v86\n\t"
  "v_cvt_pk_bf16_f32 v113, v82, v83\n\t"
  "v_mfma_f32_32x32x16_bf16 v[96:111], a[44:47], a[68:71], v[96:111]\n\t"
  "s_barrier\n\t"
  "ds_read_b128 a[44:47], v168 offset:46080\n\t"
  "s_add_u32 m0, s30, 65536\n\t"
  "s_mov_b32 s29, 0\n\t"
  "v_exp_f32 v87, v87\n\t"
  "v_exp_f32 v88, v88\n\t"
  "v_cvt_pk_bf16_f32 v114, v84, v85\n\t"
  "v_exp_f32 v89, v89\n\t"
  "v_cvt_pk_bf16_f32 v115, v86, v87\n\t"
  "v_smfmac_f32_16x16x64_bf16 v[160:163], v[172:175], v[120:127], v171\n\t"
  "buffer_load_dwordx4 v225, s[24:27], s29 offen offset:0 lds\n\t"
  "v_exp_f32 v90, v90\n\t"
  "v_exp_f32 v91, v91\n\t"
  "v_cvt_pk_bf16_f32 v116, v88, v89\n\t"
  "v_exp_f32 v92, v92\n\t"
  "v_mfma_f32_32x32x16_bf16 v[128:143], a[48:51], v[112:115], v[128:143]\n\t"
  "s_waitcnt lgkmcnt(3)\n\t"
  "ds_read_b128 a[48:51], v168 offset:43008\n\t"
  "v_exp_f32 v93, v93\n\t"
  "v_exp_f32 v94, v94\n\t"
  "v_cvt_pk_bf16_f32 v117, v90, v91\n\t"
  "v_exp_f32 v95, v95\n\t"
  "v_cvt_pk_bf16_f32 v118, v92, v93\n\t"
  "v_cvt_pk_bf16_f32 v119, v94, v95\n\t"
  "v_mfma_f32_32x32x16_bf16 v[80:95], a[32:35], a[64:67], v[48:63]\n\t"
  "ds_read_b128 a[32:35], v168 offset:49152\n\t"
  "v_exp_f32 v96, v96\n\t"
  "v_exp_f32 v97, v97\n\t"
  "v_exp_f32 v98, v98\n\t"
  "v_exp_f32 v99, v99\n\t"
  "v_mfma_f32_32x32x16_bf16 v[128:143], a[56:59], v[116:119], v[128:143]\n\t"
  "ds_read_b128 a[56:59], v168 offset:44032\n\t"
  "v_not_b32 v242, s34\n\t"
  "buffer_load_dwordx2 v[242:243], v30, s[20:23], 0 offen offset:0 sc1\n\t"
  "v_exp_f32 v100, v100\n\t"
  "v_exp_f32 v101, v101\n\t"
  "v_cvt_pk_bf16_f32 v120, v96, v97\n\t"
  "v_exp_f32 v102, v102\n\t"
  "v_cvt_pk_bf16_f32 v121, v98, v99\n\t"
  "v_mfma_f32_32x32x16_bf16 v[80:95], a[40:43], a[68:71], v[80:95]\n\t"
  "ds_read_b128 a[40:43], v168 offset:50176\n\t"
  "v_exp_f32 v103, v103\n\t"
  "v_exp_f32 v104, v104\n\t"
  "v_cvt_pk_bf16_f32 v122, v100, v101\n\t"
  "v_exp_f32 v105, v105\n\t"
  "v_cvt_pk_bf16_f32 v123, v102, v103\n\t"
  "v_smfmac_f32_16x16x64_bf16 v[160:163], v[172:175], v[112:119], v171\n\t"
  "buffer_load_dwordx4 v229, s[24:27], s29 offen offset:1024 lds\n\t"
  "v_exp_f32 v106, v106\n\t"
  "v_exp_f32 v107, v107\n\t"
  "v_cvt_pk_bf16_f32 v124, v104, v105\n\t"
  "v_exp_f32 v108, v108\n\t"
  "v_mfma_f32_32x32x16_bf16 v[128:143], a[52:55], v[120:123], v[128:143]\n\t"
  "s_waitcnt lgkmcnt(3)\n\t"
  "ds_read_b128 a[52:55], v168 offset:47104\n\t"
  "v_exp_f32 v109, v109\n\t"
  "v_exp_f32 v110, v110\n\t"
  "v_cvt_pk_bf16_f32 v125, v106, v107\n\t"
  "v_exp_f32 v111, v111\n\t"
  "v_cvt_pk_bf16_f32 v126, v108, v109\n\t"
  "v_cvt_pk_bf16_f32 v127, v110, v111\n\t"
  "v_mfma_f32_32x32x16_bf16 v[96:111], a[36:39], a[64:67], v[48:63]\n\t"
  "ds_read_b128 a[36:39], v168 offset:53248\n\t"
  "v_exp_f32 v80, v80\n\t"
  "v_exp_f32 v81, v81\n\t"
  "v_exp_f32 v82, v82\n\t"
  "v_exp_f32 v83, v83\n\t"
  "v_mfma_f32_32x32x16_bf16 v[128:143], a[60:63], v[124:127], v[128:143]\n\t"
  "ds_read_b128 a[60:63], v168 offset:48128\n\t"
  "v_exp_f32 v84, v84\n\t"
  "v_exp_f32 v85, v85\n\t"
  "v_cvt_pk_bf16_f32 v112, v80, v81\n\t"
  "v_exp_f32 v86, v86\n\t"
  "v_cvt_pk_bf16_f32 v113, v82, v83\n\t"
  "v_mfma_f32_32x32x16_bf16 v[96:111], a[44:47], a[68:71], v[96:111]\n\t"
  "ds_read_b128 a[44:47], v168 offset:54272\n\t"
  "v_exp_f32 v87, v87\n\t"
  "v_exp_f32 v88, v88\n\t"
  "v_cvt_pk_bf16_f32 v114, v84, v85\n\t"
  "v_exp_f32 v89, v89\n\t"
  "v_cvt_pk_bf16_f32 v115, v86, v87\n\t"
  "v_smfmac_f32_16x16x64_bf16 v[160:163], v[172:175], v[120:127], v171\n\t"
  "buffer_load_dwordx4 v230, s[24:27], s29 offen offset:2048 lds\n\t"
  "v_exp_f32 v90, v90\n\t"
  "v_exp_f32 v91, v91\n\t"
  "v_cvt_pk_bf16_f32 v116, v88, v89\n\t"
  "v_exp_f32 v92, v92\n\t"
  "v_mfma_f32_32x32x16_bf16 v[128:143], a[48:51], v[112:115], v[128:143]\n\t"
  "s_waitcnt lgkmcnt(3)\n\t"
  "ds_read_b128 a[48:51], v168 offset:51200\n\t"
  "v_exp_f32 v93, v93\n\t"
  "v_exp_f32 v94, v94\n\t"
  "v_cvt_pk_bf16_f32 v117, v90, v91\n\t"
  "v_exp_f32 v95, v95\n\t"
  "v_cvt_pk_bf16_f32 v118, v92, v93\n\t"
  "v_cvt_pk_bf16_f32 v119, v94, v95\n\t"
  "v_mfma_f32_32x32x16_bf16 v[80:95], a[32:35], a[64:67], v[48:63]\n\t"
  "ds_read_b128 a[32:35], v168 offset:57344\n\t"
  "v_exp_f32 v96, v96\n\t"
  "v_exp_f32 v97, v97\n\t"
  "v_exp_f32 v98, v98\n\t"
  "v_exp_f32 v99, v99\n\t"
  "v_mfma_f32_32x32x16_bf16 v[128:143], a[56:59], v[116:119], v[128:143]\n\t"
  "ds_read_b128 a[56:59], v168 offset:52224\n\t"
  "v_exp_f32 v100, v100\n\t"
  "v_exp_f32 v101, v101\n\t"
  "v_cvt_pk_bf16_f32 v120, v96, v97\n\t"
  "v_exp_f32 v102, v102\n\t"
  "v_cvt_pk_bf16_f32 v121, v98, v99\n\t"
  "v_mfma_f32_32x32x16_bf16 v[80:95], a[40:43], a[68:71], v[80:95]\n\t"
  "ds_read_b128 a[40:43], v168 offset:58368\n\t"
  "v_exp_f32 v103, v103\n\t"
  "v_exp_f32 v104, v104\n\t"
  "v_cvt_pk_bf16_f32 v122, v100, v101\n\t"
  "v_exp_f32 v105, v105\n\t"
  "v_cvt_pk_bf16_f32 v123, v102, v103\n\t"
  "v_smfmac_f32_16x16x64_bf16 v[160:163], v[172:175], v[112:119], v171\n\t"
  "buffer_load_dwordx4 v231, s[24:27], s29 offen offset:3072 lds\n\t"
  "v_exp_f32 v106, v106\n\t"
  "v_exp_f32 v107, v107\n\t"
  "v_cvt_pk_bf16_f32 v124, v104, v105\n\t"
  "v_exp_f32 v108, v108\n\t"
  "v_mfma_f32_32x32x16_bf16 v[128:143], a[52:55], v[120:123], v[128:143]\n\t"
  "s_waitcnt lgkmcnt(3)\n\t"
  "ds_read_b128 a[52:55], v168 offset:55296\n\t"
  "v_exp_f32 v109, v109\n\t"
  "v_exp_f32 v110, v110\n\t"
  "v_cvt_pk_bf16_f32 v125, v106, v107\n\t"
  "v_exp_f32 v111, v111\n\t"
  "v_cvt_pk_bf16_f32 v126, v108, v109\n\t"
  "v_cvt_pk_bf16_f32 v127, v110, v111\n\t"
  "v_mfma_f32_32x32x16_bf16 v[96:111], a[36:39], a[64:67], v[48:63]\n\t"
  "ds_read_b128 a[36:39], v168 offset:61440\n\t"
  "v_exp_f32 v80, v80\n\t"
  "v_exp_f32 v81, v81\n\t"
  "v_exp_f32 v82, v82\n\t"
  "v_exp_f32 v83, v83\n\t"
  "v_mfma_f32_32x32x16_bf16 v[128:143], a[60:63], v[124:127], v[128:143]\n\t"
  "ds_read_b128 a[60:63], v168 offset:56320\n\t"
  "v_exp_f32 v84, v84\n\t"
  "v_exp_f32 v85, v85\n\t"
  "v_cvt_pk_bf16_f32 v112, v80, v81\n\t"
  "v_exp_f32 v86, v86\n\t"
  "v_cvt_pk_bf16_f32 v113, v82, v83\n\t"
  "v_mfma_f32_32x32x16_bf16 v[96:111], a[44:47], a[68:71], v[96:111]\n\t"
  "ds_read_b128 a[44:47], v168 offset:62464\n\t"
  "s_add_u32 m0, s30, 81920\n\t"
  "s_mov_b32 s29, 262144\n\t"
  "v_exp_f32 v87, v87\n\t"
  "v_exp_f32 v88, v88\n\t"
  "v_cvt_pk_bf16_f32 v114, v84, v85\n\t"
  "v_exp_f32 v89, v89\n\t"
  "v_cvt_pk_bf16_f32 v115, v86, v87\n\t"
  "v_smfmac_f32_16x16x64_bf16 v[160:163], v[172:175], v[120:127], v171\n\t"
  "buffer_load_dwordx4 v225, s[24:27], s29 offen offset:0 lds\n\t"
  "v_exp_f32 v90, v90\n\t"
  "v_exp_f32 v91, v91\n\t"
  "v_cvt_pk_bf16_f32 v116, v88, v89\n\t"
  "v_exp_f32 v92, v92\n\t"
  "v_mfma_f32_32x32x16_bf16 v[128:143], a[48:51], v[112:115], v[128:143]\n\t"
  "s_waitcnt lgkmcnt(3)\n\t"
  "ds_read_b128 a[48:51], v168 offset:59392\n\t"
  "v_exp_f32 v93, v93\n\t"
  "v_exp_f32 v94, v94\n\t"
  "v_cvt_pk_bf16_f32 v117, v90, v91\n\t"
  "v_exp_f32 v95, v95\n\t"
  "v_cvt_pk_bf16_f32 v118, v92, v93\n\t"
  "v_cvt_pk_bf16_f32 v119, v94, v95\n\t"
  "v_mfma_f32_32x32x16_bf16 v[80:95], a[32:35], a[64:67], v[48:63]\n\t"
  "ds_read_b128 a[32:35], v168 offset:0\n\t"
  "v_exp_f32 v96, v96\n\t"
  "v_exp_f32 v97, v97\n\t"
  "v_exp_f32 v98, v98\n\t"
  "v_exp_f32 v99, v99\n\t"
  "v_mfma_f32_32x32x16_bf16 v[128:143], a[56:59], v[116:119], v[128:143]\n\t"
  "ds_read_b128 a[56:59], v168 offset:60416\n\t"
  "v_exp_f32 v100, v100\n\t"
  "v_exp_f32 v101, v101\n\t"
  "v_cvt_pk_bf16_f32 v120, v96, v97\n\t"
  "v_exp_f32 v102, v102\n\t"
  "v_cvt_pk_bf16_f32 v121, v98, v99\n\t"
  "v_mfma_f32_32x32x16_bf16 v[80:95], a[40:43], a[68:71], v[80:95]\n\t"
  "ds_read_b128 a[40:43], v168 offset:1024\n\t"
  "v_exp_f32 v103, v103\n\t"
  "v_exp_f32 v104, v104\n\t"
  "v_cvt_pk_bf16_f32 v122, v100, v101\n\t"
  "v_exp_f32 v105, v105\n\t"
  "v_cvt_pk_bf16_f32 v123, v102, v103\n\t"
  "v_smfmac_f32_16x16x64_bf16 v[160:163], v[172:175], v[112:119], v171\n\t"
  "buffer_load_dwordx4 v229, s[24:27], s29 offen offset:1024 lds\n\t"
  "v_exp_f32 v106, v106\n\t"
  "v_exp_f32 v107, v107\n\t"
  "v_cvt_pk_bf16_f32 v124, v104, v105\n\t"
  "v_exp_f32 v108, v108\n\t"
  "v_mfma_f32_32x32x16_bf16 v[128:143], a[52:55], v[120:123], v[128:143]\n\t"
  "s_waitcnt lgkmcnt(3)\n\t"
  "ds_read_b128 a[52:55], v168 offset:63488\n\t"
  "v_exp_f32 v109, v109\n\t"
  "v_exp_f32 v110, v110\n\t"
  "v_cvt_pk_bf16_f32 v125, v106, v107\n\t"
  "v_exp_f32 v111, v111\n\t"
  "v_cvt_pk_bf16_f32 v126, v108, v109\n\t"
  "v_cvt_pk_bf16_f32 v127, v110, v111\n\t"
  "v_mfma_f32_32x32x16_bf16 v[96:111], a[36:39], a[64:67], v[48:63]\n\t"
  "ds_read_b128 a[36:39], v168 offset:4096\n\t"
  "v_exp_f32 v80, v80\n\t"
  "v_exp_f32 v81, v81\n\t"
  "v_exp_f32 v82, v82\n\t"
  "v_exp_f32 v83, v83\n\t"
  "v_mfma_f32_32x32x16_bf16 v[128:143], a[60:63], v[124:127], v[128:143]\n\t"
  "ds_read_b128 a[60:63], v168 offset:64512\n\t"
  "s_mov_b32 s72, 0x20000\n\t"
  "s_not_b32 s68, s34\n\t"
  "s_mov_b32 s73, 64\n\t"
  "s_branch LPGc_%=\n\t"
  "LPGa_%=:\n\t"
  "v_not_b32 v242, s34\n\t"
  "buffer_load_dwordx2 v[242:243], v30, s[20:23], 0 offen offset:0 sc1\n\t"
  "s_mov_b32 s73, 64\n\t"
  "LPGw_%=:\n\t"
  "s_sleep 1\n\t"
  "LPGc_%=:\n\t"
  "v_cmp_eq_u32 vcc, s68, v242\n\t"
  "s_nop 1\n\t"
  "s_cmp_eq_u64 vcc, 0\n\t"
  "v_cmp_ne_u64 vcc, s[34:35], v[242:243]\n\t"
  "s_cbranch_vccz LPGb_%=\n\t"
  "s_cbranch_scc1 LPGr_%=\n\t"
  "s_sub_u32 s73, s73, 1\n\t"
  "s_cbranch_scc0 LPGw_%=\n\t"
  "s_waitcnt vmcnt(0)\n\t"
  "LPGr_%=:\n\t"
  "s_sleep 2\n\t"
  "s_sub_u32 s72, s72, 1\n\t"
  "s_cbranch_scc0 LPGa_%=\n\t"
  "LPGb_%=:\n\t"
  "v_exp_f32 v84, v84\n\t"
  "v_exp_f32 v85, v85\n\t"
  "v_cvt_pk_bf16_f32 v112, v80, v81\n\t"
  "v_exp_f32 v86, v86\n\t"
  "v_cvt_pk_bf16_f32 v113, v82, v83\n\t"
  "v_mfma_f32_32x32x16_bf16 v[96:111], a[44:47], a[68:71], v[96:111]\n\t"
  "ds_read_b128 a[44:47], v168 offset:5120\n\t"
  "v_exp_f32 v87, v87\n\t"
  "v_exp_f32 v88, v88\n\t"
  "v_cvt_pk_bf16_f32 v114, v84, v85\n\t"
  "v_exp_f32 v89, v89\n\t"
  "v_cvt_pk_bf16_f32 v115, v86, v87\n\t"
  "v_smfmac_f32_16x16x64_bf16 v[160:163], v[172:175], v[120:127], v171\n\t"
  "buffer_load_dwordx4 v230, s[24:27], s29 offen offset:2048 lds\n\t"
  "v_exp_f32 v90, v90\n\t"
  "v_exp_f32 v91, v91\n\t"
  "v_cvt_pk_bf16_f32 v116, v88, v89\n\t"
  "v_exp_f32 v92, v92\n\t"
  "v_mfma_f32_32x32x16_bf16 v[128:143], a[48:51], v[112:115], v[128:143]\n\t"
  "s_waitcnt lgkmcnt(3)\n\t"
  "ds_read_b128 a[48:51], v168 offset:2048\n\t"
  "v_exp_f32 v93, v93\n\t"
  "v_exp_f32 v94, v94\n\t"
  "v_cvt_pk_bf16_f32 v117, v90, v91\n\t"
  "v_exp_f32 v95, v95\n\t"
  "v_cvt_pk_bf16_f32 v118, v92, v93\n\t"
  "v_cvt_pk_bf16_f32 v119, v94, v95\n\t"
  "v_mfma_f32_32x32x16_bf16 v[80:95], a[32:35], a[72:75], v[64:79]\n\t"
  "ds_read_b128 a[32:35], v168 offset:8192\n\t"
  "v_exp_f32 v96, v96\n\t"
  "v_exp_f32 v97, v97\n\t"
  "v_exp_f32 v98, v98\n\t"
  "v_exp_f32 v99, v99\n\t"
  "v_mfma_f32_32x32x16_bf16 v[128:143], a[56:59], v[116:119], v[128:143]\n\t"
  "ds_read_b128 a[56:59], v168 offset:3072\n\t"
  "v_exp_f32 v100, v100\n\t"
  "v_exp_f32 v101, v101\n\t"
  "v_cvt_pk_bf16_f32 v120, v96, v97\n\t"
  "v_exp_f32 v102, v102\n\t"
  "v_cvt_pk_bf16_f32 v121, v98, v99\n\t"
  "v_mfma_f32_32x32x16_bf16 v[80:95], a[40:43], a[76:79], v[80:95]\n\t"
  "ds_read_b128 a[40:43], v168 offset:9216\n\t"
  "v_exp_f32 v103, v103\n\t"
  "v_exp_f32 v104, v104\n\t"
  "v_cvt_pk_bf16_f32 v122, v100, v101\n\t"
  "v_exp_f32 v105, v105\n\t"
  "v_cvt_pk_bf16_f32 v123, v102, v103\n\t"
  "v_smfmac_f32_16x16x64_bf16 v[160:163], v[172:175], v[112:119], v171\n\t"
  "buffer_load_dwordx4 v231, s[24:27], s29 offen offset:3072 lds\n\t"
  "v_exp_f32 v106, v106\n\t"
  "v_exp_f32 v107, v107\n\t"
  "v_cvt_pk_bf16_f32 v124, v104, v105\n\t"
  "v_exp_f32 v108, v108\n\t"
  "v_mfma_f32_32x32x16_bf16 v[128:143], a[52:55], v[120:123], v[128:143]\n\t"
  "s_waitcnt lgkmcnt(3)\n\t"
  "ds_read_b128 a[52:55], v168 offset:6144\n\t"
  "v_exp_f32 v109, v109\n\t"
  "v_exp_f32 v110, v110\n\t"
  "v_cvt_pk_bf16_f32 v125, v106, v107\n\t"
  "v_exp_f32 v111, v111\n\t"
  "v_cvt_pk_bf16_f32 v126, v108, v109\n\t"
  "v_cvt_pk_bf16_f32 v127, v110, v111\n\t"
  "v_mfma_f32_32x32x16_bf16 v[96:111], a[36:39], a[72:75], v[64:79]\n\t"
  "ds_read_b128 a[36:39], v168 offset:12288\n\t"
  "v_exp_f32 v80, v80\n\t"
  "v_exp_f32 v81, v81\n\t"
  "v_exp_f32 v82, v82\n\t"
  "v_exp_f32 v83, v83\n\t"
  "v_mfma_f32_32x32x16_bf16 v[128:143], a[60:63], v[124:127], v[128:143]\n\t"
  "ds_read_b128 a[60:63], v168 offset:7168\n\t"
  "v_exp_f32 v84, v84\n\t"
  "v_exp_f32 v85, v85\n\t"
  "v_cvt_pk_bf16_f32 v112, v80, v81\n\t"
  "v_exp_f32 v86, v86\n\t"
  "v_cvt_pk_bf16_f32 v113, v82, v83\n\t"
  "v_mfma_f32_32x32x16_bf16 v[96:111], a[44:47], a[76:79], v[96:111]\n\t"
  "ds_read_b128 a[44:47], v168 offset:13312\n\t"
  "v_exp_f32 v87, v87\n\t"
  "v_exp_f32 v88, v88\n\t"
  "v_cvt_pk_bf16_f32 v114, v84, v85\n\t"
  "v_exp_f32 v89, v89\n\t"
  "v_cvt_pk_bf16_f32 v115, v86, v87\n\t"
  "v_smfmac_f32_16x16x64_bf16 v[160:163], v[172:175], v[120:127], v171\n\t"
  "v_exp_f32 v90, v90\n\t"
  "v_exp_f32 v91, v91\n\t"
  "v_cvt_pk_bf16_f32 v116, v88, v89\n\t"
  "v_exp_f32 v92, v92\n\t"
  "v_mfma_f32_32x32x16_bf16 v[144:159], a[48:51], v[112:115], v[144:159]\n\t"
  "s_waitcnt lgkmcnt(3)\n\t"
  "ds_read_b128 a[48:51], v168 offset:10240\n\t"
  "v_exp_f32 v93, v93\n\t"
  "v_exp_f32 v94, v94\n\t"
  "v_cvt_pk_bf16_f32 v117, v90, v91\n\t"
  "v_exp_f32 v95, v95\n\t"
  "v_cvt_pk_bf16_f32 v118, v92, v93\n\t"
  "v_cvt_pk_bf16_f32 v119, v94, v95\n\t"
  "v_mfma_f32_32x32x16_bf16 v[80:95], a[32:35], a[72:75], v[64:79]\n\t"
  "ds_read_b128 a[32:35], v168 offset:16384\n\t"
  "v_exp_f32 v96, v96\n\t"
  "v_exp_f32 v97, v97\n\t"
  "v_exp_f32 v98, v98\n\t"
  "v_exp_f32 v99, v99\n\t"
  "v_mfma_f32_32x32x16_bf16 v[144:159], a[56:59], v[116:119], v[144:159]\n\t"
  "ds_read_b128 a[56:59], v168 offset:11264\n\t"
  "v_add_f32 v160, v160, v161\n\t"
  "v_rcp_f32 v162, v160\n\t"
  "v_add_f32 v161, v160, |v128|\n\t"
  "v_mul_f32 v48, v128, v162\n\t"
  "v_mul_f32 v49, v129, v162\n\t"
  "v_mul_f32 v50, v130, v162\n\t"
  "v_mul_f32 v51, v131, v162\n\t"
  "v_mul_f32 v52, v132, v162\n\t"
  "v_mul_f32 v53, v133, v162\n\t"
  "v_mul_f32 v54, v134, v162\n\t"
  "v_mul_f32 v55, v135, v162\n\t"
  "v_cvt_pk_bf16_f32 v48, v48, v49\n\t"
  "v_cvt_pk_bf16_f32 v49, v50, v51\n\t"
  "v_cvt_pk_bf16_f32 v50, v52, v53\n\t"
  "v_cvt_pk_bf16_f32 v51, v54, v55\n\t"
  "v_exp_f32 v100, v100\n\t"
  "v_exp_f32 v101, v101\n\t"
  "v_cvt_pk_bf16_f32 v120, v96, v97\n\t"
  "v_exp_f32 v102, v102\n\t"
  "v_cvt_pk_bf16_f32 v121, v98, v99\n\t"
  "v_mfma_f32_32x32x16_bf16 v[80:95], a[40:43], a[76:79], v[80:95]\n\t"
  "ds_read_b128 a[40:43], v168 offset:17408\n\t"
  "buffer_store_dwordx4 v[48:51], v219, s[20:23], 0 offen offset:0 sc1\n\t"
  "v_mul_f32 v56, v136, v162\n\t"
  "v_mul_f32 v57, v137, v162\n\t"
  "v_mul_f32 v58, v138, v162\n\t"
  "v_mul_f32 v59, v139, v162\n\t"
  "v_mul_f32 v60, v140, v162\n\t"
  "v_mul_f32 v61, v141, v162\n\t"
  "v_mul_f32 v62, v142, v162\n\t"
  "v_mul_f32 v63, v143, v162\n\t"
  "v_cvt_pk_bf16_f32 v56, v56, v57\n\t"
  "v_cvt_pk_bf16_f32 v57, v58, v59\n\t"
  "v_cvt_pk_bf16_f32 v58, v60, v61\n\t"
  "v_cvt_pk_bf16_f32 v59, v62, v63\n\t"
  "v_exp_f32 v103, v103\n\t"
  "v_exp_f32 v104, v104\n\t"
  "v_cvt_pk_bf16_f32 v122, v100, v101\n\t"
  "v_exp_f32 v105, v105\n\t"
  "v_cvt_pk_bf16_f32 v123, v102, v103\n\t"
  "v_smfmac_f32_16x16x64_bf16 v[164:167], v[172:175], v[112:119], v171\n\t"
  "buffer_store_dwordx4 v[56:59], v219, s[20:23], 0 offen offset:512 sc1\n\t"
  "v_exp_f32 v106, v106\n\t"
  "v_exp_f32 v107, v107\n\t"
  "v_cvt_pk_bf16_f32 v124, v104, v105\n\t"
  "v_exp_f32 v108, v108\n\t"
  "v_mfma_f32_32x32x16_bf16 v[144:159], a[52:55], v[120:123], v[144:159]\n\t"
  "s_waitcnt lgkmcnt(3)\n\t"
  "ds_read_b128 a[52:55], v168 offset:14336\n\t"
  "v_exp_f32 v109, v109\n\t"
  "v_exp_f32 v110, v110\n\t"
  "v_cvt_pk_bf16_f32 v125, v106, v107\n\t"
  "v_exp_f32 v111, v111\n\t"
  "v_cvt_pk_bf16_f32 v126, v108, v109\n\t"
  "v_cvt_pk_bf16_f32 v127, v110, v111\n\t"
  "v_mfma_f32_32x32x16_bf16 v[96:111], a[36:39], a[72:75], v[64:79]\n\t"
  "ds_read_b128 a[36:39], v168 offset:20480\n\t"
  "v_exp_f32 v80, v80\n\t"
  "v_exp_f32 v81, v81\n\t"
  "v_exp_f32 v82, v82\n\t"
  "v_exp_f32 v83, v83\n\t"
  "v_mfma_f32_32x32x16_bf16 v[144:159], a[60:63], v[124:127], v[144:159]\n\t"
  "ds_read_b128 a[60:63], v168 offset:15360\n\t"
  "v_exp_f32 v84, v84\n\t"
  "v_exp_f32 v85, v85\n\t"
  "v_cvt_pk_bf16_f32 v112, v80, v81\n\t"
  "v_exp_f32 v86, v86\n\t"
  "v_cvt_pk_bf16_f32 v113, v82, v83\n\t"
  "v_mfma_f32_32x32x16_bf16 v[96:111], a[44:47], a[76:79], v[96:111]\n\t"
  "ds_read_b128 a[44:47], v168 offset:21504\n\t"
  "v_exp_f32 v87, v87\n\t"
  "v_exp_f32 v88, v88\n\t"
  "v_cvt_pk_bf16_f32 v114, v84, v85\n\t"
  "v_exp_f32 v89, v89\n\t"
  "v_cvt_pk_bf16_f32 v115, v86, v87\n\t"
  "v_smfmac_f32_16x16x64_bf16 v[164:167], v[172:175], v[120:127], v171\n\t"
  "v_exp_f32 v90, v90\n\t"
  "v_exp_f32 v91, v91\n\t"
  "v_cvt_pk_bf16_f32 v116, v88, v89\n\t"
  "v_exp_f32 v92, v92\n\t"
  "v_mfma_f32_32x32x16_bf16 v[144:159], a[48:51], v[112:115], v[144:159]\n\t"
  "s_waitcnt lgkmcnt(3)\n\t"
  "ds_read_b128 a[48:51], v168 offset:18432\n\t"
  "v_add_f32 v161, v161, |v129|\n\t"
  "v_add_f32 v161, v161, |v130|\n\t"
  "v_add_f32 v161, v161, |v131|\n\t"
  "v_add_f32 v161, v161, |v132|\n\t"
  "v_add_f32 v161, v161, |v133|\n\t"
  "v_add_f32 v161, v161, |v134|\n\t"
  "v_add_f32 v161, v161, |v135|\n\t"
  "v_exp_f32 v93, v93\n\t"
  "v_exp_f32 v94, v94\n\t"
  "v_cvt_pk_bf16_f32 v117, v90, v91\n\t"
  "v_exp_f32 v95, v95\n\t"
  "v_cvt_pk_bf16_f32 v118, v92, v93\n\t"
  "v_cvt_pk_bf16_f32 v119, v94, v95\n\t"
  "v_mfma_f32_32x32x16_bf16 v[80:95], a[32:35], a[72:75], v[64:79]\n\t"
  "ds_read_b128 a[32:35], v168 offset:24576\n\t"
  "v_exp_f32 v96, v96\n\t"
  "v_exp_f32 v97, v97\n\t"
  "v_exp_f32 v98, v98\n\t"
  "v_exp_f32 v99, v99\n\t"
  "v_mfma_f32_32x32x16_bf16 v[144:159], a[56:59], v[116:119], v[144:159]\n\t"
  "ds_read_b128 a[56:59], v168 offset:19456\n\t"
  "v_exp_f32 v100, v100\n\t"
  "v_exp_f32 v101, v101\n\t"
  "v_cvt_pk_bf16_f32 v120, v96, v97\n\t"
  "v_exp_f32 v102, v102\n\t"
  "v_cvt_pk_bf16_f32 v121, v98, v99\n\t"
  "v_mfma_f32_32x32x16_bf16 v[80:95], a[40:43], a[76:79], v[80:95]\n\t"
  "ds_read_b128 a[40:43], v168 offset:25600\n\t"
  "v_exp_f32 v103, v103\n\t"
  "v_exp_f32 v104, v104\n\t"
  "v_cvt_pk_bf16_f32 v122, v100, v101\n\t"
  "v_exp_f32 v105, v105\n\t"
  "v_cvt_pk_bf16_f32 v123, v102, v103\n\t"
  "v_smfmac_f32_16x16x64_bf16 v[164:167], v[172:175], v[112:119], v171\n\t"
  "v_exp_f32 v106, v106\n\t"
  "v_exp_f32 v107, v107\n\t"
  "v_cvt_pk_bf16_f32 v124, v104, v105\n\t"
  "v_exp_f32 v108, v108\n\t"
  "v_mfma_f32_32x32x16_bf16 v[144:159], a[52:55], v[120:123], v[144:159]\n\t"
  "s_waitcnt lgkmcnt(3)\n\t"
  "ds_read_b128 a[52:55], v168 offset:22528\n\t"
  "v_add_f32 v161, v161, |v136|\n\t"
  "v_add_f32 v161, v161, |v137|\n\t"
  "v_add_f32 v161, v161, |v138|\n\t"
  "v_add_f32 v161, v161, |v139|\n\t"
  "v_add_f32 v161, v161, |v140|\n\t"
  "v_add_f32 v161, v161, |v141|\n\t"
  "v_add_f32 v161, v161, |v142|\n\t"
  "v_add_f32 v161, v161, |v143|\n\t"
  "v_cmp_nlt_f32 vcc, v161, v245\n\t"
  "v_exp_f32 v109, v109\n\t"
  "v_exp_f32 v110, v110\n\t"
  "v_cvt_pk_bf16_f32 v125, v106, v107\n\t"
  "v_exp_f32 v111, v111\n\t"
  "v_cvt_pk_bf16_f32 v126, v108, v109\n\t"
  "v_cvt_pk_bf16_f32 v127, v110, v111\n\t"
  "v_mfma_f32_32x32x16_bf16 v[96:111], a[36:39], a[72:75], v[64:79]\n\t"
  "ds_read_b128 a[36:39], v168 offset:28672\n\t"
  "v_exp_f32 v80, v80\n\t"
  "v_exp_f32 v81, v81\n\t"
  "v_exp_f32 v82, v82\n\t"
  "v_exp_f32 v83, v83\n\t"
  "v_mfma_f32_32x32x16_bf16 v[144:159], a[60:63], v[124:127], v[144:159]\n\t"
  "ds_read_b128 a[60:63], v168 offset:23552\n\t"
  "v_exp_f32 v84, v84\n\t"
  "v_exp_f32 v85, v85\n\t"
  "v_cvt_pk_bf16_f32 v112, v80, v81\n\t"
  "v_exp_f32 v86, v86\n\t"
  "v_cvt_pk_bf16_f32 v113, v82, v83\n\t"
  "v_mfma_f32_32x32x16_bf16 v[96:111], a[44:47], a[76:79], v[96:111]\n\t"
  "s_barrier\n\t"
  "ds_read_b128 a[44:47], v168 offset:29696\n\t"
  "s_add_u32 m0, s30, 0\n\t"
  "s_mov_b32 s29, 1572864\n\t"
  "v_exp_f32 v87, v87\n\t"
  "v_exp_f32 v88, v88\n\t"
  "v_cvt_pk_bf16_f32 v114, v84, v85\n\t"
  "v_exp_f32 v89, v89\n\t"
  "v_cvt_pk_bf16_f32 v115, v86, v87\n\t"
  "v_smfmac_f32_16x16x64_bf16 v[164:167], v[172:175], v[120:127], v171\n\t"
  "buffer_load_dwordx4 v225, s[24:27], s29 offen offset:0 lds\n\t"
  "v_exp_f32 v90, v90\n\t"
  "v_exp_f32 v91, v91\n\t"
  "v_cvt_pk_bf16_f32 v116, v88, v89\n\t"
  "v_exp_f32 v92, v92\n\t"
  "v_mfma_f32_32x32x16_bf16 v[144:159], a[48:51], v[112:115], v[144:159]\n\t"
  "s_waitcnt lgkmcnt(3)\n\t"
  "ds_read_b128 a[48:51], v168 offset:26624\n\t"
  "v_exp_f32 v93, v93\n\t"
  "v_exp_f32 v94, v94\n\t"
  "v_cvt_pk_bf16_f32 v117, v90, v91\n\t"
  "v_exp_f32 v95, v95\n\t"
  "v_cvt_pk_bf16_f32 v118, v92, v93\n\t"
  "v_cvt_pk_bf16_f32 v119, v94, v95\n\t"
  "v_mfma_f32_32x32x16_bf16 v[80:95], a[32:35], a[72:75], v[64:79]\n\t"
  "ds_read_b128 a[32:35], v168 offset:32768\n\t"
  "s_cmp_eq_u64 vcc, 0\n\t"
  "s_cselect_b32 s37, 0, 0x40000000\n\t"
  "v_exp_f32 v96, v96\n\t"
  "v_exp_f32 v97, v97\n\t"
  "v_exp_f32 v98, v98\n\t"
  "v_exp_f32 v99, v99\n\t"
  "v_mfma_f32_32x32x16_bf16 v[144:159], a[56:59], v[116:119], v[144:159]\n\t"
  "ds_read_b128 a[56:59], v168 offset:27648\n\t"
  "s_waitcnt vmcnt(1)\n\t"
  "buffer_store_dwordx2 v[240:241], v227, s[20:23], s37 offen offset:0 sc1\n\t"
  "v_exp_f32 v100, v100\n\t"
  "v_exp_f32 v101, v101\n\t"
  "v_cvt_pk_bf16_f32 v120, v96, v97\n\t"
  "v_exp_f32 v102, v102\n\t"
  "v_cvt_pk_bf16_f32 v121, v98, v99\n\t"
  "v_mfma_f32_32x32x16_bf16 v[80:95], a[40:43], a[76:79], v[80:95]\n\t"
  "ds_read_b128 a[40:43], v168 offset:33792\n\t"
  "v_exp_f32 v103, v103\n\t"
  "v_exp_f32 v104, v104\n\t"
  "v_cvt_pk_bf16_f32 v122, v100, v101\n\t"
  "v_exp_f32 v105, v105\n\t"
  "v_cvt_pk_bf16_f32 v123, v102, v103\n\t"
  "v_smfmac_f32_16x16x64_bf16 v[164:167], v[172:175], v[112:119], v171\n\t"
  "buffer_load_dwordx4 v229, s[24:27], s29 offen offset:1024 lds\n\t"
  "v_exp_f32 v106, v106\n\t"
  "v_exp_f32 v107, v107\n\t"
  "v_cvt_pk_bf16_f32 v124, v104, v105\n\t"
  "v_exp_f32 v108, v108\n\t"
  "v_mfma_f32_32x32x16_bf16 v[144:159], a[52:55], v[120:123], v[144:159]\n\t"
  "s_waitcnt lgkmcnt(3)\n\t"
  "ds_read_b128 a[52:55], v168 offset:30720\n\t"
  "v_exp_f32 v109, v109\n\t"
  "v_exp_f32 v110, v110\n\t"
  "v_cvt_pk_bf16_f32 v125, v106, v107\n\t"
  "v_exp_f32 v111, v111\n\t"
  "v_cvt_pk_bf16_f32 v126, v108, v109\n\t"
  "v_cvt_pk_bf16_f32 v127, v110, v111\n\t"
  "v_mfma_f32_32x32x16_bf16 v[96:111], a[36:39], a[72:75], v[64:79]\n\t"
  "ds_read_b128 a[36:39], v168 offset:36864\n\t"
  "v_exp_f32 v80, v80\n\t"
  "v_exp_f32 v81, v81\n\t"
  "v_exp_f32 v82, v82\n\t"
  "v_exp_f32 v83, v83\n\t"
  "v_mfma_f32_32x32x16_bf16 v[144:159], a[60:63], v[124:127], v[144:159]\n\t"
  "ds_read_b128 a[60:63], v168 offset:31744\n\t"
  "v_exp_f32 v84, v84\n\t"
  "v_exp_f32 v85, v85\n\t"
  "v_cvt_pk_bf16_f32 v112, v80, v81\n\t"
  "v_exp_f32 v86, v86\n\t"
  "v_cvt_pk_bf16_f32 v113, v82, v83\n\t"
  "v_mfma_f32_32x32x16_bf16 v[96:111], a[44:47], a[76:79], v[96:111]\n\t"
  "ds_read_b128 a[44:47], v168 offset:37888\n\t"
  "v_exp_f32 v87, v87\n\t"
  "v_exp_f32 v88, v88\n\t"
  "v_cvt_pk_bf16_f32 v114, v84, v85\n\t"
  "v_exp_f32 v89, v89\n\t"
  "v_cvt_pk_bf16_f32 v115, v86, v87\n\t"
  "v_smfmac_f32_16x16x64_bf16 v[164:167], v[172:175], v[120:127], v171\n\t"
  "buffer_load_dwordx4 v230, s[24:27], s29 offen offset:2048 lds\n\t"
  "v_exp_f32 v90, v90\n\t"
  "v_exp_f32 v91, v91\n\t"
  "v_cvt_pk_bf16_f32 v116, v88, v89\n\t"
  "v_exp_f32 v92, v92\n\t"
  "v_mfma_f32_32x32x16_bf16 v[144:159], a[48:51], v[112:115], v[144:159]\n\t"
  "s_waitcnt lgkmcnt(3)\n\t"
  "ds_read_b128 a[48:51], v168 offset:34816\n\t"
  "v_exp_f32 v93, v93\n\t"
  "v_exp_f32 v94, v94\n\t"
  "v_cvt_pk_bf16_f32 v117, v90, v91\n\t"
  "v_exp_f32 v95, v95\n\t"
  "v_cvt_pk_bf16_f32 v118, v92, v93\n\t"
  "v_cvt_pk_bf16_f32 v119, v94, v95\n\t"
  "v_mfma_f32_32x32x16_bf16 v[80:95], a[32:35], a[72:75], v[64:79]\n\t"
  "ds_read_b128 a[32:35], v168 offset:40960\n\t"
  "v_exp_f32 v96, v96\n\t"
  "v_exp_f32 v97, v97\n\t"
  "v_exp_f32 v98, v98\n\t"
  "v_exp_f32 v99, v99\n\t"
  "v_mfma_f32_32x32x16_bf16 v[144:159], a[56:59], v[116:119], v[144:159]\n\t"
  "ds_read_b128 a[56:59], v168 offset:35840\n\t"
  "v_exp_f32 v100, v100\n\t"
  "v_exp_f32 v101, v101\n\t"
  "v_cvt_pk_bf16_f32 v120, v96, v97\n\t"
  "v_exp_f32 v102, v102\n\t"
  "v_cvt_pk_bf16_f32 v121, v98, v99\n\t"
  "v_mfma_f32_32x32x16_bf16 v[80:95], a[40:43], a[76:79], v[80:95]\n\t"
  "ds_read_b128 a[40:43], v168 offset:41984\n\t"
  "v_exp_f32 v103, v103\n\t"
  "v_exp_f32 v104, v104\n\t"
  "v_cvt_pk_bf16_f32 v122, v100, v101\n\t"
  "v_exp_f32 v105, v105\n\t"
  "v_cvt_pk_bf16_f32 v123, v102, v103\n\t"
  "v_smfmac_f32_16x16x64_bf16 v[164:167], v[172:175], v[112:119], v171\n\t"
  "buffer_load_dwordx4 v231, s[24:27], s29 offen offset:3072 lds\n\t"
  "v_exp_f32 v106, v106\n\t"
  "v_exp_f32 v107, v107\n\t"
  "v_cvt_pk_bf16_f32 v124, v104, v105\n\t"
  "v_exp_f32 v108, v108\n\t"
  "v_mfma_f32_32x32x16_bf16 v[144:159], a[52:55], v[120:123], v[144:159]\n\t"
  "s_waitcnt lgkmcnt(3)\n\t"
  "ds_read_b128 a[52:55], v168 offset:38912\n\t"
  "v_exp_f32 v109, v109\n\t"
  "v_exp_f32 v110, v110\n\t"
  "v_cvt_pk_bf16_f32 v125, v106, v107\n\t"
  "v_exp_f32 v111, v111\n\t"
  "v_cvt_pk_bf16_f32 v126, v108, v109\n\t"
  "v_cvt_pk_bf16_f32 v127, v110, v111\n\t"
  "v_mfma_f32_32x32x16_bf16 v[96:111], a[36:39], a[72:75], v[64:79]\n\t"
  "ds_read_b128 a[36:39], v168 offset:45056\n\t"
  "v_exp_f32 v80, v80\n\t"
  "v_exp_f32 v81, v81\n\t"
  "v_exp_f32 v82, v82\n\t"
  "v_exp_f32 v83, v83\n\t"
  "v_mfma_f32_32x32x16_bf16 v[144:159], a[60:63], v[124:127], v[144:159]\n\t"
  "ds_read_b128 a[60:63], v168 offset:39936\n\t"
  "v_exp_f32 v84, v84\n\t"
  "v_exp_f32 v85, v85\n\t"
  "v_cvt_pk_bf16_f32 v112, v80, v81\n\t"
  "v_exp_f32 v86, v86\n\t"
  "v_cvt_pk_bf16_f32 v113, v82, v83\n\t"
  "v_mfma_f32_32x32x16_bf16 v[96:111], a[44:47], a[76:79], v[96:111]\n\t"
  "s_barrier\n\t"
  "ds_read_b128 a[44:47], v168 offset:46080\n\t"
  "s_add_u32 m0, s30, 16384\n\t"
  "s_mov_b32 s29, 1835008\n\t"
  "v_exp_f32 v87, v87\n\t"
  "v_exp_f32 v88, v88\n\t"
  "v_cvt_pk_bf16_f32 v114, v84, v85\n\t"
  "v_exp_f32 v89, v89\n\t"
  "v_cvt_pk_bf16_f32 v115, v86, v87\n\t"
  "v_smfmac_f32_16x16x64_bf16 v[164:167], v[172:175], v[120:127], v171\n\t"
  "buffer_load_dwordx4 v225, s[24:27], s29 offen offset:0 lds\n\t"
  "v_exp_f32 v90, v90\n\t"
  "v_exp_f32 v91, v91\n\t"
  "v_cvt_pk_bf16_f32 v116, v88, v89\n\t"
  "v_exp_f32 v92, v92\n\t"
  "v_mfma_f32_32x32x16_bf16 v[144:159], a[48:51], v[112:115], v[144:159]\n\t"
  "s_waitcnt lgkmcnt(3)\n\t"
  "ds_read_b128 a[48:51], v168 offset:43008\n\t"
  "v_exp_f32 v93, v93\n\t"
  "v_exp_f32 v94, v94\n\t"
  "v_cvt_pk_bf16_f32 v117, v90, v91\n\t"
  "v_exp_f32 v95, v95\n\t"
  "v_cvt_pk_bf16_f32 v118, v92, v93\n\t"
  "v_cvt_pk_bf16_f32 v119, v94, v95\n\t"
  "v_mfma_f32_32x32x16_bf16 v[80:95], a[32:35], a[72:75], v[64:79]\n\t"
  "ds_read_b128 a[32:35], v168 offset:49152\n\t"
  "v_exp_f32 v96, v96\n\t"
  "v_exp_f32 v97, v97\n\t"
  "v_exp_f32 v98, v98\n\t"
  "v_exp_f32 v99, v99\n\t"
  "v_mfma_f32_32x32x16_bf16 v[144:159], a[56:59], v[116:119], v[144:159]\n\t"
  "ds_read_b128 a[56:59], v168 offset:44032\n\t"
  "buffer_store_dwordx2 v[240:241], v220, s[20:23], 0 offen offset:8 sc1\n\t"
  "v_exp_f32 v100, v100\n\t"
  "v_exp_f32 v101, v101\n\t"
  "v_cvt_pk_bf16_f32 v120, v96, v97\n\t"
  "v_exp_f32 v102, v102\n\t"
  "v_cvt_pk_bf16_f32 v121, v98, v99\n\t"
  "v_mfma_f32_32x32x16_bf16 v[80:95], a[40:43], a[76:79], v[80:95]\n\t"
  "ds_read_b128 a[40:43], v168 offset:50176\n\t"
  "v_exp_f32 v103, v103\n\t"
  "v_exp_f32 v104, v104\n\t"
  "v_cvt_pk_bf16_f32 v122, v100, v101\n\t"
  "v_exp_f32 v105, v105\n\t"
  "v_cvt_pk_bf16_f32 v123, v102, v103\n\t"
  "v_smfmac_f32_16x16x64_bf16 v[164:167], v[172:175], v[112:119], v171\n\t"
  "buffer_load_dwordx4 v229, s[24:27], s29 offen offset:1024 lds\n\t"
  "v_exp_f32 v106, v106\n\t"
  "v_exp_f32 v107, v107\n\t"
  "v_cvt_pk_bf16_f32 v124, v104, v105\n\t"
  "v_exp_f32 v108, v108\n\t"
  "v_mfma_f32_32x32x16_bf16 v[144:159], a[52:55], v[120:123], v[144:159]\n\t"
  "s_waitcnt lgkmcnt(3)\n\t"
  "ds_read_b128 a[52:55], v168 offset:47104\n\t"
  "v_exp_f32 v109, v109\n\t"
  "v_exp_f32 v110, v110\n\t"
  "v_cvt_pk_bf16_f32 v125, v106, v107\n\t"
  "v_exp_f32 v111, v111\n\t"
  "v_cvt_pk_bf16_f32 v126, v108, v109\n\t"
  "v_cvt_pk_bf16_f32 v127, v110, v111\n\t"
  "v_mfma_f32_32x32x16_bf16 v[96:111], a[36:39], a[72:75], v[64:79]\n\t"
  "ds_read_b128 a[36:39], v168 offset:53248\n\t"
  "v_exp_f32 v80, v80\n\t"
  "v_exp_f32 v81, v81\n\t"
  "v_exp_f32 v82, v82\n\t"
  "v_exp_f32 v83, v83\n\t"
  "v_mfma_f32_32x32x16_bf16 v[144:159], a[60:63], v[124:127], v[144:159]\n\t"
  "ds_read_b128 a[60:63], v168 offset:48128\n\t"
  "v_exp_f32 v84, v84\n\t"
  "v_exp_f32 v85, v85\n\t"
  "v_cvt_pk_bf16_f32 v112, v80, v81\n\t"
  "v_exp_f32 v86, v86\n\t"
  "v_cvt_pk_bf16_f32 v113, v82, v83\n\t"
  "v_mfma_f32_32x32x16_bf16 v[96:111], a[44:47], a[76:79], v[96:111]\n\t"
  "ds_read_b128 a[44:47], v168 offset:54272\n\t"
  "v_exp_f32 v87, v87\n\t"
  "v_exp_f32 v88, v88\n\t"
  "v_cvt_pk_bf16_f32 v114, v84, v85\n\t"
  "v_exp_f32 v89, v89\n\t"
  "v_cvt_pk_bf16_f32 v115, v86, v87\n\t"
  "v_smfmac_f32_16x16x64_bf16 v[164:167], v[172:175], v[120:127], v171\n\t"
  "buffer_load_dwordx4 v230, s[24:27], s29 offen offset:2048 lds\n\t"
  "v_exp_f32 v90, v90\n\t"
  "v_exp_f32 v91, v91\n\t"
  "v_cvt_pk_bf16_f32 v116, v88, v89\n\t"
  "v_exp_f32 v92, v92\n\t"
  "v_mfma_f32_32x32x16_bf16 v[144:159], a[48:51], v[112:115], v[144:159]\n\t"
  "s_waitcnt lgkmcnt(3)\n\t"
  "ds_read_b128 a[48:51], v168 offset:51200\n\t"
  "v_exp_f32 v93, v93\n\t"
  "v_exp_f32 v94, v94\n\t"
  "v_cvt_pk_bf16_f32 v117, v90, v91\n\t"
  "v_exp_f32 v95, v95\n\t"
  "v_cvt_pk_bf16_f32 v118, v92, v93\n\t"
  "v_cvt_pk_bf16_f32 v119, v94, v95\n\t"
  "v_mfma_f32_32x32x16_bf16 v[80:95], a[32:35], a[72:75], v[64:79]\n\t"
  "ds_read_b128 a[32:35], v168 offset:57344\n\t"
  "v_exp_f32 v96, v96\n\t"
  "v_exp_f32 v97, v97\n\t"
  "v_exp_f32 v98, v98\n\t"
  "v_exp_f32 v99, v99\n\t"
  "v_mfma_f32_32x32x16_bf16 v[144:159], a[56:59], v[116:119], v[144:159]\n\t"
  "ds_read_b128 a[56:59], v168 offset:52224\n\t"
  "v_exp_f32 v100, v100\n\t"
  "v_exp_f32 v101, v101\n\t"
  "v_cvt_pk_bf16_f32 v120, v96, v97\n\t"
  "v_exp_f32 v102, v102\n\t"
  "v_cvt_pk_bf16_f32 v121, v98, v99\n\t"
  "v_mfma_f32_32x32x16_bf16 v[80:95], a[40:43], a[76:79], v[80:95]\n\t"
  "ds_read_b128 a[40:43], v168 offset:58368\n\t"
  "v_exp_f32 v103, v103\n\t"
  "v_exp_f32 v104, v104\n\t"
  "v_cvt_pk_bf16_f32 v122, v100, v101\n\t"
  "v_exp_f32 v105, v105\n\t"
  "v_cvt_pk_bf16_f32 v123, v102, v103\n\t"
  "v_smfmac_f32_16x16x64_bf16 v[164:167], v[172:175], v[112:119], v171\n\t"
  "buffer_load_dwordx4 v231, s[24:27], s29 offen offset:3072 lds\n\t"
  "v_exp_f32 v106, v106\n\t"
  "v_exp_f32 v107, v107\n\t"
  "v_cvt_pk_bf16_f32 v124, v104, v105\n\t"
  "v_exp_f32 v108, v108\n\t"
  "v_mfma_f32_32x32x16_bf16 v[144:159], a[52:55], v[120:123], v[144:159]\n\t"
  "s_waitcnt lgkmcnt(3)\n\t"
  "ds_read_b128 a[52:55], v168 offset:55296\n\t"
  "v_exp_f32 v109, v109\n\t"
  "v_exp_f32 v110, v110\n\t"
  "v_cvt_pk_bf16_f32 v125, v106, v107\n\t"
  "v_exp_f32 v111, v111\n\t"
  "v_cvt_pk_bf16_f32 v126, v108, v109\n\t"
  "v_cvt_pk_bf16_f32 v127, v110, v111\n\t"
  "v_mfma_f32_32x32x16_bf16 v[96:111], a[36:39], a[72:75], v[64:79]\n\t"
  "ds_read_b128 a[36:39], v168 offset:61440\n\t"
  "v_exp_f32 v80, v80\n\t"
  "v_exp_f32 v81, v81\n\t"
  "v_exp_f32 v82, v82\n\t"
  "v_exp_f32 v83, v83\n\t"
  "v_mfma_f32_32x32x16_bf16 v[144:159], a[60:63], v[124:127], v[144:159]\n\t"
  "ds_read_b128 a[60:63], v168 offset:56320\n\t"
  "v_exp_f32 v84, v84\n\t"
  "v_exp_f32 v85, v85\n\t"
  "v_cvt_pk_bf16_f32 v112, v80, v81\n\t"
  "v_exp_f32 v86, v86\n\t"
  "v_cvt_pk_bf16_f32 v113, v82, v83\n\t"
  "v_mfma_f32_32x32x16_bf16 v[96:111], a[44:47], a[76:79], v[96:111]\n\t"
  "s_barrier\n\t"
  "ds_read_b128 a[44:47], v168 offset:62464\n\t"
  "s_add_u32 m0, s55, 32768\n\t"
  "v_exp_f32 v87, v87\n\t"
  "v_exp_f32 v88, v88\n\t"
  "v_cvt_pk_bf16_f32 v114, v84, v85\n\t"
  "v_exp_f32 v89, v89\n\t"
  "v_cvt_pk_bf16_f32 v115, v86, v87\n\t"
  "v_smfmac_f32_16x16x64_bf16 v[164:167], v[172:175], v[120:127], v171\n\t"
  "buffer_load_dwordx4 v226, s[20:23], 0 offen sc1 lds\n\t"
  "v_exp_f32 v90, v90\n\t"
  "v_exp_f32 v91, v91\n\t"
  "v_cvt_pk_bf16_f32 v116, v88, v89\n\t"
  "v_exp_f32 v92, v92\n\t"
  "v_mfma_f32_32x32x16_bf16 v[144:159], a[48:51], v[112:115], v[144:159]\n\t"
  "s_waitcnt lgkmcnt(3)\n\t"
  "ds_read_b128 a[48:51], v168 offset:59392\n\t"
  "v_exp_f32 v93, v93\n\t"
  "v_exp_f32 v94, v94\n\t"
  "v_cvt_pk_bf16_f32 v117, v90, v91\n\t"
  "v_exp_f32 v95, v95\n\t"
  "v_cvt_pk_bf16_f32 v118, v92, v93\n\t"
  "v_cvt_pk_bf16_f32 v119, v94, v95\n\t"
  "v_mfma_f32_32x32x16_bf16 v[80:95], a[32:35], a[72:75], v[64:79]\n\t"
  "v_exp_f32 v96, v96\n\t"
  "v_exp_f32 v97, v97\n\t"
  "v_exp_f32 v98, v98\n\t"
  "v_exp_f32 v99, v99\n\t"
  "v_mfma_f32_32x32x16_bf16 v[144:159], a[56:59], v[116:119], v[144:159]\n\t"
  "ds_read_b128 a[56:59], v168 offset:60416\n\t"
  "v_not_b32 v242, s34\n\t"
  "buffer_load_dwordx2 v[242:243], v228, s[20:23], 0 offen offset:0 sc1\n\t"
  "v_exp_f32 v100, v100\n\t"
  "v_exp_f32 v101, v101\n\t"
  "v_cvt_pk_bf16_f32 v120, v96, v97\n\t"
  "v_exp_f32 v102, v102\n\t"
  "v_cvt_pk_bf16_f32 v121, v98, v99\n\t"
  "v_mfma_f32_32x32x16_bf16 v[80:95], a[40:43], a[76:79], v[80:95]\n\t"
  "v_exp_f32 v103, v103\n\t"
  "v_exp_f32 v104, v104\n\t"
  "v_cvt_pk_bf16_f32 v122, v100, v101\n\t"
  "v_exp_f32 v105, v105\n\t"
  "v_cvt_pk_bf16_f32 v123, v102, v103\n\t"
  "v_smfmac_f32_16x16x64_bf16 v[164:167], v[172:175], v[112:119], v171\n\t"
  "v_exp_f32 v106, v106\n\t"
  "v_exp_f32 v107, v107\n\t"
  "v_cvt_pk_bf16_f32 v124, v104, v105\n\t"
  "v_exp_f32 v108, v108\n\t"
  "v_mfma_f32_32x32x16_bf16 v[144:159], a[52:55], v[120:123], v[144:159]\n\t"
  "s_waitcnt lgkmcnt(1)\n\t"
  "ds_read_b128 a[52:55], v168 offset:63488\n\t"
  "v_exp_f32 v109, v109\n\t"
  "v_exp_f32 v110, v110\n\t"
  "v_cvt_pk_bf16_f32 v125, v106, v107\n\t"
  "v_exp_f32 v111, v111\n\t"
  "v_cvt_pk_bf16_f32 v126, v108, v109\n\t"
  "v_cvt_pk_bf16_f32 v127, v110, v111\n\t"
  "v_mfma_f32_32x32x16_bf16 v[96:111], a[36:39], a[72:75], v[64:79]\n\t"
  "v_exp_f32 v80, v80\n\t"
  "v_exp_f32 v81, v81\n\t"
  "v_exp_f32 v82, v82\n\t"
  "v_exp_f32 v83, v83\n\t"
  "v_mfma_f32_32x32x16_bf16 v[144:159], a[60:63], v[124:127], v[144:159]\n\t"
  "ds_read_b128 a[60:63], v168 offset:64512\n\t"
  "v_exp_f32 v84, v84\n\t"
  "v_exp_f32 v85, v85\n\t"
  "v_cvt_pk_bf16_f32 v112, v80, v81\n\t"
  "v_exp_f32 v86, v86\n\t"
  "v_cvt_pk_bf16_f32 v113, v82, v83\n\t"
  "v_mfma_f32_32x32x16_bf16 v[96:111], a[44:47], a[76:79], v[96:111]\n\t"
  "v_exp_f32 v87, v87\n\t"
  "v_exp_f32 v88, v88\n\t"
  "v_cvt_pk_bf16_f32 v114, v84, v85\n\t"
  "v_exp_f32 v89, v89\n\t"
  "v_cvt_pk_bf16_f32 v115, v86, v87\n\t"
  "v_smfmac_f32_16x16x64_bf16 v[164:167], v[172:175], v[120:127], v171\n\t"
  "v_exp_f32 v90, v90\n\t"
  "v_exp_f32 v91, v91\n\t"
  "v_cvt_pk_bf16_f32 v116, v88, v89\n\t"
  "v_exp_f32 v92, v92\n\t"
  "v_mfma_f32_32x32x16_bf16 v[144:159], a[48:51], v[112:115], v[144:159]\n\t"
  "s_waitcnt lgkmcnt(1)\n\t"
  "v_exp_f32 v93, v93\n\t"
  "v_exp_f32 v94, v94\n\t"
  "v_cvt_pk_bf16_f32 v117, v90, v91\n\t"
  "v_exp_f32 v95, v95\n\t"
  "v_cvt_pk_bf16_f32 v118, v92, v93\n\t"
  "v_cvt_pk_bf16_f32 v119, v94, v95\n\t"
  "v_exp_f32 v96, v96\n\t"
  "v_exp_f32 v97, v97\n\t"
  "v_exp_f32 v98, v98\n\t"
  "v_exp_f32 v99, v99\n\t"
  "v_mfma_f32_32x32x16_bf16 v[144:159], a[56:59], v[116:119], v[144:159]\n\t"
  "v_exp_f32 v100, v100\n\t"
  "v_exp_f32 v101, v101\n\t"
  "v_cvt_pk_bf16_f32 v120, v96, v97\n\t"
  "v_exp_f32 v102, v102\n\t"
  "v_cvt_pk_bf16_f32 v121, v98, v99\n\t"
  "v_exp_f32 v103, v103\n\t"
  "v_exp_f32 v104, v104\n\t"
  "v_cvt_pk_bf16_f32 v122, v100, v101\n\t"
  "v_exp_f32 v105, v105\n\t"
  "v_cvt_pk_bf16_f32 v123, v102, v103\n\t"
  "v_smfmac_f32_16x16x64_bf16 v[164:167], v[172:175], v[112:119], v171\n\t"
  "v_exp_f32 v106, v106\n\t"
  "v_exp_f32 v107, v107\n\t"
  "v_cvt_pk_bf16_f32 v124, v104, v105\n\t"
  "v_exp_f32 v108, v108\n\t"
  "v_mfma_f32_32x32x16_bf16 v[144:159], a[52:55], v[120:123], v[144:159]\n\t"
  "s_waitcnt lgkmcnt(0)\n\t"
  "v_exp_f32 v109, v109\n\t"
  "v_exp_f32 v110, v110\n\t"
  "v_cvt_pk_bf16_f32 v125, v106, v107\n\t"
  "v_exp_f32 v111, v111\n\t"
  "v_cvt_pk_bf16_f32 v126, v108, v109\n\t"
  "v_cvt_pk_bf16_f32 v127, v110, v111\n\t"
  "s_nop 1\n\t"
  "v_mfma_f32_32x32x16_bf16 v[144:159], a[60:63], v[124:127], v[144:159]\n\t"
  "v_smfmac_f32_16x16x64_bf16 v[164:167], v[172:175], v[120:127], v171\n\t"
  "s_cmp_lg_u32 s37, 0\n\t"
  "s_cbranch_scc1 LATTn_%=\n\t"
  "s_mov_b32 s72, 0x20000\n\t"
  "s_not_b32 s68, s34\n\t"
  "s_mov_b32 s73, 64\n\t"
  "s_branch LPXc_%=\n\t"
  "LPXa_%=:\n\t"
  "v_not_b32 v242, s34\n\t"
  "buffer_load_dwordx2 v[242:243], v228, s[20:23], 0 offen offset:0 sc1\n\t"
  "s_mov_b32 s73, 64\n\t"
  "LPXw_%=:\n\t"
  "s_sleep 1\n\t"
  "LPXc_%=:\n\t"
  "v_cmp_eq_u32 vcc, s68, v242\n\t"
  "s_nop 1\n\t"
  "s_cmp_eq_u64 vcc, 0\n\t"
  "v_cmp_ne_u64 vcc, s[34:35], v[242:243]\n\t"
  "s_cbranch_vccz LPXb_%=\n\t"
  "s_cbranch_scc1 LPXr_%=\n\t"
  "s_sub_u32 s73, s73, 1\n\t"
  "s_cbranch_scc0 LPXw_%=\n\t"
  "s_waitcnt vmcnt(0)\n\t"
  "LPXr_%=:\n\t"
  "s_sleep 2\n\t"
  "s_sub_u32 s72, s72, 1\n\t"
  "s_cbranch_scc0 LPXa_%=\n\t"
  "LPXb_%=:\n\t"
  "buffer_load_dwordx4 a[192:195], v246, s[20:23], s38 offen offset:0 sc1\n\t"
  "buffer_load_dwordx4 a[196:199], v247, s[20:23], s38 offen offset:0 sc1\n\t"
  "s_add_u32 s38, s38, 0x40000\n\t"
  "buffer_load_dwordx4 a[200:203], v246, s[20:23], s38 offen offset:0 sc1\n\t"
  "buffer_load_dwordx4 a[204:207], v247, s[20:23], s38 offen offset:0 sc1\n\t"
  "s_add_u32 s38, s38, 0x40000\n\t"
  "buffer_load_dwordx4 a[208:211], v246, s[20:23], s38 offen offset:0 sc1\n\t"
  "buffer_load_dwordx4 a[212:215], v247, s[20:23], s38 offen offset:0 sc1\n\t"
  "s_add_u32 s38, s38, 0x40000\n\t"
  "buffer_load_dwordx4 a[216:219], v246, s[20:23], s38 offen offset:0 sc1\n\t"
  "buffer_load_dwordx4 a[220:223], v247, s[20:23], s38 offen offset:0 sc1\n\t"
  "s_add_u32 s38, s38, 0x40000\n\t"
  "buffer_load_dwordx4 a[224:227], v246, s[20:23], s38 offen offset:0 sc1\n\t"
  "buffer_load_dwordx4 a[228:231], v247, s[20:23], s38 offen offset:0 sc1\n\t"
  "s_add_u32 s38, s38, 0x40000\n\t"
  "buffer_load_dwordx4 a[232:235], v246, s[20:23], s38 offen offset:0 sc1\n\t"
  "buffer_load_dwordx4 a[236:239], v247, s[20:23], s38 offen offset:0 sc1\n\t"
  "s_add_u32 s38, s38, 0x40000\n\t"
  "LATTn_%=:\n\t"
  "s_nop 11\n\t"
  "v_add_f32 v164, v164, v165\n\t"
  "v_rcp_f32 v166, v164\n\t"
  "s_nop 0\n\t"
  "v_pk_mul_f32 v[64:65], v[144:145], v[166:167] op_sel_hi:[1,0]\n\t"
  "v_pk_mul_f32 v[66:67], v[146:147], v[166:167] op_sel_hi:[1,0]\n\t"
  "v_pk_mul_f32 v[68:69], v[148:149], v[166:167] op_sel_hi:[1,0]\n\t"
  "v_pk_mul_f32 v[70:71], v[150:151], v[166:167] op_sel_hi:[1,0]\n\t"
  "v_pk_mul_f32 v[72:73], v[152:153], v[166:167] op_sel_hi:[1,0]\n\t"
  "v_pk_mul_f32 v[74:75], v[154:155], v[166:167] op_sel_hi:[1,0]\n\t"
  "v_pk_mul_f32 v[76:77], v[156:157], v[166:167] op_sel_hi:[1,0]\n\t"
  "v_pk_mul_f32 v[78:79], v[158:159], v[166:167] op_sel_hi:[1,0]\n\t"
  "v_cvt_pk_bf16_f32 v64, v64, v65\n\t"
  "v_cvt_pk_bf16_f32 v65, v66, v67\n\t"
  "v_cvt_pk_bf16_f32 v66, v68, v69\n\t"
  "v_cvt_pk_bf16_f32 v67, v70, v71\n\t"
  "v_cvt_pk_bf16_f32 v72, v72, v73\n\t"
  "v_cvt_pk_bf16_f32 v73, v74, v75\n\t"
  "v_cvt_pk_bf16_f32 v74, v76, v77\n\t"
  "v_cvt_pk_bf16_f32 v75, v78, v79\n\t"
  "buffer_store_dwordx4 v[64:67], v219, s[20:23], 0 offen offset:2048 sc1\n\t"
  "buffer_store_dwordx4 v[72:75], v219, s[20:23], 0 offen offset:2560 sc1\n\t"
  "v_pk_add_f32 v[144:145], v[144:145], v[152:153]\n\t"
  "v_pk_add_f32 v[146:147], v[146:147], v[154:155]\n\t"
  "v_pk_add_f32 v[148:149], v[148:149], v[156:157]\n\t"
  "v_pk_add_f32 v[150:151], v[150:151], v[158:159]\n\t"
  "v_pk_add_f32 v[144:145], v[144:145], v[148:149]\n\t"
  "v_pk_add_f32 v[146:147], v[146:147], v[150:151]\n\t"
  "v_pk_add_f32 v[144:145], v[144:145], v[146:147]\n\t"
  "v_add_f32 v164, v164, |v144|\n\t"
  "v_add_f32 v164, v164, |v145|\n\t"
  "v_cmp_nlt_f32 vcc, v164, v245\n\t"
  "s_lshr_b32 s31, s37, 30\n\t"
  "s_cmp_eq_u64 vcc, 0\n\t"
  "s_cselect_b32 s36, 0, 2\n\t"
  "s_or_b32 s36, s36, s31\n\t"
     : "={s36}"(bad), "={s37}"(late), "+{s38}"(so8), "={v[32:47]}"(ia), "={v[200:215]}"(ib), "={v[216:217]}"(xm), "={v[196:199]}"(b1), "={v[252:255]}"(b2),
       "+{v245}"(pv), "+{v[176:191]}"(rv), "+{v[224:239]}"(wv), "+{s[24:27]}"(sw), "+{s[40:47]}"(sp), "+{s[48:55]}"(sq)
     : "{s[20:23]}"(sws), "{s30}"(wid * 4096), "{v[168:175]}"(tv), "{v[192:195]}"(rw), "{v218}"(wm8), "{v219}"(ov), "{v250}"(bv), "{v29}"(kv), "{v30}"(gp), "{v[16:23]}"(fw), "{v251}"(co), "{s[74:75]}"(ca), "{v[246:247]}"(wx), "{v[220:221]}"(wb2), "{v244}"(qv), "{s[34:35]}"(tag)
     : "memory", "scc", "vcc", "s28", "s29", "s31", "s39", "s68", "s72", "s73",
       "v240","v241","v242","v243","v48","v49","v50","v51","v52","v53","v54","v55","v56","v57","v58","v59","v60","v61","v62","v63","v64","v65","v66","v67","v68","v69","v70","v71","v72","v73","v74","v75","v76","v77","v78","v79","v80","v81","v82","v83","v84","v85","v86","v87","v88","v89","v90","v91","v92","v93","v94","v95","v96","v97","v98","v99","v100","v101","v102","v103","v104","v105","v106","v107","v108","v109","v110","v111","v112","v113","v114","v115","v116","v117","v118","v119","v120","v121","v122","v123","v124","v125","v126","v127","v128","v129","v130","v131","v132","v133","v134","v135","v136","v137","v138","v139","v140","v141","v142","v143","v144","v145","v146","v147","v148","v149","v150","v151","v152","v153","v154","v155","v156","v157","v158","v159","v160","v161","v162","v163","v164","v165","v166","v167","a0","a1","a2","a3","a4","a5","a6","a7","a8","a9","a10","a11","a12","a13","a14","a15","a16","a17","a18","a19","a32","a33","a34","a35","a36","a37","a38","a39","a40","a41","a42","a43","a44","a45","a46","a47","a48","a49","a50","a51","a52","a53","a54","a55","a56","a57","a58","a59","a60","a61","a62","a63","a64","a65","a66","a67","a68","a69","a70","a71","a72","a73","a74","a75","a76","a77","a78","a79","a192","a193","a194","a195","a196","a197","a198","a199","a200","a201","a202","a203","a204","a205","a206","a207","a208","a209","a210","a211","a212","a213","a214","a215","a216","a217","a218","a219","a220","a221","a222","a223","a224","a225","a226","a227","a228","a229","a230","a231","a232","a233","a234","a235","a236","a237","a238","a239","a240","a241","a242","a243","a244","a245","a246","a247","a248","a249","a250","a251","a252","a253","a254","a255");
    if (__builtin_expect(bad != 0, 0)) {
      asm volatile("s_waitcnt vmcnt(0)" ::: "memory"); late = 1; so8 = kh * 131072;
      __builtin_amdgcn_fence(__ATOMIC_ACQUIRE, "agent");
#pragma unroll 1
      for (int e = 0; e < 2; e++) if ((bad >> e) & 1) {
        f32x16 o; float l;
        att_slow(ws + WS_Q + (size_t)(head * 64 + qblk + e) * 2048, ws + WS_KV + (size_t)head * 262144, lane_id(), o, l);
        const float rl = 1.f / l;
        asm volatile("s_waitcnt vmcnt(0)" ::: "memory");
#pragma unroll
        for (int a = 0; a < 2; a++) {
          u32x4 t;
#pragma unroll
          for (int i = 0; i < 4; i++) t[i] = cvtpk(o[8 * a + 2 * i] * rl, o[8 * a + 2 * i + 1] * rl);
          st16_dev(ws + ov + e * 2048 + a * 512, t);
        }
        if (e == 0) asm volatile("s_waitcnt vmcnt(0)\n\tbuffer_store_dwordx2 %0, %1, %2, 0 offen sc1" :: "v"(tag), "v"(wv[3]), "s"(sws) : "memory");
      }
    }
  }
  {
    asm volatile(
  "kfd_begin:\n\t"
  "s_cmp_eq_u32 s37, 0\n\t"
  "s_cbranch_scc1 LH_%=\n\t"
  "s_mov_b32 s72, 0x20000\n\t"
  "s_not_b32 s68, s34\n\t"
  "s_mov_b32 s73, 64\n\t"
  "LP0a_%=:\n\t"
  "v_not_b32 v24, s34\n\t"
  "buffer_load_dwordx2 v[24:25], v228, s[20:23], 0 offen offset:0 sc1\n\t"
  "s_mov_b32 s73, 64\n\t"
  "LP0w_%=:\n\t"
  "s_sleep 1\n\t"
  "LP0c_%=:\n\t"
  "v_cmp_eq_u32 vcc, s68, v24\n\t"
  "s_nop 1\n\t"
  "s_cmp_eq_u64 vcc, 0\n\t"
  "v_cmp_ne_u64 vcc, s[34:35], v[24:25]\n\t"
  "s_cbranch_vccz LP0b_%=\n\t"
  "s_cbranch_scc1 LP0r_%=\n\t"
  "s_sub_u32 s73, s73, 1\n\t"
  "s_cbranch_scc0 LP0w_%=\n\t"
  "s_waitcnt vmcnt(0)\n\t"
  "LP0r_%=:\n\t"
  "s_sleep 2\n\t"
  "s_sub_u32 s72, s72, 1\n\t"
  "s_cbranch_scc0 LP0a_%=\n\t"
  "LP0b_%=:\n\t"
  "buffer_load_dwordx4 a[192:195], v246, s[20:23], s38 offen offset:0 sc1\n\t"
  "buffer_load_dwordx4 a[196:199], v247, s[20:23], s38 offen offset:0 sc1\n\t"
  "s_add_u32 s38, s38, 0x40000\n\t"
  "buffer_load_dwordx4 a[200:203], v246, s[20:23], s38 offen offset:0 sc1\n\t"
  "buffer_load_dwordx4 a[204:207], v247, s[20:23], s38 offen offset:0 sc1\n\t"
  "s_add_u32 s38, s38, 0x40000\n\t"
  "buffer_load_dwordx4 a[208:211], v246, s[20:23], s38 offen offset:0 sc1\n\t"
  "buffer_load_dwordx4 a[212:215], v247, s[20:23], s38 offen offset:0 sc1\n\t"
  "s_add_u32 s38, s38, 0x40000\n\t"
  "buffer_load_dwordx4 a[216:219], v246, s[20:23], s38 offen offset:0 sc1\n\t"
  "buffer_load_dwordx4 a[220:223], v247, s[20:23], s38 offen offset:0 sc1\n\t"
  "s_add_u32 s38, s38, 0x40000\n\t"
  "buffer_load_dwordx4 a[224:227], v246, s[20:23], s38 offen offset:0 sc1\n\t"
  "buffer_load_dwordx4 a[228:231], v247, s[20:23], s38 offen offset:0 sc1\n\t"
  "s_add_u32 s38, s38, 0x40000\n\t"
  "buffer_load_dwordx4 a[232:235], v246, s[20:23], s38 offen offset:0 sc1\n\t"
  "buffer_load_dwordx4 a[236:239], v247, s[20:23], s38 offen offset:0 sc1\n\t"
  "s_add_u32 s38, s38, 0x40000\n\t"
  "s_waitcnt vmcnt(12)\n\t"
  "buffer_store_dwordx2 v[16:17], v227, s[20:23], 0 offen offset:8 sc1\n\t"
  "buffer_store_dwordx2 v[16:17], v227, s[20:23], 0 offen offset:8 sc1\n\t"
  "LH_%=:\n\t"
  "s_waitcnt vmcnt(14)\n\t"
  "s_barrier\n\t"
  "ds_read_b64_tr_b16 a[128:129], v196 offset:0\n\t"
  "ds_read_b64_tr_b16 a[130:131], v196 offset:2048\n\t"
  "ds_read_b64_tr_b16 a[132:133], v197 offset:0\n\t"
  "ds_read_b64_tr_b16 a[134:135], v197 offset:2048\n\t"
  "ds_read_b64_tr_b16 a[136:137], v198 offset:0\n\t"
  "ds_read_b64_tr_b16 a[138:139], v198 offset:2048\n\t"
  "ds_read_b64_tr_b16 a[140:141], v199 offset:0\n\t"
  "ds_read_b64_tr_b16 a[142:143], v199 offset:2048\n\t"
  "ds_read_b128 a[144:147], v222 offset:0\n\t"
  "ds_read_b64_tr_b16 a[148:149], v196 offset:8192\n\t"
  "ds_read_b64_tr_b16 a[150:151], v196 offset:10240\n\t"
  "s_mov_b32 s28, 0\n\t"
  "s_mov_b32 s29, 0\n\t"
  "s_mov_b32 s39, 0\n\t"
  "s_mov_b32 s41, 0x40000000\n\t"
  "s_waitcnt lgkmcnt(7) vmcnt(12)\n\t"
  "v_mfma_f32_16x16x32_bf16 v[32:35], a[128:131], a[192:195], v[32:35]\n\t"
  "ds_read_b64_tr_b16 a[152:153], v197 offset:8192\n\t"
  "v_mfma_f32_16x16x32_bf16 v[80:83], a[128:131], a[196:199], 0\n\t"
  "ds_read_b64_tr_b16 a[154:155], v197 offset:10240\n\t"
  "v_mfma_f32_16x16x32_bf16 v[84:87], a[132:135], a[196:199], 0\n\t"
  "ds_read_b64_tr_b16 a[156:157], v198 offset:8192\n\t"
  "s_waitcnt lgkmcnt(6)\n\t"
  "v_mfma_f32_16x16x32_bf16 v[36:39], a[132:135], a[192:195], v[36:39]\n\t"
  "ds_read_b64_tr_b16 a[158:159], v198 offset:10240\n\t"
  "buffer_load_dwordx4 a[240:243], v246, s[20:23], s38 offen offset:0 sc1\n\t"
  "v_mfma_f32_16x16x32_bf16 v[40:43], a[136:139], a[192:195], v[40:43]\n\t"
  "ds_read_b64_tr_b16 a[128:129], v199 offset:8192\n\t"
  "v_mfma_f32_16x16x32_bf16 v[88:91], a[136:139], a[196:199], 0\n\t"
  "ds_read_b64_tr_b16 a[130:131], v199 offset:10240\n\t"
  "v_mfma_f32_16x16x32_bf16 v[92:95], a[140:143], a[196:199], 0\n\t"
  "ds_read_b128 a[132:135], v222 offset:128\n\t"
  "s_waitcnt lgkmcnt(9)\n\t"
  "v_mfma_f32_16x16x32_bf16 v[44:47], a[140:143], a[192:195], v[44:47]\n\t"
  "buffer_load_dwordx4 a[244:247], v247, s[20:23], s38 offen offset:0 sc1\n\t"
  "v_mfma_f32_16x16x32_bf16 v[48:51], a[144:147], a[192:195], 0\n\t"
  "ds_read_b64_tr_b16 a[136:137], v196 offset:16384\n\t"
  "s_add_u32 s38, s38, 0x40000\n\t"
  "v_mfma_f32_16x16x32_bf16 v[96:99], a[144:147], a[196:199], 0\n\t"
  "ds_read_b64_tr_b16 a[138:139], v196 offset:18432\n\t"
  "s_waitcnt lgkmcnt(7) vmcnt(12)\n\t"
  "v_mfma_f32_16x16x32_bf16 v[80:83], a[148:151], a[204:207], v[80:83]\n\t"
  "ds_read_b64_tr_b16 a[140:141], v197 offset:16384\n\t"
  "v_mfma_f32_16x16x32_bf16 v[32:35], a[148:151], a[200:203], v[32:35]\n\t"
  "ds_read_b64_tr_b16 a[142:143], v197 offset:18432\n\t"
  "v_mfma_f32_16x16x32_bf16 v[36:39], a[152:155], a[200:203], v[36:39]\n\t"
  "ds_read_b64_tr_b16 a[144:145], v198 offset:16384\n\t"
  "s_waitcnt lgkmcnt(6)\n\t"
  "v_mfma_f32_16x16x32_bf16 v[84:87], a[152:155], a[204:207], v[84:87]\n\t"
  "ds_read_b64_tr_b16 a[146:147], v198 offset:18432\n\t"
  "buffer_load_dwordx4 a[248:251], v246, s[20:23], s38 offen offset:0 sc1\n\t"
  "v_mfma_f32_16x16x32_bf16 v[88:91], a[156:159], a[204:207], v[88:91]\n\t"
  "ds_read_b64_tr_b16 a[148:149], v199 offset:16384\n\t"
  "v_mfma_f32_16x16x32_bf16 v[40:43], a[156:159], a[200:203], v[40:43]\n\t"
  "ds_read_b64_tr_b16 a[150:151], v199 offset:18432\n\t"
  "v_mfma_f32_16x16x32_bf16 v[44:47], a[128:131], a[200:203], v[44:47]\n\t"
  "ds_read_b128 a[152:155], v222 offset:256\n\t"
  "s_waitcnt lgkmcnt(9)\n\t"
  "v_mfma_f32_16x16x32_bf16 v[92:95], a[128:131], a[204:207], v[92:95]\n\t"
  "buffer_load_dwordx4 a[252:255], v247, s[20:23], s38 offen offset:0 sc1\n\t"
  "v_mfma_f32_16x16x32_bf16 v[96:99], a[132:135], a[204:207], v[96:99]\n\t"
  "ds_read_b64_tr_b16 a[156:157], v196 offset:24576\n\t"
  "s_add_u32 s38, s38, 0x40000\n\t"
  "v_mfma_f32_16x16x32_bf16 v[48:51], a[132:135], a[200:203], v[48:51]\n\t"
  "ds_read_b64_tr_b16 a[158:159], v196 offset:26624\n\t"
  "s_waitcnt lgkmcnt(7) vmcnt(12)\n\t"
  "v_mfma_f32_16x16x32_bf16 v[32:35], a[136:139], a[208:211], v[32:35]\n\t"
  "ds_read_b64_tr_b16 a[128:129], v197 offset:24576\n\t"
  "v_mfma_f32_16x16x32_bf16 v[80:83], a[136:139], a[212:215], v[80:83]\n\t"
  "ds_read_b64_tr_b16 a[130:131], v197 offset:26624\n\t"
  "v_mfma_f32_16x16x32_bf16 v[84:87], a[140:143], a[212:215], v[84:87]\n\t"
  "ds_read_b64_tr_b16 a[132:133], v198 offset:24576\n\t"
  "s_waitcnt lgkmcnt(6)\n\t"
  "v_mfma_f32_16x16x32_bf16 v[36:39], a[140:143], a[208:211], v[36:39]\n\t"
  "ds_read_b64_tr_b16 a[134:135], v198 offset:26624\n\t"
  "buffer_load_dwordx4 a[192:195], v246, s[20:23], s38 offen offset:0 sc1\n\t"
  "v_mfma_f32_16x16x32_bf16 v[40:43], a[144:147], a[208:211], v[40:43]\n\t"
  "ds_read_b64_tr_b16 a[136:137], v199 offset:24576\n\t"
  "v_mfma_f32_16x16x32_bf16 v[88:91], a[144:147], a[212:215], v[88:91]\n\t"
  "ds_read_b64_tr_b16 a[138:139], v199 offset:26624\n\t"
  "v_mfma_f32_16x16x32_bf16 v[92:95], a[148:151], a[212:215], v[92:95]\n\t"
  "ds_read_b128 a[140:143], v222 offset:384\n\t"
  "s_waitcnt lgkmcnt(9)\n\t"
  "v_mfma_f32_16x16x32_bf16 v[44:47], a[148:151], a[208:211], v[44:47]\n\t"
  "buffer_load_dwordx4 a[196:199], v247, s[20:23], s38 offen offset:0 sc1\n\t"
  "v_mfma_f32_16x16x32_bf16 v[48:51], a[152:155], a[208:211], v[48:51]\n\t"
  "ds_read_b64_tr_b16 a[144:145], v196 offset:32768\n\t"
  "s_add_u32 s38, s38, 0x40000\n\t"
  "v_mfma_f32_16x16x32_bf16 v[96:99], a[152:155], a[212:215], v[96:99]\n\t"
  "ds_read_b64_tr_b16 a[146:147], v196 offset:34816\n\t"
  "s_waitcnt lgkmcnt(7) vmcnt(12)\n\t"
  "v_mfma_f32_16x16x32_bf16 v[80:83], a[156:159], a[220:223], v[80:83]\n\t"
  "ds_read_b64_tr_b16 a[148:149], v197 offset:32768\n\t"
  "v_mfma_f32_16x16x32_bf16 v[32:35], a[156:159], a[216:219], v[32:35]\n\t"
  "ds_read_b64_tr_b16 a[150:151], v197 offset:34816\n\t"
  "v_mfma_f32_16x16x32_bf16 v[36:39], a[128:131], a[216:219], v[36:39]\n\t"
  "ds_read_b64_tr_b16 a[152:153], v198 offset:32768\n\t"
  "s_waitcnt lgkmcnt(6)\n\t"
  "v_mfma_f32_16x16x32_bf16 v[84:87], a[128:131], a[220:223], v[84:87]\n\t"
  "ds_read_b64_tr_b16 a[154:155], v198 offset:34816\n\t"
  "buffer_load_dwordx4 a[200:203], v246, s[20:23], s38 offen offset:0 sc1\n\t"
  "v_mfma_f32_16x16x32_bf16 v[88:91], a[132:135], a[220:223], v[88:91]\n\t"
  "ds_read_b64_tr_b16 a[156:157], v199 offset:32768\n\t"
  "v_mfma_f32_16x16x32_bf16 v[40:43], a[132:135], a[216:219], v[40:43]\n\t"
  "ds_read_b64_tr_b16 a[158:159], v199 offset:34816\n\t"
  "v_mfma_f32_16x16x32_bf16 v[44:47], a[136:139], a[216:219], v[44:47]\n\t"
  "ds_read_b128 a[128:131], v222 offset:512\n\t"
  "s_waitcnt lgkmcnt(9)\n\t"
  "v_mfma_f32_16x16x32_bf16 v[92:95], a[136:139], a[220:223], v[92:95]\n\t"
  "buffer_load_dwordx4 a[204:207], v247, s[20:23], s38 offen offset:0 sc1\n\t"
  "v_mfma_f32_16x16x32_bf16 v[96:99], a[140:143], a[220:223], v[96:99]\n\t"
  "ds_read_b64_tr_b16 a[132:133], v196 offset:40960\n\t"
  "s_add_u32 s38, s38, 0x40000\n\t"
  "v_mfma_f32_16x16x32_bf16 v[48:51], a[140:143], a[216:219], v[48:51]\n\t"
  "ds_read_b64_tr_b16 a[134:135], v196 offset:43008\n\t"
  "s_waitcnt lgkmcnt(7) vmcnt(12)\n\t"
  "v_mfma_f32_16x16x32_bf16 v[32:35], a[144:147], a[224:227], v[32:35]\n\t"
  "ds_read_b64_tr_b16 a[136:137], v197 offset:40960\n\t"
  "s_waitcnt vmcnt(8)\n\t"
  "buffer_store_dwordx2 v[16:17], v227, s[20:23], 0 offen offset:8 sc1\n\t"
  "v_mfma_f32_16x16x32_bf16 v[80:83], a[144:147], a[228:231], v[80:83]\n\t"
  "ds_read_b64_tr_b16 a[138:139], v197 offset:43008\n\t"
  "v_mfma_f32_16x16x32_bf16 v[84:87], a[148:151], a[228:231], v[84:87]\n\t"
  "ds_read_b64_tr_b16 a[140:141], v198 offset:40960\n\t"
  "s_waitcnt lgkmcnt(6)\n\t"
  "v_mfma_f32_16x16x32_bf16 v[36:39], a[148:151], a[224:227], v[36:39]\n\t"
  "ds_read_b64_tr_b16 a[142:143], v198 offset:43008\n\t"
  "buffer_load_dwordx4 a[208:211], v246, s[20:23], s38 offen offset:0 sc1\n\t"
  "v_mfma_f32_16x16x32_bf16 v[40:43], a[152:155], a[224:227], v[40:43]\n\t"
  "ds_read_b64_tr_b16 a[144:145], v199 offset:40960\n\t"
  "v_mfma_f32_16x16x32_bf16 v[88:91], a[152:155], a[228:231], v[88:91]\n\t"
  "ds_read_b64_tr_b16 a[146:147], v199 offset:43008\n\t"
  "v_mfma_f32_16x16x32_bf16 v[92:95], a[156:159], a[228:231], v[92:95]\n\t"
  "ds_read_b128 a[148:151], v222 offset:640\n\t"
  "s_waitcnt lgkmcnt(9)\n\t"
  "v_mfma_f32_16x16x32_bf16 v[44:47], a[156:159], a[224:227], v[44:47]\n\t"
  "buffer_load_dwordx4 a[212:215], v247, s[20:23], s38 offen offset:0 sc1\n\t"
  "v_mfma_f32_16x16x32_bf16 v[48:51], a[128:131], a[224:227], v[48:51]\n\t"
  "ds_read_b64_tr_b16 a[152:153], v196 offset:49152\n\t"
  "s_add_u32 s38, s38, 0x40000\n\t"
  "v_mfma_f32_16x16x32_bf16 v[96:99], a[128:131], a[228:231], v[96:99]\n\t"
  "ds_read_b64_tr_b16 a[154:155], v196 offset:51200\n\t"
  "s_waitcnt lgkmcnt(7)\n\t"
  "v_mfma_f32_16x16x32_bf16 v[80:83], a[132:135], a[236:239], v[80:83]\n\t"
  "ds_read_b64_tr_b16 a[156:157], v197 offset:49152\n\t"
  "v_mfma_f32_16x16x32_bf16 v[32:35], a[132:135], a[232:235], v[32:35]\n\t"
  "ds_read_b64_tr_b16 a[158:159], v197 offset:51200\n\t"
  "v_mfma_f32_16x16x32_bf16 v[36:39], a[136:139], a[232:235], v[36:39]\n\t"
  "ds_read_b64_tr_b16 a[128:129], v198 offset:49152\n\t"
  "s_waitcnt lgkmcnt(6)\n\t"
  "v_mfma_f32_16x16x32_bf16 v[84:87], a[136:139], a[236:239], v[84:87]\n\t"
  "ds_read_b64_tr_b16 a[130:131], v198 offset:51200\n\t"
  "buffer_load_dwordx4 a[216:219], v246, s[20:23], s38 offen offset:0 sc1\n\t"
  "v_mfma_f32_16x16x32_bf16 v[88:91], a[140:143], a[236:239], v[88:91]\n\t"
  "ds_read_b64_tr_b16 a[132:133], v199 offset:49152\n\t"
  "v_mfma_f32_16x16x32_bf16 v[40:43], a[140:143], a[232:235], v[40:43]\n\t"
  "ds_read_b64_tr_b16 a[134:135], v199 offset:51200\n\t"
  "v_mfma_f32_16x16x32_bf16 v[44:47], a[144:147], a[232:235], v[44:47]\n\t"
  "ds_read_b128 a[136:139], v222 offset:768\n\t"
  "s_waitcnt lgkmcnt(9)\n\t"
  "v_mfma_f32_16x16x32_bf16 v[92:95], a[144:147], a[236:239], v[92:95]\n\t"
  "buffer_load_dwordx4 a[220:223], v247, s[20:23], s38 offen offset:0 sc1\n\t"
  "v_mfma_f32_16x16x32_bf16 v[96:99], a[148:151], a[236:239], v[96:99]\n\t"
  "ds_read_b64_tr_b16 a[140:141], v196 offset:57344\n\t"
  "s_add_u32 s38, s38, 0x40000\n\t"
  "v_mfma_f32_16x16x32_bf16 v[48:51], a[148:151], a[232:235], v[48:51]\n\t"
  "ds_read_b64_tr_b16 a[142:143], v196 offset:59392\n\t"
  "s_waitcnt lgkmcnt(7) vmcnt(11)\n\t"
  "v_mfma_f32_16x16x32_bf16 v[32:35], a[152:155], a[240:243], v[32:35]\n\t"
  "ds_read_b64_tr_b16 a[144:145], v197 offset:57344\n\t"
  "v_mfma_f32_16x16x32_bf16 v[80:83], a[152:155], a[244:247], v[80:83]\n\t"
  "ds_read_b64_tr_b16 a[146:147], v197 offset:59392\n\t"
  "v_mfma_f32_16x16x32_bf16 v[84:87], a[156:159], a[244:247], v[84:87]\n\t"
  "ds_read_b64_tr_b16 a[148:149], v198 offset:57344\n\t"
  "s_waitcnt lgkmcnt(6)\n\t"
  "v_mfma_f32_16x16x32_bf16 v[36:39], a[156:159], a[240:243], v[36:39]\n\t"
  "ds_read_b64_tr_b16 a[150:151], v198 offset:59392\n\t"
  "buffer_load_dwordx4 a[224:227], v246, s[20:23], s38 offen offset:0 sc1\n\t"
  "v_mfma_f32_16x16x32_bf16 v[40:43], a[128:131], a[240:243], v[40:43]\n\t"
  "ds_read_b64_tr_b16 a[152:153], v199 offset:57344\n\t"
  "v_mfma_f32_16x16x32_bf16 v[88:91], a[128:131], a[244:247], v[88:91]\n\t"
  "ds_read_b64_tr_b16 a[154:155], v199 offset:59392\n\t"
  "v_mfma_f32_16x16x32_bf16 v[92:95], a[132:135], a[244:247], v[92:95]\n\t"
  "ds_read_b128 a[156:159], v222 offset:896\n\t"
  "s_waitcnt lgkmcnt(9)\n\t"
  "v_mfma_f32_16x16x32_bf16 v[44:47], a[132:135], a[240:243], v[44:47]\n\t"
  "buffer_load_dwordx4 a[228:231], v247, s[20:23], s38 offen offset:0 sc1\n\t"
  "v_mfma_f32_16x16x32_bf16 v[48:51], a[136:139], a[240:243], v[48:51]\n\t"
  "ds_read_b64_tr_b16 a[128:129], v252 offset:0\n\t"
  "s_add_u32 s38, s38, 0x40000\n\t"
  "v_mfma_f32_16x16x32_bf16 v[96:99], a[136:139], a[244:247], v[96:99]\n\t"
  "ds_read_b64_tr_b16 a[130:131], v252 offset:2048\n\t"
  "s_waitcnt lgkmcnt(7) vmcnt(11)\n\t"
  "v_mfma_f32_16x16x32_bf16 v[80:83], a[140:143], a[252:255], v[80:83]\n\t"
  "ds_read_b64_tr_b16 a[132:133], v253 offset:0\n\t"
  "v_mfma_f32_16x16x32_bf16 v[32:35], a[140:143], a[248:251], v[32:35]\n\t"
  "ds_read_b64_tr_b16 a[134:135], v253 offset:2048\n\t"
  "v_mfma_f32_16x16x32_bf16 v[36:39], a[144:147], a[248:251], v[36:39]\n\t"
  "ds_read_b64_tr_b16 a[136:137], v254 offset:0\n\t"
  "s_waitcnt lgkmcnt(6)\n\t"
  "v_mfma_f32_16x16x32_bf16 v[84:87], a[144:147], a[252:255], v[84:87]\n\t"
  "ds_read_b64_tr_b16 a[138:139], v254 offset:2048\n\t"
  "buffer_load_dwordx4 a[232:235], v246, s[20:23], s38 offen offset:0 sc1\n\t"
  "v_mfma_f32_16x16x32_bf16 v[88:91], a[148:151], a[252:255], v[88:91]\n\t"
  "ds_read_b64_tr_b16 a[140:141], v255 offset:0\n\t"
  "v_mfma_f32_16x16x32_bf16 v[40:43], a[148:151], a[248:251], v[40:43]\n\t"
  "ds_read_b64_tr_b16 a[142:143], v255 offset:2048\n\t"
  "v_mfma_f32_16x16x32_bf16 v[44:47], a[152:155], a[248:251], v[44:47]\n\t"
  "ds_read_b128 a[144:147], v222 offset:1024\n\t"
  "s_waitcnt lgkmcnt(9)\n\t"
  "v_mfma_f32_16x16x32_bf16 v[92:95], a[152:155], a[252:255], v[92:95]\n\t"
  "buffer_load_dwordx4 a[236:239], v247, s[20:23], s38 offen offset:0 sc1\n\t"
  "v_mfma_f32_16x16x32_bf16 v[96:99], a[156:159], a[252:255], v[96:99]\n\t"
  "ds_read_b64_tr_b16 a[148:149], v252 offset:8192\n\t"
  "s_add_u32 s38, s38, 0x40000\n\t"
  "v_mfma_f32_16x16x32_bf16 v[48:51], a[156:159], a[248:251], v[48:51]\n\t"
  "ds_read_b64_tr_b16 a[150:151], v252 offset:10240\n\t"
  "s_waitcnt lgkmcnt(7) vmcnt(11)\n\t"
  "v_mfma_f32_16x16x32_bf16 v[32:35], a[128:131], a[192:195], v[32:35]\n\t"
  "ds_read_b64_tr_b16 a[152:153], v253 offset:8192\n\t"
  "v_mfma_f32_16x16x32_bf16 v[80:83], a[128:131], a[196:199], v[80:83]\n\t"
  "ds_read_b64_tr_b16 a[154:155], v253 offset:10240\n\t"
  "v_mfma_f32_16x16x32_bf16 v[84:87], a[132:135], a[196:199], v[84:87]\n\t"
  "ds_read_b64_tr_b16 a[156:157], v254 offset:8192\n\t"
  "s_waitcnt lgkmcnt(6)\n\t"
  "v_mfma_f32_16x16x32_bf16 v[36:39], a[132:135], a[192:195], v[36:39]\n\t"
  "ds_read_b64_tr_b16 a[158:159], v254 offset:10240\n\t"
  "buffer_load_dwordx4 a[240:243], v246, s[20:23], s38 offen offset:0 sc1\n\t"
  "v_mfma_f32_16x16x32_bf16 v[40:43], a[136:139], a[192:195], v[40:43]\n\t"
  "ds_read_b64_tr_b16 a[128:129], v255 offset:8192\n\t"
  "v_mfma_f32_16x16x32_bf16 v[88:91], a[136:139], a[196:199], v[88:91]\n\t"
  "ds_read_b64_tr_b16 a[130:131], v255 offset:10240\n\t"
  "v_mfma_f32_16x16x32_bf16 v[92:95], a[140:143], a[196:199], v[92:95]\n\t"
  "ds_read_b128 a[132:135], v222 offset:1152\n\t"
  "s_waitcnt lgkmcnt(9)\n\t"
  "v_mfma_f32_16x16x32_bf16 v[44:47], a[140:143], a[192:195], v[44:47]\n\t"
  "buffer_load_dwordx4 a[244:247], v247, s[20:23], s38 offen offset:0 sc1\n\t"
  "v_mfma_f32_16x16x32_bf16 v[48:51], a[144:147], a[192:195], v[48:51]\n\t"
  "ds_read_b64_tr_b16 a[136:137], v252 offset:16384\n\t"
  "s_add_u32 s38, s38, 0x40000\n\t"
  "v_mfma_f32_16x16x32_bf16 v[96:99], a[144:147], a[196:199], v[96:99]\n\t"
  "ds_read_b64_tr_b16 a[138:139], v252 offset:18432\n\t"
  "s_waitcnt lgkmcnt(7) vmcnt(11)\n\t"
  "v_mfma_f32_16x16x32_bf16 v[80:83], a[148:151], a[204:207], v[80:83]\n\t"
  "ds_read_b64_tr_b16 a[140:141], v253 offset:16384\n\t"
  "v_mfma_f32_16x16x32_bf16 v[32:35], a[148:151], a[200:203], v[32:35]\n\t"
  "ds_read_b64_tr_b16 a[142:143], v253 offset:18432\n\t"
  "v_mfma_f32_16x16x32_bf16 v[36:39], a[152:155], a[200:203], v[36:39]\n\t"
  "ds_read_b64_tr_b16 a[144:145], v254 offset:16384\n\t"
  "s_waitcnt lgkmcnt(6)\n\t"
  "v_mfma_f32_16x16x32_bf16 v[84:87], a[152:155], a[204:207], v[84:87]\n\t"
  "ds_read_b64_tr_b16 a[146:147], v254 offset:18432\n\t"
  "buffer_load_dwordx4 a[248:251], v246, s[20:23], s38 offen offset:0 sc1\n\t"
  "v_mfma_f32_16x16x32_bf16 v[88:91], a[156:159], a[204:207], v[88:91]\n\t"
  "ds_read_b64_tr_b16 a[148:149], v255 offset:16384\n\t"
  "v_mfma_f32_16x16x32_bf16 v[40:43], a[156:159], a[200:203], v[40:43]\n\t"
  "ds_read_b64_tr_b16 a[150:151], v255 offset:18432\n\t"
  "v_mfma_f32_16x16x32_bf16 v[44:47], a[128:131], a[200:203], v[44:47]\n\t"
  "ds_read_b128 a[152:155], v222 offset:1280\n\t"
  "s_waitcnt lgkmcnt(9)\n\t"
  "v_mfma_f32_16x16x32_bf16 v[92:95], a[128:131], a[204:207], v[92:95]\n\t"
  "buffer_load_dwordx4 a[252:255], v247, s[20:23], s38 offen offset:0 sc1\n\t"
  "v_mfma_f32_16x16x32_bf16 v[96:99], a[132:135], a[204:207], v[96:99]\n\t"
  "ds_read_b64_tr_b16 a[156:157], v252 offset:24576\n\t"
  "s_add_u32 s38, s38, 0xffc40000\n\t"
  "v_mfma_f32_16x16x32_bf16 v[48:51], a[132:135], a[200:203], v[48:51]\n\t"
  "ds_read_b64_tr_b16 a[158:159], v252 offset:26624\n\t"
  "s_waitcnt lgkmcnt(7) vmcnt(10)\n\t"
  "v_mfma_f32_16x16x32_bf16 v[32:35], a[136:139], a[208:211], v[32:35]\n\t"
  "ds_read_b64_tr_b16 a[128:129], v253 offset:24576\n\t"
  "v_mfma_f32_16x16x32_bf16 v[80:83], a[136:139], a[212:215], v[80:83]\n\t"
  "ds_read_b64_tr_b16 a[130:131], v253 offset:26624\n\t"
  "v_mfma_f32_16x16x32_bf16 v[84:87], a[140:143], a[212:215], v[84:87]\n\t"
  "ds_read_b64_tr_b16 a[132:133], v254 offset:24576\n\t"
  "v_mfma_f32_16x16x32_bf16 v[36:39], a[140:143], a[208:211], v[36:39]\n\t"
  "ds_read_b64_tr_b16 a[134:135], v254 offset:26624\n\t"
  "s_waitcnt lgkmcnt(7)\n\t"
  "v_mfma_f32_16x16x32_bf16 v[40:43], a[144:147], a[208:211], v[40:43]\n\t"
  "ds_read_b64_tr_b16 a[136:137], v255 offset:24576\n\t"
  "v_mfma_f32_16x16x32_bf16 v[88:91], a[144:147], a[212:215], v[88:91]\n\t"
  "ds_read_b64_tr_b16 a[138:139], v255 offset:26624\n\t"
  "v_mfma_f32_16x16x32_bf16 v[92:95], a[148:151], a[212:215], v[92:95]\n\t"
  "ds_read_b128 a[140:143], v222 offset:1408\n\t"
  "v_mfma_f32_16x16x32_bf16 v[44:47], a[148:151], a[208:211], v[44:47]\n\t"
  "s_waitcnt lgkmcnt(9)\n\t"
  "v_mfma_f32_16x16x32_bf16 v[48:51], a[152:155], a[208:211], v[48:51]\n\t"
  "ds_read_b64_tr_b16 a[144:145], v232 offset:0\n\t"
  "v_mfma_f32_16x16x32_bf16 v[96:99], a[152:155], a[212:215], v[96:99]\n\t"
  "ds_read_b64_tr_b16 a[146:147], v232 offset:2048\n\t"
  "s_waitcnt lgkmcnt(7) vmcnt(8)\n\t"
  "v_mfma_f32_16x16x32_bf16 v[80:83], a[156:159], a[220:223], v[80:83]\n\t"
  "ds_read_b64_tr_b16 a[148:149], v233 offset:0\n\t"
  "v_mfma_f32_16x16x32_bf16 v[32:35], a[156:159], a[216:219], v[32:35]\n\t"
  "ds_read_b64_tr_b16 a[150:151], v233 offset:2048\n\t"
  "v_mfma_f32_16x16x32_bf16 v[36:39], a[128:131], a[216:219], v[36:39]\n\t"
  "ds_read_b64_tr_b16 a[152:153], v234 offset:0\n\t"
  "v_mfma_f32_16x16x32_bf16 v[84:87], a[128:131], a[220:223], v[84:87]\n\t"
  "ds_read_b64_tr_b16 a[154:155], v234 offset:2048\n\t"
  "s_waitcnt lgkmcnt(7)\n\t"
  "v_mfma_f32_16x16x32_bf16 v[88:91], a[132:135], a[220:223], v[88:91]\n\t"
  "ds_read_b64_tr_b16 a[156:157], v235 offset:0\n\t"
  "v_mfma_f32_16x16x32_bf16 v[40:43], a[132:135], a[216:219], v[40:43]\n\t"
  "ds_read_b64_tr_b16 a[158:159], v235 offset:2048\n\t"
  "v_mfma_f32_16x16x32_bf16 v[44:47], a[136:139], a[216:219], v[44:47]\n\t"
  "ds_read_b128 a[128:131], v222 offset:1536\n\t"
  "v_mfma_f32_16x16x32_bf16 v[92:95], a[136:139], a[220:223], v[92:95]\n\t"
  "s_waitcnt lgkmcnt(9)\n\t"
  "v_mfma_f32_16x16x32_bf16 v[96:99], a[140:143], a[220:223], v[96:99]\n\t"
  "ds_read_b64_tr_b16 a[132:133], v232 offset:8192\n\t"
  "v_mfma_f32_16x16x32_bf16 v[48:51], a[140:143], a[216:219], v[48:51]\n\t"
  "ds_read_b64_tr_b16 a[134:135], v232 offset:10240\n\t"
  "s_waitcnt lgkmcnt(7) vmcnt(6)\n\t"
  "v_mfma_f32_16x16x32_bf16 v[32:35], a[144:147], a[224:227], v[32:35]\n\t"
  "ds_read_b64_tr_b16 a[136:137], v233 offset:8192\n\t"
  "v_mfma_f32_16x16x32_bf16 v[80:83], a[144:147], a[228:231], v[80:83]\n\t"
  "ds_read_b64_tr_b16 a[138:139], v233 offset:10240\n\t"
  "v_mfma_f32_16x16x32_bf16 v[84:87], a[148:151], a[228:231], v[84:87]\n\t"
  "ds_read_b64_tr_b16 a[140:141], v234 offset:8192\n\t"
  "v_mfma_f32_16x16x32_bf16 v[36:39], a[148:151], a[224:227], v[36:39]\n\t"
  "ds_read_b64_tr_b16 a[142:143], v234 offset:10240\n\t"
  "s_waitcnt lgkmcnt(7)\n\t"
  "v_mfma_f32_16x16x32_bf16 v[40:43], a[152:155], a[224:227], v[40:43]\n\t"
  "ds_read_b64_tr_b16 a[144:145], v235 offset:8192\n\t"
  "v_mfma_f32_16x16x32_bf16 v[88:91], a[152:155], a[228:231], v[88:91]\n\t"
  "ds_read_b64_tr_b16 a[146:147], v235 offset:10240\n\t"
  "v_mfma_f32_16x16x32_bf16 v[92:95], a[156:159], a[228:231], v[92:95]\n\t"
  "ds_read_b128 a[148:151], v222 offset:1664\n\t"
  "v_mfma_f32_16x16x32_bf16 v[44:47], a[156:159], a[224:227], v[44:47]\n\t"
  "s_waitcnt lgkmcnt(9)\n\t"
  "v_mfma_f32_16x16x32_bf16 v[48:51], a[128:131], a[224:227], v[48:51]\n\t"
  "ds_read_b64_tr_b16 a[152:153], v232 offset:16384\n\t"
  "v_mfma_f32_16x16x32_bf16 v[96:99], a[128:131], a[228:231], v[96:99]\n\t"
  "ds_read_b64_tr_b16 a[154:155], v232 offset:18432\n\t"
  "s_waitcnt lgkmcnt(7) vmcnt(4)\n\t"
  "v_mfma_f32_16x16x32_bf16 v[80:83], a[132:135], a[236:239], v[80:83]\n\t"
  "ds_read_b64_tr_b16 a[156:157], v233 offset:16384\n\t"
  "v_mfma_f32_16x16x32_bf16 v[32:35], a[132:135], a[232:235], v[32:35]\n\t"
  "ds_read_b64_tr_b16 a[158:159], v233 offset:18432\n\t"
  "v_mfma_f32_16x16x32_bf16 v[36:39], a[136:139], a[232:235], v[36:39]\n\t"
  "ds_read_b64_tr_b16 a[128:129], v234 offset:16384\n\t"
  "v_mfma_f32_16x16x32_bf16 v[84:87], a[136:139], a[236:239], v[84:87]\n\t"
  "ds_read_b64_tr_b16 a[130:131], v234 offset:18432\n\t"
  "s_waitcnt lgkmcnt(7)\n\t"
  "v_mfma_f32_16x16x32_bf16 v[88:91], a[140:143], a[236:239], v[88:91]\n\t"
  "ds_read_b64_tr_b16 a[132:133], v235 offset:16384\n\t"
  "v_mfma_f32_16x16x32_bf16 v[40:43], a[140:143], a[232:235], v[40:43]\n\t"
  "ds_read_b64_tr_b16 a[134:135], v235 offset:18432\n\t"
  "v_mfma_f32_16x16x32_bf16 v[44:47], a[144:147], a[232:235], v[44:47]\n\t"
  "ds_read_b128 a[136:139], v222 offset:1792\n\t"
  "v_mfma_f32_16x16x32_bf16 v[92:95], a[144:147], a[236:239], v[92:95]\n\t"
  "s_waitcnt lgkmcnt(9)\n\t"
  "v_mfma_f32_16x16x32_bf16 v[96:99], a[148:151], a[236:239], v[96:99]\n\t"
  "ds_read_b64_tr_b16 a[140:141], v232 offset:24576\n\t"
  "v_mfma_f32_16x16x32_bf16 v[48:51], a[148:151], a[232:235], v[48:51]\n\t"
  "ds_read_b64_tr_b16 a[142:143], v232 offset:26624\n\t"
  "s_waitcnt lgkmcnt(7) vmcnt(2)\n\t"
  "v_mfma_f32_16x16x32_bf16 v[32:35], a[152:155], a[240:243], v[32:35]\n\t"
  "ds_read_b64_tr_b16 a[144:145], v233 offset:24576\n\t"
  "v_not_b32 v24, s34\n\t"
  "v_mfma_f32_16x16x32_bf16 v[80:83], a[152:155], a[244:247], v[80:83]\n\t"
  "ds_read_b64_tr_b16 a[146:147], v233 offset:26624\n\t"
  "buffer_load_dwordx2 v[24:25], v228, s[20:23], 0 offen offset:8 sc1\n\t"
  "v_mfma_f32_16x16x32_bf16 v[84:87], a[156:159], a[244:247], v[84:87]\n\t"
  "ds_read_b64_tr_b16 a[148:149], v234 offset:24576\n\t"
  "v_mfma_f32_16x16x32_bf16 v[36:39], a[156:159], a[240:243], v[36:39]\n\t"
  "ds_read_b64_tr_b16 a[150:151], v234 offset:26624\n\t"
  "s_waitcnt lgkmcnt(7)\n\t"
  "v_mfma_f32_16x16x32_bf16 v[40:43], a[128:131], a[240:243], v[40:43]\n\t"
  "ds_read_b64_tr_b16 a[152:153], v235 offset:24576\n\t"
  "v_mfma_f32_16x16x32_bf16 v[88:91], a[128:131], a[244:247], v[88:91]\n\t"
  "ds_read_b64_tr_b16 a[154:155], v235 offset:26624\n\t"
  "v_mfma_f32_16x16x32_bf16 v[92:95], a[132:135], a[244:247], v[92:95]\n\t"
  "ds_read_b128 a[156:159], v222 offset:1920\n\t"
  "v_mfma_f32_16x16x32_bf16 v[44:47], a[132:135], a[240:243], v[44:47]\n\t"
  "s_waitcnt lgkmcnt(9)\n\t"
  "v_mfma_f32_16x16x32_bf16 v[48:51], a[136:139], a[240:243], v[48:51]\n\t"
  "ds_read_b64_tr_b16 a[128:129], v196 offset:0\n\t"
  "v_mfma_f32_16x16x32_bf16 v[96:99], a[136:139], a[244:247], v[96:99]\n\t"
  "ds_read_b64_tr_b16 a[130:131], v196 offset:2048\n\t"
  "s_waitcnt lgkmcnt(7) vmcnt(1)\n\t"
  "v_mfma_f32_16x16x32_bf16 v[80:83], a[140:143], a[252:255], v[80:83]\n\t"
  "ds_read_b64_tr_b16 a[132:133], v197 offset:0\n\t"
  "v_mfma_f32_16x16x32_bf16 v[32:35], a[140:143], a[248:251], v[32:35]\n\t"
  "ds_read_b64_tr_b16 a[134:135], v197 offset:2048\n\t"
  "v_mfma_f32_16x16x32_bf16 v[36:39], a[144:147], a[248:251], v[36:39]\n\t"
  "ds_read_b64_tr_b16 a[136:137], v198 offset:0\n\t"
  "v_mfma_f32_16x16x32_bf16 v[84:87], a[144:147], a[252:255], v[84:87]\n\t"
  "ds_read_b64_tr_b16 a[138:139], v198 offset:2048\n\t"
  "s_waitcnt lgkmcnt(7)\n\t"
  "v_mfma_f32_16x16x32_bf16 v[88:91], a[148:151], a[252:255], v[88:91]\n\t"
  "ds_read_b64_tr_b16 a[140:141], v199 offset:0\n\t"
  "v_mfma_f32_16x16x32_bf16 v[40:43], a[148:151], a[248:251], v[40:43]\n\t"
  "ds_read_b64_tr_b16 a[142:143], v199 offset:2048\n\t"
  "v_mfma_f32_16x16x32_bf16 v[44:47], a[152:155], a[248:251], v[44:47]\n\t"
  "ds_read_b128 a[144:147], v222 offset:0\n\t"
  "v_mfma_f32_16x16x32_bf16 v[92:95], a[152:155], a[252:255], v[92:95]\n\t"
  "s_waitcnt lgkmcnt(9)\n\t"
  "v_mfma_f32_16x16x32_bf16 v[96:99], a[156:159], a[252:255], v[96:99]\n\t"
  "ds_read_b64_tr_b16 a[148:149], v196 offset:8192\n\t"
  "ds_write_b128 v248, v[80:83] offset:0\n\t"
  "v_mfma_f32_16x16x32_bf16 v[48:51], a[156:159], a[248:251], v[48:51]\n\t"
  "ds_read_b64_tr_b16 a[150:151], v196 offset:10240\n\t"
  "s_nop 2\n\t"
  "ds_write_b128 v248, v[84:87] offset:1024\n\t"
  "ds_write_b128 v248, v[88:91] offset:2048\n\t"
  "ds_write_b128 v248, v[92:95] offset:3072\n\t"
  "ds_write_b128 v248, v[96:99] offset:4096\n\t"
  "s_mov_b32 s72, 0x20000\n\t"
  "s_not_b32 s68, s34\n\t"
  "s_mov_b32 s73, 64\n\t"
  "s_branch LP2c_%=\n\t"
  "LP2a_%=:\n\t"
  "v_not_b32 v24, s34\n\t"
  "buffer_load_dwordx2 v[24:25], v228, s[20:23], 0 offen offset:8 sc1\n\t"
  "s_mov_b32 s73, 64\n\t"
  "LP2w_%=:\n\t"
  "s_sleep 1\n\t"
  "LP2c_%=:\n\t"
  "v_cmp_eq_u32 vcc, s68, v24\n\t"
  "s_nop 1\n\t"
  "s_cmp_eq_u64 vcc, 0\n\t"
  "v_cmp_ne_u64 vcc, s[34:35], v[24:25]\n\t"
  "s_cbranch_vccz LP2b_%=\n\t"
  "s_cbranch_scc1 LP2r_%=\n\t"
  "s_sub_u32 s73, s73, 1\n\t"
  "s_cbranch_scc0 LP2w_%=\n\t"
  "s_waitcnt vmcnt(0)\n\t"
  "LP2r_%=:\n\t"
  "s_sleep 2\n\t"
  "s_sub_u32 s72, s72, 1\n\t"
  "s_cbranch_scc0 LP2a_%=\n\t"
  "LP2b_%=:\n\t"
  "buffer_load_dwordx4 a[192:195], v246, s[20:23], s38 offen offset:2048 sc1\n\t"
  "buffer_load_dwordx4 a[196:199], v247, s[20:23], s38 offen offset:2048 sc1\n\t"
  "s_add_u32 s38, s38, 0x40000\n\t"
  "buffer_load_dwordx4 a[200:203], v246, s[20:23], s38 offen offset:2048 sc1\n\t"
  "buffer_load_dwordx4 a[204:207], v247, s[20:23], s38 offen offset:2048 sc1\n\t"
  "s_add_u32 s38, s38, 0x40000\n\t"
  "buffer_load_dwordx4 a[208:211], v246, s[20:23], s38 offen offset:2048 sc1\n\t"
  "buffer_load_dwordx4 a[212:215], v247, s[20:23], s38 offen offset:2048 sc1\n\t"
  "s_add_u32 s38, s38, 0x40000\n\t"
  "buffer_load_dwordx4 a[216:219], v246, s[20:23], s38 offen offset:2048 sc1\n\t"
  "buffer_load_dwordx4 a[220:223], v247, s[20:23], s38 offen offset:2048 sc1\n\t"
  "s_add_u32 s38, s38, 0x40000\n\t"
  "buffer_load_dwordx4 a[224:227], v246, s[20:23], s38 offen offset:2048 sc1\n\t"
  "buffer_load_dwordx4 a[228:231], v247, s[20:23], s38 offen offset:2048 sc1\n\t"
  "s_add_u32 s38, s38, 0x40000\n\t"
  "buffer_load_dwordx4 a[232:235], v246, s[20:23], s38 offen offset:2048 sc1\n\t"
  "buffer_load_dwordx4 a[236:239], v247, s[20:23], s38 offen offset:2048 sc1\n\t"
  "s_add_u32 s38, s38, 0x40000\n\t"
  "s_waitcnt lgkmcnt(0)\n\t"
  "s_barrier\n\t"
  "ds_read_b128 v[128:131], v249 offset:4096\n\t"
  "ds_read_b128 v[112:115], v249 offset:0\n\t"
  "ds_read_b128 v[116:119], v249 offset:1024\n\t"
  "ds_read_b128 v[120:123], v249 offset:2048\n\t"
  "ds_read_b128 v[124:127], v249 offset:3072\n\t"
  "s_waitcnt lgkmcnt(0)\n\t"
  "v_pk_add_f32 v[48:49], v[48:49], v[128:129]\n\t"
  "v_add_f32 v48, v48, v49\n\t"
  "v_pk_add_f32 v[32:33], v[32:33], v[112:113]\n\t"
  "v_pk_add_f32 v[34:35], v[34:35], v[114:115]\n\t"
  "v_pk_add_f32 v[36:37], v[36:37], v[116:117]\n\t"
  "v_pk_add_f32 v[38:39], v[38:39], v[118:119]\n\t"
  "v_pk_add_f32 v[32:33], v[32:33], v[48:49] op_sel_hi:[1,0] neg_lo:[0,1] neg_hi:[0,1]\n\t"
  "v_pk_add_f32 v[34:35], v[34:35], v[48:49] op_sel_hi:[1,0] neg_lo:[0,1] neg_hi:[0,1]\n\t"
  "v_pk_add_f32 v[36:37], v[36:37], v[48:49] op_sel_hi:[1,0] neg_lo:[0,1] neg_hi:[0,1]\n\t"
  "v_pk_add_f32 v[38:39], v[38:39], v[48:49] op_sel_hi:[1,0] neg_lo:[0,1] neg_hi:[0,1]\n\t"
  "v_cvt_pk_bf16_f32 v132, v32, v33\n\t"
  "v_cvt_pk_bf16_f32 v133, v34, v35\n\t"
  "v_cvt_pk_bf16_f32 v134, v36, v37\n\t"
  "v_cvt_pk_bf16_f32 v135, v38, v39\n\t"
  "v_pk_add_f32 v[40:41], v[40:41], v[120:121]\n\t"
  "v_pk_add_f32 v[42:43], v[42:43], v[122:123]\n\t"
  "v_pk_add_f32 v[44:45], v[44:45], v[124:125]\n\t"
  "v_pk_add_f32 v[46:47], v[46:47], v[126:127]\n\t"
  "v_pk_add_f32 v[40:41], v[40:41], v[48:49] op_sel_hi:[1,0] neg_lo:[0,1] neg_hi:[0,1]\n\t"
  "v_pk_add_f32 v[42:43], v[42:43], v[48:49] op_sel_hi:[1,0] neg_lo:[0,1] neg_hi:[0,1]\n\t"
  "v_pk_add_f32 v[44:45], v[44:45], v[48:49] op_sel_hi:[1,0] neg_lo:[0,1] neg_hi:[0,1]\n\t"
  "v_pk_add_f32 v[46:47], v[46:47], v[48:49] op_sel_hi:[1,0] neg_lo:[0,1] neg_hi:[0,1]\n\t"
  "v_cvt_pk_bf16_f32 v136, v40, v41\n\t"
  "v_cvt_pk_bf16_f32 v137, v42, v43\n\t"
  "v_cvt_pk_bf16_f32 v138, v44, v45\n\t"
  "v_cvt_pk_bf16_f32 v139, v46, v47\n\t"
  "buffer_store_dwordx4 v[132:135], v223, s[20:23], 0 offen offset:0 sc1\n\t"
  "buffer_store_dwordx4 v[136:139], v223, s[20:23], s59 offen offset:0 sc1\n\t"
  "v_pk_mul_f32 v[112:113], v[32:33], v[32:33]\n\t"
  "v_pk_mul_f32 v[114:115], v[34:35], v[34:35]\n\t"
  "v_pk_fma_f32 v[112:113], v[36:37], v[36:37], v[112:113]\n\t"
  "v_pk_fma_f32 v[114:115], v[38:39], v[38:39], v[114:115]\n\t"
  "v_pk_fma_f32 v[112:113], v[40:41], v[40:41], v[112:113]\n\t"
  "v_pk_fma_f32 v[114:115], v[42:43], v[42:43], v[114:115]\n\t"
  "v_pk_fma_f32 v[112:113], v[44:45], v[44:45], v[112:113]\n\t"
  "v_pk_fma_f32 v[114:115], v[46:47], v[46:47], v[114:115]\n\t"
  "v_pk_add_f32 v[112:113], v[112:113], v[114:115]\n\t"
  "v_add_f32 v48, v48, v216\n\t"
  "v_add_f32 v112, v112, v113\n\t"
  "v_mov_b32 v113, v112\n\t"
  "v_accvgpr_write_b32 a160, v132\n\t"
  "v_accvgpr_write_b32 a161, v133\n\t"
  "v_permlane16_swap_b32 v112, v113\n\t"
  "v_accvgpr_write_b32 a162, v134\n\t"
  "v_accvgpr_write_b32 a163, v135\n\t"
  "v_add_f32 v112, v112, v113\n\t"
  "v_mov_b32 v113, v112\n\t"
  "v_accvgpr_write_b32 a164, v136\n\t"
  "v_accvgpr_write_b32 a165, v137\n\t"
  "v_permlane32_swap_b32 v112, v113\n\t"
  "v_accvgpr_write_b32 a166, v138\n\t"
  "v_accvgpr_write_b32 a167, v139\n\t"
  "v_add_f32 v112, v112, v113\n\t"
  "buffer_store_dword v112, v27, s[20:23], 0 offen offset:0 sc1\n\t"
  "v_accvgpr_write_b32 a176, v48\n\t"
  "s_waitcnt vmcnt(13)\n\t"
  "v_mfma_f32_16x16x32_bf16 v[200:203], a[128:131], a[192:195], v[200:203]\n\t"
  "ds_read_b64_tr_b16 a[152:153], v197 offset:8192\n\t"
  "v_mfma_f32_16x16x32_bf16 v[80:83], a[128:131], a[196:199], 0\n\t"
  "ds_read_b64_tr_b16 a[154:155], v197 offset:10240\n\t"
  "v_mfma_f32_16x16x32_bf16 v[84:87], a[132:135], a[196:199], 0\n\t"
  "ds_read_b64_tr_b16 a[156:157], v198 offset:8192\n\t"
  "v_mfma_f32_16x16x32_bf16 v[204:207], a[132:135], a[192:195], v[204:207]\n\t"
  "ds_read_b64_tr_b16 a[158:159], v198 offset:10240\n\t"
  "buffer_load_dwordx4 a[240:243], v246, s[20:23], s38 offen offset:2048 sc1\n\t"
  "v_mfma_f32_16x16x32_bf16 v[208:211], a[136:139], a[192:195], v[208:211]\n\t"
  "ds_read_b64_tr_b16 a[128:129], v199 offset:8192\n\t"
  "v_mfma_f32_16x16x32_bf16 v[88:91], a[136:139], a[196:199], 0\n\t"
  "ds_read_b64_tr_b16 a[130:131], v199 offset:10240\n\t"
  "v_mfma_f32_16x16x32_bf16 v[92:95], a[140:143], a[196:199], 0\n\t"
  "ds_read_b128 a[132:135], v222 offset:128\n\t"
  "v_mfma_f32_16x16x32_bf16 v[212:215], a[140:143], a[192:195], v[212:215]\n\t"
  "buffer_load_dwordx4 a[244:247], v247, s[20:23], s38 offen offset:2048 sc1\n\t"
  "v_mfma_f32_16x16x32_bf16 v[52:55], a[144:147], a[192:195], 0\n\t"
  "ds_read_b64_tr_b16 a[136:137], v196 offset:16384\n\t"
  "s_add_u32 s38, s38, 0x40000\n\t"
  "v_mfma_f32_16x16x32_bf16 v[96:99], a[144:147], a[196:199], 0\n\t"
  "ds_read_b64_tr_b16 a[138:139], v196 offset:18432\n\t"
  "s_waitcnt lgkmcnt(7) vmcnt(13)\n\t"
  "v_mfma_f32_16x16x32_bf16 v[80:83], a[148:151], a[204:207], v[80:83]\n\t"
  "ds_read_b64_tr_b16 a[140:141], v197 offset:16384\n\t"
  "v_mfma_f32_16x16x32_bf16 v[200:203], a[148:151], a[200:203], v[200:203]\n\t"
  "ds_read_b64_tr_b16 a[142:143], v197 offset:18432\n\t"
  "v_mfma_f32_16x16x32_bf16 v[204:207], a[152:155], a[200:203], v[204:207]\n\t"
  "ds_read_b64_tr_b16 a[144:145], v198 offset:16384\n\t"
  "s_waitcnt lgkmcnt(6)\n\t"
  "v_mfma_f32_16x16x32_bf16 v[84:87], a[152:155], a[204:207], v[84:87]\n\t"
  "ds_read_b64_tr_b16 a[146:147], v198 offset:18432\n\t"
  "buffer_load_dwordx4 a[248:251], v246, s[20:23], s38 offen offset:2048 sc1\n\t"
  "v_mfma_f32_16x16x32_bf16 v[88:91], a[156:159], a[204:207], v[88:91]\n\t"
  "ds_read_b64_tr_b16 a[148:149], v199 offset:16384\n\t"
  "v_mfma_f32_16x16x32_bf16 v[208:211], a[156:159], a[200:203], v[208:211]\n\t"
  "ds_read_b64_tr_b16 a[150:151], v199 offset:18432\n\t"
  "v_mfma_f32_16x16x32_bf16 v[212:215], a[128:131], a[200:203], v[212:215]\n\t"
  "ds_read_b128 a[152:155], v222 offset:256\n\t"
  "s_waitcnt lgkmcnt(9)\n\t"
  "v_mfma_f32_16x16x32_bf16 v[92:95], a[128:131], a[204:207], v[92:95]\n\t"
  "buffer_load_dwordx4 a[252:255], v247, s[20:23], s38 offen offset:2048 sc1\n\t"
  "v_mfma_f32_16x16x32_bf16 v[96:99], a[132:135], a[204:207], v[96:99]\n\t"
  "ds_read_b64_tr_b16 a[156:157], v196 offset:24576\n\t"
  "s_add_u32 s38, s38, 0x40000\n\t"
  "v_mfma_f32_16x16x32_bf16 v[52:55], a[132:135], a[200:203], v[52:55]\n\t"
  "ds_read_b64_tr_b16 a[158:159], v196 offset:26624\n\t"
  "s_waitcnt lgkmcnt(7) vmcnt(13)\n\t"
  "v_mfma_f32_16x16x32_bf16 v[200:203], a[136:139], a[208:211], v[200:203]\n\t"
  "ds_read_b64_tr_b16 a[128:129], v197 offset:24576\n\t"
  "v_mfma_f32_16x16x32_bf16 v[80:83], a[136:139], a[212:215], v[80:83]\n\t"
  "ds_read_b64_tr_b16 a[130:131], v197 offset:26624\n\t"
  "v_mfma_f32_16x16x32_bf16 v[84:87], a[140:143], a[212:215], v[84:87]\n\t"
  "ds_read_b64_tr_b16 a[132:133], v198 offset:24576\n\t"
  "s_waitcnt lgkmcnt(6)\n\t"
  "v_mfma_f32_16x16x32_bf16 v[204:207], a[140:143], a[208:211], v[204:207]\n\t"
  "ds_read_b64_tr_b16 a[134:135], v198 offset:26624\n\t"
  "buffer_load_dwordx4 a[192:195], v246, s[20:23], s38 offen offset:2048 sc1\n\t"
  "v_mfma_f32_16x16x32_bf16 v[208:211], a[144:147], a[208:211], v[208:211]\n\t"
  "ds_read_b64_tr_b16 a[136:137], v199 offset:24576\n\t"
  "v_mfma_f32_16x16x32_bf16 v[88:91], a[144:147], a[212:215], v[88:91]\n\t"
  "ds_read_b64_tr_b16 a[138:139], v199 offset:26624\n\t"
  "v_mfma_f32_16x16x32_bf16 v[92:95], a[148:151], a[212:215], v[92:95]\n\t"
  "ds_read_b128 a[140:143], v222 offset:384\n\t"
  "s_waitcnt lgkmcnt(9)\n\t"
  "v_mfma_f32_16x16x32_bf16 v[212:215], a[148:151], a[208:211], v[212:215]\n\t"
  "buffer_load_dwordx4 a[196:199], v247, s[20:23], s38 offen offset:2048 sc1\n\t"
  "v_mfma_f32_16x16x32_bf16 v[52:55], a[152:155], a[208:211], v[52:55]\n\t"
  "ds_read_b64_tr_b16 a[144:145], v196 offset:32768\n\t"
  "s_add_u32 s38, s38, 0x40000\n\t"
  "v_mfma_f32_16x16x32_bf16 v[96:99], a[152:155], a[212:215], v[96:99]\n\t"
  "ds_read_b64_tr_b16 a[146:147], v196 offset:34816\n\t"
  "s_waitcnt lgkmcnt(7) vmcnt(13)\n\t"
  "v_mfma_f32_16x16x32_bf16 v[80:83], a[156:159], a[220:223], v[80:83]\n\t"
  "ds_read_b64_tr_b16 a[148:149], v197 offset:32768\n\t"
  "v_mfma_f32_16x16x32_bf16 v[200:203], a[156:159], a[216:219], v[200:203]\n\t"
  "ds_read_b64_tr_b16 a[150:151], v197 offset:34816\n\t"
  "v_mfma_f32_16x16x32_bf16 v[204:207], a[128:131], a[216:219], v[204:207]\n\t"
  "ds_read_b64_tr_b16 a[152:153], v198 offset:32768\n\t"
  "s_waitcnt lgkmcnt(6)\n\t"
  "v_mfma_f32_16x16x32_bf16 v[84:87], a[128:131], a[220:223], v[84:87]\n\t"
  "ds_read_b64_tr_b16 a[154:155], v198 offset:34816\n\t"
  "buffer_load_dwordx4 a[200:203], v246, s[20:23], s38 offen offset:2048 sc1\n\t"
  "v_mfma_f32_16x16x32_bf16 v[88:91], a[132:135], a[220:223], v[88:91]\n\t"
  "ds_read_b64_tr_b16 a[156:157], v199 offset:32768\n\t"
  "v_mfma_f32_16x16x32_bf16 v[208:211], a[132:135], a[216:219], v[208:211]\n\t"
  "ds_read_b64_tr_b16 a[158:159], v199 offset:34816\n\t"
  "v_mfma_f32_16x16x32_bf16 v[212:215], a[136:139], a[216:219], v[212:215]\n\t"
  "ds_read_b128 a[128:131], v222 offset:512\n\t"
  "s_waitcnt lgkmcnt(9)\n\t"
  "v_mfma_f32_16x16x32_bf16 v[92:95], a[136:139], a[220:223], v[92:95]\n\t"
  "buffer_load_dwordx4 a[204:207], v247, s[20:23], s38 offen offset:2048 sc1\n\t"
  "v_mfma_f32_16x16x32_bf16 v[96:99], a[140:143], a[220:223], v[96:99]\n\t"
  "ds_read_b64_tr_b16 a[132:133], v196 offset:40960\n\t"
  "s_add_u32 s38, s38, 0x40000\n\t"
  "v_mfma_f32_16x16x32_bf16 v[52:55], a[140:143], a[216:219], v[52:55]\n\t"
  "ds_read_b64_tr_b16 a[134:135], v196 offset:43008\n\t"
  "s_waitcnt lgkmcnt(7) vmcnt(13)\n\t"
  "v_mfma_f32_16x16x32_bf16 v[200:203], a[144:147], a[224:227], v[200:203]\n\t"
  "ds_read_b64_tr_b16 a[136:137], v197 offset:40960\n\t"
  "s_waitcnt vmcnt(8)\n\t"
  "buffer_store_dwordx2 v[16:17], v8, s[20:23], 0 offen offset:0 sc1\n\t"
  "v_mfma_f32_16x16x32_bf16 v[80:83], a[144:147], a[228:231], v[80:83]\n\t"
  "ds_read_b64_tr_b16 a[138:139], v197 offset:43008\n\t"
  "v_mfma_f32_16x16x32_bf16 v[84:87], a[148:151], a[228:231], v[84:87]\n\t"
  "ds_read_b64_tr_b16 a[140:141], v198 offset:40960\n\t"
  "s_waitcnt lgkmcnt(6)\n\t"
  "v_mfma_f32_16x16x32_bf16 v[204:207], a[148:151], a[224:227], v[204:207]\n\t"
  "ds_read_b64_tr_b16 a[142:143], v198 offset:43008\n\t"
  "buffer_load_dwordx4 a[208:211], v246, s[20:23], s38 offen offset:2048 sc1\n\t"
  "v_mfma_f32_16x16x32_bf16 v[208:211], a[152:155], a[224:227], v[208:211]\n\t"
  "ds_read_b64_tr_b16 a[144:145], v199 offset:40960\n\t"
  "v_mfma_f32_16x16x32_bf16 v[88:91], a[152:155], a[228:231], v[88:91]\n\t"
  "ds_read_b64_tr_b16 a[146:147], v199 offset:43008\n\t"
  "v_mfma_f32_16x16x32_bf16 v[92:95], a[156:159], a[228:231], v[92:95]\n\t"
  "ds_read_b128 a[148:151], v222 offset:640\n\t"
  "s_waitcnt lgkmcnt(9)\n\t"
  "v_mfma_f32_16x16x32_bf16 v[212:215], a[156:159], a[224:227], v[212:215]\n\t"
  "buffer_load_dwordx4 a[212:215], v247, s[20:23], s38 offen offset:2048 sc1\n\t"
  "v_mfma_f32_16x16x32_bf16 v[52:55], a[128:131], a[224:227], v[52:55]\n\t"
  "ds_read_b64_tr_b16 a[152:153], v196 offset:49152\n\t"
  "s_add_u32 s38, s38, 0x40000\n\t"
  "v_mfma_f32_16x16x32_bf16 v[96:99], a[128:131], a[228:231], v[96:99]\n\t"
  "ds_read_b64_tr_b16 a[154:155], v196 offset:51200\n\t"
  "s_waitcnt lgkmcnt(7)\n\t"
  "v_mfma_f32_16x16x32_bf16 v[80:83], a[132:135], a[236:239], v[80:83]\n\t"
  "ds_read_b64_tr_b16 a[156:157], v197 offset:49152\n\t"
  "v_mfma_f32_16x16x32_bf16 v[200:203], a[132:135], a[232:235], v[200:203]\n\t"
  "ds_read_b64_tr_b16 a[158:159], v197 offset:51200\n\t"
  "v_mfma_f32_16x16x32_bf16 v[204:207], a[136:139], a[232:235], v[204:207]\n\t"
  "ds_read_b64_tr_b16 a[128:129], v198 offset:49152\n\t"
  "s_waitcnt lgkmcnt(6)\n\t"
  "v_mfma_f32_16x16x32_bf16 v[84:87], a[136:139], a[236:239], v[84:87]\n\t"
  "ds_read_b64_tr_b16 a[130:131], v198 offset:51200\n\t"
  "buffer_load_dwordx4 a[216:219], v246, s[20:23], s38 offen offset:2048 sc1\n\t"
  "v_mfma_f32_16x16x32_bf16 v[88:91], a[140:143], a[236:239], v[88:91]\n\t"
  "ds_read_b64_tr_b16 a[132:133], v199 offset:49152\n\t"
  "v_mfma_f32_16x16x32_bf16 v[208:211], a[140:143], a[232:235], v[208:211]\n\t"
  "ds_read_b64_tr_b16 a[134:135], v199 offset:51200\n\t"
  "v_mfma_f32_16x16x32_bf16 v[212:215], a[144:147], a[232:235], v[212:215]\n\t"
  "ds_read_b128 a[136:139], v222 offset:768\n\t"
  "s_waitcnt lgkmcnt(9)\n\t"
  "v_mfma_f32_16x16x32_bf16 v[92:95], a[144:147], a[236:239], v[92:95]\n\t"
  "buffer_load_dwordx4 a[220:223], v247, s[20:23], s38 offen offset:2048 sc1\n\t"
  "v_mfma_f32_16x16x32_bf16 v[96:99], a[148:151], a[236:239], v[96:99]\n\t"
  "ds_read_b64_tr_b16 a[140:141], v196 offset:57344\n\t"
  "s_add_u32 s38, s38, 0x40000\n\t"
  "v_mfma_f32_16x16x32_bf16 v[52:55], a[148:151], a[232:235], v[52:55]\n\t"
  "ds_read_b64_tr_b16 a[142:143], v196 offset:59392\n\t"
  "s_waitcnt lgkmcnt(7) vmcnt(11)\n\t"
  "v_mfma_f32_16x16x32_bf16 v[200:203], a[152:155], a[240:243], v[200:203]\n\t"
  "ds_read_b64_tr_b16 a[144:145], v197 offset:57344\n\t"
  "v_mfma_f32_16x16x32_bf16 v[80:83], a[152:155], a[244:247], v[80:83]\n\t"
  "ds_read_b64_tr_b16 a[146:147], v197 offset:59392\n\t"
  "v_mfma_f32_16x16x32_bf16 v[84:87], a[156:159], a[244:247], v[84:87]\n\t"
  "ds_read_b64_tr_b16 a[148:149], v198 offset:57344\n\t"
  "s_waitcnt lgkmcnt(6)\n\t"
  "v_mfma_f32_16x16x32_bf16 v[204:207], a[156:159], a[240:243], v[204:207]\n\t"
  "ds_read_b64_tr_b16 a[150:151], v198 offset:59392\n\t"
  "buffer_load_dwordx4 a[224:227], v246, s[20:23], s38 offen offset:2048 sc1\n\t"
  "v_mfma_f32_16x16x32_bf16 v[208:211], a[128:131], a[240:243], v[208:211]\n\t"
  "ds_read_b64_tr_b16 a[152:153], v199 offset:57344\n\t"
  "v_mfma_f32_16x16x32_bf16 v[88:91], a[128:131], a[244:247], v[88:91]\n\t"
  "ds_read_b64_tr_b16 a[154:155], v199 offset:59392\n\t"
  "v_mfma_f32_16x16x32_bf16 v[92:95], a[132:135], a[244:247], v[92:95]\n\t"
  "ds_read_b128 a[156:159], v222 offset:896\n\t"
  "s_waitcnt lgkmcnt(9)\n\t"
  "v_mfma_f32_16x16x32_bf16 v[212:215], a[132:135], a[240:243], v[212:215]\n\t"
  "buffer_load_dwordx4 a[228:231], v247, s[20:23], s38 offen offset:2048 sc1\n\t"
  "v_mfma_f32_16x16x32_bf16 v[52:55], a[136:139], a[240:243], v[52:55]\n\t"
  "ds_read_b64_tr_b16 a[128:129], v252 offset:0\n\t"
  "s_add_u32 s38, s38, 0x40000\n\t"
  "v_mfma_f32_16x16x32_bf16 v[96:99], a[136:139], a[244:247], v[96:99]\n\t"
  "ds_read_b64_tr_b16 a[130:131], v252 offset:2048\n\t"
  "s_waitcnt lgkmcnt(7) vmcnt(11)\n\t"
  "v_mfma_f32_16x16x32_bf16 v[80:83], a[140:143], a[252:255], v[80:83]\n\t"
  "ds_read_b64_tr_b16 a[132:133], v253 offset:0\n\t"
  "v_mfma_f32_16x16x32_bf16 v[200:203], a[140:143], a[248:251], v[200:203]\n\t"
  "ds_read_b64_tr_b16 a[134:135], v253 offset:2048\n\t"
  "v_mfma_f32_16x16x32_bf16 v[204:207], a[144:147], a[248:251], v[204:207]\n\t"
  "ds_read_b64_tr_b16 a[136:137], v254 offset:0\n\t"
  "s_waitcnt lgkmcnt(6)\n\t"
  "v_mfma_f32_16x16x32_bf16 v[84:87], a[144:147], a[252:255], v[84:87]\n\t"
  "ds_read_b64_tr_b16 a[138:139], v254 offset:2048\n\t"
  "buffer_load_dwordx4 a[232:235], v246, s[20:23], s38 offen offset:2048 sc1\n\t"
  "v_mfma_f32_16x16x32_bf16 v[88:91], a[148:151], a[252:255], v[88:91]\n\t"
  "ds_read_b64_tr_b16 a[140:141], v255 offset:0\n\t"
  "v_mfma_f32_16x16x32_bf16 v[208:211], a[148:151], a[248:251], v[208:211]\n\t"
  "ds_read_b64_tr_b16 a[142:143], v255 offset:2048\n\t"
  "v_mfma_f32_16x16x32_bf16 v[212:215], a[152:155], a[248:251], v[212:215]\n\t"
  "ds_read_b128 a[144:147], v222 offset:1024\n\t"
  "s_waitcnt lgkmcnt(9)\n\t"
  "v_mfma_f32_16x16x32_bf16 v[92:95], a[152:155], a[252:255], v[92:95]\n\t"
  "buffer_load_dwordx4 a[236:239], v247, s[20:23], s38 offen offset:2048 sc1\n\t"
  "v_mfma_f32_16x16x32_bf16 v[96:99], a[156:159], a[252:255], v[96:99]\n\t"
  "ds_read_b64_tr_b16 a[148:149], v252 offset:8192\n\t"
  "s_add_u32 s38, s38, 0x40000\n\t"
  "v_mfma_f32_16x16x32_bf16 v[52:55], a[156:159], a[248:251], v[52:55]\n\t"
  "ds_read_b64_tr_b16 a[150:151], v252 offset:10240\n\t"
  "s_waitcnt lgkmcnt(7) vmcnt(11)\n\t"
  "v_mfma_f32_16x16x32_bf16 v[200:203], a[128:131], a[192:195], v[200:203]\n\t"
  "s_barrier\n\t"
  "ds_read_b64_tr_b16 a[152:153], v253 offset:8192\n\t"
  "v_mfma_f32_16x16x32_bf16 v[80:83], a[128:131], a[196:199], v[80:83]\n\t"
  "ds_read_b64_tr_b16 a[154:155], v253 offset:10240\n\t"
  "v_mfma_f32_16x16x32_bf16 v[84:87], a[132:135], a[196:199], v[84:87]\n\t"
  "ds_read_b64_tr_b16 a[156:157], v254 offset:8192\n\t"
  "s_waitcnt lgkmcnt(6)\n\t"
  "v_mfma_f32_16x16x32_bf16 v[204:207], a[132:135], a[192:195], v[204:207]\n\t"
  "ds_read_b64_tr_b16 a[158:159], v254 offset:10240\n\t"
  "buffer_load_dwordx4 a[240:243], v246, s[20:23], s38 offen offset:2048 sc1\n\t"
  "v_mfma_f32_16x16x32_bf16 v[208:211], a[136:139], a[192:195], v[208:211]\n\t"
  "ds_read_b64_tr_b16 a[128:129], v255 offset:8192\n\t"
  "v_mfma_f32_16x16x32_bf16 v[88:91], a[136:139], a[196:199], v[88:91]\n\t"
  "ds_read_b64_tr_b16 a[130:131], v255 offset:10240\n\t"
  "v_mfma_f32_16x16x32_bf16 v[92:95], a[140:143], a[196:199], v[92:95]\n\t"
  "ds_read_b128 a[132:135], v222 offset:1152\n\t"
  "s_waitcnt lgkmcnt(9)\n\t"
  "v_mfma_f32_16x16x32_bf16 v[212:215], a[140:143], a[192:195], v[212:215]\n\t"
  "buffer_load_dwordx4 a[244:247], v247, s[20:23], s38 offen offset:2048 sc1\n\t"
  "v_mfma_f32_16x16x32_bf16 v[52:55], a[144:147], a[192:195], v[52:55]\n\t"
  "ds_read_b64_tr_b16 a[136:137], v252 offset:16384\n\t"
  "s_add_u32 s38, s38, 0x40000\n\t"
  "v_mfma_f32_16x16x32_bf16 v[96:99], a[144:147], a[196:199], v[96:99]\n\t"
  "ds_read_b64_tr_b16 a[138:139], v252 offset:18432\n\t"
  "s_waitcnt lgkmcnt(7) vmcnt(11)\n\t"
  "v_mfma_f32_16x16x32_bf16 v[80:83], a[148:151], a[204:207], v[80:83]\n\t"
  "ds_read_b64_tr_b16 a[140:141], v253 offset:16384\n\t"
  "v_mfma_f32_16x16x32_bf16 v[200:203], a[148:151], a[200:203], v[200:203]\n\t"
  "ds_read_b64_tr_b16 a[142:143], v253 offset:18432\n\t"
  "v_mfma_f32_16x16x32_bf16 v[204:207], a[152:155], a[200:203], v[204:207]\n\t"
  "ds_read_b64_tr_b16 a[144:145], v254 offset:16384\n\t"
  "s_waitcnt lgkmcnt(6)\n\t"
  "v_mfma_f32_16x16x32_bf16 v[84:87], a[152:155], a[204:207], v[84:87]\n\t"
  "ds_read_b64_tr_b16 a[146:147], v254 offset:18432\n\t"
  "buffer_load_dwordx4 a[248:251], v246, s[20:23], s38 offen offset:2048 sc1\n\t"
  "v_mfma_f32_16x16x32_bf16 v[88:91], a[156:159], a[204:207], v[88:91]\n\t"
  "ds_read_b64_tr_b16 a[148:149], v255 offset:16384\n\t"
  "v_mfma_f32_16x16x32_bf16 v[208:211], a[156:159], a[200:203], v[208:211]\n\t"
  "ds_read_b64_tr_b16 a[150:151], v255 offset:18432\n\t"
  "v_mfma_f32_16x16x32_bf16 v[212:215], a[128:131], a[200:203], v[212:215]\n\t"
  "ds_read_b128 a[152:155], v222 offset:1280\n\t"
  "s_waitcnt lgkmcnt(9)\n\t"
  "v_mfma_f32_16x16x32_bf16 v[92:95], a[128:131], a[204:207], v[92:95]\n\t"
  "buffer_load_dwordx4 a[252:255], v247, s[20:23], s38 offen offset:2048 sc1\n\t"
  "v_mfma_f32_16x16x32_bf16 v[96:99], a[132:135], a[204:207], v[96:99]\n\t"
  "ds_read_b64_tr_b16 a[156:157], v252 offset:24576\n\t"
  "v_mfma_f32_16x16x32_bf16 v[52:55], a[132:135], a[200:203], v[52:55]\n\t"
  "ds_read_b64_tr_b16 a[158:159], v252 offset:26624\n\t"
  "s_waitcnt lgkmcnt(7) vmcnt(10)\n\t"
  "v_mfma_f32_16x16x32_bf16 v[200:203], a[136:139], a[208:211], v[200:203]\n\t"
  "ds_read_b64_tr_b16 a[128:129], v253 offset:24576\n\t"
  "s_add_u32 m0, s30, 86016\n\t"
  "v_mfma_f32_16x16x32_bf16 v[80:83], a[136:139], a[212:215], v[80:83]\n\t"
  "ds_read_b64_tr_b16 a[130:131], v253 offset:26624\n\t"
  "buffer_load_dwordx4 v1, s[20:23], s29 offen offset:0 sc1 lds\n\t"
  "v_mfma_f32_16x16x32_bf16 v[84:87], a[140:143], a[212:215], v[84:87]\n\t"
  "ds_read_b64_tr_b16 a[132:133], v254 offset:24576\n\t"
  "v_mfma_f32_16x16x32_bf16 v[204:207], a[140:143], a[208:211], v[204:207]\n\t"
  "ds_read_b64_tr_b16 a[134:135], v254 offset:26624\n\t"
  "s_waitcnt lgkmcnt(7)\n\t"
  "v_mfma_f32_16x16x32_bf16 v[208:211], a[144:147], a[208:211], v[208:211]\n\t"
  "ds_read_b64_tr_b16 a[136:137], v255 offset:24576\n\t"
  "v_mfma_f32_16x16x32_bf16 v[88:91], a[144:147], a[212:215], v[88:91]\n\t"
  "ds_read_b64_tr_b16 a[138:139], v255 offset:26624\n\t"
  "buffer_load_dwordx4 v1, s[20:23], s29 offen offset:1024 sc1 lds\n\t"
  "v_mfma_f32_16x16x32_bf16 v[92:95], a[148:151], a[212:215], v[92:95]\n\t"
  "ds_read_b128 a[140:143], v222 offset:1408\n\t"
  "v_mfma_f32_16x16x32_bf16 v[212:215], a[148:151], a[208:211], v[212:215]\n\t"
  "s_waitcnt lgkmcnt(9)\n\t"
  "v_mfma_f32_16x16x32_bf16 v[52:55], a[152:155], a[208:211], v[52:55]\n\t"
  "ds_read_b64_tr_b16 a[144:145], v232 offset:0\n\t"
  "s_waitcnt lgkmcnt(6) vmcnt(10)\n\t"
  "v_mfma_f32_16x16x32_bf16 v[96:99], a[152:155], a[212:215], v[96:99]\n\t"
  "ds_read_b64_tr_b16 a[146:147], v232 offset:2048\n\t"
  "buffer_load_dwordx4 v1, s[20:23], s29 offen offset:2048 sc1 lds\n\t"
  "v_mfma_f32_16x16x32_bf16 v[80:83], a[156:159], a[220:223], v[80:83]\n\t"
  "ds_read_b64_tr_b16 a[148:149], v233 offset:0\n\t"
  "v_mfma_f32_16x16x32_bf16 v[200:203], a[156:159], a[216:219], v[200:203]\n\t"
  "ds_read_b64_tr_b16 a[150:151], v233 offset:2048\n\t"
  "v_mfma_f32_16x16x32_bf16 v[204:207], a[128:131], a[216:219], v[204:207]\n\t"
  "ds_read_b64_tr_b16 a[152:153], v234 offset:0\n\t"
  "s_waitcnt lgkmcnt(6)\n\t"
  "v_mfma_f32_16x16x32_bf16 v[84:87], a[128:131], a[220:223], v[84:87]\n\t"
  "ds_read_b64_tr_b16 a[154:155], v234 offset:2048\n\t"
  "buffer_load_dwordx4 v1, s[20:23], s29 offen offset:3072 sc1 lds\n\t"
  "v_mfma_f32_16x16x32_bf16 v[88:91], a[132:135], a[220:223], v[88:91]\n\t"
  "ds_read_b64_tr_b16 a[156:157], v235 offset:0\n\t"
  "v_mfma_f32_16x16x32_bf16 v[208:211], a[132:135], a[216:219], v[208:211]\n\t"
  "ds_read_b64_tr_b16 a[158:159], v235 offset:2048\n\t"
  "v_mfma_f32_16x16x32_bf16 v[212:215], a[136:139], a[216:219], v[212:215]\n\t"
  "ds_read_b128 a[128:131], v222 offset:1536\n\t"
  "s_add_u32 m0, s30, 90112\n\t"
  "s_waitcnt lgkmcnt(9)\n\t"
  "v_mfma_f32_16x16x32_bf16 v[92:95], a[136:139], a[220:223], v[92:95]\n\t"
  "buffer_load_dwordx4 v2, s[20:23], s29 offen offset:0 sc1 lds\n\t"
  "v_mfma_f32_16x16x32_bf16 v[96:99], a[140:143], a[220:223], v[96:99]\n\t"
  "ds_read_b64_tr_b16 a[132:133], v232 offset:8192\n\t"
  "v_mfma_f32_16x16x32_bf16 v[52:55], a[140:143], a[216:219], v[52:55]\n\t"
  "ds_read_b64_tr_b16 a[134:135], v232 offset:10240\n\t"
  "s_waitcnt lgkmcnt(7) vmcnt(11)\n\t"
  "v_mfma_f32_16x16x32_bf16 v[200:203], a[144:147], a[224:227], v[200:203]\n\t"
  "ds_read_b64_tr_b16 a[136:137], v233 offset:8192\n\t"
  "v_mfma_f32_16x16x32_bf16 v[80:83], a[144:147], a[228:231], v[80:83]\n\t"
  "ds_read_b64_tr_b16 a[138:139], v233 offset:10240\n\t"
  "buffer_load_dwordx4 v2, s[20:23], s29 offen offset:1024 sc1 lds\n\t"
  "v_mfma_f32_16x16x32_bf16 v[84:87], a[148:151], a[228:231], v[84:87]\n\t"
  "ds_read_b64_tr_b16 a[140:141], v234 offset:8192\n\t"
  "v_mfma_f32_16x16x32_bf16 v[204:207], a[148:151], a[224:227], v[204:207]\n\t"
  "ds_read_b64_tr_b16 a[142:143], v234 offset:10240\n\t"
  "s_waitcnt lgkmcnt(7)\n\t"
  "v_mfma_f32_16x16x32_bf16 v[208:211], a[152:155], a[224:227], v[208:211]\n\t"
  "ds_read_b64_tr_b16 a[144:145], v235 offset:8192\n\t"
  "v_mfma_f32_16x16x32_bf16 v[88:91], a[152:155], a[228:231], v[88:91]\n\t"
  "ds_read_b64_tr_b16 a[146:147], v235 offset:10240\n\t"
  "buffer_load_dwordx4 v2, s[20:23], s29 offen offset:2048 sc1 lds\n\t"
  "v_mfma_f32_16x16x32_bf16 v[92:95], a[156:159], a[228:231], v[92:95]\n\t"
  "ds_read_b128 a[148:151], v222 offset:1664\n\t"
  "s_add_u32 s29, s29, 28672\n\t"
  "v_mfma_f32_16x16x32_bf16 v[212:215], a[156:159], a[224:227], v[212:215]\n\t"
  "s_waitcnt lgkmcnt(9)\n\t"
  "v_mfma_f32_16x16x32_bf16 v[52:55], a[128:131], a[224:227], v[52:55]\n\t"
  "ds_read_b64_tr_b16 a[152:153], v232 offset:16384\n\t"
  "s_add_u32 m0, s30, 57344\n\t"
  "s_waitcnt lgkmcnt(6) vmcnt(11)\n\t"
  "v_mfma_f32_16x16x32_bf16 v[96:99], a[128:131], a[228:231], v[96:99]\n\t"
  "ds_read_b64_tr_b16 a[154:155], v232 offset:18432\n\t"
  "buffer_load_dwordx4 v1, s[20:23], s29 offen offset:0 sc1 lds\n\t"
  "v_mfma_f32_16x16x32_bf16 v[80:83], a[132:135], a[236:239], v[80:83]\n\t"
  "ds_read_b64_tr_b16 a[156:157], v233 offset:16384\n\t"
  "v_mfma_f32_16x16x32_bf16 v[200:203], a[132:135], a[232:235], v[200:203]\n\t"
  "ds_read_b64_tr_b16 a[158:159], v233 offset:18432\n\t"
  "v_mfma_f32_16x16x32_bf16 v[204:207], a[136:139], a[232:235], v[204:207]\n\t"
  "ds_read_b64_tr_b16 a[128:129], v234 offset:16384\n\t"
  "s_waitcnt lgkmcnt(6)\n\t"
  "v_mfma_f32_16x16x32_bf16 v[84:87], a[136:139], a[236:239], v[84:87]\n\t"
  "ds_read_b64_tr_b16 a[130:131], v234 offset:18432\n\t"
  "buffer_load_dwordx4 v1, s[20:23], s29 offen offset:1024 sc1 lds\n\t"
  "v_mfma_f32_16x16x32_bf16 v[88:91], a[140:143], a[236:239], v[88:91]\n\t"
  "ds_read_b64_tr_b16 a[132:133], v235 offset:16384\n\t"
  "v_mfma_f32_16x16x32_bf16 v[208:211], a[140:143], a[232:235], v[208:211]\n\t"
  "ds_read_b64_tr_b16 a[134:135], v235 offset:18432\n\t"
  "v_mfma_f32_16x16x32_bf16 v[212:215], a[144:147], a[232:235], v[212:215]\n\t"
  "ds_read_b128 a[136:139], v222 offset:1792\n\t"
  "s_waitcnt lgkmcnt(9)\n\t"
  "v_mfma_f32_16x16x32_bf16 v[92:95], a[144:147], a[236:239], v[92:95]\n\t"
  "buffer_load_dwordx4 v1, s[20:23], s29 offen offset:2048 sc1 lds\n\t"
  "v_mfma_f32_16x16x32_bf16 v[96:99], a[148:151], a[236:239], v[96:99]\n\t"
  "ds_read_b64_tr_b16 a[140:141], v232 offset:24576\n\t"
  "v_mfma_f32_16x16x32_bf16 v[52:55], a[148:151], a[232:235], v[52:55]\n\t"
  "ds_read_b64_tr_b16 a[142:143], v232 offset:26624\n\t"
  "s_waitcnt lgkmcnt(7) vmcnt(12)\n\t"
  "v_mfma_f32_16x16x32_bf16 v[200:203], a[152:155], a[240:243], v[200:203]\n\t"
  "ds_read_b64_tr_b16 a[144:145], v233 offset:24576\n\t"
  "v_mfma_f32_16x16x32_bf16 v[80:83], a[152:155], a[244:247], v[80:83]\n\t"
  "ds_read_b64_tr_b16 a[146:147], v233 offset:26624\n\t"
  "buffer_load_dwordx4 v1, s[20:23], s29 offen offset:3072 sc1 lds\n\t"
  "v_mfma_f32_16x16x32_bf16 v[84:87], a[156:159], a[244:247], v[84:87]\n\t"
  "ds_read_b64_tr_b16 a[148:149], v234 offset:24576\n\t"
  "s_waitcnt lgkmcnt(6)\n\t"
  "v_not_b32 v24, s34\n\t"
  "v_mfma_f32_16x16x32_bf16 v[204:207], a[156:159], a[240:243], v[204:207]\n\t"
  "ds_read_b64_tr_b16 a[150:151], v234 offset:26624\n\t"
  "buffer_load_dwordx2 v[24:25], v9, s[20:23], 0 offen offset:0 sc1\n\t"
  "v_mfma_f32_16x16x32_bf16 v[208:211], a[128:131], a[240:243], v[208:211]\n\t"
  "ds_read_b64_tr_b16 a[152:153], v235 offset:24576\n\t"
  "s_add_u32 m0, s30, 61440\n\t"
  "v_mfma_f32_16x16x32_bf16 v[88:91], a[128:131], a[244:247], v[88:91]\n\t"
  "ds_read_b64_tr_b16 a[154:155], v235 offset:26624\n\t"
  "buffer_load_dwordx4 v2, s[20:23], s29 offen offset:0 sc1 lds\n\t"
  "v_mfma_f32_16x16x32_bf16 v[92:95], a[132:135], a[244:247], v[92:95]\n\t"
  "ds_read_b128 a[156:159], v222 offset:1920\n\t"
  "v_mfma_f32_16x16x32_bf16 v[212:215], a[132:135], a[240:243], v[212:215]\n\t"
  "s_waitcnt lgkmcnt(9)\n\t"
  "v_mfma_f32_16x16x32_bf16 v[52:55], a[136:139], a[240:243], v[52:55]\n\t"
  "s_waitcnt lgkmcnt(5) vmcnt(13)\n\t"
  "v_mfma_f32_16x16x32_bf16 v[96:99], a[136:139], a[244:247], v[96:99]\n\t"
  "buffer_load_dwordx4 v2, s[20:23], s29 offen offset:1024 sc1 lds\n\t"
  "v_mfma_f32_16x16x32_bf16 v[80:83], a[140:143], a[252:255], v[80:83]\n\t"
  "v_mfma_f32_16x16x32_bf16 v[200:203], a[140:143], a[248:251], v[200:203]\n\t"
  "v_mfma_f32_16x16x32_bf16 v[204:207], a[144:147], a[248:251], v[204:207]\n\t"
  "s_waitcnt lgkmcnt(1)\n\t"
  "v_mfma_f32_16x16x32_bf16 v[84:87], a[144:147], a[252:255], v[84:87]\n\t"
  "buffer_load_dwordx4 v2, s[20:23], s29 offen offset:2048 sc1 lds\n\t"
  "v_mfma_f32_16x16x32_bf16 v[88:91], a[148:151], a[252:255], v[88:91]\n\t"
  "s_add_u32 s29, s29, 28672\n\t"
  "v_mfma_f32_16x16x32_bf16 v[208:211], a[148:151], a[248:251], v[208:211]\n\t"
  "v_mfma_f32_16x16x32_bf16 v[212:215], a[152:155], a[248:251], v[212:215]\n\t"
  "v_mfma_f32_16x16x32_bf16 v[92:95], a[152:155], a[252:255], v[92:95]\n\t"
  "s_waitcnt lgkmcnt(0)\n\t"
  "v_mfma_f32_16x16x32_bf16 v[96:99], a[156:159], a[252:255], v[96:99]\n\t"
  "ds_write_b128 v248, v[80:83] offset:0\n\t"
  "v_mfma_f32_16x16x32_bf16 v[52:55], a[156:159], a[248:251], v[52:55]\n\t"
  "s_nop 2\n\t"
  "ds_write_b128 v248, v[84:87] offset:1024\n\t"
  "ds_write_b128 v248, v[88:91] offset:2048\n\t"
  "ds_write_b128 v248, v[92:95] offset:3072\n\t"
  "ds_write_b128 v248, v[96:99] offset:4096\n\t"
  "s_mov_b32 s72, 0x20000\n\t"
  "s_not_b32 s68, s34\n\t"
  "s_mov_b32 s73, 64\n\t"
  "s_branch LP3c_%=\n\t"
  "LP3a_%=:\n\t"
  "v_not_b32 v24, s34\n\t"
  "buffer_load_dwordx2 v[24:25], v9, s[20:23], 0 offen offset:0 sc1\n\t"
  "s_mov_b32 s73, 64\n\t"
  "LP3w_%=:\n\t"
  "s_sleep 1\n\t"
  "LP3c_%=:\n\t"
  "v_cmp_eq_u32 vcc, s68, v24\n\t"
  "s_nop 1\n\t"
  "s_cmp_eq_u64 vcc, 0\n\t"
  "v_cmp_ne_u64 vcc, s[34:35], v[24:25]\n\t"
  "s_cbranch_vccz LP3b_%=\n\t"
  "s_cbranch_scc1 LP3r_%=\n\t"
  "s_sub_u32 s73, s73, 1\n\t"
  "s_cbranch_scc0 LP3w_%=\n\t"
  "s_waitcnt vmcnt(0)\n\t"
  "LP3r_%=:\n\t"
  "s_sleep 2\n\t"
  "s_sub_u32 s72, s72, 1\n\t"
  "s_cbranch_scc0 LP3a_%=\n\t"
  "LP3b_%=:\n\t"
  "buffer_load_dwordx4 a[0:3], v0, s[20:23], s28 offen offset:0 sc1\n\t"
  "buffer_load_dwordx4 a[4:7], v0, s[20:23], s28 offen offset:1024 sc1\n\t"
  "s_add_u32 s28, s28, 131072\n\t"
  "buffer_load_dwordx4 a[16:19], v0, s[20:23], s28 offen offset:0 sc1\n\t"
  "buffer_load_dwordx4 a[20:23], v0, s[20:23], s28 offen offset:1024 sc1\n\t"
  "s_add_u32 s28, s28, 131072\n\t"
  "buffer_load_dwordx4 a[32:35], v0, s[20:23], s28 offen offset:0 sc1\n\t"
  "buffer_load_dwordx4 a[36:39], v0, s[20:23], s28 offen offset:1024 sc1\n\t"
  "s_add_u32 s28, s28, 131072\n\t"
  "s_waitcnt lgkmcnt(0)\n\t"
  "s_barrier\n\t"
  "ds_read_b128 v[128:131], v249 offset:4096\n\t"
  "ds_read_b128 v[112:115], v249 offset:0\n\t"
  "ds_read_b128 v[116:119], v249 offset:1024\n\t"
  "ds_read_b128 v[120:123], v249 offset:2048\n\t"
  "ds_read_b128 v[124:127], v249 offset:3072\n\t"
  "s_waitcnt lgkmcnt(0)\n\t"
  "v_pk_add_f32 v[52:53], v[52:53], v[128:129]\n\t"
  "v_add_f32 v52, v52, v53\n\t"
  "v_pk_add_f32 v[200:201], v[200:201], v[112:113]\n\t"
  "v_pk_add_f32 v[202:203], v[202:203], v[114:115]\n\t"
  "v_pk_add_f32 v[204:205], v[204:205], v[116:117]\n\t"
  "v_pk_add_f32 v[206:207], v[206:207], v[118:119]\n\t"
  "v_pk_add_f32 v[200:201], v[200:201], v[52:53] op_sel_hi:[1,0] neg_lo:[0,1] neg_hi:[0,1]\n\t"
  "v_pk_add_f32 v[202:203], v[202:203], v[52:53] op_sel_hi:[1,0] neg_lo:[0,1] neg_hi:[0,1]\n\t"
  "v_pk_add_f32 v[204:205], v[204:205], v[52:53] op_sel_hi:[1,0] neg_lo:[0,1] neg_hi:[0,1]\n\t"
  "v_pk_add_f32 v[206:207], v[206:207], v[52:53] op_sel_hi:[1,0] neg_lo:[0,1] neg_hi:[0,1]\n\t"
  "v_cvt_pk_bf16_f32 v132, v200, v201\n\t"
  "v_cvt_pk_bf16_f32 v133, v202, v203\n\t"
  "v_cvt_pk_bf16_f32 v134, v204, v205\n\t"
  "v_cvt_pk_bf16_f32 v135, v206, v207\n\t"
  "v_pk_add_f32 v[208:209], v[208:209], v[120:121]\n\t"
  "v_pk_add_f32 v[210:211], v[210:211], v[122:123]\n\t"
  "v_pk_add_f32 v[212:213], v[212:213], v[124:125]\n\t"
  "v_pk_add_f32 v[214:215], v[214:215], v[126:127]\n\t"
  "v_pk_add_f32 v[208:209], v[208:209], v[52:53] op_sel_hi:[1,0] neg_lo:[0,1] neg_hi:[0,1]\n\t"
  "v_pk_add_f32 v[210:211], v[210:211], v[52:53] op_sel_hi:[1,0] neg_lo:[0,1] neg_hi:[0,1]\n\t"
  "v_pk_add_f32 v[212:213], v[212:213], v[52:53] op_sel_hi:[1,0] neg_lo:[0,1] neg_hi:[0,1]\n\t"
  "v_pk_add_f32 v[214:215], v[214:215], v[52:53] op_sel_hi:[1,0] neg_lo:[0,1] neg_hi:[0,1]\n\t"
  "v_cvt_pk_bf16_f32 v136, v208, v209\n\t"
  "v_cvt_pk_bf16_f32 v137, v210, v211\n\t"
  "v_cvt_pk_bf16_f32 v138, v212, v213\n\t"
  "v_cvt_pk_bf16_f32 v139, v214, v215\n\t"
  "buffer_store_dwordx4 v[132:135], v223, s[20:23], 0 offen offset:2048 sc1\n\t"
  "buffer_store_dwordx4 v[136:139], v223, s[20:23], s59 offen offset:2048 sc1\n\t"
  "v_pk_mul_f32 v[112:113], v[200:201], v[200:201]\n\t"
  "v_pk_mul_f32 v[114:115], v[202:203], v[202:203]\n\t"
  "v_pk_fma_f32 v[112:113], v[204:205], v[204:205], v[112:113]\n\t"
  "v_pk_fma_f32 v[114:115], v[206:207], v[206:207], v[114:115]\n\t"
  "v_pk_fma_f32 v[112:113], v[208:209], v[208:209], v[112:113]\n\t"
  "v_pk_fma_f32 v[114:115], v[210:211], v[210:211], v[114:115]\n\t"
  "v_pk_fma_f32 v[112:113], v[212:213], v[212:213], v[112:113]\n\t"
  "v_pk_fma_f32 v[114:115], v[214:215], v[214:215], v[114:115]\n\t"
  "v_pk_add_f32 v[112:113], v[112:113], v[114:115]\n\t"
  "v_add_f32 v52, v52, v217\n\t"
  "v_add_f32 v112, v112, v113\n\t"
  "v_mov_b32 v113, v112\n\t"
  "v_accvgpr_write_b32 a168, v132\n\t"
  "v_accvgpr_write_b32 a169, v133\n\t"
  "v_permlane16_swap_b32 v112, v113\n\t"
  "v_accvgpr_write_b32 a170, v134\n\t"
  "v_accvgpr_write_b32 a171, v135\n\t"
  "v_add_f32 v112, v112, v113\n\t"
  "v_mov_b32 v113, v112\n\t"
  "v_accvgpr_write_b32 a172, v136\n\t"
  "v_accvgpr_write_b32 a173, v137\n\t"
  "v_permlane32_swap_b32 v112, v113\n\t"
  "v_accvgpr_write_b32 a174, v138\n\t"
  "v_accvgpr_write_b32 a175, v139\n\t"
  "v_add_f32 v112, v112, v113\n\t"
  "buffer_store_dword v112, v27, s[20:23], 0 offen offset:2048 sc1\n\t"
  "v_accvgpr_write_b32 a177, v52\n\t"
  "s_waitcnt vmcnt(17)\n\t"
  "s_barrier\n\t"
  "ds_read_b128 a[128:131], v4 offset:28672\n\t"
  "ds_read_b128 a[132:135], v4 offset:29696\n\t"
  "ds_read_b128 a[136:139], v4 offset:30720\n\t"
  "ds_read_b128 a[140:143], v4 offset:31744\n\t"
  "ds_read_b128 a[144:147], v4 offset:32768\n\t"
  "ds_read_b128 a[148:151], v4 offset:33792\n\t"
  "s_waitcnt lgkmcnt(4) vmcnt(7)\n\t"
  "v_mfma_f32_16x16x32_bf16 v[32:35], a[128:131], a[0:3], 0\n\t"
  "ds_read_b128 a[152:155], v4 offset:34816\n\t"
  "v_mfma_f32_16x16x32_bf16 v[36:39], a[128:131], a[4:7], 0\n\t"
  "buffer_load_dwordx4 a[48:51], v0, s[20:23], s28 offen offset:0 sc1\n\t"
  "v_mfma_f32_16x16x32_bf16 v[52:55], a[132:135], a[4:7], 0\n\t"
  "ds_read_b128 a[156:159], v4 offset:35840\n\t"
  "v_mfma_f32_16x16x32_bf16 v[48:51], a[132:135], a[0:3], 0\n\t"
  "s_waitcnt lgkmcnt(4)\n\t"
  "v_mfma_f32_16x16x32_bf16 v[64:67], a[136:139], a[0:3], 0\n\t"
  "ds_read_b128 a[128:131], v4 offset:36864\n\t"
  "v_mfma_f32_16x16x32_bf16 v[68:71], a[136:139], a[4:7], 0\n\t"
  "v_mfma_f32_16x16x32_bf16 v[84:87], a[140:143], a[4:7], 0\n\t"
  "ds_read_b128 a[132:135], v4 offset:37888\n\t"
  "s_waitcnt lgkmcnt(4)\n\t"
  "v_mfma_f32_16x16x32_bf16 v[80:83], a[140:143], a[0:3], 0\n\t"
  "buffer_load_dwordx4 a[52:55], v0, s[20:23], s28 offen offset:1024 sc1\n\t"
  "v_mfma_f32_16x16x32_bf16 v[96:99], a[144:147], a[0:3], 0\n\t"
  "ds_read_b128 a[136:139], v4 offset:38912\n\t"
  "s_add_u32 s28, s28, 131072\n\t"
  "v_mfma_f32_16x16x32_bf16 v[100:103], a[144:147], a[4:7], 0\n\t"
  "v_mfma_f32_16x16x32_bf16 v[116:119], a[148:151], a[4:7], 0\n\t"
  "ds_read_b128 a[140:143], v4 offset:39936\n\t"
  "v_mfma_f32_16x16x32_bf16 v[112:115], a[148:151], a[0:3], 0\n\t"
  "s_waitcnt lgkmcnt(4)\n\t"
  "v_mfma_f32_16x16x32_bf16 v[128:131], a[152:155], a[0:3], 0\n\t"
  "ds_read_b128 a[144:147], v4 offset:40960\n\t"
  "s_add_u32 m0, s30, 0\n\t"
  "v_mfma_f32_16x16x32_bf16 v[132:135], a[152:155], a[4:7], 0\n\t"
  "buffer_load_dwordx4 v1, s[20:23], s29 offen offset:0 sc1 lds\n\t"
  "v_mfma_f32_16x16x32_bf16 v[148:151], a[156:159], a[4:7], 0\n\t"
  "ds_read_b128 a[148:151], v4 offset:41984\n\t"
  "v_mfma_f32_16x16x32_bf16 v[144:147], a[156:159], a[0:3], 0\n\t"
  "s_waitcnt lgkmcnt(4)\n\t"
  "v_mfma_f32_16x16x32_bf16 v[160:163], a[128:131], a[0:3], 0\n\t"
  "ds_read_b128 a[152:155], v4 offset:43008\n\t"
  "v_mfma_f32_16x16x32_bf16 v[164:167], a[128:131], a[4:7], 0\n\t"
  "v_mfma_f32_16x16x32_bf16 v[180:183], a[132:135], a[4:7], 0\n\t"
  "ds_read_b128 a[156:159], v4 offset:44032\n\t"
  "s_waitcnt lgkmcnt(4)\n\t"
  "v_mfma_f32_16x16x32_bf16 v[176:179], a[132:135], a[0:3], 0\n\t"
  "buffer_load_dwordx4 v1, s[20:23], s29 offen offset:1024 sc1 lds\n\t"
  "v_mfma_f32_16x16x32_bf16 v[192:195], a[136:139], a[0:3], 0\n\t"
  "ds_read_b128 a[128:131], v4 offset:45056\n\t"
  "v_mfma_f32_16x16x32_bf16 v[196:199], a[136:139], a[4:7], 0\n\t"
  "v_mfma_f32_16x16x32_bf16 v[212:215], a[140:143], a[4:7], 0\n\t"
  "ds_read_b128 a[132:135], v4 offset:46080\n\t"
  "v_mfma_f32_16x16x32_bf16 v[208:211], a[140:143], a[0:3], 0\n\t"
  "s_waitcnt lgkmcnt(4)\n\t"
  "v_mfma_f32_16x16x32_bf16 v[224:227], a[144:147], a[0:3], 0\n\t"
  "ds_read_b128 a[136:139], v4 offset:47104\n\t"
  "v_mfma_f32_16x16x32_bf16 v[228:231], a[144:147], a[4:7], 0\n\t"
  "buffer_load_dwordx4 v1, s[20:23], s29 offen offset:2048 sc1 lds\n\t"
  "v_mfma_f32_16x16x32_bf16 v[244:247], a[148:151], a[4:7], 0\n\t"
  "ds_read_b128 a[140:143], v4 offset:48128\n\t"
  "v_mfma_f32_16x16x32_bf16 v[240:243], a[148:151], a[0:3], 0\n\t"
  "s_waitcnt lgkmcnt(4) vmcnt(10)\n\t"
  "v_mfma_f32_16x16x32_bf16 v[32:35], a[152:155], a[16:19], v[32:35]\n\t"
  "ds_read_b128 a[144:147], v4 offset:49152\n\t"
  "v_mfma_f32_16x16x32_bf16 v[36:39], a[152:155], a[20:23], v[36:39]\n\t"
  "v_mfma_f32_16x16x32_bf16 v[52:55], a[156:159], a[20:23], v[52:55]\n\t"
  "ds_read_b128 a[148:151], v4 offset:50176\n\t"
  "s_waitcnt lgkmcnt(4)\n\t"
  "v_mfma_f32_16x16x32_bf16 v[48:51], a[156:159], a[16:19], v[48:51]\n\t"
  "buffer_load_dwordx4 v1, s[20:23], s29 offen offset:3072 sc1 lds\n\t"
  "v_mfma_f32_16x16x32_bf16 v[64:67], a[128:131], a[16:19], v[64:67]\n\t"
  "ds_read_b128 a[152:155], v4 offset:51200\n\t"
  "v_mfma_f32_16x16x32_bf16 v[68:71], a[128:131], a[20:23], v[68:71]\n\t"
  "v_mfma_f32_16x16x32_bf16 v[84:87], a[132:135], a[20:23], v[84:87]\n\t"
  "ds_read_b128 a[156:159], v4 offset:52224\n\t"
  "v_mfma_f32_16x16x32_bf16 v[80:83], a[132:135], a[16:19], v[80:83]\n\t"
  "s_waitcnt lgkmcnt(4)\n\t"
  "v_mfma_f32_16x16x32_bf16 v[96:99], a[136:139], a[16:19], v[96:99]\n\t"
  "ds_read_b128 a[128:131], v4 offset:53248\n\t"
  "s_add_u32 m0, s30, 4096\n\t"
  "v_mfma_f32_16x16x32_bf16 v[100:103], a[136:139], a[20:23], v[100:103]\n\t"
  "buffer_load_dwordx4 v2, s[20:23], s29 offen offset:0 sc1 lds\n\t"
  "v_mfma_f32_16x16x32_bf16 v[116:119], a[140:143], a[20:23], v[116:119]\n\t"
  "ds_read_b128 a[132:135], v4 offset:54272\n\t"
  "v_mfma_f32_16x16x32_bf16 v[112:115], a[140:143], a[16:19], v[112:115]\n\t"
  "s_waitcnt lgkmcnt(4)\n\t"
  "v_mfma_f32_16x16x32_bf16 v[128:131], a[144:147], a[16:19], v[128:131]\n\t"
  "ds_read_b128 a[136:139], v4 offset:55296\n\t"
  "v_mfma_f32_16x16x32_bf16 v[132:135], a[144:147], a[20:23], v[132:135]\n\t"
  "s_barrier\n\t"
  "v_mfma_f32_16x16x32_bf16 v[148:151], a[148:151], a[20:23], v[148:151]\n\t"
  "ds_read_b128 a[140:143], v4 offset:56320\n\t"
  "s_waitcnt vmcnt(7)\n\t"
  "buffer_store_dwordx2 v[16:17], v8, s[20:23], 0 offen offset:8 sc1\n\t"
  "v_mfma_f32_16x16x32_bf16 v[144:147], a[148:151], a[16:19], v[144:147]\n\t"
  "s_waitcnt lgkmcnt(4)\n\t"
  "v_mfma_f32_16x16x32_bf16 v[160:163], a[152:155], a[16:19], v[160:163]\n\t"
  "ds_read_b128 a[144:147], v4 offset:0\n\t"
  "v_mfma_f32_16x16x32_bf16 v[164:167], a[152:155], a[20:23], v[164:167]\n\t"
  "buffer_load_dwordx4 v2, s[20:23], s29 offen offset:1024 sc1 lds\n\t"
  "v_mfma_f32_16x16x32_bf16 v[180:183], a[156:159], a[20:23], v[180:183]\n\t"
  "ds_read_b128 a[148:151], v4 offset:1024\n\t"
  "s_waitcnt lgkmcnt(4)\n\t"
  "v_mfma_f32_16x16x32_bf16 v[176:179], a[156:159], a[16:19], v[176:179]\n\t"
  "buffer_load_dwordx4 v2, s[20:23], s29 offen offset:2048 sc1 lds\n\t"
  "v_mfma_f32_16x16x32_bf16 v[192:195], a[128:131], a[16:19], v[192:195]\n\t"
  "ds_read_b128 a[152:155], v4 offset:2048\n\t"
  "s_add_u32 s29, s29, 28672\n\t"
  "v_mfma_f32_16x16x32_bf16 v[196:199], a[128:131], a[20:23], v[196:199]\n\t"
  "v_mfma_f32_16x16x32_bf16 v[212:215], a[132:135], a[20:23], v[212:215]\n\t"
  "ds_read_b128 a[156:159], v4 offset:3072\n\t"
  "v_mfma_f32_16x16x32_bf16 v[208:211], a[132:135], a[16:19], v[208:211]\n\t"
  "s_waitcnt lgkmcnt(4)\n\t"
  "v_mfma_f32_16x16x32_bf16 v[224:227], a[136:139], a[16:19], v[224:227]\n\t"
  "ds_read_b128 a[128:131], v4 offset:4096\n\t"
  "v_mfma_f32_16x16x32_bf16 v[228:231], a[136:139], a[20:23], v[228:231]\n\t"
  "v_mfma_f32_16x16x32_bf16 v[244:247], a[140:143], a[20:23], v[244:247]\n\t"
  "ds_read_b128 a[132:135], v4 offset:5120\n\t"
  "v_mfma_f32_16x16x32_bf16 v[240:243], a[140:143], a[16:19], v[240:243]\n\t"
  "s_waitcnt lgkmcnt(4)\n\t"
  "v_mfma_f32_16x16x32_bf16 v[32:35], a[144:147], a[32:35], v[32:35]\n\t"
  "ds_read_b128 a[136:139], v4 offset:6144\n\t"
  "v_mfma_f32_16x16x32_bf16 v[36:39], a[144:147], a[36:39], v[36:39]\n\t"
  "buffer_load_dwordx4 a[64:67], v0, s[20:23], s28 offen offset:0 sc1\n\t"
  "v_mfma_f32_16x16x32_bf16 v[52:55], a[148:151], a[36:39], v[52:55]\n\t"
  "ds_read_b128 a[140:143], v4 offset:7168\n\t"
  "s_waitcnt lgkmcnt(4)\n\t"
  "v_mfma_f32_16x16x32_bf16 v[48:51], a[148:151], a[32:35], v[48:51]\n\t"
  "buffer_load_dwordx4 a[68:71], v0, s[20:23], s28 offen offset:1024 sc1\n\t"
  "v_mfma_f32_16x16x32_bf16 v[64:67], a[152:155], a[32:35], v[64:67]\n\t"
  "ds_read_b128 a[144:147], v4 offset:8192\n\t"
  "s_add_u32 s28, s28, 131072\n\t"
  "v_mfma_f32_16x16x32_bf16 v[68:71], a[152:155], a[36:39], v[68:71]\n\t"
  "v_mfma_f32_16x16x32_bf16 v[84:87], a[156:159], a[36:39], v[84:87]\n\t"
  "ds_read_b128 a[148:151], v4 offset:9216\n\t"
  "v_mfma_f32_16x16x32_bf16 v[80:83], a[156:159], a[32:35], v[80:83]\n\t"
  "s_waitcnt lgkmcnt(4)\n\t"
  "v_mfma_f32_16x16x32_bf16 v[96:99], a[128:131], a[32:35], v[96:99]\n\t"
  "ds_read_b128 a[152:155], v4 offset:10240\n\t"
  "v_mfma_f32_16x16x32_bf16 v[100:103], a[128:131], a[36:39], v[100:103]\n\t"
  "v_mfma_f32_16x16x32_bf16 v[116:119], a[132:135], a[36:39], v[116:119]\n\t"
  "ds_read_b128 a[156:159], v4 offset:11264\n\t"
  "v_mfma_f32_16x16x32_bf16 v[112:115], a[132:135], a[32:35], v[112:115]\n\t"
  "s_waitcnt lgkmcnt(4)\n\t"
  "v_mfma_f32_16x16x32_bf16 v[128:131], a[136:139], a[32:35], v[128:131]\n\t"
  "ds_read_b128 a[128:131], v4 offset:12288\n\t"
  "v_mfma_f32_16x16x32_bf16 v[132:135], a[136:139], a[36:39], v[132:135]\n\t"
  "buffer_load_dwordx4 a[80:83], v0, s[20:23], s28 offen offset:0 sc1\n\t"
  "v_mfma_f32_16x16x32_bf16 v[148:151], a[140:143], a[36:39], v[148:151]\n\t"
  "ds_read_b128 a[132:135], v4 offset:13312\n\t"
  "v_mfma_f32_16x16x32_bf16 v[144:147], a[140:143], a[32:35], v[144:147]\n\t"
  "s_waitcnt lgkmcnt(4)\n\t"
  "v_mfma_f32_16x16x32_bf16 v[160:163], a[144:147], a[32:35], v[160:163]\n\t"
  "ds_read_b128 a[136:139], v4 offset:14336\n\t"
  "v_mfma_f32_16x16x32_bf16 v[164:167], a[144:147], a[36:39], v[164:167]\n\t"
  "v_mfma_f32_16x16x32_bf16 v[180:183], a[148:151], a[36:39], v[180:183]\n\t"
  "ds_read_b128 a[140:143], v4 offset:15360\n\t"
  "s_waitcnt lgkmcnt(4)\n\t"
  "v_mfma_f32_16x16x32_bf16 v[176:179], a[148:151], a[32:35], v[176:179]\n\t"
  "buffer_load_dwordx4 a[84:87], v0, s[20:23], s28 offen offset:1024 sc1\n\t"
  "v_mfma_f32_16x16x32_bf16 v[192:195], a[152:155], a[32:35], v[192:195]\n\t"
  "ds_read_b128 a[144:147], v4 offset:16384\n\t"
  "s_add_u32 s28, s28, 131072\n\t"
  "v_mfma_f32_16x16x32_bf16 v[196:199], a[152:155], a[36:39], v[196:199]\n\t"
  "v_mfma_f32_16x16x32_bf16 v[212:215], a[156:159], a[36:39], v[212:215]\n\t"
  "ds_read_b128 a[148:151], v4 offset:17408\n\t"
  "v_mfma_f32_16x16x32_bf16 v[208:211], a[156:159], a[32:35], v[208:211]\n\t"
  "s_waitcnt lgkmcnt(4)\n\t"
  "v_mfma_f32_16x16x32_bf16 v[224:227], a[128:131], a[32:35], v[224:227]\n\t"
  "ds_read_b128 a[152:155], v4 offset:18432\n\t"
  "v_mfma_f32_16x16x32_bf16 v[228:231], a[128:131], a[36:39], v[228:231]\n\t"
  "v_mfma_f32_16x16x32_bf16 v[244:247], a[132:135], a[36:39], v[244:247]\n\t"
  "ds_read_b128 a[156:159], v4 offset:19456\n\t"
  "v_mfma_f32_16x16x32_bf16 v[240:243], a[132:135], a[32:35], v[240:243]\n\t"
  "s_waitcnt lgkmcnt(4) vmcnt(12)\n\t"
  "v_mfma_f32_16x16x32_bf16 v[32:35], a[136:139], a[48:51], v[32:35]\n\t"
  "ds_read_b128 a[128:131], v4 offset:20480\n\t"
  "v_mfma_f32_16x16x32_bf16 v[36:39], a[136:139], a[52:55], v[36:39]\n\t"
  "v_mfma_f32_16x16x32_bf16 v[52:55], a[140:143], a[52:55], v[52:55]\n\t"
  "ds_read_b128 a[132:135], v4 offset:21504\n\t"
  "v_mfma_f32_16x16x32_bf16 v[48:51], a[140:143], a[48:51], v[48:51]\n\t"
  "s_waitcnt lgkmcnt(4)\n\t"
  "v_mfma_f32_16x16x32_bf16 v[64:67], a[144:147], a[48:51], v[64:67]\n\t"
  "ds_read_b128 a[136:139], v4 offset:22528\n\t"
  "v_mfma_f32_16x16x32_bf16 v[68:71], a[144:147], a[52:55], v[68:71]\n\t"
  "v_mfma_f32_16x16x32_bf16 v[84:87], a[148:151], a[52:55], v[84:87]\n\t"
  "ds_read_b128 a[140:143], v4 offset:23552\n\t"
  "v_mfma_f32_16x16x32_bf16 v[80:83], a[148:151], a[48:51], v[80:83]\n\t"
  "s_waitcnt lgkmcnt(4)\n\t"
  "v_mfma_f32_16x16x32_bf16 v[96:99], a[152:155], a[48:51], v[96:99]\n\t"
  "ds_read_b128 a[144:147], v4 offset:24576\n\t"
  "v_mfma_f32_16x16x32_bf16 v[100:103], a[152:155], a[52:55], v[100:103]\n\t"
  "v_mfma_f32_16x16x32_bf16 v[116:119], a[156:159], a[52:55], v[116:119]\n\t"
  "ds_read_b128 a[148:151], v4 offset:25600\n\t"
  "v_mfma_f32_16x16x32_bf16 v[112:115], a[156:159], a[48:51], v[112:115]\n\t"
  "s_waitcnt lgkmcnt(4)\n\t"
  "v_mfma_f32_16x16x32_bf16 v[128:131], a[128:131], a[48:51], v[128:131]\n\t"
  "ds_read_b128 a[152:155], v4 offset:26624\n\t"
  "s_waitcnt vmcnt(4)\n\t"
  "v_mfma_f32_16x16x32_bf16 v[132:135], a[128:131], a[52:55], v[132:135]\n\t"
  "s_barrier\n\t"
  "v_mfma_f32_16x16x32_bf16 v[148:151], a[132:135], a[52:55], v[148:151]\n\t"
  "ds_read_b128 a[156:159], v4 offset:27648\n\t"
  "v_mfma_f32_16x16x32_bf16 v[144:147], a[132:135], a[48:51], v[144:147]\n\t"
  "s_waitcnt lgkmcnt(4)\n\t"
  "v_mfma_f32_16x16x32_bf16 v[160:163], a[136:139], a[48:51], v[160:163]\n\t"
  "ds_read_b128 a[128:131], v3 offset:0\n\t"
  "v_mfma_f32_16x16x32_bf16 v[164:167], a[136:139], a[52:55], v[164:167]\n\t"
  "v_mfma_f32_16x16x32_bf16 v[180:183], a[140:143], a[52:55], v[180:183]\n\t"
  "ds_read_b128 a[132:135], v3 offset:1024\n\t"
  "v_mfma_f32_16x16x32_bf16 v[176:179], a[140:143], a[48:51], v[176:179]\n\t"
  "s_waitcnt lgkmcnt(4)\n\t"
  "v_mfma_f32_16x16x32_bf16 v[192:195], a[144:147], a[48:51], v[192:195]\n\t"
  "ds_read_b128 a[136:139], v3 offset:2048\n\t"
  "v_mfma_f32_16x16x32_bf16 v[196:199], a[144:147], a[52:55], v[196:199]\n\t"
  "v_mfma_f32_16x16x32_bf16 v[212:215], a[148:151], a[52:55], v[212:215]\n\t"
  "ds_read_b128 a[140:143], v3 offset:3072\n\t"
  "v_mfma_f32_16x16x32_bf16 v[208:211], a[148:151], a[48:51], v[208:211]\n\t"
  "s_waitcnt lgkmcnt(4)\n\t"
  "v_mfma_f32_16x16x32_bf16 v[224:227], a[152:155], a[48:51], v[224:227]\n\t"
  "ds_read_b128 a[144:147], v3 offset:4096\n\t"
  "v_mfma_f32_16x16x32_bf16 v[228:231], a[152:155], a[52:55], v[228:231]\n\t"
  "v_mfma_f32_16x16x32_bf16 v[244:247], a[156:159], a[52:55], v[244:247]\n\t"
  "ds_read_b128 a[148:151], v3 offset:5120\n\t"
  "v_mfma_f32_16x16x32_bf16 v[240:243], a[156:159], a[48:51], v[240:243]\n\t"
  "s_waitcnt lgkmcnt(4) vmcnt(2)\n\t"
  "v_mfma_f32_16x16x32_bf16 v[32:35], a[128:131], a[64:67], v[32:35]\n\t"
  "ds_read_b128 a[152:155], v3 offset:6144\n\t"
  "v_mfma_f32_16x16x32_bf16 v[36:39], a[128:131], a[68:71], v[36:39]\n\t"
  "v_mfma_f32_16x16x32_bf16 v[52:55], a[132:135], a[68:71], v[52:55]\n\t"
  "ds_read_b128 a[156:159], v3 offset:7168\n\t"
  "v_mfma_f32_16x16x32_bf16 v[48:51], a[132:135], a[64:67], v[48:51]\n\t"
  "s_waitcnt lgkmcnt(4)\n\t"
  "v_mfma_f32_16x16x32_bf16 v[64:67], a[136:139], a[64:67], v[64:67]\n\t"
  "ds_read_b128 a[128:131], v3 offset:8192\n\t"
  "v_not_b32 v24, s34\n\t"
  "buffer_load_dwordx2 v[24:25], v9, s[20:23], 0 offen offset:8 sc1\n\t"
  "v_mfma_f32_16x16x32_bf16 v[68:71], a[136:139], a[68:71], v[68:71]\n\t"
  "v_mfma_f32_16x16x32_bf16 v[84:87], a[140:143], a[68:71], v[84:87]\n\t"
  "ds_read_b128 a[132:135], v3 offset:9216\n\t"
  "v_mfma_f32_16x16x32_bf16 v[80:83], a[140:143], a[64:67], v[80:83]\n\t"
  "s_waitcnt lgkmcnt(4)\n\t"
  "v_mfma_f32_16x16x32_bf16 v[96:99], a[144:147], a[64:67], v[96:99]\n\t"
  "ds_read_b128 a[136:139], v3 offset:10240\n\t"
  "v_mfma_f32_16x16x32_bf16 v[100:103], a[144:147], a[68:71], v[100:103]\n\t"
  "v_mfma_f32_16x16x32_bf16 v[116:119], a[148:151], a[68:71], v[116:119]\n\t"
  "ds_read_b128 a[140:143], v3 offset:11264\n\t"
  "v_mfma_f32_16x16x32_bf16 v[112:115], a[148:151], a[64:67], v[112:115]\n\t"
  "s_waitcnt lgkmcnt(4)\n\t"
  "v_mfma_f32_16x16x32_bf16 v[128:131], a[152:155], a[64:67], v[128:131]\n\t"
  "ds_read_b128 a[144:147], v3 offset:12288\n\t"
  "v_mfma_f32_16x16x32_bf16 v[132:135], a[152:155], a[68:71], v[132:135]\n\t"
  "v_mfma_f32_16x16x32_bf16 v[148:151], a[156:159], a[68:71], v[148:151]\n\t"
  "ds_read_b128 a[148:151], v3 offset:13312\n\t"
  "v_mfma_f32_16x16x32_bf16 v[144:147], a[156:159], a[64:67], v[144:147]\n\t"
  "s_waitcnt lgkmcnt(4)\n\t"
  "v_mfma_f32_16x16x32_bf16 v[160:163], a[128:131], a[64:67], v[160:163]\n\t"
  "ds_read_b128 a[152:155], v3 offset:14336\n\t"
  "v_mfma_f32_16x16x32_bf16 v[164:167], a[128:131], a[68:71], v[164:167]\n\t"
  "v_mfma_f32_16x16x32_bf16 v[180:183], a[132:135], a[68:71], v[180:183]\n\t"
  "ds_read_b128 a[156:159], v3 offset:15360\n\t"
  "v_mfma_f32_16x16x32_bf16 v[176:179], a[132:135], a[64:67], v[176:179]\n\t"
  "s_waitcnt lgkmcnt(4)\n\t"
  "v_mfma_f32_16x16x32_bf16 v[192:195], a[136:139], a[64:67], v[192:195]\n\t"
  "ds_read_b128 a[128:131], v3 offset:16384\n\t"
  "v_mfma_f32_16x16x32_bf16 v[196:199], a[136:139], a[68:71], v[196:199]\n\t"
  "v_mfma_f32_16x16x32_bf16 v[212:215], a[140:143], a[68:71], v[212:215]\n\t"
  "ds_read_b128 a[132:135], v3 offset:17408\n\t"
  "v_mfma_f32_16x16x32_bf16 v[208:211], a[140:143], a[64:67], v[208:211]\n\t"
  "s_waitcnt lgkmcnt(4)\n\t"
  "v_mfma_f32_16x16x32_bf16 v[224:227], a[144:147], a[64:67], v[224:227]\n\t"
  "ds_read_b128 a[136:139], v3 offset:18432\n\t"
  "v_mfma_f32_16x16x32_bf16 v[228:231], a[144:147], a[68:71], v[228:231]\n\t"
  "v_mfma_f32_16x16x32_bf16 v[244:247], a[148:151], a[68:71], v[244:247]\n\t"
  "ds_read_b128 a[140:143], v3 offset:19456\n\t"
  "v_mfma_f32_16x16x32_bf16 v[240:243], a[148:151], a[64:67], v[240:243]\n\t"
  "s_waitcnt lgkmcnt(4) vmcnt(1)\n\t"
  "v_mfma_f32_16x16x32_bf16 v[32:35], a[152:155], a[80:83], v[32:35]\n\t"
  "ds_read_b128 a[144:147], v3 offset:20480\n\t"
  "v_mfma_f32_16x16x32_bf16 v[36:39], a[152:155], a[84:87], v[36:39]\n\t"
  "v_mfma_f32_16x16x32_bf16 v[52:55], a[156:159], a[84:87], v[52:55]\n\t"
  "ds_read_b128 a[148:151], v3 offset:21504\n\t"
  "s_mov_b32 s72, 0x20000\n\t"
  "s_not_b32 s68, s34\n\t"
  "s_mov_b32 s73, 64\n\t"
  "s_branch LP4c_%=\n\t"
  "LP4a_%=:\n\t"
  "v_not_b32 v24, s34\n\t"
  "buffer_load_dwordx2 v[24:25], v9, s[20:23], 0 offen offset:8 sc1\n\t"
  "s_mov_b32 s73, 64\n\t"
  "LP4w_%=:\n\t"
  "s_sleep 1\n\t"
  "LP4c_%=:\n\t"
  "v_cmp_eq_u32 vcc, s68, v24\n\t"
  "s_nop 1\n\t"
  "s_cmp_eq_u64 vcc, 0\n\t"
  "v_cmp_ne_u64 vcc, s[34:35], v[24:25]\n\t"
  "s_cbranch_vccz LP4b_%=\n\t"
  "s_cbranch_scc1 LP4r_%=\n\t"
  "s_sub_u32 s73, s73, 1\n\t"
  "s_cbranch_scc0 LP4w_%=\n\t"
  "s_waitcnt vmcnt(0)\n\t"
  "LP4r_%=:\n\t"
  "s_sleep 2\n\t"
  "s_sub_u32 s72, s72, 1\n\t"
  "s_cbranch_scc0 LP4a_%=\n\t"
  "LP4b_%=:\n\t"
  "s_waitcnt lgkmcnt(4)\n\t"
  "v_mfma_f32_16x16x32_bf16 v[48:51], a[156:159], a[80:83], v[48:51]\n\t"
  "buffer_load_dwordx4 a[8:11], v0, s[20:23], s39 offen offset:2048 sc1\n\t"
  "v_mfma_f32_16x16x32_bf16 v[64:67], a[128:131], a[80:83], v[64:67]\n\t"
  "ds_read_b128 a[152:155], v3 offset:22528\n\t"
  "v_mfma_f32_16x16x32_bf16 v[68:71], a[128:131], a[84:87], v[68:71]\n\t"
  "buffer_load_dwordx4 a[12:15], v0, s[20:23], s39 offen offset:3072 sc1\n\t"
  "v_mfma_f32_16x16x32_bf16 v[84:87], a[132:135], a[84:87], v[84:87]\n\t"
  "ds_read_b128 a[156:159], v3 offset:23552\n\t"
  "s_add_u32 s39, s39, 131072\n\t"
  "s_waitcnt lgkmcnt(4)\n\t"
  "v_mfma_f32_16x16x32_bf16 v[80:83], a[132:135], a[80:83], v[80:83]\n\t"
  "buffer_load_dwordx4 a[24:27], v0, s[20:23], s39 offen offset:2048 sc1\n\t"
  "v_mfma_f32_16x16x32_bf16 v[96:99], a[136:139], a[80:83], v[96:99]\n\t"
  "ds_read_b128 a[128:131], v3 offset:24576\n\t"
  "v_mfma_f32_16x16x32_bf16 v[100:103], a[136:139], a[84:87], v[100:103]\n\t"
  "buffer_load_dwordx4 a[28:31], v0, s[20:23], s39 offen offset:3072 sc1\n\t"
  "v_mfma_f32_16x16x32_bf16 v[116:119], a[140:143], a[84:87], v[116:119]\n\t"
  "ds_read_b128 a[132:135], v3 offset:25600\n\t"
  "s_add_u32 s39, s39, 131072\n\t"
  "s_waitcnt lgkmcnt(4)\n\t"
  "v_mfma_f32_16x16x32_bf16 v[112:115], a[140:143], a[80:83], v[112:115]\n\t"
  "buffer_load_dwordx4 a[40:43], v0, s[20:23], s39 offen offset:2048 sc1\n\t"
  "v_mfma_f32_16x16x32_bf16 v[128:131], a[144:147], a[80:83], v[128:131]\n\t"
  "ds_read_b128 a[136:139], v3 offset:26624\n\t"
  "v_mfma_f32_16x16x32_bf16 v[132:135], a[144:147], a[84:87], v[132:135]\n\t"
  "buffer_load_dwordx4 a[44:47], v0, s[20:23], s39 offen offset:3072 sc1\n\t"
  "v_mfma_f32_16x16x32_bf16 v[148:151], a[148:151], a[84:87], v[148:151]\n\t"
  "ds_read_b128 a[140:143], v3 offset:27648\n\t"
  "s_add_u32 s39, s39, 131072\n\t"
  "s_waitcnt lgkmcnt(4)\n\t"
  "v_mfma_f32_16x16x32_bf16 v[144:147], a[148:151], a[80:83], v[144:147]\n\t"
  "buffer_load_dwordx4 a[56:59], v0, s[20:23], s39 offen offset:2048 sc1\n\t"
  "v_mfma_f32_16x16x32_bf16 v[160:163], a[152:155], a[80:83], v[160:163]\n\t"
  "ds_read_b128 a[144:147], v4 offset:28672\n\t"
  "v_mfma_f32_16x16x32_bf16 v[164:167], a[152:155], a[84:87], v[164:167]\n\t"
  "buffer_load_dwordx4 a[60:63], v0, s[20:23], s39 offen offset:3072 sc1\n\t"
  "v_mfma_f32_16x16x32_bf16 v[180:183], a[156:159], a[84:87], v[180:183]\n\t"
  "ds_read_b128 a[148:151], v4 offset:29696\n\t"
  "s_add_u32 s39, s39, 131072\n\t"
  "s_waitcnt lgkmcnt(4)\n\t"
  "v_mfma_f32_16x16x32_bf16 v[176:179], a[156:159], a[80:83], v[176:179]\n\t"
  "buffer_load_dwordx4 a[72:75], v0, s[20:23], s39 offen offset:2048 sc1\n\t"
  "v_mfma_f32_16x16x32_bf16 v[192:195], a[128:131], a[80:83], v[192:195]\n\t"
  "ds_read_b128 a[152:155], v4 offset:30720\n\t"
  "v_mfma_f32_16x16x32_bf16 v[196:199], a[128:131], a[84:87], v[196:199]\n\t"
  "buffer_load_dwordx4 a[76:79], v0, s[20:23], s39 offen offset:3072 sc1\n\t"
  "v_mfma_f32_16x16x32_bf16 v[212:215], a[132:135], a[84:87], v[212:215]\n\t"
  "ds_read_b128 a[156:159], v4 offset:31744\n\t"
  "s_add_u32 s39, s39, 131072\n\t"
  "s_waitcnt lgkmcnt(4)\n\t"
  "v_mfma_f32_16x16x32_bf16 v[208:211], a[132:135], a[80:83], v[208:211]\n\t"
  "buffer_load_dwordx4 a[88:91], v0, s[20:23], s39 offen offset:2048 sc1\n\t"
  "v_mfma_f32_16x16x32_bf16 v[224:227], a[136:139], a[80:83], v[224:227]\n\t"
  "ds_read_b128 a[128:131], v4 offset:32768\n\t"
  "v_mfma_f32_16x16x32_bf16 v[228:231], a[136:139], a[84:87], v[228:231]\n\t"
  "buffer_load_dwordx4 a[92:95], v0, s[20:23], s39 offen offset:3072 sc1\n\t"
  "v_mfma_f32_16x16x32_bf16 v[244:247], a[140:143], a[84:87], v[244:247]\n\t"
  "ds_read_b128 a[132:135], v4 offset:33792\n\t"
  "s_add_u32 s39, s39, 131072\n\t"
  "s_add_u32 m0, s30, 28672\n\t"
  "s_waitcnt lgkmcnt(4) vmcnt(10)\n\t"
  "v_mfma_f32_16x16x32_bf16 v[240:243], a[140:143], a[80:83], v[240:243]\n\t"
  "buffer_load_dwordx4 v1, s[20:23], s29 offen offset:0 sc1 lds\n\t"
  "v_mfma_f32_16x16x32_bf16 v[40:43], a[144:147], a[8:11], 0\n\t"
  "ds_read_b128 a[136:139], v4 offset:34816\n\t"
  "v_mfma_f32_16x16x32_bf16 v[44:47], a[144:147], a[12:15], 0\n\t"
  "buffer_load_dwordx4 v1, s[20:23], s29 offen offset:1024 sc1 lds\n\t"
  "v_mfma_f32_16x16x32_bf16 v[60:63], a[148:151], a[12:15], 0\n\t"
  "ds_read_b128 a[140:143], v4 offset:35840\n\t"
  "s_waitcnt lgkmcnt(4)\n\t"
  "v_mfma_f32_16x16x32_bf16 v[56:59], a[148:151], a[8:11], 0\n\t"
  "buffer_load_dwordx4 v1, s[20:23], s29 offen offset:2048 sc1 lds\n\t"
  "v_mfma_f32_16x16x32_bf16 v[72:75], a[152:155], a[8:11], 0\n\t"
  "ds_read_b128 a[144:147], v4 offset:36864\n\t"
  "v_mfma_f32_16x16x32_bf16 v[76:79], a[152:155], a[12:15], 0\n\t"
  "v_mfma_f32_16x16x32_bf16 v[92:95], a[156:159], a[12:15], 0\n\t"
  "ds_read_b128 a[148:151], v4 offset:37888\n\t"
  "v_mfma_f32_16x16x32_bf16 v[88:91], a[156:159], a[8:11], 0\n\t"
  "s_waitcnt lgkmcnt(4)\n\t"
  "v_mfma_f32_16x16x32_bf16 v[104:107], a[128:131], a[8:11], 0\n\t"
  "ds_read_b128 a[152:155], v4 offset:38912\n\t"
  "v_mfma_f32_16x16x32_bf16 v[108:111], a[128:131], a[12:15], 0\n\t"
  "buffer_load_dwordx4 v1, s[20:23], s29 offen offset:3072 sc1 lds\n\t"
  "v_mfma_f32_16x16x32_bf16 v[124:127], a[132:135], a[12:15], 0\n\t"
  "ds_read_b128 a[156:159], v4 offset:39936\n\t"
  "v_mfma_f32_16x16x32_bf16 v[120:123], a[132:135], a[8:11], 0\n\t"
  "s_waitcnt lgkmcnt(4)\n\t"
  "v_mfma_f32_16x16x32_bf16 v[136:139], a[136:139], a[8:11], 0\n\t"
  "ds_read_b128 a[128:131], v4 offset:40960\n\t"
  "v_mfma_f32_16x16x32_bf16 v[140:143], a[136:139], a[12:15], 0\n\t"
  "v_mfma_f32_16x16x32_bf16 v[156:159], a[140:143], a[12:15], 0\n\t"
  "ds_read_b128 a[132:135], v4 offset:41984\n\t"
  "s_add_u32 m0, s30, 32768\n\t"
  "s_waitcnt lgkmcnt(4)\n\t"
  "v_mfma_f32_16x16x32_bf16 v[152:155], a[140:143], a[8:11], 0\n\t"
  "buffer_load_dwordx4 v2, s[20:23], s29 offen offset:0 sc1 lds\n\t"
  "v_mfma_f32_16x16x32_bf16 v[168:171], a[144:147], a[8:11], 0\n\t"
  "ds_read_b128 a[136:139], v4 offset:43008\n\t"
  "v_mfma_f32_16x16x32_bf16 v[172:175], a[144:147], a[12:15], 0\n\t"
  "v_mfma_f32_16x16x32_bf16 v[188:191], a[148:151], a[12:15], 0\n\t"
  "ds_read_b128 a[140:143], v4 offset:44032\n\t"
  "v_mfma_f32_16x16x32_bf16 v[184:187], a[148:151], a[8:11], 0\n\t"
  "s_waitcnt lgkmcnt(4)\n\t"
  "v_mfma_f32_16x16x32_bf16 v[200:203], a[152:155], a[8:11], 0\n\t"
  "ds_read_b128 a[144:147], v4 offset:45056\n\t"
  "v_mfma_f32_16x16x32_bf16 v[204:207], a[152:155], a[12:15], 0\n\t"
  "buffer_load_dwordx4 v2, s[20:23], s29 offen offset:1024 sc1 lds\n\t"
  "v_mfma_f32_16x16x32_bf16 v[220:223], a[156:159], a[12:15], 0\n\t"
  "ds_read_b128 a[148:151], v4 offset:46080\n\t"
  "v_mfma_f32_16x16x32_bf16 v[216:219], a[156:159], a[8:11], 0\n\t"
  "s_waitcnt lgkmcnt(4)\n\t"
  "v_mfma_f32_16x16x32_bf16 v[232:235], a[128:131], a[8:11], 0\n\t"
  "ds_read_b128 a[152:155], v4 offset:47104\n\t"
  "v_mfma_f32_16x16x32_bf16 v[236:239], a[128:131], a[12:15], 0\n\t"
  "v_mfma_f32_16x16x32_bf16 v[252:255], a[132:135], a[12:15], 0\n\t"
  "ds_read_b128 a[156:159], v4 offset:48128\n\t"
  "v_mfma_f32_16x16x32_bf16 v[248:251], a[132:135], a[8:11], 0\n\t"
  "s_waitcnt lgkmcnt(4) vmcnt(14)\n\t"
  "v_mfma_f32_16x16x32_bf16 v[40:43], a[136:139], a[24:27], v[40:43]\n\t"
  "ds_read_b128 a[128:131], v4 offset:49152\n\t"
  "v_mfma_f32_16x16x32_bf16 v[44:47], a[136:139], a[28:31], v[44:47]\n\t"
  "buffer_load_dwordx4 v2, s[20:23], s29 offen offset:2048 sc1 lds\n\t"
  "v_mfma_f32_16x16x32_bf16 v[60:63], a[140:143], a[28:31], v[60:63]\n\t"
  "ds_read_b128 a[132:135], v4 offset:50176\n\t"
  "s_add_u32 s29, s29, 28672\n\t"
  "v_mfma_f32_16x16x32_bf16 v[56:59], a[140:143], a[24:27], v[56:59]\n\t"
  "s_waitcnt lgkmcnt(4)\n\t"
  "v_mfma_f32_16x16x32_bf16 v[72:75], a[144:147], a[24:27], v[72:75]\n\t"
  "ds_read_b128 a[136:139], v4 offset:51200\n\t"
  "v_mfma_f32_16x16x32_bf16 v[76:79], a[144:147], a[28:31], v[76:79]\n\t"
  "v_mfma_f32_16x16x32_bf16 v[92:95], a[148:151], a[28:31], v[92:95]\n\t"
  "ds_read_b128 a[140:143], v4 offset:52224\n\t"
  "s_waitcnt lgkmcnt(4)\n\t"
  "v_mfma_f32_16x16x32_bf16 v[88:91], a[148:151], a[24:27], v[88:91]\n\t"
  "buffer_load_dwordx4 a[96:99], v0, s[20:23], s28 offen offset:0 sc1\n\t"
  "v_mfma_f32_16x16x32_bf16 v[104:107], a[152:155], a[24:27], v[104:107]\n\t"
  "ds_read_b128 a[144:147], v4 offset:53248\n\t"
  "v_mfma_f32_16x16x32_bf16 v[108:111], a[152:155], a[28:31], v[108:111]\n\t"
  "v_mfma_f32_16x16x32_bf16 v[124:127], a[156:159], a[28:31], v[124:127]\n\t"
  "ds_read_b128 a[148:151], v4 offset:54272\n\t"
  "v_mfma_f32_16x16x32_bf16 v[120:123], a[156:159], a[24:27], v[120:123]\n\t"
  "s_waitcnt lgkmcnt(4)\n\t"
  "v_mfma_f32_16x16x32_bf16 v[136:139], a[128:131], a[24:27], v[136:139]\n\t"
  "ds_read_b128 a[152:155], v4 offset:55296\n\t"
  "v_mfma_f32_16x16x32_bf16 v[140:143], a[128:131], a[28:31], v[140:143]\n\t"
  "buffer_load_dwordx4 a[100:103], v0, s[20:23], s28 offen offset:1024 sc1\n\t"
  "v_mfma_f32_16x16x32_bf16 v[156:159], a[132:135], a[28:31], v[156:159]\n\t"
  "ds_read_b128 a[156:159], v4 offset:56320\n\t"
  "v_mfma_f32_16x16x32_bf16 v[152:155], a[132:135], a[24:27], v[152:155]\n\t"
  "s_waitcnt lgkmcnt(4)\n\t"
  "v_mfma_f32_16x16x32_bf16 v[168:171], a[136:139], a[24:27], v[168:171]\n\t"
  "ds_read_b128 a[128:131], v4 offset:0\n\t"
  "v_mfma_f32_16x16x32_bf16 v[172:175], a[136:139], a[28:31], v[172:175]\n\t"
  "v_mfma_f32_16x16x32_bf16 v[188:191], a[140:143], a[28:31], v[188:191]\n\t"
  "ds_read_b128 a[132:135], v4 offset:1024\n\t"
  "s_waitcnt lgkmcnt(4)\n\t"
  "v_mfma_f32_16x16x32_bf16 v[184:187], a[140:143], a[24:27], v[184:187]\n\t"
  "buffer_load_dwordx4 a[104:107], v0, s[20:23], s28 offen offset:2048 sc1\n\t"
  "v_mfma_f32_16x16x32_bf16 v[200:203], a[144:147], a[24:27], v[200:203]\n\t"
  "ds_read_b128 a[136:139], v4 offset:2048\n\t"
  "v_mfma_f32_16x16x32_bf16 v[204:207], a[144:147], a[28:31], v[204:207]\n\t"
  "v_mfma_f32_16x16x32_bf16 v[220:223], a[148:151], a[28:31], v[220:223]\n\t"
  "ds_read_b128 a[140:143], v4 offset:3072\n\t"
  "v_mfma_f32_16x16x32_bf16 v[216:219], a[148:151], a[24:27], v[216:219]\n\t"
  "s_waitcnt lgkmcnt(4)\n\t"
  "v_mfma_f32_16x16x32_bf16 v[232:235], a[152:155], a[24:27], v[232:235]\n\t"
  "ds_read_b128 a[144:147], v4 offset:4096\n\t"
  "v_mfma_f32_16x16x32_bf16 v[236:239], a[152:155], a[28:31], v[236:239]\n\t"
  "buffer_load_dwordx4 a[108:111], v0, s[20:23], s28 offen offset:3072 sc1\n\t"
  "v_mfma_f32_16x16x32_bf16 v[252:255], a[156:159], a[28:31], v[252:255]\n\t"
  "ds_read_b128 a[148:151], v4 offset:5120\n\t"
  "s_add_u32 s28, s28, 131072\n\t"
  "v_mfma_f32_16x16x32_bf16 v[248:251], a[156:159], a[24:27], v[248:251]\n\t"
  "s_barrier\n\t"
  "s_waitcnt lgkmcnt(4) vmcnt(17)\n\t"
  "v_mfma_f32_16x16x32_bf16 v[40:43], a[128:131], a[40:43], v[40:43]\n\t"
  "ds_read_b128 a[152:155], v4 offset:6144\n\t"
  "v_mfma_f32_16x16x32_bf16 v[44:47], a[128:131], a[44:47], v[44:47]\n\t"
  "v_mfma_f32_16x16x32_bf16 v[60:63], a[132:135], a[44:47], v[60:63]\n\t"
  "ds_read_b128 a[156:159], v4 offset:7168\n\t"
  "s_waitcnt lgkmcnt(4)\n\t"
  "v_mfma_f32_16x16x32_bf16 v[56:59], a[132:135], a[40:43], v[56:59]\n\t"
  "buffer_load_dwordx4 a[112:115], v0, s[20:23], s28 offen offset:0 sc1\n\t"
  "v_mfma_f32_16x16x32_bf16 v[72:75], a[136:139], a[40:43], v[72:75]\n\t"
  "ds_read_b128 a[128:131], v4 offset:8192\n\t"
  "v_mfma_f32_16x16x32_bf16 v[76:79], a[136:139], a[44:47], v[76:79]\n\t"
  "v_mfma_f32_16x16x32_bf16 v[92:95], a[140:143], a[44:47], v[92:95]\n\t"
  "ds_read_b128 a[132:135], v4 offset:9216\n\t"
  "v_mfma_f32_16x16x32_bf16 v[88:91], a[140:143], a[40:43], v[88:91]\n\t"
  "s_waitcnt lgkmcnt(4)\n\t"
  "v_mfma_f32_16x16x32_bf16 v[104:107], a[144:147], a[40:43], v[104:107]\n\t"
  "ds_read_b128 a[136:139], v4 offset:10240\n\t"
  "v_mfma_f32_16x16x32_bf16 v[108:111], a[144:147], a[44:47], v[108:111]\n\t"
  "v_mfma_f32_16x16x32_bf16 v[124:127], a[148:151], a[44:47], v[124:127]\n\t"
  "ds_read_b128 a[140:143], v4 offset:11264\n\t"
  "s_waitcnt lgkmcnt(4)\n\t"
  "v_mfma_f32_16x16x32_bf16 v[120:123], a[148:151], a[40:43], v[120:123]\n\t"
  "buffer_load_dwordx4 a[116:119], v0, s[20:23], s28 offen offset:1024 sc1\n\t"
  "v_mfma_f32_16x16x32_bf16 v[136:139], a[152:155], a[40:43], v[136:139]\n\t"
  "ds_read_b128 a[144:147], v4 offset:12288\n\t"
  "v_mfma_f32_16x16x32_bf16 v[140:143], a[152:155], a[44:47], v[140:143]\n\t"
  "v_mfma_f32_16x16x32_bf16 v[156:159], a[156:159], a[44:47], v[156:159]\n\t"
  "ds_read_b128 a[148:151], v4 offset:13312\n\t"
  "v_mfma_f32_16x16x32_bf16 v[152:155], a[156:159], a[40:43], v[152:155]\n\t"
  "s_waitcnt lgkmcnt(4)\n\t"
  "v_mfma_f32_16x16x32_bf16 v[168:171], a[128:131], a[40:43], v[168:171]\n\t"
  "ds_read_b128 a[152:155], v4 offset:14336\n\t"
  "v_mfma_f32_16x16x32_bf16 v[172:175], a[128:131], a[44:47], v[172:175]\n\t"
  "buffer_load_dwordx4 a[120:123], v0, s[20:23], s28 offen offset:2048 sc1\n\t"
  "v_mfma_f32_16x16x32_bf16 v[188:191], a[132:135], a[44:47], v[188:191]\n\t"
  "ds_read_b128 a[156:159], v4 offset:15360\n\t"
  "v_mfma_f32_16x16x32_bf16 v[184:187], a[132:135], a[40:43], v[184:187]\n\t"
  "s_waitcnt lgkmcnt(4)\n\t"
  "v_mfma_f32_16x16x32_bf16 v[200:203], a[136:139], a[40:43], v[200:203]\n\t"
  "ds_read_b128 a[128:131], v4 offset:16384\n\t"
  "v_mfma_f32_16x16x32_bf16 v[204:207], a[136:139], a[44:47], v[204:207]\n\t"
  "v_mfma_f32_16x16x32_bf16 v[220:223], a[140:143], a[44:47], v[220:223]\n\t"
  "ds_read_b128 a[132:135], v4 offset:17408\n\t"
  "s_waitcnt lgkmcnt(4)\n\t"
  "v_mfma_f32_16x16x32_bf16 v[216:219], a[140:143], a[40:43], v[216:219]\n\t"
  "buffer_load_dwordx4 a[124:127], v0, s[20:23], s28 offen offset:3072 sc1\n\t"
  "v_mfma_f32_16x16x32_bf16 v[232:235], a[144:147], a[40:43], v[232:235]\n\t"
  "ds_read_b128 a[136:139], v4 offset:18432\n\t"
  "s_add_u32 s28, s28, 131072\n\t"
  "v_mfma_f32_16x16x32_bf16 v[236:239], a[144:147], a[44:47], v[236:239]\n\t"
  "v_mfma_f32_16x16x32_bf16 v[252:255], a[148:151], a[44:47], v[252:255]\n\t"
  "ds_read_b128 a[140:143], v4 offset:19456\n\t"
  "v_mfma_f32_16x16x32_bf16 v[248:251], a[148:151], a[40:43], v[248:251]\n\t"
  "s_waitcnt lgkmcnt(4) vmcnt(19)\n\t"
  "v_mfma_f32_16x16x32_bf16 v[40:43], a[152:155], a[56:59], v[40:43]\n\t"
  "ds_read_b128 a[144:147], v4 offset:20480\n\t"
  "s_add_u32 m0, s30, 86016\n\t"
  "v_mfma_f32_16x16x32_bf16 v[44:47], a[152:155], a[60:63], v[44:47]\n\t"
  "buffer_load_dwordx4 v1, s[20:23], s29 offen offset:0 sc1 lds\n\t"
  "v_mfma_f32_16x16x32_bf16 v[60:63], a[156:159], a[60:63], v[60:63]\n\t"
  "ds_read_b128 a[148:151], v4 offset:21504\n\t"
  "v_mfma_f32_16x16x32_bf16 v[56:59], a[156:159], a[56:59], v[56:59]\n\t"
  "s_waitcnt lgkmcnt(4)\n\t"
  "v_mfma_f32_16x16x32_bf16 v[72:75], a[128:131], a[56:59], v[72:75]\n\t"
  "ds_read_b128 a[152:155], v4 offset:22528\n\t"
  "v_mfma_f32_16x16x32_bf16 v[76:79], a[128:131], a[60:63], v[76:79]\n\t"
  "v_mfma_f32_16x16x32_bf16 v[92:95], a[132:135], a[60:63], v[92:95]\n\t"
  "ds_read_b128 a[156:159], v4 offset:23552\n\t"
  "s_waitcnt lgkmcnt(4)\n\t"
  "v_mfma_f32_16x16x32_bf16 v[88:91], a[132:135], a[56:59], v[88:91]\n\t"
  "buffer_load_dwordx4 v1, s[20:23], s29 offen offset:1024 sc1 lds\n\t"
  "v_mfma_f32_16x16x32_bf16 v[104:107], a[136:139], a[56:59], v[104:107]\n\t"
  "ds_read_b128 a[128:131], v4 offset:24576\n\t"
  "v_mfma_f32_16x16x32_bf16 v[108:111], a[136:139], a[60:63], v[108:111]\n\t"
  "v_mfma_f32_16x16x32_bf16 v[124:127], a[140:143], a[60:63], v[124:127]\n\t"
  "ds_read_b128 a[132:135], v4 offset:25600\n\t"
  "v_mfma_f32_16x16x32_bf16 v[120:123], a[140:143], a[56:59], v[120:123]\n\t"
  "s_waitcnt lgkmcnt(4)\n\t"
  "v_mfma_f32_16x16x32_bf16 v[136:139], a[144:147], a[56:59], v[136:139]\n\t"
  "ds_read_b128 a[136:139], v4 offset:26624\n\t"
  "v_mfma_f32_16x16x32_bf16 v[140:143], a[144:147], a[60:63], v[140:143]\n\t"
  "buffer_load_dwordx4 v1, s[20:23], s29 offen offset:2048 sc1 lds\n\t"
  "v_mfma_f32_16x16x32_bf16 v[156:159], a[148:151], a[60:63], v[156:159]\n\t"
  "ds_read_b128 a[140:143], v4 offset:27648\n\t"
  "v_mfma_f32_16x16x32_bf16 v[152:155], a[148:151], a[56:59], v[152:155]\n\t"
  "s_waitcnt lgkmcnt(4)\n\t"
  "v_mfma_f32_16x16x32_bf16 v[168:171], a[152:155], a[56:59], v[168:171]\n\t"
  "ds_read_b128 a[144:147], v3 offset:0\n\t"
  "v_mfma_f32_16x16x32_bf16 v[172:175], a[152:155], a[60:63], v[172:175]\n\t"
  "v_mfma_f32_16x16x32_bf16 v[188:191], a[156:159], a[60:63], v[188:191]\n\t"
  "ds_read_b128 a[148:151], v3 offset:1024\n\t"
  "v_mfma_f32_16x16x32_bf16 v[184:187], a[156:159], a[56:59], v[184:187]\n\t"
  "s_waitcnt lgkmcnt(4)\n\t"
  "v_mfma_f32_16x16x32_bf16 v[200:203], a[128:131], a[56:59], v[200:203]\n\t"
  "ds_read_b128 a[152:155], v3 offset:2048\n\t"
  "v_mfma_f32_16x16x32_bf16 v[204:207], a[128:131], a[60:63], v[204:207]\n\t"
  "buffer_load_dwordx4 v1, s[20:23], s29 offen offset:3072 sc1 lds\n\t"
  "v_mfma_f32_16x16x32_bf16 v[220:223], a[132:135], a[60:63], v[220:223]\n\t"
  "ds_read_b128 a[156:159], v3 offset:3072\n\t"
  "v_mfma_f32_16x16x32_bf16 v[216:219], a[132:135], a[56:59], v[216:219]\n\t"
  "s_waitcnt lgkmcnt(4)\n\t"
  "v_mfma_f32_16x16x32_bf16 v[232:235], a[136:139], a[56:59], v[232:235]\n\t"
  "ds_read_b128 a[128:131], v3 offset:4096\n\t"
  "v_mfma_f32_16x16x32_bf16 v[236:239], a[136:139], a[60:63], v[236:239]\n\t"
  "v_mfma_f32_16x16x32_bf16 v[252:255], a[140:143], a[60:63], v[252:255]\n\t"
  "ds_read_b128 a[132:135], v3 offset:5120\n\t"
  "s_add_u32 m0, s30, 90112\n\t"
  "s_waitcnt lgkmcnt(4) vmcnt(21)\n\t"
  "v_mfma_f32_16x16x32_bf16 v[248:251], a[140:143], a[56:59], v[248:251]\n\t"
  "buffer_load_dwordx4 v2, s[20:23], s29 offen offset:0 sc1 lds\n\t"
  "v_mfma_f32_16x16x32_bf16 v[40:43], a[144:147], a[72:75], v[40:43]\n\t"
  "ds_read_b128 a[136:139], v3 offset:6144\n\t"
  "v_mfma_f32_16x16x32_bf16 v[44:47], a[144:147], a[76:79], v[44:47]\n\t"
  "v_mfma_f32_16x16x32_bf16 v[60:63], a[148:151], a[76:79], v[60:63]\n\t"
  "ds_read_b128 a[140:143], v3 offset:7168\n\t"
  "v_mfma_f32_16x16x32_bf16 v[56:59], a[148:151], a[72:75], v[56:59]\n\t"
  "s_waitcnt lgkmcnt(4)\n\t"
  "v_mfma_f32_16x16x32_bf16 v[72:75], a[152:155], a[72:75], v[72:75]\n\t"
  "ds_read_b128 a[144:147], v3 offset:8192\n\t"
  "v_mfma_f32_16x16x32_bf16 v[76:79], a[152:155], a[76:79], v[76:79]\n\t"
  "buffer_load_dwordx4 v2, s[20:23], s29 offen offset:1024 sc1 lds\n\t"
  "v_mfma_f32_16x16x32_bf16 v[92:95], a[156:159], a[76:79], v[92:95]\n\t"
  "ds_read_b128 a[148:151], v3 offset:9216\n\t"
  "v_mfma_f32_16x16x32_bf16 v[88:91], a[156:159], a[72:75], v[88:91]\n\t"
  "s_waitcnt lgkmcnt(4)\n\t"
  "v_mfma_f32_16x16x32_bf16 v[104:107], a[128:131], a[72:75], v[104:107]\n\t"
  "ds_read_b128 a[152:155], v3 offset:10240\n\t"
  "v_mfma_f32_16x16x32_bf16 v[108:111], a[128:131], a[76:79], v[108:111]\n\t"
  "v_mfma_f32_16x16x32_bf16 v[124:127], a[132:135], a[76:79], v[124:127]\n\t"
  "ds_read_b128 a[156:159], v3 offset:11264\n\t"
  "s_waitcnt lgkmcnt(4)\n\t"
  "v_mfma_f32_16x16x32_bf16 v[120:123], a[132:135], a[72:75], v[120:123]\n\t"
  "buffer_load_dwordx4 v2, s[20:23], s29 offen offset:2048 sc1 lds\n\t"
  "v_mfma_f32_16x16x32_bf16 v[136:139], a[136:139], a[72:75], v[136:139]\n\t"
  "ds_read_b128 a[128:131], v3 offset:12288\n\t"
  "s_add_u32 s29, s29, 28672\n\t"
  "v_mfma_f32_16x16x32_bf16 v[140:143], a[136:139], a[76:79], v[140:143]\n\t"
  "v_mfma_f32_16x16x32_bf16 v[156:159], a[140:143], a[76:79], v[156:159]\n\t"
  "ds_read_b128 a[132:135], v3 offset:13312\n\t"
  "v_mfma_f32_16x16x32_bf16 v[152:155], a[140:143], a[72:75], v[152:155]\n\t"
  "s_waitcnt lgkmcnt(4)\n\t"
  "v_mfma_f32_16x16x32_bf16 v[168:171], a[144:147], a[72:75], v[168:171]\n\t"
  "ds_read_b128 a[136:139], v3 offset:14336\n\t"
  "v_mfma_f32_16x16x32_bf16 v[172:175], a[144:147], a[76:79], v[172:175]\n\t"
  "buffer_load_dwordx4 a[0:3], v0, s[20:23], s28 offen offset:0 sc1\n\t"
  "v_mfma_f32_16x16x32_bf16 v[188:191], a[148:151], a[76:79], v[188:191]\n\t"
  "ds_read_b128 a[140:143], v3 offset:15360\n\t"
  "v_mfma_f32_16x16x32_bf16 v[184:187], a[148:151], a[72:75], v[184:187]\n\t"
  "s_waitcnt lgkmcnt(4)\n\t"
  "v_mfma_f32_16x16x32_bf16 v[200:203], a[152:155], a[72:75], v[200:203]\n\t"
  "ds_read_b128 a[144:147], v3 offset:16384\n\t"
  "v_mfma_f32_16x16x32_bf16 v[204:207], a[152:155], a[76:79], v[204:207]\n\t"
  "v_mfma_f32_16x16x32_bf16 v[220:223], a[156:159], a[76:79], v[220:223]\n\t"
  "ds_read_b128 a[148:151], v3 offset:17408\n\t"
  "s_waitcnt lgkmcnt(4)\n\t"
  "v_mfma_f32_16x16x32_bf16 v[216:219], a[156:159], a[72:75], v[216:219]\n\t"
  "buffer_load_dwordx4 a[4:7], v0, s[20:23], s28 offen offset:1024 sc1\n\t"
  "v_mfma_f32_16x16x32_bf16 v[232:235], a[128:131], a[72:75], v[232:235]\n\t"
  "ds_read_b128 a[152:155], v3 offset:18432\n\t"
  "v_mfma_f32_16x16x32_bf16 v[236:239], a[128:131], a[76:79], v[236:239]\n\t"
  "v_mfma_f32_16x16x32_bf16 v[252:255], a[132:135], a[76:79], v[252:255]\n\t"
  "ds_read_b128 a[156:159], v3 offset:19456\n\t"
  "v_mfma_f32_16x16x32_bf16 v[248:251], a[132:135], a[72:75], v[248:251]\n\t"
  "s_waitcnt lgkmcnt(4) vmcnt(24)\n\t"
  "v_mfma_f32_16x16x32_bf16 v[40:43], a[136:139], a[88:91], v[40:43]\n\t"
  "ds_read_b128 a[128:131], v3 offset:20480\n\t"
  "v_mfma_f32_16x16x32_bf16 v[44:47], a[136:139], a[92:95], v[44:47]\n\t"
  "v_mfma_f32_16x16x32_bf16 v[60:63], a[140:143], a[92:95], v[60:63]\n\t"
  "ds_read_b128 a[132:135], v3 offset:21504\n\t"
  "s_waitcnt lgkmcnt(4)\n\t"
  "v_mfma_f32_16x16x32_bf16 v[56:59], a[140:143], a[88:91], v[56:59]\n\t"
  "buffer_load_dwordx4 a[8:11], v0, s[20:23], s28 offen offset:2048 sc1\n\t"
  "v_mfma_f32_16x16x32_bf16 v[72:75], a[144:147], a[88:91], v[72:75]\n\t"
  "ds_read_b128 a[136:139], v3 offset:22528\n\t"
  "v_mfma_f32_16x16x32_bf16 v[76:79], a[144:147], a[92:95], v[76:79]\n\t"
  "v_mfma_f32_16x16x32_bf16 v[92:95], a[148:151], a[92:95], v[92:95]\n\t"
  "ds_read_b128 a[140:143], v3 offset:23552\n\t"
  "v_mfma_f32_16x16x32_bf16 v[88:91], a[148:151], a[88:91], v[88:91]\n\t"
  "s_waitcnt lgkmcnt(4)\n\t"
  "v_mfma_f32_16x16x32_bf16 v[104:107], a[152:155], a[88:91], v[104:107]\n\t"
  "ds_read_b128 a[144:147], v3 offset:24576\n\t"
  "v_mfma_f32_16x16x32_bf16 v[108:111], a[152:155], a[92:95], v[108:111]\n\t"
  "buffer_load_dwordx4 a[12:15], v0, s[20:23], s28 offen offset:3072 sc1\n\t"
  "v_mfma_f32_16x16x32_bf16 v[124:127], a[156:159], a[92:95], v[124:127]\n\t"
  "ds_read_b128 a[148:151], v3 offset:25600\n\t"
  "s_add_u32 s28, s28, 131072\n\t"
  "v_mfma_f32_16x16x32_bf16 v[120:123], a[156:159], a[88:91], v[120:123]\n\t"
  "s_waitcnt lgkmcnt(4)\n\t"
  "v_mfma_f32_16x16x32_bf16 v[136:139], a[128:131], a[88:91], v[136:139]\n\t"
  "ds_read_b128 a[152:155], v3 offset:26624\n\t"
  "v_mfma_f32_16x16x32_bf16 v[140:143], a[128:131], a[92:95], v[140:143]\n\t"
  "v_mfma_f32_16x16x32_bf16 v[156:159], a[132:135], a[92:95], v[156:159]\n\t"
  "ds_read_b128 a[156:159], v3 offset:27648\n\t"
  "s_waitcnt lgkmcnt(4)\n\t"
  "v_mfma_f32_16x16x32_bf16 v[152:155], a[132:135], a[88:91], v[152:155]\n\t"
  "buffer_load_dwordx4 a[16:19], v0, s[20:23], s28 offen offset:0 sc1\n\t"
  "v_mfma_f32_16x16x32_bf16 v[168:171], a[136:139], a[88:91], v[168:171]\n\t"
  "v_mfma_f32_16x16x32_bf16 v[172:175], a[136:139], a[92:95], v[172:175]\n\t"
  "s_waitcnt vmcnt(20)\n\t"
  "v_mfma_f32_16x16x32_bf16 v[188:191], a[140:143], a[92:95], v[188:191]\n\t"
  "s_barrier\n\t"
  "v_mfma_f32_16x16x32_bf16 v[184:187], a[140:143], a[88:91], v[184:187]\n\t"
  "s_waitcnt lgkmcnt(2)\n\t"
  "v_mfma_f32_16x16x32_bf16 v[200:203], a[144:147], a[88:91], v[200:203]\n\t"
  "ds_read_b128 a[128:131], v3 offset:28672\n\t"
  "v_mfma_f32_16x16x32_bf16 v[204:207], a[144:147], a[92:95], v[204:207]\n\t"
  "buffer_load_dwordx4 a[20:23], v0, s[20:23], s28 offen offset:1024 sc1\n\t"
  "v_mfma_f32_16x16x32_bf16 v[220:223], a[148:151], a[92:95], v[220:223]\n\t"
  "ds_read_b128 a[132:135], v3 offset:29696\n\t"
  "v_mfma_f32_16x16x32_bf16 v[216:219], a[148:151], a[88:91], v[216:219]\n\t"
  "s_waitcnt lgkmcnt(2)\n\t"
  "v_mfma_f32_16x16x32_bf16 v[232:235], a[152:155], a[88:91], v[232:235]\n\t"
  "ds_read_b128 a[136:139], v3 offset:30720\n\t"
  "v_mfma_f32_16x16x32_bf16 v[236:239], a[152:155], a[92:95], v[236:239]\n\t"
  "v_mfma_f32_16x16x32_bf16 v[252:255], a[156:159], a[92:95], v[252:255]\n\t"
  "ds_read_b128 a[140:143], v3 offset:31744\n\t"
  "s_waitcnt lgkmcnt(3) vmcnt(17)\n\t"
  "v_mfma_f32_16x16x32_bf16 v[248:251], a[156:159], a[88:91], v[248:251]\n\t"
  "buffer_load_dwordx4 a[24:27], v0, s[20:23], s28 offen offset:2048 sc1\n\t"
  "v_mfma_f32_16x16x32_bf16 v[32:35], a[128:131], a[96:99], v[32:35]\n\t"
  "ds_read_b128 a[144:147], v3 offset:32768\n\t"
  "v_mfma_f32_16x16x32_bf16 v[36:39], a[128:131], a[100:103], v[36:39]\n\t"
  "v_mfma_f32_16x16x32_bf16 v[40:43], a[128:131], a[104:107], v[40:43]\n\t"
  "v_mfma_f32_16x16x32_bf16 v[44:47], a[128:131], a[108:111], v[44:47]\n\t"
  "s_waitcnt lgkmcnt(3)\n\t"
  "v_mfma_f32_16x16x32_bf16 v[60:63], a[132:135], a[108:111], v[60:63]\n\t"
  "ds_read_b128 a[148:151], v3 offset:33792\n\t"
  "v_mfma_f32_16x16x32_bf16 v[56:59], a[132:135], a[104:107], v[56:59]\n\t"
  "v_mfma_f32_16x16x32_bf16 v[52:55], a[132:135], a[100:103], v[52:55]\n\t"
  "buffer_load_dwordx4 a[28:31], v0, s[20:23], s28 offen offset:3072 sc1\n\t"
  "v_mfma_f32_16x16x32_bf16 v[48:51], a[132:135], a[96:99], v[48:51]\n\t"
  "s_waitcnt lgkmcnt(3)\n\t"
  "v_mfma_f32_16x16x32_bf16 v[64:67], a[136:139], a[96:99], v[64:67]\n\t"
  "ds_read_b128 a[152:155], v3 offset:34816\n\t"
  "s_add_u32 s28, s28, 131072\n\t"
  "v_mfma_f32_16x16x32_bf16 v[68:71], a[136:139], a[100:103], v[68:71]\n\t"
  "v_mfma_f32_16x16x32_bf16 v[72:75], a[136:139], a[104:107], v[72:75]\n\t"
  "v_mfma_f32_16x16x32_bf16 v[76:79], a[136:139], a[108:111], v[76:79]\n\t"
  "s_waitcnt lgkmcnt(3)\n\t"
  "v_mfma_f32_16x16x32_bf16 v[92:95], a[140:143], a[108:111], v[92:95]\n\t"
  "ds_read_b128 a[156:159], v3 offset:35840\n\t"
  "v_mfma_f32_16x16x32_bf16 v[88:91], a[140:143], a[104:107], v[88:91]\n\t"
  "s_add_u32 m0, s30, 57344\n\t"
  "v_mfma_f32_16x16x32_bf16 v[84:87], a[140:143], a[100:103], v[84:87]\n\t"
  "buffer_load_dwordx4 v1, s[20:23], s29 offen offset:0 sc1 lds\n\t"
  "v_mfma_f32_16x16x32_bf16 v[80:83], a[140:143], a[96:99], v[80:83]\n\t"
  "s_waitcnt lgkmcnt(3)\n\t"
  "v_mfma_f32_16x16x32_bf16 v[96:99], a[144:147], a[96:99], v[96:99]\n\t"
  "ds_read_b128 a[128:131], v3 offset:36864\n\t"
  "v_mfma_f32_16x16x32_bf16 v[100:103], a[144:147], a[100:103], v[100:103]\n\t"
  "v_mfma_f32_16x16x32_bf16 v[104:107], a[144:147], a[104:107], v[104:107]\n\t"
  "buffer_load_dwordx4 v1, s[20:23], s29 offen offset:1024 sc1 lds\n\t"
  "v_mfma_f32_16x16x32_bf16 v[108:111], a[144:147], a[108:111], v[108:111]\n\t"
  "s_waitcnt lgkmcnt(3)\n\t"
  "v_mfma_f32_16x16x32_bf16 v[124:127], a[148:151], a[108:111], v[124:127]\n\t"
  "ds_read_b128 a[132:135], v3 offset:37888\n\t"
  "v_mfma_f32_16x16x32_bf16 v[120:123], a[148:151], a[104:107], v[120:123]\n\t"
  "v_mfma_f32_16x16x32_bf16 v[116:119], a[148:151], a[100:103], v[116:119]\n\t"
  "v_mfma_f32_16x16x32_bf16 v[112:115], a[148:151], a[96:99], v[112:115]\n\t"
  "s_waitcnt lgkmcnt(3)\n\t"
  "v_mfma_f32_16x16x32_bf16 v[128:131], a[152:155], a[96:99], v[128:131]\n\t"
  "ds_read_b128 a[136:139], v3 offset:38912\n\t"
  "v_mfma_f32_16x16x32_bf16 v[132:135], a[152:155], a[100:103], v[132:135]\n\t"
  "v_mfma_f32_16x16x32_bf16 v[136:139], a[152:155], a[104:107], v[136:139]\n\t"
  "buffer_load_dwordx4 v1, s[20:23], s29 offen offset:2048 sc1 lds\n\t"
  "v_mfma_f32_16x16x32_bf16 v[140:143], a[152:155], a[108:111], v[140:143]\n\t"
  "s_waitcnt lgkmcnt(3)\n\t"
  "v_mfma_f32_16x16x32_bf16 v[156:159], a[156:159], a[108:111], v[156:159]\n\t"
  "ds_read_b128 a[140:143], v3 offset:39936\n\t"
  "v_mfma_f32_16x16x32_bf16 v[152:155], a[156:159], a[104:107], v[152:155]\n\t"
  "v_mfma_f32_16x16x32_bf16 v[148:151], a[156:159], a[100:103], v[148:151]\n\t"
  "buffer_load_dwordx4 v1, s[20:23], s29 offen offset:3072 sc1 lds\n\t"
  "v_mfma_f32_16x16x32_bf16 v[144:147], a[156:159], a[96:99], v[144:147]\n\t"
  "s_waitcnt lgkmcnt(3)\n\t"
  "v_mfma_f32_16x16x32_bf16 v[160:163], a[128:131], a[96:99], v[160:163]\n\t"
  "ds_read_b128 a[144:147], v3 offset:40960\n\t"
  "v_mfma_f32_16x16x32_bf16 v[164:167], a[128:131], a[100:103], v[164:167]\n\t"
  "v_mfma_f32_16x16x32_bf16 v[168:171], a[128:131], a[104:107], v[168:171]\n\t"
  "v_mfma_f32_16x16x32_bf16 v[172:175], a[128:131], a[108:111], v[172:175]\n\t"
  "s_waitcnt lgkmcnt(3)\n\t"
  "v_mfma_f32_16x16x32_bf16 v[188:191], a[132:135], a[108:111], v[188:191]\n\t"
  "ds_read_b128 a[148:151], v3 offset:41984\n\t"
  "v_mfma_f32_16x16x32_bf16 v[184:187], a[132:135], a[104:107], v[184:187]\n\t"
  "s_add_u32 m0, s30, 61440\n\t"
  "v_mfma_f32_16x16x32_bf16 v[180:183], a[132:135], a[100:103], v[180:183]\n\t"
  "buffer_load_dwordx4 v2, s[20:23], s29 offen offset:0 sc1 lds\n\t"
  "v_mfma_f32_16x16x32_bf16 v[176:179], a[132:135], a[96:99], v[176:179]\n\t"
  "s_waitcnt lgkmcnt(3)\n\t"
  "v_mfma_f32_16x16x32_bf16 v[192:195], a[136:139], a[96:99], v[192:195]\n\t"
  "ds_read_b128 a[152:155], v3 offset:43008\n\t"
  "v_mfma_f32_16x16x32_bf16 v[196:199], a[136:139], a[100:103], v[196:199]\n\t"
  "v_mfma_f32_16x16x32_bf16 v[200:203], a[136:139], a[104:107], v[200:203]\n\t"
  "buffer_load_dwordx4 v2, s[20:23], s29 offen offset:1024 sc1 lds\n\t"
  "v_mfma_f32_16x16x32_bf16 v[204:207], a[136:139], a[108:111], v[204:207]\n\t"
  "s_waitcnt lgkmcnt(3)\n\t"
  "v_mfma_f32_16x16x32_bf16 v[220:223], a[140:143], a[108:111], v[220:223]\n\t"
  "ds_read_b128 a[156:159], v3 offset:44032\n\t"
  "v_mfma_f32_16x16x32_bf16 v[216:219], a[140:143], a[104:107], v[216:219]\n\t"
  "v_mfma_f32_16x16x32_bf16 v[212:215], a[140:143], a[100:103], v[212:215]\n\t"
  "v_mfma_f32_16x16x32_bf16 v[208:211], a[140:143], a[96:99], v[208:211]\n\t"
  "s_waitcnt lgkmcnt(3)\n\t"
  "v_mfma_f32_16x16x32_bf16 v[224:227], a[144:147], a[96:99], v[224:227]\n\t"
  "ds_read_b128 a[128:131], v3 offset:45056\n\t"
  "v_mfma_f32_16x16x32_bf16 v[228:231], a[144:147], a[100:103], v[228:231]\n\t"
  "v_mfma_f32_16x16x32_bf16 v[232:235], a[144:147], a[104:107], v[232:235]\n\t"
  "buffer_load_dwordx4 v2, s[20:23], s29 offen offset:2048 sc1 lds\n\t"
  "v_mfma_f32_16x16x32_bf16 v[236:239], a[144:147], a[108:111], v[236:239]\n\t"
  "s_waitcnt lgkmcnt(3)\n\t"
  "v_mfma_f32_16x16x32_bf16 v[252:255], a[148:151], a[108:111], v[252:255]\n\t"
  "ds_read_b128 a[132:135], v3 offset:46080\n\t"
  "s_add_u32 s29, s29, 28672\n\t"
  "v_mfma_f32_16x16x32_bf16 v[248:251], a[148:151], a[104:107], v[248:251]\n\t"
  "v_mfma_f32_16x16x32_bf16 v[244:247], a[148:151], a[100:103], v[244:247]\n\t"
  "v_mfma_f32_16x16x32_bf16 v[240:243], a[148:151], a[96:99], v[240:243]\n\t"
  "s_waitcnt lgkmcnt(3) vmcnt(22)\n\t"
  "v_mfma_f32_16x16x32_bf16 v[32:35], a[152:155], a[112:115], v[32:35]\n\t"
  "ds_read_b128 a[136:139], v3 offset:47104\n\t"
  "v_mfma_f32_16x16x32_bf16 v[36:39], a[152:155], a[116:119], v[36:39]\n\t"
  "v_mfma_f32_16x16x32_bf16 v[40:43], a[152:155], a[120:123], v[40:43]\n\t"
  "buffer_load_dwordx4 a[32:35], v0, s[20:23], s28 offen offset:0 sc1\n\t"
  "v_mfma_f32_16x16x32_bf16 v[44:47], a[152:155], a[124:127], v[44:47]\n\t"
  "s_waitcnt lgkmcnt(3)\n\t"
  "v_mfma_f32_16x16x32_bf16 v[60:63], a[156:159], a[124:127], v[60:63]\n\t"
  "ds_read_b128 a[140:143], v3 offset:48128\n\t"
  "v_mfma_f32_16x16x32_bf16 v[56:59], a[156:159], a[120:123], v[56:59]\n\t"
  "v_mfma_f32_16x16x32_bf16 v[52:55], a[156:159], a[116:119], v[52:55]\n\t"
  "buffer_load_dwordx4 a[36:39], v0, s[20:23], s28 offen offset:1024 sc1\n\t"
  "v_mfma_f32_16x16x32_bf16 v[48:51], a[156:159], a[112:115], v[48:51]\n\t"
  "s_waitcnt lgkmcnt(3)\n\t"
  "v_mfma_f32_16x16x32_bf16 v[64:67], a[128:131], a[112:115], v[64:67]\n\t"
  "ds_read_b128 a[144:147], v3 offset:49152\n\t"
  "v_mfma_f32_16x16x32_bf16 v[68:71], a[128:131], a[116:119], v[68:71]\n\t"
  "v_mfma_f32_16x16x32_bf16 v[72:75], a[128:131], a[120:123], v[72:75]\n\t"
  "v_mfma_f32_16x16x32_bf16 v[76:79], a[128:131], a[124:127], v[76:79]\n\t"
  "s_waitcnt lgkmcnt(3)\n\t"
  "v_mfma_f32_16x16x32_bf16 v[92:95], a[132:135], a[124:127], v[92:95]\n\t"
  "ds_read_b128 a[148:151], v3 offset:50176\n\t"
  "v_mfma_f32_16x16x32_bf16 v[88:91], a[132:135], a[120:123], v[88:91]\n\t"
  "v_mfma_f32_16x16x32_bf16 v[84:87], a[132:135], a[116:119], v[84:87]\n\t"
  "buffer_load_dwordx4 a[40:43], v0, s[20:23], s28 offen offset:2048 sc1\n\t"
  "v_mfma_f32_16x16x32_bf16 v[80:83], a[132:135], a[112:115], v[80:83]\n\t"
  "s_waitcnt lgkmcnt(3)\n\t"
  "v_mfma_f32_16x16x32_bf16 v[96:99], a[136:139], a[112:115], v[96:99]\n\t"
  "ds_read_b128 a[152:155], v3 offset:51200\n\t"
  "v_mfma_f32_16x16x32_bf16 v[100:103], a[136:139], a[116:119], v[100:103]\n\t"
  "v_mfma_f32_16x16x32_bf16 v[104:107], a[136:139], a[120:123], v[104:107]\n\t"
  "buffer_load_dwordx4 a[44:47], v0, s[20:23], s28 offen offset:3072 sc1\n\t"
  "v_mfma_f32_16x16x32_bf16 v[108:111], a[136:139], a[124:127], v[108:111]\n\t"
  "s_waitcnt lgkmcnt(3)\n\t"
  "v_mfma_f32_16x16x32_bf16 v[124:127], a[140:143], a[124:127], v[124:127]\n\t"
  "ds_read_b128 a[156:159], v3 offset:52224\n\t"
  "s_add_u32 s28, s28, 131072\n\t"
  "v_mfma_f32_16x16x32_bf16 v[120:123], a[140:143], a[120:123], v[120:123]\n\t"
  "v_mfma_f32_16x16x32_bf16 v[116:119], a[140:143], a[116:119], v[116:119]\n\t"
  "v_mfma_f32_16x16x32_bf16 v[112:115], a[140:143], a[112:115], v[112:115]\n\t"
  "s_waitcnt lgkmcnt(3)\n\t"
  "v_mfma_f32_16x16x32_bf16 v[128:131], a[144:147], a[112:115], v[128:131]\n\t"
  "ds_read_b128 a[128:131], v3 offset:53248\n\t"
  "v_mfma_f32_16x16x32_bf16 v[132:135], a[144:147], a[116:119], v[132:135]\n\t"
  "v_mfma_f32_16x16x32_bf16 v[136:139], a[144:147], a[120:123], v[136:139]\n\t"
  "global_load_dword v26, v7, s[70:71]\n\t"
  "v_mfma_f32_16x16x32_bf16 v[140:143], a[144:147], a[124:127], v[140:143]\n\t"
  "s_waitcnt lgkmcnt(3)\n\t"
  "v_mfma_f32_16x16x32_bf16 v[156:159], a[148:151], a[124:127], v[156:159]\n\t"
  "ds_read_b128 a[132:135], v3 offset:54272\n\t"
  "v_mfma_f32_16x16x32_bf16 v[152:155], a[148:151], a[120:123], v[152:155]\n\t"
  "v_mfma_f32_16x16x32_bf16 v[148:151], a[148:151], a[116:119], v[148:151]\n\t"
  "buffer_load_dwordx4 a[48:51], v0, s[20:23], s28 offen offset:0 sc1\n\t"
  "v_mfma_f32_16x16x32_bf16 v[144:147], a[148:151], a[112:115], v[144:147]\n\t"
  "s_waitcnt lgkmcnt(3)\n\t"
  "v_mfma_f32_16x16x32_bf16 v[160:163], a[152:155], a[112:115], v[160:163]\n\t"
  "ds_read_b128 a[136:139], v3 offset:55296\n\t"
  "v_mfma_f32_16x16x32_bf16 v[164:167], a[152:155], a[116:119], v[164:167]\n\t"
  "v_mfma_f32_16x16x32_bf16 v[168:171], a[152:155], a[120:123], v[168:171]\n\t"
  "v_mfma_f32_16x16x32_bf16 v[172:175], a[152:155], a[124:127], v[172:175]\n\t"
  "s_waitcnt lgkmcnt(3)\n\t"
  "v_mfma_f32_16x16x32_bf16 v[188:191], a[156:159], a[124:127], v[188:191]\n\t"
  "ds_read_b128 a[140:143], v3 offset:56320\n\t"
  "s_waitcnt vmcnt(21)\n\t"
  "v_mfma_f32_16x16x32_bf16 v[184:187], a[156:159], a[120:123], v[184:187]\n\t"
  "s_barrier\n\t"
  "v_mfma_f32_16x16x32_bf16 v[180:183], a[156:159], a[116:119], v[180:183]\n\t"
  "buffer_load_dwordx4 a[52:55], v0, s[20:23], s28 offen offset:1024 sc1\n\t"
  "v_mfma_f32_16x16x32_bf16 v[176:179], a[156:159], a[112:115], v[176:179]\n\t"
  "s_waitcnt lgkmcnt(3)\n\t"
  "v_mfma_f32_16x16x32_bf16 v[192:195], a[128:131], a[112:115], v[192:195]\n\t"
  "ds_read_b128 a[144:147], v4 offset:28672\n\t"
  "v_mfma_f32_16x16x32_bf16 v[196:199], a[128:131], a[116:119], v[196:199]\n\t"
  "v_mfma_f32_16x16x32_bf16 v[200:203], a[128:131], a[120:123], v[200:203]\n\t"
  "v_mfma_f32_16x16x32_bf16 v[204:207], a[128:131], a[124:127], v[204:207]\n\t"
  "s_waitcnt lgkmcnt(3)\n\t"
  "v_mfma_f32_16x16x32_bf16 v[220:223], a[132:135], a[124:127], v[220:223]\n\t"
  "ds_read_b128 a[148:151], v4 offset:29696\n\t"
  "v_mfma_f32_16x16x32_bf16 v[216:219], a[132:135], a[120:123], v[216:219]\n\t"
  "v_mfma_f32_16x16x32_bf16 v[212:215], a[132:135], a[116:119], v[212:215]\n\t"
  "buffer_load_dwordx4 a[56:59], v0, s[20:23], s28 offen offset:2048 sc1\n\t"
  "v_mfma_f32_16x16x32_bf16 v[208:211], a[132:135], a[112:115], v[208:211]\n\t"
  "s_waitcnt lgkmcnt(3)\n\t"
  "v_mfma_f32_16x16x32_bf16 v[224:227], a[136:139], a[112:115], v[224:227]\n\t"
  "ds_read_b128 a[152:155], v4 offset:30720\n\t"
  "v_mfma_f32_16x16x32_bf16 v[228:231], a[136:139], a[116:119], v[228:231]\n\t"
  "v_mfma_f32_16x16x32_bf16 v[232:235], a[136:139], a[120:123], v[232:235]\n\t"
  "buffer_load_dwordx4 a[60:63], v0, s[20:23], s28 offen offset:3072 sc1\n\t"
  "v_mfma_f32_16x16x32_bf16 v[236:239], a[136:139], a[124:127], v[236:239]\n\t"
  "s_waitcnt lgkmcnt(3)\n\t"
  "v_mfma_f32_16x16x32_bf16 v[252:255], a[140:143], a[124:127], v[252:255]\n\t"
  "ds_read_b128 a[156:159], v4 offset:31744\n\t"
  "s_add_u32 s28, s28, 131072\n\t"
  "v_mfma_f32_16x16x32_bf16 v[248:251], a[140:143], a[120:123], v[248:251]\n\t"
  "v_mfma_f32_16x16x32_bf16 v[244:247], a[140:143], a[116:119], v[244:247]\n\t"
  "v_mfma_f32_16x16x32_bf16 v[240:243], a[140:143], a[112:115], v[240:243]\n\t"
  "s_waitcnt lgkmcnt(3) vmcnt(20)\n\t"
  "v_mfma_f32_16x16x32_bf16 v[32:35], a[144:147], a[0:3], v[32:35]\n\t"
  "ds_read_b128 a[128:131], v4 offset:32768\n\t"
  "v_mfma_f32_16x16x32_bf16 v[36:39], a[144:147], a[4:7], v[36:39]\n\t"
  "s_add_u32 m0, s30, 0\n\t"
  "v_mfma_f32_16x16x32_bf16 v[40:43], a[144:147], a[8:11], v[40:43]\n\t"
  "buffer_load_dwordx4 v1, s[20:23], s29 offen offset:0 sc1 lds\n\t"
  "v_mfma_f32_16x16x32_bf16 v[44:47], a[144:147], a[12:15], v[44:47]\n\t"
  "s_waitcnt lgkmcnt(3)\n\t"
  "v_mfma_f32_16x16x32_bf16 v[60:63], a[148:151], a[12:15], v[60:63]\n\t"
  "ds_read_b128 a[132:135], v4 offset:33792\n\t"
  "v_mfma_f32_16x16x32_bf16 v[56:59], a[148:151], a[8:11], v[56:59]\n\t"
  "v_mfma_f32_16x16x32_bf16 v[52:55], a[148:151], a[4:7], v[52:55]\n\t"
  "buffer_load_dwordx4 v1, s[20:23], s29 offen offset:1024 sc1 lds\n\t"
  "v_mfma_f32_16x16x32_bf16 v[48:51], a[148:151], a[0:3], v[48:51]\n\t"
  "s_waitcnt lgkmcnt(3)\n\t"
  "v_mfma_f32_16x16x32_bf16 v[64:67], a[152:155], a[0:3], v[64:67]\n\t"
  "ds_read_b128 a[136:139], v4 offset:34816\n\t"
  "v_mfma_f32_16x16x32_bf16 v[68:71], a[152:155], a[4:7], v[68:71]\n\t"
  "v_mfma_f32_16x16x32_bf16 v[72:75], a[152:155], a[8:11], v[72:75]\n\t"
  "v_mfma_f32_16x16x32_bf16 v[76:79], a[152:155], a[12:15], v[76:79]\n\t"
  "s_waitcnt lgkmcnt(3)\n\t"
  "v_mfma_f32_16x16x32_bf16 v[92:95], a[156:159], a[12:15], v[92:95]\n\t"
  "ds_read_b128 a[140:143], v4 offset:35840\n\t"
  "v_mfma_f32_16x16x32_bf16 v[88:91], a[156:159], a[8:11], v[88:91]\n\t"
  "v_mfma_f32_16x16x32_bf16 v[84:87], a[156:159], a[4:7], v[84:87]\n\t"
  "buffer_load_dwordx4 v1, s[20:23], s29 offen offset:2048 sc1 lds\n\t"
  "v_mfma_f32_16x16x32_bf16 v[80:83], a[156:159], a[0:3], v[80:83]\n\t"
  "s_waitcnt lgkmcnt(3)\n\t"
  "v_mfma_f32_16x16x32_bf16 v[96:99], a[128:131], a[0:3], v[96:99]\n\t"
  "ds_read_b128 a[144:147], v4 offset:36864\n\t"
  "v_mfma_f32_16x16x32_bf16 v[100:103], a[128:131], a[4:7], v[100:103]\n\t"
  "v_mfma_f32_16x16x32_bf16 v[104:107], a[128:131], a[8:11], v[104:107]\n\t"
  "v_mfma_f32_16x16x32_bf16 v[108:111], a[128:131], a[12:15], v[108:111]\n\t"
  "s_waitcnt lgkmcnt(3)\n\t"
  "v_mfma_f32_16x16x32_bf16 v[124:127], a[132:135], a[12:15], v[124:127]\n\t"
  "ds_read_b128 a[148:151], v4 offset:37888\n\t"
  "v_mfma_f32_16x16x32_bf16 v[120:123], a[132:135], a[8:11], v[120:123]\n\t"
  "v_mfma_f32_16x16x32_bf16 v[116:119], a[132:135], a[4:7], v[116:119]\n\t"
  "buffer_load_dwordx4 v1, s[20:23], s29 offen offset:3072 sc1 lds\n\t"
  "v_mfma_f32_16x16x32_bf16 v[112:115], a[132:135], a[0:3], v[112:115]\n\t"
  "s_waitcnt lgkmcnt(3)\n\t"
  "v_mfma_f32_16x16x32_bf16 v[128:131], a[136:139], a[0:3], v[128:131]\n\t"
  "ds_read_b128 a[152:155], v4 offset:38912\n\t"
  "v_mfma_f32_16x16x32_bf16 v[132:135], a[136:139], a[4:7], v[132:135]\n\t"
  "s_add_u32 m0, s30, 4096\n\t"
  "v_mfma_f32_16x16x32_bf16 v[136:139], a[136:139], a[8:11], v[136:139]\n\t"
  "buffer_load_dwordx4 v2, s[20:23], s29 offen offset:0 sc1 lds\n\t"
  "v_mfma_f32_16x16x32_bf16 v[140:143], a[136:139], a[12:15], v[140:143]\n\t"
  "s_waitcnt lgkmcnt(3)\n\t"
  "v_mfma_f32_16x16x32_bf16 v[156:159], a[140:143], a[12:15], v[156:159]\n\t"
  "ds_read_b128 a[156:159], v4 offset:39936\n\t"
  "v_mfma_f32_16x16x32_bf16 v[152:155], a[140:143], a[8:11], v[152:155]\n\t"
  "v_mfma_f32_16x16x32_bf16 v[148:151], a[140:143], a[4:7], v[148:151]\n\t"
  "v_mfma_f32_16x16x32_bf16 v[144:147], a[140:143], a[0:3], v[144:147]\n\t"
  "s_waitcnt lgkmcnt(3)\n\t"
  "v_mfma_f32_16x16x32_bf16 v[160:163], a[144:147], a[0:3], v[160:163]\n\t"
  "ds_read_b128 a[128:131], v4 offset:40960\n\t"
  "v_mfma_f32_16x16x32_bf16 v[164:167], a[144:147], a[4:7], v[164:167]\n\t"
  "v_mfma_f32_16x16x32_bf16 v[168:171], a[144:147], a[8:11], v[168:171]\n\t"
  "buffer_load_dwordx4 v2, s[20:23], s29 offen offset:1024 sc1 lds\n\t"
  "v_mfma_f32_16x16x32_bf16 v[172:175], a[144:147], a[12:15], v[172:175]\n\t"
  "s_waitcnt lgkmcnt(3)\n\t"
  "v_mfma_f32_16x16x32_bf16 v[188:191], a[148:151], a[12:15], v[188:191]\n\t"
  "ds_read_b128 a[132:135], v4 offset:41984\n\t"
  "v_mfma_f32_16x16x32_bf16 v[184:187], a[148:151], a[8:11], v[184:187]\n\t"
  "v_mfma_f32_16x16x32_bf16 v[180:183], a[148:151], a[4:7], v[180:183]\n\t"
  "buffer_load_dwordx4 v2, s[20:23], s29 offen offset:2048 sc1 lds\n\t"
  "v_mfma_f32_16x16x32_bf16 v[176:179], a[148:151], a[0:3], v[176:179]\n\t"
  "s_waitcnt lgkmcnt(3)\n\t"
  "v_mfma_f32_16x16x32_bf16 v[192:195], a[152:155], a[0:3], v[192:195]\n\t"
  "ds_read_b128 a[136:139], v4 offset:43008\n\t"
  "s_add_u32 s29, s29, 28672\n\t"
  "v_mfma_f32_16x16x32_bf16 v[196:199], a[152:155], a[4:7], v[196:199]\n\t"
  "v_mfma_f32_16x16x32_bf16 v[200:203], a[152:155], a[8:11], v[200:203]\n\t"
  "v_mfma_f32_16x16x32_bf16 v[204:207], a[152:155], a[12:15], v[204:207]\n\t"
  "s_waitcnt lgkmcnt(3)\n\t"
  "v_mfma_f32_16x16x32_bf16 v[220:223], a[156:159], a[12:15], v[220:223]\n\t"
  "ds_read_b128 a[140:143], v4 offset:44032\n\t"
  "v_mfma_f32_16x16x32_bf16 v[216:219], a[156:159], a[8:11], v[216:219]\n\t"
  "v_mfma_f32_16x16x32_bf16 v[212:215], a[156:159], a[4:7], v[212:215]\n\t"
  "buffer_load_dwordx4 a[64:67], v0, s[20:23], s28 offen offset:0 sc1\n\t"
  "v_mfma_f32_16x16x32_bf16 v[208:211], a[156:159], a[0:3], v[208:211]\n\t"
  "s_waitcnt lgkmcnt(3)\n\t"
  "v_mfma_f32_16x16x32_bf16 v[224:227], a[128:131], a[0:3], v[224:227]\n\t"
  "ds_read_b128 a[144:147], v4 offset:45056\n\t"
  "v_mfma_f32_16x16x32_bf16 v[228:231], a[128:131], a[4:7], v[228:231]\n\t"
  "v_mfma_f32_16x16x32_bf16 v[232:235], a[128:131], a[8:11], v[232:235]\n\t"
  "buffer_load_dwordx4 a[68:71], v0, s[20:23], s28 offen offset:1024 sc1\n\t"
  "v_mfma_f32_16x16x32_bf16 v[236:239], a[128:131], a[12:15], v[236:239]\n\t"
  "s_waitcnt lgkmcnt(3)\n\t"
  "v_mfma_f32_16x16x32_bf16 v[252:255], a[132:135], a[12:15], v[252:255]\n\t"
  "ds_read_b128 a[148:151], v4 offset:46080\n\t"
  "v_mfma_f32_16x16x32_bf16 v[248:251], a[132:135], a[8:11], v[248:251]\n\t"
  "v_mfma_f32_16x16x32_bf16 v[244:247], a[132:135], a[4:7], v[244:247]\n\t"
  "v_mfma_f32_16x16x32_bf16 v[240:243], a[132:135], a[0:3], v[240:243]\n\t"
  "s_waitcnt lgkmcnt(3) vmcnt(25)\n\t"
  "v_mfma_f32_16x16x32_bf16 v[32:35], a[136:139], a[16:19], v[32:35]\n\t"
  "ds_read_b128 a[152:155], v4 offset:47104\n\t"
  "v_mfma_f32_16x16x32_bf16 v[36:39], a[136:139], a[20:23], v[36:39]\n\t"
  "v_mfma_f32_16x16x32_bf16 v[40:43], a[136:139], a[24:27], v[40:43]\n\t"
  "buffer_load_dwordx4 a[72:75], v0, s[20:23], s28 offen offset:2048 sc1\n\t"
  "v_mfma_f32_16x16x32_bf16 v[44:47], a[136:139], a[28:31], v[44:47]\n\t"
  "s_waitcnt lgkmcnt(3)\n\t"
  "v_mfma_f32_16x16x32_bf16 v[60:63], a[140:143], a[28:31], v[60:63]\n\t"
  "ds_read_b128 a[156:159], v4 offset:48128\n\t"
  "v_mfma_f32_16x16x32_bf16 v[56:59], a[140:143], a[24:27], v[56:59]\n\t"
  "v_mfma_f32_16x16x32_bf16 v[52:55], a[140:143], a[20:23], v[52:55]\n\t"
  "v_mfma_f32_16x16x32_bf16 v[48:51], a[140:143], a[16:19], v[48:51]\n\t"
  "s_waitcnt lgkmcnt(3)\n\t"
  "v_mfma_f32_16x16x32_bf16 v[64:67], a[144:147], a[16:19], v[64:67]\n\t"
  "ds_read_b128 a[128:131], v4 offset:49152\n\t"
  "v_mfma_f32_16x16x32_bf16 v[68:71], a[144:147], a[20:23], v[68:71]\n\t"
  "v_mfma_f32_16x16x32_bf16 v[72:75], a[144:147], a[24:27], v[72:75]\n\t"
  "buffer_load_dwordx4 a[76:79], v0, s[20:23], s28 offen offset:3072 sc1\n\t"
  "v_mfma_f32_16x16x32_bf16 v[76:79], a[144:147], a[28:31], v[76:79]\n\t"
  "s_waitcnt lgkmcnt(3)\n\t"
  "v_mfma_f32_16x16x32_bf16 v[92:95], a[148:151], a[28:31], v[92:95]\n\t"
  "ds_read_b128 a[132:135], v4 offset:50176\n\t"
  "s_add_u32 s28, s28, 131072\n\t"
  "v_mfma_f32_16x16x32_bf16 v[88:91], a[148:151], a[24:27], v[88:91]\n\t"
  "v_mfma_f32_16x16x32_bf16 v[84:87], a[148:151], a[20:23], v[84:87]\n\t"
  "buffer_load_dwordx4 a[80:83], v0, s[20:23], s28 offen offset:0 sc1\n\t"
  "v_mfma_f32_16x16x32_bf16 v[80:83], a[148:151], a[16:19], v[80:83]\n\t"
  "s_waitcnt lgkmcnt(3)\n\t"
  "v_mfma_f32_16x16x32_bf16 v[96:99], a[152:155], a[16:19], v[96:99]\n\t"
  "ds_read_b128 a[136:139], v4 offset:51200\n\t"
  "v_mfma_f32_16x16x32_bf16 v[100:103], a[152:155], a[20:23], v[100:103]\n\t"
  "v_mfma_f32_16x16x32_bf16 v[104:107], a[152:155], a[24:27], v[104:107]\n\t"
  "v_mfma_f32_16x16x32_bf16 v[108:111], a[152:155], a[28:31], v[108:111]\n\t"
  "s_waitcnt lgkmcnt(3)\n\t"
  "v_mfma_f32_16x16x32_bf16 v[124:127], a[156:159], a[28:31], v[124:127]\n\t"
  "ds_read_b128 a[140:143], v4 offset:52224\n\t"
  "v_mfma_f32_16x16x32_bf16 v[120:123], a[156:159], a[24:27], v[120:123]\n\t"
  "v_mfma_f32_16x16x32_bf16 v[116:119], a[156:159], a[20:23], v[116:119]\n\t"
  "buffer_load_dwordx4 a[84:87], v0, s[20:23], s28 offen offset:1024 sc1\n\t"
  "v_mfma_f32_16x16x32_bf16 v[112:115], a[156:159], a[16:19], v[112:115]\n\t"
  "s_waitcnt lgkmcnt(3)\n\t"
  "v_mfma_f32_16x16x32_bf16 v[128:131], a[128:131], a[16:19], v[128:131]\n\t"
  "ds_read_b128 a[144:147], v4 offset:53248\n\t"
  "v_mfma_f32_16x16x32_bf16 v[132:135], a[128:131], a[20:23], v[132:135]\n\t"
  "v_mfma_f32_16x16x32_bf16 v[136:139], a[128:131], a[24:27], v[136:139]\n\t"
  "buffer_load_dwordx4 a[88:91], v0, s[20:23], s28 offen offset:2048 sc1\n\t"
  "v_mfma_f32_16x16x32_bf16 v[140:143], a[128:131], a[28:31], v[140:143]\n\t"
  "s_waitcnt lgkmcnt(3)\n\t"
  "v_mfma_f32_16x16x32_bf16 v[156:159], a[132:135], a[28:31], v[156:159]\n\t"
  "ds_read_b128 a[148:151], v4 offset:54272\n\t"
  "v_mfma_f32_16x16x32_bf16 v[152:155], a[132:135], a[24:27], v[152:155]\n\t"
  "v_mfma_f32_16x16x32_bf16 v[148:151], a[132:135], a[20:23], v[148:151]\n\t"
  "v_mfma_f32_16x16x32_bf16 v[144:147], a[132:135], a[16:19], v[144:147]\n\t"
  "s_waitcnt lgkmcnt(3)\n\t"
  "v_mfma_f32_16x16x32_bf16 v[160:163], a[136:139], a[16:19], v[160:163]\n\t"
  "ds_read_b128 a[152:155], v4 offset:55296\n\t"
  "v_mfma_f32_16x16x32_bf16 v[164:167], a[136:139], a[20:23], v[164:167]\n\t"
  "v_mfma_f32_16x16x32_bf16 v[168:171], a[136:139], a[24:27], v[168:171]\n\t"
  "buffer_load_dwordx4 a[92:95], v0, s[20:23], s28 offen offset:3072 sc1\n\t"
  "v_mfma_f32_16x16x32_bf16 v[172:175], a[136:139], a[28:31], v[172:175]\n\t"
  "s_waitcnt lgkmcnt(3)\n\t"
  "v_mfma_f32_16x16x32_bf16 v[188:191], a[140:143], a[28:31], v[188:191]\n\t"
  "ds_read_b128 a[156:159], v4 offset:56320\n\t"
  "s_add_u32 s28, s28, 131072\n\t"
  "v_mfma_f32_16x16x32_bf16 v[184:187], a[140:143], a[24:27], v[184:187]\n\t"
  "s_waitcnt vmcnt(24)\n\t"
  "v_mfma_f32_16x16x32_bf16 v[180:183], a[140:143], a[20:23], v[180:183]\n\t"
  "s_barrier\n\t"
  "v_mfma_f32_16x16x32_bf16 v[176:179], a[140:143], a[16:19], v[176:179]\n\t"
  "s_waitcnt lgkmcnt(3)\n\t"
  "v_mfma_f32_16x16x32_bf16 v[192:195], a[144:147], a[16:19], v[192:195]\n\t"
  "ds_read_b128 a[128:131], v4 offset:0\n\t"
  "v_mfma_f32_16x16x32_bf16 v[196:199], a[144:147], a[20:23], v[196:199]\n\t"
  "s_add_u32 m0, s30, 28672\n\t"
  "v_mfma_f32_16x16x32_bf16 v[200:203], a[144:147], a[24:27], v[200:203]\n\t"
  "buffer_load_dwordx4 v1, s[20:23], s29 offen offset:0 sc1 lds\n\t"
  "v_mfma_f32_16x16x32_bf16 v[204:207], a[144:147], a[28:31], v[204:207]\n\t"
  "s_waitcnt lgkmcnt(3)\n\t"
  "v_mfma_f32_16x16x32_bf16 v[220:223], a[148:151], a[28:31], v[220:223]\n\t"
  "ds_read_b128 a[132:135], v4 offset:1024\n\t"
  "v_mfma_f32_16x16x32_bf16 v[216:219], a[148:151], a[24:27], v[216:219]\n\t"
  "v_mfma_f32_16x16x32_bf16 v[212:215], a[148:151], a[20:23], v[212:215]\n\t"
  "buffer_load_dwordx4 v1, s[20:23], s29 offen offset:1024 sc1 lds\n\t"
  "v_mfma_f32_16x16x32_bf16 v[208:211], a[148:151], a[16:19], v[208:211]\n\t"
  "s_waitcnt lgkmcnt(3)\n\t"
  "v_mfma_f32_16x16x32_bf16 v[224:227], a[152:155], a[16:19], v[224:227]\n\t"
  "ds_read_b128 a[136:139], v4 offset:2048\n\t"
  "v_mfma_f32_16x16x32_bf16 v[228:231], a[152:155], a[20:23], v[228:231]\n\t"
  "v_mfma_f32_16x16x32_bf16 v[232:235], a[152:155], a[24:27], v[232:235]\n\t"
  "v_mfma_f32_16x16x32_bf16 v[236:239], a[152:155], a[28:31], v[236:239]\n\t"
  "s_waitcnt lgkmcnt(3)\n\t"
  "v_mfma_f32_16x16x32_bf16 v[252:255], a[156:159], a[28:31], v[252:255]\n\t"
  "ds_read_b128 a[140:143], v4 offset:3072\n\t"
  "v_mfma_f32_16x16x32_bf16 v[248:251], a[156:159], a[24:27], v[248:251]\n\t"
  "v_mfma_f32_16x16x32_bf16 v[244:247], a[156:159], a[20:23], v[244:247]\n\t"
  "buffer_load_dwordx4 v1, s[20:23], s29 offen offset:2048 sc1 lds\n\t"
  "v_mfma_f32_16x16x32_bf16 v[240:243], a[156:159], a[16:19], v[240:243]\n\t"
  "s_waitcnt lgkmcnt(3) vmcnt(23)\n\t"
  "v_mfma_f32_16x16x32_bf16 v[32:35], a[128:131], a[32:35], v[32:35]\n\t"
  "ds_read_b128 a[144:147], v4 offset:4096\n\t"
  "v_mfma_f32_16x16x32_bf16 v[36:39], a[128:131], a[36:39], v[36:39]\n\t"
  "v_mfma_f32_16x16x32_bf16 v[40:43], a[128:131], a[40:43], v[40:43]\n\t"
  "buffer_load_dwordx4 v1, s[20:23], s29 offen offset:3072 sc1 lds\n\t"
  "v_mfma_f32_16x16x32_bf16 v[44:47], a[128:131], a[44:47], v[44:47]\n\t"
  "s_waitcnt lgkmcnt(3)\n\t"
  "v_mfma_f32_16x16x32_bf16 v[60:63], a[132:135], a[44:47], v[60:63]\n\t"
  "ds_read_b128 a[148:151], v4 offset:5120\n\t"
  "v_mfma_f32_16x16x32_bf16 v[56:59], a[132:135], a[40:43], v[56:59]\n\t"
  "v_mfma_f32_16x16x32_bf16 v[52:55], a[132:135], a[36:39], v[52:55]\n\t"
  "v_mfma_f32_16x16x32_bf16 v[48:51], a[132:135], a[32:35], v[48:51]\n\t"
  "s_waitcnt lgkmcnt(3)\n\t"
  "v_mfma_f32_16x16x32_bf16 v[64:67], a[136:139], a[32:35], v[64:67]\n\t"
  "ds_read_b128 a[152:155], v4 offset:6144\n\t"
  "v_mfma_f32_16x16x32_bf16 v[68:71], a[136:139], a[36:39], v[68:71]\n\t"
  "s_add_u32 m0, s30, 32768\n\t"
  "v_mfma_f32_16x16x32_bf16 v[72:75], a[136:139], a[40:43], v[72:75]\n\t"
  "buffer_load_dwordx4 v2, s[20:23], s29 offen offset:0 sc1 lds\n\t"
  "v_mfma_f32_16x16x32_bf16 v[76:79], a[136:139], a[44:47], v[76:79]\n\t"
  "s_waitcnt lgkmcnt(3)\n\t"
  "v_mfma_f32_16x16x32_bf16 v[92:95], a[140:143], a[44:47], v[92:95]\n\t"
  "ds_read_b128 a[156:159], v4 offset:7168\n\t"
  "v_mfma_f32_16x16x32_bf16 v[88:91], a[140:143], a[40:43], v[88:91]\n\t"
  "v_mfma_f32_16x16x32_bf16 v[84:87], a[140:143], a[36:39], v[84:87]\n\t"
  "buffer_load_dwordx4 v2, s[20:23], s29 offen offset:1024 sc1 lds\n\t"
  "v_mfma_f32_16x16x32_bf16 v[80:83], a[140:143], a[32:35], v[80:83]\n\t"
  "s_waitcnt lgkmcnt(3)\n\t"
  "v_mfma_f32_16x16x32_bf16 v[96:99], a[144:147], a[32:35], v[96:99]\n\t"
  "ds_read_b128 a[128:131], v4 offset:8192\n\t"
  "v_mfma_f32_16x16x32_bf16 v[100:103], a[144:147], a[36:39], v[100:103]\n\t"
  "v_mfma_f32_16x16x32_bf16 v[104:107], a[144:147], a[40:43], v[104:107]\n\t"
  "v_mfma_f32_16x16x32_bf16 v[108:111], a[144:147], a[44:47], v[108:111]\n\t"
  "s_waitcnt lgkmcnt(3)\n\t"
  "v_mfma_f32_16x16x32_bf16 v[124:127], a[148:151], a[44:47], v[124:127]\n\t"
  "ds_read_b128 a[132:135], v4 offset:9216\n\t"
  "v_mfma_f32_16x16x32_bf16 v[120:123], a[148:151], a[40:43], v[120:123]\n\t"
  "v_mfma_f32_16x16x32_bf16 v[116:119], a[148:151], a[36:39], v[116:119]\n\t"
  "buffer_load_dwordx4 v2, s[20:23], s29 offen offset:2048 sc1 lds\n\t"
  "v_mfma_f32_16x16x32_bf16 v[112:115], a[148:151], a[32:35], v[112:115]\n\t"
  "s_waitcnt lgkmcnt(3)\n\t"
  "v_mfma_f32_16x16x32_bf16 v[128:131], a[152:155], a[32:35], v[128:131]\n\t"
  "ds_read_b128 a[136:139], v4 offset:10240\n\t"
  "s_add_u32 s29, s29, 28672\n\t"
  "v_mfma_f32_16x16x32_bf16 v[132:135], a[152:155], a[36:39], v[132:135]\n\t"
  "v_mfma_f32_16x16x32_bf16 v[136:139], a[152:155], a[40:43], v[136:139]\n\t"
  "v_mfma_f32_16x16x32_bf16 v[140:143], a[152:155], a[44:47], v[140:143]\n\t"
  "s_waitcnt lgkmcnt(3)\n\t"
  "v_mfma_f32_16x16x32_bf16 v[156:159], a[156:159], a[44:47], v[156:159]\n\t"
  "ds_read_b128 a[140:143], v4 offset:11264\n\t"
  "v_mfma_f32_16x16x32_bf16 v[152:155], a[156:159], a[40:43], v[152:155]\n\t"
  "v_mfma_f32_16x16x32_bf16 v[148:151], a[156:159], a[36:39], v[148:151]\n\t"
  "buffer_load_dwordx4 a[96:99], v0, s[20:23], s28 offen offset:0 sc1\n\t"
  "v_mfma_f32_16x16x32_bf16 v[144:147], a[156:159], a[32:35], v[144:147]\n\t"
  "s_waitcnt lgkmcnt(3)\n\t"
  "v_mfma_f32_16x16x32_bf16 v[160:163], a[128:131], a[32:35], v[160:163]\n\t"
  "ds_read_b128 a[144:147], v4 offset:12288\n\t"
  "v_mfma_f32_16x16x32_bf16 v[164:167], a[128:131], a[36:39], v[164:167]\n\t"
  "v_mfma_f32_16x16x32_bf16 v[168:171], a[128:131], a[40:43], v[168:171]\n\t"
  "buffer_load_dwordx4 a[100:103], v0, s[20:23], s28 offen offset:1024 sc1\n\t"
  "v_mfma_f32_16x16x32_bf16 v[172:175], a[128:131], a[44:47], v[172:175]\n\t"
  "s_waitcnt lgkmcnt(3)\n\t"
  "v_mfma_f32_16x16x32_bf16 v[188:191], a[132:135], a[44:47], v[188:191]\n\t"
  "ds_read_b128 a[148:151], v4 offset:13312\n\t"
  "v_mfma_f32_16x16x32_bf16 v[184:187], a[132:135], a[40:43], v[184:187]\n\t"
  "v_mfma_f32_16x16x32_bf16 v[180:183], a[132:135], a[36:39], v[180:183]\n\t"
  "v_mfma_f32_16x16x32_bf16 v[176:179], a[132:135], a[32:35], v[176:179]\n\t"
  "s_waitcnt lgkmcnt(3)\n\t"
  "v_mfma_f32_16x16x32_bf16 v[192:195], a[136:139], a[32:35], v[192:195]\n\t"
  "ds_read_b128 a[152:155], v4 offset:14336\n\t"
  "v_mfma_f32_16x16x32_bf16 v[196:199], a[136:139], a[36:39], v[196:199]\n\t"
  "v_mfma_f32_16x16x32_bf16 v[200:203], a[136:139], a[40:43], v[200:203]\n\t"
  "buffer_load_dwordx4 a[104:107], v0, s[20:23], s28 offen offset:2048 sc1\n\t"
  "v_mfma_f32_16x16x32_bf16 v[204:207], a[136:139], a[44:47], v[204:207]\n\t"
  "s_waitcnt lgkmcnt(3)\n\t"
  "v_mfma_f32_16x16x32_bf16 v[220:223], a[140:143], a[44:47], v[220:223]\n\t"
  "ds_read_b128 a[156:159], v4 offset:15360\n\t"
  "v_mfma_f32_16x16x32_bf16 v[216:219], a[140:143], a[40:43], v[216:219]\n\t"
  "v_mfma_f32_16x16x32_bf16 v[212:215], a[140:143], a[36:39], v[212:215]\n\t"
  "buffer_load_dwordx4 a[108:111], v0, s[20:23], s28 offen offset:3072 sc1\n\t"
  "v_mfma_f32_16x16x32_bf16 v[208:211], a[140:143], a[32:35], v[208:211]\n\t"
  "s_waitcnt lgkmcnt(3)\n\t"
  "v_mfma_f32_16x16x32_bf16 v[224:227], a[144:147], a[32:35], v[224:227]\n\t"
  "ds_read_b128 a[128:131], v4 offset:16384\n\t"
  "s_add_u32 s28, s28, 131072\n\t"
  "v_mfma_f32_16x16x32_bf16 v[228:231], a[144:147], a[36:39], v[228:231]\n\t"
  "v_mfma_f32_16x16x32_bf16 v[232:235], a[144:147], a[40:43], v[232:235]\n\t"
  "v_mfma_f32_16x16x32_bf16 v[236:239], a[144:147], a[44:47], v[236:239]\n\t"
  "s_waitcnt lgkmcnt(3)\n\t"
  "v_mfma_f32_16x16x32_bf16 v[252:255], a[148:151], a[44:47], v[252:255]\n\t"
  "ds_read_b128 a[132:135], v4 offset:17408\n\t"
  "v_mfma_f32_16x16x32_bf16 v[248:251], a[148:151], a[40:43], v[248:251]\n\t"
  "v_mfma_f32_16x16x32_bf16 v[244:247], a[148:151], a[36:39], v[244:247]\n\t"
  "buffer_load_dwordx4 a[112:115], v0, s[20:23], s28 offen offset:0 sc1\n\t"
  "v_mfma_f32_16x16x32_bf16 v[240:243], a[148:151], a[32:35], v[240:243]\n\t"
  "s_waitcnt lgkmcnt(3) vmcnt(27)\n\t"
  "v_mfma_f32_16x16x32_bf16 v[32:35], a[152:155], a[48:51], v[32:35]\n\t"
  "ds_read_b128 a[136:139], v4 offset:18432\n\t"
  "v_mfma_f32_16x16x32_bf16 v[36:39], a[152:155], a[52:55], v[36:39]\n\t"
  "v_mfma_f32_16x16x32_bf16 v[40:43], a[152:155], a[56:59], v[40:43]\n\t"
  "v_mfma_f32_16x16x32_bf16 v[44:47], a[152:155], a[60:63], v[44:47]\n\t"
  "s_waitcnt lgkmcnt(3)\n\t"
  "v_mfma_f32_16x16x32_bf16 v[60:63], a[156:159], a[60:63], v[60:63]\n\t"
  "ds_read_b128 a[140:143], v4 offset:19456\n\t"
  "v_mfma_f32_16x16x32_bf16 v[56:59], a[156:159], a[56:59], v[56:59]\n\t"
  "v_mfma_f32_16x16x32_bf16 v[52:55], a[156:159], a[52:55], v[52:55]\n\t"
  "buffer_load_dwordx4 a[116:119], v0, s[20:23], s28 offen offset:1024 sc1\n\t"
  "v_mfma_f32_16x16x32_bf16 v[48:51], a[156:159], a[48:51], v[48:51]\n\t"
  "s_waitcnt lgkmcnt(3)\n\t"
  "v_mfma_f32_16x16x32_bf16 v[64:67], a[128:131], a[48:51], v[64:67]\n\t"
  "ds_read_b128 a[144:147], v4 offset:20480\n\t"
  "v_mfma_f32_16x16x32_bf16 v[68:71], a[128:131], a[52:55], v[68:71]\n\t"
  "v_mfma_f32_16x16x32_bf16 v[72:75], a[128:131], a[56:59], v[72:75]\n\t"
  "buffer_load_dwordx4 a[120:123], v0, s[20:23], s28 offen offset:2048 sc1\n\t"
  "v_mfma_f32_16x16x32_bf16 v[76:79], a[128:131], a[60:63], v[76:79]\n\t"
  "s_waitcnt lgkmcnt(3)\n\t"
  "v_mfma_f32_16x16x32_bf16 v[92:95], a[132:135], a[60:63], v[92:95]\n\t"
  "ds_read_b128 a[148:151], v4 offset:21504\n\t"
  "v_mfma_f32_16x16x32_bf16 v[88:91], a[132:135], a[56:59], v[88:91]\n\t"
  "v_mfma_f32_16x16x32_bf16 v[84:87], a[132:135], a[52:55], v[84:87]\n\t"
  "v_mfma_f32_16x16x32_bf16 v[80:83], a[132:135], a[48:51], v[80:83]\n\t"
  "s_waitcnt lgkmcnt(3)\n\t"
  "v_mfma_f32_16x16x32_bf16 v[96:99], a[136:139], a[48:51], v[96:99]\n\t"
  "ds_read_b128 a[152:155], v4 offset:22528\n\t"
  "v_mfma_f32_16x16x32_bf16 v[100:103], a[136:139], a[52:55], v[100:103]\n\t"
  "v_mfma_f32_16x16x32_bf16 v[104:107], a[136:139], a[56:59], v[104:107]\n\t"
  "buffer_load_dwordx4 a[124:127], v0, s[20:23], s28 offen offset:3072 sc1\n\t"
  "v_mfma_f32_16x16x32_bf16 v[108:111], a[136:139], a[60:63], v[108:111]\n\t"
  "s_waitcnt lgkmcnt(3)\n\t"
  "v_mfma_f32_16x16x32_bf16 v[124:127], a[140:143], a[60:63], v[124:127]\n\t"
  "ds_read_b128 a[156:159], v4 offset:23552\n\t"
  "s_add_u32 s28, s28, 131072\n\t"
  "v_mfma_f32_16x16x32_bf16 v[120:123], a[140:143], a[56:59], v[120:123]\n\t"
  "v_mfma_f32_16x16x32_bf16 v[116:119], a[140:143], a[52:55], v[116:119]\n\t"
  "v_mfma_f32_16x16x32_bf16 v[112:115], a[140:143], a[48:51], v[112:115]\n\t"
  "s_waitcnt lgkmcnt(3)\n\t"
  "v_mfma_f32_16x16x32_bf16 v[128:131], a[144:147], a[48:51], v[128:131]\n\t"
  "ds_read_b128 a[128:131], v4 offset:24576\n\t"
  "v_mfma_f32_16x16x32_bf16 v[132:135], a[144:147], a[52:55], v[132:135]\n\t"
  "v_mfma_f32_16x16x32_bf16 v[136:139], a[144:147], a[56:59], v[136:139]\n\t"
  "v_mfma_f32_16x16x32_bf16 v[140:143], a[144:147], a[60:63], v[140:143]\n\t"
  "s_waitcnt lgkmcnt(3)\n\t"
  "v_mfma_f32_16x16x32_bf16 v[156:159], a[148:151], a[60:63], v[156:159]\n\t"
  "ds_read_b128 a[132:135], v4 offset:25600\n\t"
  "v_mfma_f32_16x16x32_bf16 v[152:155], a[148:151], a[56:59], v[152:155]\n\t"
  "v_mfma_f32_16x16x32_bf16 v[148:151], a[148:151], a[52:55], v[148:151]\n\t"
  "v_mfma_f32_16x16x32_bf16 v[144:147], a[148:151], a[48:51], v[144:147]\n\t"
  "s_waitcnt lgkmcnt(3)\n\t"
  "v_mfma_f32_16x16x32_bf16 v[160:163], a[152:155], a[48:51], v[160:163]\n\t"
  "ds_read_b128 a[136:139], v4 offset:26624\n\t"
  "v_mfma_f32_16x16x32_bf16 v[164:167], a[152:155], a[52:55], v[164:167]\n\t"
  "v_mfma_f32_16x16x32_bf16 v[168:171], a[152:155], a[56:59], v[168:171]\n\t"
  "v_mfma_f32_16x16x32_bf16 v[172:175], a[152:155], a[60:63], v[172:175]\n\t"
  "s_waitcnt lgkmcnt(3)\n\t"
  "v_mfma_f32_16x16x32_bf16 v[188:191], a[156:159], a[60:63], v[188:191]\n\t"
  "ds_read_b128 a[140:143], v4 offset:27648\n\t"
  "v_mfma_f32_16x16x32_bf16 v[184:187], a[156:159], a[56:59], v[184:187]\n\t"
  "s_waitcnt vmcnt(23)\n\t"
  "v_mfma_f32_16x16x32_bf16 v[180:183], a[156:159], a[52:55], v[180:183]\n\t"
  "s_barrier\n\t"
  "v_mfma_f32_16x16x32_bf16 v[176:179], a[156:159], a[48:51], v[176:179]\n\t"
  "s_waitcnt lgkmcnt(3)\n\t"
  "v_mfma_f32_16x16x32_bf16 v[192:195], a[128:131], a[48:51], v[192:195]\n\t"
  "ds_read_b128 a[144:147], v3 offset:0\n\t"
  "v_mfma_f32_16x16x32_bf16 v[196:199], a[128:131], a[52:55], v[196:199]\n\t"
  "s_add_u32 m0, s30, 86016\n\t"
  "v_mfma_f32_16x16x32_bf16 v[200:203], a[128:131], a[56:59], v[200:203]\n\t"
  "buffer_load_dwordx4 v1, s[20:23], s29 offen offset:0 sc1 lds\n\t"
  "v_mfma_f32_16x16x32_bf16 v[204:207], a[128:131], a[60:63], v[204:207]\n\t"
  "s_waitcnt lgkmcnt(3)\n\t"
  "v_mfma_f32_16x16x32_bf16 v[220:223], a[132:135], a[60:63], v[220:223]\n\t"
  "ds_read_b128 a[148:151], v3 offset:1024\n\t"
  "v_mfma_f32_16x16x32_bf16 v[216:219], a[132:135], a[56:59], v[216:219]\n\t"
  "v_mfma_f32_16x16x32_bf16 v[212:215], a[132:135], a[52:55], v[212:215]\n\t"
  "buffer_load_dwordx4 v1, s[20:23], s29 offen offset:1024 sc1 lds\n\t"
  "v_mfma_f32_16x16x32_bf16 v[208:211], a[132:135], a[48:51], v[208:211]\n\t"
  "s_waitcnt lgkmcnt(3)\n\t"
  "v_mfma_f32_16x16x32_bf16 v[224:227], a[136:139], a[48:51], v[224:227]\n\t"
  "ds_read_b128 a[152:155], v3 offset:2048\n\t"
  "v_mfma_f32_16x16x32_bf16 v[228:231], a[136:139], a[52:55], v[228:231]\n\t"
  "v_mfma_f32_16x16x32_bf16 v[232:235], a[136:139], a[56:59], v[232:235]\n\t"
  "buffer_load_dwordx4 v1, s[20:23], s29 offen offset:2048 sc1 lds\n\t"
  "v_mfma_f32_16x16x32_bf16 v[236:239], a[136:139], a[60:63], v[236:239]\n\t"
  "s_waitcnt lgkmcnt(3)\n\t"
  "v_mfma_f32_16x16x32_bf16 v[252:255], a[140:143], a[60:63], v[252:255]\n\t"
  "ds_read_b128 a[156:159], v3 offset:3072\n\t"
  "v_mfma_f32_16x16x32_bf16 v[248:251], a[140:143], a[56:59], v[248:251]\n\t"
  "v_mfma_f32_16x16x32_bf16 v[244:247], a[140:143], a[52:55], v[244:247]\n\t"
  "buffer_load_dwordx4 v1, s[20:23], s29 offen offset:3072 sc1 lds\n\t"
  "v_mfma_f32_16x16x32_bf16 v[240:243], a[140:143], a[48:51], v[240:243]\n\t"
  "s_waitcnt lgkmcnt(3) vmcnt(23)\n\t"
  "v_mfma_f32_16x16x32_bf16 v[32:35], a[144:147], a[64:67], v[32:35]\n\t"
  "ds_read_b128 a[128:131], v3 offset:4096\n\t"
  "v_mfma_f32_16x16x32_bf16 v[36:39], a[144:147], a[68:71], v[36:39]\n\t"
  "s_add_u32 m0, s30, 90112\n\t"
  "v_mfma_f32_16x16x32_bf16 v[40:43], a[144:147], a[72:75], v[40:43]\n\t"
  "buffer_load_dwordx4 v2, s[20:23], s29 offen offset:0 sc1 lds\n\t"
  "v_mfma_f32_16x16x32_bf16 v[44:47], a[144:147], a[76:79], v[44:47]\n\t"
  "s_waitcnt lgkmcnt(3)\n\t"
  "v_mfma_f32_16x16x32_bf16 v[60:63], a[148:151], a[76:79], v[60:63]\n\t"
  "ds_read_b128 a[132:135], v3 offset:5120\n\t"
  "v_mfma_f32_16x16x32_bf16 v[56:59], a[148:151], a[72:75], v[56:59]\n\t"
  "v_mfma_f32_16x16x32_bf16 v[52:55], a[148:151], a[68:71], v[52:55]\n\t"
  "buffer_load_dwordx4 v2, s[20:23], s29 offen offset:1024 sc1 lds\n\t"
  "v_mfma_f32_16x16x32_bf16 v[48:51], a[148:151], a[64:67], v[48:51]\n\t"
  "s_waitcnt lgkmcnt(3)\n\t"
  "v_mfma_f32_16x16x32_bf16 v[64:67], a[152:155], a[64:67], v[64:67]\n\t"
  "ds_read_b128 a[136:139], v3 offset:6144\n\t"
  "v_mfma_f32_16x16x32_bf16 v[68:71], a[152:155], a[68:71], v[68:71]\n\t"
  "v_mfma_f32_16x16x32_bf16 v[72:75], a[152:155], a[72:75], v[72:75]\n\t"
  "buffer_load_dwordx4 v2, s[20:23], s29 offen offset:2048 sc1 lds\n\t"
  "v_mfma_f32_16x16x32_bf16 v[76:79], a[152:155], a[76:79], v[76:79]\n\t"
  "s_waitcnt lgkmcnt(3)\n\t"
  "v_mfma_f32_16x16x32_bf16 v[92:95], a[156:159], a[76:79], v[92:95]\n\t"
  "ds_read_b128 a[140:143], v3 offset:7168\n\t"
  "s_add_u32 s29, s29, 28672\n\t"
  "v_mfma_f32_16x16x32_bf16 v[88:91], a[156:159], a[72:75], v[88:91]\n\t"
  "v_mfma_f32_16x16x32_bf16 v[84:87], a[156:159], a[68:71], v[84:87]\n\t"
  "buffer_load_dwordx4 a[0:3], v0, s[20:23], s28 offen offset:0 sc1\n\t"
  "v_mfma_f32_16x16x32_bf16 v[80:83], a[156:159], a[64:67], v[80:83]\n\t"
  "s_waitcnt lgkmcnt(3)\n\t"
  "v_mfma_f32_16x16x32_bf16 v[96:99], a[128:131], a[64:67], v[96:99]\n\t"
  "ds_read_b128 a[144:147], v3 offset:8192\n\t"
  "v_mfma_f32_16x16x32_bf16 v[100:103], a[128:131], a[68:71], v[100:103]\n\t"
  "v_mfma_f32_16x16x32_bf16 v[104:107], a[128:131], a[72:75], v[104:107]\n\t"
  "buffer_load_dwordx4 a[4:7], v0, s[20:23], s28 offen offset:1024 sc1\n\t"
  "v_mfma_f32_16x16x32_bf16 v[108:111], a[128:131], a[76:79], v[108:111]\n\t"
  "s_waitcnt lgkmcnt(3)\n\t"
  "v_mfma_f32_16x16x32_bf16 v[124:127], a[132:135], a[76:79], v[124:127]\n\t"
  "ds_read_b128 a[148:151], v3 offset:9216\n\t"
  "v_mfma_f32_16x16x32_bf16 v[120:123], a[132:135], a[72:75], v[120:123]\n\t"
  "v_mfma_f32_16x16x32_bf16 v[116:119], a[132:135], a[68:71], v[116:119]\n\t"
  "v_mfma_f32_16x16x32_bf16 v[112:115], a[132:135], a[64:67], v[112:115]\n\t"
  "s_waitcnt lgkmcnt(3)\n\t"
  "v_mfma_f32_16x16x32_bf16 v[128:131], a[136:139], a[64:67], v[128:131]\n\t"
  "ds_read_b128 a[152:155], v3 offset:10240\n\t"
  "v_mfma_f32_16x16x32_bf16 v[132:135], a[136:139], a[68:71], v[132:135]\n\t"
  "v_mfma_f32_16x16x32_bf16 v[136:139], a[136:139], a[72:75], v[136:139]\n\t"
  "buffer_load_dwordx4 a[8:11], v0, s[20:23], s28 offen offset:2048 sc1\n\t"
  "v_mfma_f32_16x16x32_bf16 v[140:143], a[136:139], a[76:79], v[140:143]\n\t"
  "s_waitcnt lgkmcnt(3)\n\t"
  "v_mfma_f32_16x16x32_bf16 v[156:159], a[140:143], a[76:79], v[156:159]\n\t"
  "ds_read_b128 a[156:159], v3 offset:11264\n\t"
  "v_mfma_f32_16x16x32_bf16 v[152:155], a[140:143], a[72:75], v[152:155]\n\t"
  "v_mfma_f32_16x16x32_bf16 v[148:151], a[140:143], a[68:71], v[148:151]\n\t"
  "buffer_load_dwordx4 a[12:15], v0, s[20:23], s28 offen offset:3072 sc1\n\t"
  "v_mfma_f32_16x16x32_bf16 v[144:147], a[140:143], a[64:67], v[144:147]\n\t"
  "s_waitcnt lgkmcnt(3)\n\t"
  "v_mfma_f32_16x16x32_bf16 v[160:163], a[144:147], a[64:67], v[160:163]\n\t"
  "ds_read_b128 a[128:131], v3 offset:12288\n\t"
  "s_add_u32 s28, s28, 131072\n\t"
  "v_mfma_f32_16x16x32_bf16 v[164:167], a[144:147], a[68:71], v[164:167]\n\t"
  "v_mfma_f32_16x16x32_bf16 v[168:171], a[144:147], a[72:75], v[168:171]\n\t"
  "v_mfma_f32_16x16x32_bf16 v[172:175], a[144:147], a[76:79], v[172:175]\n\t"
  "s_waitcnt lgkmcnt(3)\n\t"
  "v_mfma_f32_16x16x32_bf16 v[188:191], a[148:151], a[76:79], v[188:191]\n\t"
  "ds_read_b128 a[132:135], v3 offset:13312\n\t"
  "v_mfma_f32_16x16x32_bf16 v[184:187], a[148:151], a[72:75], v[184:187]\n\t"
  "v_mfma_f32_16x16x32_bf16 v[180:183], a[148:151], a[68:71], v[180:183]\n\t"
  "global_load_dword v26, v7, s[70:71]\n\t"
  "v_mfma_f32_16x16x32_bf16 v[176:179], a[148:151], a[64:67], v[176:179]\n\t"
  "s_waitcnt lgkmcnt(3)\n\t"
  "v_mfma_f32_16x16x32_bf16 v[192:195], a[152:155], a[64:67], v[192:195]\n\t"
  "ds_read_b128 a[136:139], v3 offset:14336\n\t"
  "v_mfma_f32_16x16x32_bf16 v[196:199], a[152:155], a[68:71], v[196:199]\n\t"
  "v_mfma_f32_16x16x32_bf16 v[200:203], a[152:155], a[72:75], v[200:203]\n\t"
  "buffer_load_dwordx4 a[16:19], v0, s[20:23], s28 offen offset:0 sc1\n\t"
  "v_mfma_f32_16x16x32_bf16 v[204:207], a[152:155], a[76:79], v[204:207]\n\t"
  "s_waitcnt lgkmcnt(3)\n\t"
  "v_mfma_f32_16x16x32_bf16 v[220:223], a[156:159], a[76:79], v[220:223]\n\t"
  "ds_read_b128 a[140:143], v3 offset:15360\n\t"
  "v_mfma_f32_16x16x32_bf16 v[216:219], a[156:159], a[72:75], v[216:219]\n\t"
  "v_mfma_f32_16x16x32_bf16 v[212:215], a[156:159], a[68:71], v[212:215]\n\t"
  "v_mfma_f32_16x16x32_bf16 v[208:211], a[156:159], a[64:67], v[208:211]\n\t"
  "s_waitcnt lgkmcnt(3)\n\t"
  "v_mfma_f32_16x16x32_bf16 v[224:227], a[128:131], a[64:67], v[224:227]\n\t"
  "ds_read_b128 a[144:147], v3 offset:16384\n\t"
  "v_mfma_f32_16x16x32_bf16 v[228:231], a[128:131], a[68:71], v[228:231]\n\t"
  "v_mfma_f32_16x16x32_bf16 v[232:235], a[128:131], a[72:75], v[232:235]\n\t"
  "buffer_load_dwordx4 a[20:23], v0, s[20:23], s28 offen offset:1024 sc1\n\t"
  "v_mfma_f32_16x16x32_bf16 v[236:239], a[128:131], a[76:79], v[236:239]\n\t"
  "s_waitcnt lgkmcnt(3)\n\t"
  "v_mfma_f32_16x16x32_bf16 v[252:255], a[132:135], a[76:79], v[252:255]\n\t"
  "ds_read_b128 a[148:151], v3 offset:17408\n\t"
  "v_mfma_f32_16x16x32_bf16 v[248:251], a[132:135], a[72:75], v[248:251]\n\t"
  "v_mfma_f32_16x16x32_bf16 v[244:247], a[132:135], a[68:71], v[244:247]\n\t"
  "buffer_load_dwordx4 a[24:27], v0, s[20:23], s28 offen offset:2048 sc1\n\t"
  "v_mfma_f32_16x16x32_bf16 v[240:243], a[132:135], a[64:67], v[240:243]\n\t"
  "s_waitcnt lgkmcnt(3) vmcnt(30)\n\t"
  "v_mfma_f32_16x16x32_bf16 v[32:35], a[136:139], a[80:83], v[32:35]\n\t"
  "ds_read_b128 a[152:155], v3 offset:18432\n\t"
  "v_mfma_f32_16x16x32_bf16 v[36:39], a[136:139], a[84:87], v[36:39]\n\t"
  "v_mfma_f32_16x16x32_bf16 v[40:43], a[136:139], a[88:91], v[40:43]\n\t"
  "v_mfma_f32_16x16x32_bf16 v[44:47], a[136:139], a[92:95], v[44:47]\n\t"
  "s_waitcnt lgkmcnt(3)\n\t"
  "v_mfma_f32_16x16x32_bf16 v[60:63], a[140:143], a[92:95], v[60:63]\n\t"
  "ds_read_b128 a[156:159], v3 offset:19456\n\t"
  "v_mfma_f32_16x16x32_bf16 v[56:59], a[140:143], a[88:91], v[56:59]\n\t"
  "v_mfma_f32_16x16x32_bf16 v[52:55], a[140:143], a[84:87], v[52:55]\n\t"
  "buffer_load_dwordx4 a[28:31], v0, s[20:23], s28 offen offset:3072 sc1\n\t"
  "v_mfma_f32_16x16x32_bf16 v[48:51], a[140:143], a[80:83], v[48:51]\n\t"
  "s_waitcnt lgkmcnt(3)\n\t"
  "v_mfma_f32_16x16x32_bf16 v[64:67], a[144:147], a[80:83], v[64:67]\n\t"
  "ds_read_b128 a[128:131], v3 offset:20480\n\t"
  "s_add_u32 s28, s28, 131072\n\t"
  "v_mfma_f32_16x16x32_bf16 v[68:71], a[144:147], a[84:87], v[68:71]\n\t"
  "v_mfma_f32_16x16x32_bf16 v[72:75], a[144:147], a[88:91], v[72:75]\n\t"
  "v_mfma_f32_16x16x32_bf16 v[76:79], a[144:147], a[92:95], v[76:79]\n\t"
  "s_waitcnt lgkmcnt(3)\n\t"
  "v_mfma_f32_16x16x32_bf16 v[92:95], a[148:151], a[92:95], v[92:95]\n\t"
  "ds_read_b128 a[132:135], v3 offset:21504\n\t"
  "v_mfma_f32_16x16x32_bf16 v[88:91], a[148:151], a[88:91], v[88:91]\n\t"
  "v_mfma_f32_16x16x32_bf16 v[84:87], a[148:151], a[84:87], v[84:87]\n\t"
  "v_mfma_f32_16x16x32_bf16 v[80:83], a[148:151], a[80:83], v[80:83]\n\t"
  "s_waitcnt lgkmcnt(3)\n\t"
  "v_mfma_f32_16x16x32_bf16 v[96:99], a[152:155], a[80:83], v[96:99]\n\t"
  "ds_read_b128 a[136:139], v3 offset:22528\n\t"
  "v_mfma_f32_16x16x32_bf16 v[100:103], a[152:155], a[84:87], v[100:103]\n\t"
  "v_mfma_f32_16x16x32_bf16 v[104:107], a[152:155], a[88:91], v[104:107]\n\t"
  "v_mfma_f32_16x16x32_bf16 v[108:111], a[152:155], a[92:95], v[108:111]\n\t"
  "s_waitcnt lgkmcnt(3)\n\t"
  "v_mfma_f32_16x16x32_bf16 v[124:127], a[156:159], a[92:95], v[124:127]\n\t"
  "ds_read_b128 a[140:143], v3 offset:23552\n\t"
  "v_mfma_f32_16x16x32_bf16 v[120:123], a[156:159], a[88:91], v[120:123]\n\t"
  "v_mfma_f32_16x16x32_bf16 v[116:119], a[156:159], a[84:87], v[116:119]\n\t"
  "v_mfma_f32_16x16x32_bf16 v[112:115], a[156:159], a[80:83], v[112:115]\n\t"
  "s_waitcnt lgkmcnt(3)\n\t"
  "v_mfma_f32_16x16x32_bf16 v[128:131], a[128:131], a[80:83], v[128:131]\n\t"
  "ds_read_b128 a[144:147], v3 offset:24576\n\t"
  "v_mfma_f32_16x16x32_bf16 v[132:135], a[128:131], a[84:87], v[132:135]\n\t"
  "v_mfma_f32_16x16x32_bf16 v[136:139], a[128:131], a[88:91], v[136:139]\n\t"
  "v_mfma_f32_16x16x32_bf16 v[140:143], a[128:131], a[92:95], v[140:143]\n\t"
  "s_waitcnt lgkmcnt(3)\n\t"
  "v_mfma_f32_16x16x32_bf16 v[156:159], a[132:135], a[92:95], v[156:159]\n\t"
  "ds_read_b128 a[148:151], v3 offset:25600\n\t"
  "v_mfma_f32_16x16x32_bf16 v[152:155], a[132:135], a[88:91], v[152:155]\n\t"
  "v_mfma_f32_16x16x32_bf16 v[148:151], a[132:135], a[84:87], v[148:151]\n\t"
  "v_mfma_f32_16x16x32_bf16 v[144:147], a[132:135], a[80:83], v[144:147]\n\t"
  "s_waitcnt lgkmcnt(3)\n\t"
  "v_mfma_f32_16x16x32_bf16 v[160:163], a[136:139], a[80:83], v[160:163]\n\t"
  "ds_read_b128 a[152:155], v3 offset:26624\n\t"
  "v_mfma_f32_16x16x32_bf16 v[164:167], a[136:139], a[84:87], v[164:167]\n\t"
  "v_mfma_f32_16x16x32_bf16 v[168:171], a[136:139], a[88:91], v[168:171]\n\t"
  "v_mfma_f32_16x16x32_bf16 v[172:175], a[136:139], a[92:95], v[172:175]\n\t"
  "s_waitcnt lgkmcnt(3)\n\t"
  "v_mfma_f32_16x16x32_bf16 v[188:191], a[140:143], a[92:95], v[188:191]\n\t"
  "ds_read_b128 a[156:159], v3 offset:27648\n\t"
  "v_mfma_f32_16x16x32_bf16 v[184:187], a[140:143], a[88:91], v[184:187]\n\t"
  "s_waitcnt vmcnt(24)\n\t"
  "v_mfma_f32_16x16x32_bf16 v[180:183], a[140:143], a[84:87], v[180:183]\n\t"
  "s_barrier\n\t"
  "v_mfma_f32_16x16x32_bf16 v[176:179], a[140:143], a[80:83], v[176:179]\n\t"
  "s_waitcnt lgkmcnt(3)\n\t"
  "v_mfma_f32_16x16x32_bf16 v[192:195], a[144:147], a[80:83], v[192:195]\n\t"
  "ds_read_b128 a[128:131], v3 offset:28672\n\t"
  "v_mfma_f32_16x16x32_bf16 v[196:199], a[144:147], a[84:87], v[196:199]\n\t"
  "s_add_u32 m0, s30, 57344\n\t"
  "v_mfma_f32_16x16x32_bf16 v[200:203], a[144:147], a[88:91], v[200:203]\n\t"
  "buffer_load_dwordx4 v1, s[20:23], s29 offen offset:0 sc1 lds\n\t"
  "v_mfma_f32_16x16x32_bf16 v[204:207], a[144:147], a[92:95], v[204:207]\n\t"
  "s_waitcnt lgkmcnt(3)\n\t"
  "v_mfma_f32_16x16x32_bf16 v[220:223], a[148:151], a[92:95], v[220:223]\n\t"
  "ds_read_b128 a[132:135], v3 offset:29696\n\t"
  "v_mfma_f32_16x16x32_bf16 v[216:219], a[148:151], a[88:91], v[216:219]\n\t"
  "v_mfma_f32_16x16x32_bf16 v[212:215], a[148:151], a[84:87], v[212:215]\n\t"
  "buffer_load_dwordx4 v1, s[20:23], s29 offen offset:1024 sc1 lds\n\t"
  "v_mfma_f32_16x16x32_bf16 v[208:211], a[148:151], a[80:83], v[208:211]\n\t"
  "s_waitcnt lgkmcnt(3)\n\t"
  "v_mfma_f32_16x16x32_bf16 v[224:227], a[152:155], a[80:83], v[224:227]\n\t"
  "ds_read_b128 a[136:139], v3 offset:30720\n\t"
  "v_mfma_f32_16x16x32_bf16 v[228:231], a[152:155], a[84:87], v[228:231]\n\t"
  "v_mfma_f32_16x16x32_bf16 v[232:235], a[152:155], a[88:91], v[232:235]\n\t"
  "buffer_load_dwordx4 v1, s[20:23], s29 offen offset:2048 sc1 lds\n\t"
  "v_mfma_f32_16x16x32_bf16 v[236:239], a[152:155], a[92:95], v[236:239]\n\t"
  "s_waitcnt lgkmcnt(3)\n\t"
  "v_mfma_f32_16x16x32_bf16 v[252:255], a[156:159], a[92:95], v[252:255]\n\t"
  "ds_read_b128 a[140:143], v3 offset:31744\n\t"
  "v_mfma_f32_16x16x32_bf16 v[248:251], a[156:159], a[88:91], v[248:251]\n\t"
  "v_mfma_f32_16x16x32_bf16 v[244:247], a[156:159], a[84:87], v[244:247]\n\t"
  "buffer_load_dwordx4 v1, s[20:23], s29 offen offset:3072 sc1 lds\n\t"
  "v_mfma_f32_16x16x32_bf16 v[240:243], a[156:159], a[80:83], v[240:243]\n\t"
  "s_waitcnt lgkmcnt(3) vmcnt(24)\n\t"
  "v_mfma_f32_16x16x32_bf16 v[32:35], a[128:131], a[96:99], v[32:35]\n\t"
  "ds_read_b128 a[144:147], v3 offset:32768\n\t"
  "v_mfma_f32_16x16x32_bf16 v[36:39], a[128:131], a[100:103], v[36:39]\n\t"
  "s_add_u32 m0, s30, 61440\n\t"
  "v_mfma_f32_16x16x32_bf16 v[40:43], a[128:131], a[104:107], v[40:43]\n\t"
  "buffer_load_dwordx4 v2, s[20:23], s29 offen offset:0 sc1 lds\n\t"
  "v_mfma_f32_16x16x32_bf16 v[44:47], a[128:131], a[108:111], v[44:47]\n\t"
  "s_waitcnt lgkmcnt(3)\n\t"
  "v_mfma_f32_16x16x32_bf16 v[60:63], a[132:135], a[108:111], v[60:63]\n\t"
  "ds_read_b128 a[148:151], v3 offset:33792\n\t"
  "v_mfma_f32_16x16x32_bf16 v[56:59], a[132:135], a[104:107], v[56:59]\n\t"
  "buffer_load_dwordx2 v[24:25], v30, s[20:23], 0 offen offset:0 sc1\n\t"
  "v_mfma_f32_16x16x32_bf16 v[52:55], a[132:135], a[100:103], v[52:55]\n\t"
  "buffer_load_dwordx4 v2, s[20:23], s29 offen offset:1024 sc1 lds\n\t"
  "v_mfma_f32_16x16x32_bf16 v[48:51], a[132:135], a[96:99], v[48:51]\n\t"
  "s_waitcnt lgkmcnt(3)\n\t"
  "v_mfma_f32_16x16x32_bf16 v[64:67], a[136:139], a[96:99], v[64:67]\n\t"
  "ds_read_b128 a[152:155], v3 offset:34816\n\t"
  "v_mfma_f32_16x16x32_bf16 v[68:71], a[136:139], a[100:103], v[68:71]\n\t"
  "v_mfma_f32_16x16x32_bf16 v[72:75], a[136:139], a[104:107], v[72:75]\n\t"
  "buffer_load_dwordx4 v2, s[20:23], s29 offen offset:2048 sc1 lds\n\t"
  "v_mfma_f32_16x16x32_bf16 v[76:79], a[136:139], a[108:111], v[76:79]\n\t"
  "s_waitcnt lgkmcnt(3)\n\t"
  "v_mfma_f32_16x16x32_bf16 v[92:95], a[140:143], a[108:111], v[92:95]\n\t"
  "ds_read_b128 a[156:159], v3 offset:35840\n\t"
  "s_add_u32 s29, s29, 28672\n\t"
  "v_mfma_f32_16x16x32_bf16 v[88:91], a[140:143], a[104:107], v[88:91]\n\t"
  "v_mfma_f32_16x16x32_bf16 v[84:87], a[140:143], a[100:103], v[84:87]\n\t"
  "buffer_load_dwordx4 a[32:35], v0, s[20:23], s28 offen offset:0 sc1\n\t"
  "v_mfma_f32_16x16x32_bf16 v[80:83], a[140:143], a[96:99], v[80:83]\n\t"
  "s_waitcnt lgkmcnt(3)\n\t"
  "v_mfma_f32_16x16x32_bf16 v[96:99], a[144:147], a[96:99], v[96:99]\n\t"
  "ds_read_b128 a[128:131], v3 offset:36864\n\t"
  "v_mfma_f32_16x16x32_bf16 v[100:103], a[144:147], a[100:103], v[100:103]\n\t"
  "v_mfma_f32_16x16x32_bf16 v[104:107], a[144:147], a[104:107], v[104:107]\n\t"
  "buffer_load_dwordx4 a[36:39], v0, s[20:23], s28 offen offset:1024 sc1\n\t"
  "v_mfma_f32_16x16x32_bf16 v[108:111], a[144:147], a[108:111], v[108:111]\n\t"
  "s_waitcnt lgkmcnt(3)\n\t"
  "v_mfma_f32_16x16x32_bf16 v[124:127], a[148:151], a[108:111], v[124:127]\n\t"
  "ds_read_b128 a[132:135], v3 offset:37888\n\t"
  "v_mfma_f32_16x16x32_bf16 v[120:123], a[148:151], a[104:107], v[120:123]\n\t"
  "v_mfma_f32_16x16x32_bf16 v[116:119], a[148:151], a[100:103], v[116:119]\n\t"
  "buffer_load_dwordx4 a[40:43], v0, s[20:23], s28 offen offset:2048 sc1\n\t"
  "v_mfma_f32_16x16x32_bf16 v[112:115], a[148:151], a[96:99], v[112:115]\n\t"
  "s_waitcnt lgkmcnt(3)\n\t"
  "v_mfma_f32_16x16x32_bf16 v[128:131], a[152:155], a[96:99], v[128:131]\n\t"
  "ds_read_b128 a[136:139], v3 offset:38912\n\t"
  "v_mfma_f32_16x16x32_bf16 v[132:135], a[152:155], a[100:103], v[132:135]\n\t"
  "v_mfma_f32_16x16x32_bf16 v[136:139], a[152:155], a[104:107], v[136:139]\n\t"
  "buffer_load_dwordx4 a[44:47], v0, s[20:23], s28 offen offset:3072 sc1\n\t"
  "v_mfma_f32_16x16x32_bf16 v[140:143], a[152:155], a[108:111], v[140:143]\n\t"
  "s_waitcnt lgkmcnt(3)\n\t"
  "v_mfma_f32_16x16x32_bf16 v[156:159], a[156:159], a[108:111], v[156:159]\n\t"
  "ds_read_b128 a[140:143], v3 offset:39936\n\t"
  "s_add_u32 s28, s28, 131072\n\t"
  "v_mfma_f32_16x16x32_bf16 v[152:155], a[156:159], a[104:107], v[152:155]\n\t"
  "v_mfma_f32_16x16x32_bf16 v[148:151], a[156:159], a[100:103], v[148:151]\n\t"
  "buffer_load_dwordx4 a[48:51], v0, s[20:23], s28 offen offset:0 sc1\n\t"
  "v_mfma_f32_16x16x32_bf16 v[144:147], a[156:159], a[96:99], v[144:147]\n\t"
  "s_waitcnt lgkmcnt(3)\n\t"
  "v_mfma_f32_16x16x32_bf16 v[160:163], a[128:131], a[96:99], v[160:163]\n\t"
  "ds_read_b128 a[144:147], v3 offset:40960\n\t"
  "v_mfma_f32_16x16x32_bf16 v[164:167], a[128:131], a[100:103], v[164:167]\n\t"
  "v_mfma_f32_16x16x32_bf16 v[168:171], a[128:131], a[104:107], v[168:171]\n\t"
  "buffer_load_dwordx4 a[52:55], v0, s[20:23], s28 offen offset:1024 sc1\n\t"
  "v_mfma_f32_16x16x32_bf16 v[172:175], a[128:131], a[108:111], v[172:175]\n\t"
  "s_waitcnt lgkmcnt(3)\n\t"
  "v_mfma_f32_16x16x32_bf16 v[188:191], a[132:135], a[108:111], v[188:191]\n\t"
  "ds_read_b128 a[148:151], v3 offset:41984\n\t"
  "v_mfma_f32_16x16x32_bf16 v[184:187], a[132:135], a[104:107], v[184:187]\n\t"
  "v_mfma_f32_16x16x32_bf16 v[180:183], a[132:135], a[100:103], v[180:183]\n\t"
  "buffer_load_dwordx4 a[56:59], v0, s[20:23], s28 offen offset:2048 sc1\n\t"
  "v_mfma_f32_16x16x32_bf16 v[176:179], a[132:135], a[96:99], v[176:179]\n\t"
  "s_waitcnt lgkmcnt(3)\n\t"
  "v_mfma_f32_16x16x32_bf16 v[192:195], a[136:139], a[96:99], v[192:195]\n\t"
  "ds_read_b128 a[152:155], v3 offset:43008\n\t"
  "v_mfma_f32_16x16x32_bf16 v[196:199], a[136:139], a[100:103], v[196:199]\n\t"
  "v_mfma_f32_16x16x32_bf16 v[200:203], a[136:139], a[104:107], v[200:203]\n\t"
  "v_mfma_f32_16x16x32_bf16 v[204:207], a[136:139], a[108:111], v[204:207]\n\t"
  "s_waitcnt lgkmcnt(3)\n\t"
  "v_mfma_f32_16x16x32_bf16 v[220:223], a[140:143], a[108:111], v[220:223]\n\t"
  "ds_read_b128 a[156:159], v3 offset:44032\n\t"
  "v_mfma_f32_16x16x32_bf16 v[216:219], a[140:143], a[104:107], v[216:219]\n\t"
  "v_mfma_f32_16x16x32_bf16 v[212:215], a[140:143], a[100:103], v[212:215]\n\t"
  "buffer_load_dwordx4 a[60:63], v0, s[20:23], s28 offen offset:3072 sc1\n\t"
  "v_mfma_f32_16x16x32_bf16 v[208:211], a[140:143], a[96:99], v[208:211]\n\t"
  "s_waitcnt lgkmcnt(3)\n\t"
  "v_mfma_f32_16x16x32_bf16 v[224:227], a[144:147], a[96:99], v[224:227]\n\t"
  "ds_read_b128 a[128:131], v3 offset:45056\n\t"
  "s_add_u32 s28, s28, 131072\n\t"
  "v_mfma_f32_16x16x32_bf16 v[228:231], a[144:147], a[100:103], v[228:231]\n\t"
  "v_mfma_f32_16x16x32_bf16 v[232:235], a[144:147], a[104:107], v[232:235]\n\t"
  "v_mfma_f32_16x16x32_bf16 v[236:239], a[144:147], a[108:111], v[236:239]\n\t"
  "s_waitcnt lgkmcnt(3)\n\t"
  "v_mfma_f32_16x16x32_bf16 v[252:255], a[148:151], a[108:111], v[252:255]\n\t"
  "ds_read_b128 a[132:135], v3 offset:46080\n\t"
  "v_mfma_f32_16x16x32_bf16 v[248:251], a[148:151], a[104:107], v[248:251]\n\t"
  "v_mfma_f32_16x16x32_bf16 v[244:247], a[148:151], a[100:103], v[244:247]\n\t"
  "v_mfma_f32_16x16x32_bf16 v[240:243], a[148:151], a[96:99], v[240:243]\n\t"
  "s_waitcnt lgkmcnt(3) vmcnt(32)\n\t"
  "v_mfma_f32_16x16x32_bf16 v[32:35], a[152:155], a[112:115], v[32:35]\n\t"
  "ds_read_b128 a[136:139], v3 offset:47104\n\t"
  "v_mfma_f32_16x16x32_bf16 v[36:39], a[152:155], a[116:119], v[36:39]\n\t"
  "v_mfma_f32_16x16x32_bf16 v[40:43], a[152:155], a[120:123], v[40:43]\n\t"
  "v_mfma_f32_16x16x32_bf16 v[44:47], a[152:155], a[124:127], v[44:47]\n\t"
  "s_waitcnt lgkmcnt(3)\n\t"
  "v_mfma_f32_16x16x32_bf16 v[60:63], a[156:159], a[124:127], v[60:63]\n\t"
  "ds_read_b128 a[140:143], v3 offset:48128\n\t"
  "v_mfma_f32_16x16x32_bf16 v[56:59], a[156:159], a[120:123], v[56:59]\n\t"
  "v_mfma_f32_16x16x32_bf16 v[52:55], a[156:159], a[116:119], v[52:55]\n\t"
  "v_mfma_f32_16x16x32_bf16 v[48:51], a[156:159], a[112:115], v[48:51]\n\t"
  "s_waitcnt lgkmcnt(3)\n\t"
  "v_mfma_f32_16x16x32_bf16 v[64:67], a[128:131], a[112:115], v[64:67]\n\t"
  "ds_read_b128 a[144:147], v3 offset:49152\n\t"
  "v_mfma_f32_16x16x32_bf16 v[68:71], a[128:131], a[116:119], v[68:71]\n\t"
  "v_mfma_f32_16x16x32_bf16 v[72:75], a[128:131], a[120:123], v[72:75]\n\t"
  "v_mfma_f32_16x16x32_bf16 v[76:79], a[128:131], a[124:127], v[76:79]\n\t"
  "s_waitcnt lgkmcnt(3)\n\t"
  "v_mfma_f32_16x16x32_bf16 v[92:95], a[132:135], a[124:127], v[92:95]\n\t"
  "ds_read_b128 a[148:151], v3 offset:50176\n\t"
  "v_mfma_f32_16x16x32_bf16 v[88:91], a[132:135], a[120:123], v[88:91]\n\t"
  "v_mfma_f32_16x16x32_bf16 v[84:87], a[132:135], a[116:119], v[84:87]\n\t"
  "v_mfma_f32_16x16x32_bf16 v[80:83], a[132:135], a[112:115], v[80:83]\n\t"
  "s_waitcnt lgkmcnt(3)\n\t"
  "v_mfma_f32_16x16x32_bf16 v[96:99], a[136:139], a[112:115], v[96:99]\n\t"
  "ds_read_b128 a[152:155], v3 offset:51200\n\t"
  "v_mfma_f32_16x16x32_bf16 v[100:103], a[136:139], a[116:119], v[100:103]\n\t"
  "v_mfma_f32_16x16x32_bf16 v[104:107], a[136:139], a[120:123], v[104:107]\n\t"
  "v_mfma_f32_16x16x32_bf16 v[108:111], a[136:139], a[124:127], v[108:111]\n\t"
  "s_waitcnt lgkmcnt(3)\n\t"
  "v_mfma_f32_16x16x32_bf16 v[124:127], a[140:143], a[124:127], v[124:127]\n\t"
  "ds_read_b128 a[156:159], v3 offset:52224\n\t"
  "v_mfma_f32_16x16x32_bf16 v[120:123], a[140:143], a[120:123], v[120:123]\n\t"
  "v_mfma_f32_16x16x32_bf16 v[116:119], a[140:143], a[116:119], v[116:119]\n\t"
  "v_mfma_f32_16x16x32_bf16 v[112:115], a[140:143], a[112:115], v[112:115]\n\t"
  "s_waitcnt lgkmcnt(3)\n\t"
  "v_mfma_f32_16x16x32_bf16 v[128:131], a[144:147], a[112:115], v[128:131]\n\t"
  "ds_read_b128 a[128:131], v3 offset:53248\n\t"
  "v_mfma_f32_16x16x32_bf16 v[132:135], a[144:147], a[116:119], v[132:135]\n\t"
  "v_mfma_f32_16x16x32_bf16 v[136:139], a[144:147], a[120:123], v[136:139]\n\t"
  "v_mfma_f32_16x16x32_bf16 v[140:143], a[144:147], a[124:127], v[140:143]\n\t"
  "s_waitcnt lgkmcnt(3)\n\t"
  "v_mfma_f32_16x16x32_bf16 v[156:159], a[148:151], a[124:127], v[156:159]\n\t"
  "ds_read_b128 a[132:135], v3 offset:54272\n\t"
  "v_mfma_f32_16x16x32_bf16 v[152:155], a[148:151], a[120:123], v[152:155]\n\t"
  "v_mfma_f32_16x16x32_bf16 v[148:151], a[148:151], a[116:119], v[148:151]\n\t"
  "v_mfma_f32_16x16x32_bf16 v[144:147], a[148:151], a[112:115], v[144:147]\n\t"
  "s_waitcnt lgkmcnt(3)\n\t"
  "v_mfma_f32_16x16x32_bf16 v[160:163], a[152:155], a[112:115], v[160:163]\n\t"
  "ds_read_b128 a[136:139], v3 offset:55296\n\t"
  "v_mfma_f32_16x16x32_bf16 v[164:167], a[152:155], a[116:119], v[164:167]\n\t"
  "v_mfma_f32_16x16x32_bf16 v[168:171], a[152:155], a[120:123], v[168:171]\n\t"
  "v_mfma_f32_16x16x32_bf16 v[172:175], a[152:155], a[124:127], v[172:175]\n\t"
  "s_waitcnt lgkmcnt(3)\n\t"
  "v_mfma_f32_16x16x32_bf16 v[188:191], a[156:159], a[124:127], v[188:191]\n\t"
  "ds_read_b128 a[140:143], v3 offset:56320\n\t"
  "v_mfma_f32_16x16x32_bf16 v[184:187], a[156:159], a[120:123], v[184:187]\n\t"
  "s_waitcnt vmcnt(25)\n\t"
  "v_mfma_f32_16x16x32_bf16 v[180:183], a[156:159], a[116:119], v[180:183]\n\t"
  "s_barrier\n\t"
  "v_mfma_f32_16x16x32_bf16 v[176:179], a[156:159], a[112:115], v[176:179]\n\t"
  "s_waitcnt lgkmcnt(3)\n\t"
  "v_mfma_f32_16x16x32_bf16 v[192:195], a[128:131], a[112:115], v[192:195]\n\t"
  "ds_read_b128 a[144:147], v4 offset:28672\n\t"
  "v_mfma_f32_16x16x32_bf16 v[196:199], a[128:131], a[116:119], v[196:199]\n\t"
  "s_add_u32 m0, s30, 0\n\t"
  "v_mfma_f32_16x16x32_bf16 v[200:203], a[128:131], a[120:123], v[200:203]\n\t"
  "buffer_load_dwordx4 v1, s[20:23], s29 offen offset:0 sc1 lds\n\t"
  "v_mfma_f32_16x16x32_bf16 v[204:207], a[128:131], a[124:127], v[204:207]\n\t"
  "s_waitcnt lgkmcnt(3)\n\t"
  "v_mfma_f32_16x16x32_bf16 v[220:223], a[132:135], a[124:127], v[220:223]\n\t"
  "ds_read_b128 a[148:151], v4 offset:29696\n\t"
  "v_mfma_f32_16x16x32_bf16 v[216:219], a[132:135], a[120:123], v[216:219]\n\t"
  "v_mfma_f32_16x16x32_bf16 v[212:215], a[132:135], a[116:119], v[212:215]\n\t"
  "buffer_load_dwordx4 v1, s[20:23], s29 offen offset:1024 sc1 lds\n\t"
  "v_mfma_f32_16x16x32_bf16 v[208:211], a[132:135], a[112:115], v[208:211]\n\t"
  "s_waitcnt lgkmcnt(3)\n\t"
  "v_mfma_f32_16x16x32_bf16 v[224:227], a[136:139], a[112:115], v[224:227]\n\t"
  "ds_read_b128 a[152:155], v4 offset:30720\n\t"
  "v_mfma_f32_16x16x32_bf16 v[228:231], a[136:139], a[116:119], v[228:231]\n\t"
  "v_mfma_f32_16x16x32_bf16 v[232:235], a[136:139], a[120:123], v[232:235]\n\t"
  "buffer_load_dwordx4 v1, s[20:23], s29 offen offset:2048 sc1 lds\n\t"
  "v_mfma_f32_16x16x32_bf16 v[236:239], a[136:139], a[124:127], v[236:239]\n\t"
  "s_waitcnt lgkmcnt(3)\n\t"
  "v_mfma_f32_16x16x32_bf16 v[252:255], a[140:143], a[124:127], v[252:255]\n\t"
  "ds_read_b128 a[156:159], v4 offset:31744\n\t"
  "v_mfma_f32_16x16x32_bf16 v[248:251], a[140:143], a[120:123], v[248:251]\n\t"
  "v_mfma_f32_16x16x32_bf16 v[244:247], a[140:143], a[116:119], v[244:247]\n\t"
  "buffer_load_dwordx4 v1, s[20:23], s29 offen offset:3072 sc1 lds\n\t"
  "v_mfma_f32_16x16x32_bf16 v[240:243], a[140:143], a[112:115], v[240:243]\n\t"
  "s_waitcnt lgkmcnt(3) vmcnt(25)\n\t"
  "v_mfma_f32_16x16x32_bf16 v[32:35], a[144:147], a[0:3], v[32:35]\n\t"
  "ds_read_b128 a[128:131], v4 offset:32768\n\t"
  "v_mfma_f32_16x16x32_bf16 v[36:39], a[144:147], a[4:7], v[36:39]\n\t"
  "s_add_u32 m0, s30, 4096\n\t"
  "v_mfma_f32_16x16x32_bf16 v[40:43], a[144:147], a[8:11], v[40:43]\n\t"
  "buffer_load_dwordx4 v2, s[20:23], s29 offen offset:0 sc1 lds\n\t"
  "v_mfma_f32_16x16x32_bf16 v[44:47], a[144:147], a[12:15], v[44:47]\n\t"
  "s_waitcnt lgkmcnt(3)\n\t"
  "v_mfma_f32_16x16x32_bf16 v[60:63], a[148:151], a[12:15], v[60:63]\n\t"
  "ds_read_b128 a[132:135], v4 offset:33792\n\t"
  "v_mfma_f32_16x16x32_bf16 v[56:59], a[148:151], a[8:11], v[56:59]\n\t"
  "v_mfma_f32_16x16x32_bf16 v[52:55], a[148:151], a[4:7], v[52:55]\n\t"
  "buffer_load_dwordx4 v2, s[20:23], s29 offen offset:1024 sc1 lds\n\t"
  "v_mfma_f32_16x16x32_bf16 v[48:51], a[148:151], a[0:3], v[48:51]\n\t"
  "s_waitcnt lgkmcnt(3)\n\t"
  "v_mfma_f32_16x16x32_bf16 v[64:67], a[152:155], a[0:3], v[64:67]\n\t"
  "ds_read_b128 a[136:139], v4 offset:34816\n\t"
  "v_mfma_f32_16x16x32_bf16 v[68:71], a[152:155], a[4:7], v[68:71]\n\t"
  "v_mfma_f32_16x16x32_bf16 v[72:75], a[152:155], a[8:11], v[72:75]\n\t"
  "buffer_load_dwordx4 v2, s[20:23], s29 offen offset:2048 sc1 lds\n\t"
  "v_mfma_f32_16x16x32_bf16 v[76:79], a[152:155], a[12:15], v[76:79]\n\t"
  "s_waitcnt lgkmcnt(3)\n\t"
  "v_mfma_f32_16x16x32_bf16 v[92:95], a[156:159], a[12:15], v[92:95]\n\t"
  "ds_read_b128 a[140:143], v4 offset:35840\n\t"
  "s_add_u32 s29, s29, 28672\n\t"
  "v_mfma_f32_16x16x32_bf16 v[88:91], a[156:159], a[8:11], v[88:91]\n\t"
  "v_mfma_f32_16x16x32_bf16 v[84:87], a[156:159], a[4:7], v[84:87]\n\t"
  "buffer_load_dwordx4 a[64:67], v0, s[20:23], s28 offen offset:0 sc1\n\t"
  "v_mfma_f32_16x16x32_bf16 v[80:83], a[156:159], a[0:3], v[80:83]\n\t"
  "s_waitcnt lgkmcnt(3)\n\t"
  "v_mfma_f32_16x16x32_bf16 v[96:99], a[128:131], a[0:3], v[96:99]\n\t"
  "ds_read_b128 a[144:147], v4 offset:36864\n\t"
  "v_mfma_f32_16x16x32_bf16 v[100:103], a[128:131], a[4:7], v[100:103]\n\t"
  "v_mfma_f32_16x16x32_bf16 v[104:107], a[128:131], a[8:11], v[104:107]\n\t"
  "buffer_load_dwordx4 a[68:71], v0, s[20:23], s28 offen offset:1024 sc1\n\t"
  "v_mfma_f32_16x16x32_bf16 v[108:111], a[128:131], a[12:15], v[108:111]\n\t"
  "s_waitcnt lgkmcnt(3)\n\t"
  "v_mfma_f32_16x16x32_bf16 v[124:127], a[132:135], a[12:15], v[124:127]\n\t"
  "ds_read_b128 a[148:151], v4 offset:37888\n\t"
  "v_mfma_f32_16x16x32_bf16 v[120:123], a[132:135], a[8:11], v[120:123]\n\t"
  "v_mfma_f32_16x16x32_bf16 v[116:119], a[132:135], a[4:7], v[116:119]\n\t"
  "buffer_load_dwordx4 a[72:75], v0, s[20:23], s28 offen offset:2048 sc1\n\t"
  "v_mfma_f32_16x16x32_bf16 v[112:115], a[132:135], a[0:3], v[112:115]\n\t"
  "s_waitcnt lgkmcnt(3)\n\t"
  "v_mfma_f32_16x16x32_bf16 v[128:131], a[136:139], a[0:3], v[128:131]\n\t"
  "ds_read_b128 a[152:155], v4 offset:38912\n\t"
  "v_mfma_f32_16x16x32_bf16 v[132:135], a[136:139], a[4:7], v[132:135]\n\t"
  "v_mfma_f32_16x16x32_bf16 v[136:139], a[136:139], a[8:11], v[136:139]\n\t"
  "buffer_load_dwordx4 a[76:79], v0, s[20:23], s28 offen offset:3072 sc1\n\t"
  "v_mfma_f32_16x16x32_bf16 v[140:143], a[136:139], a[12:15], v[140:143]\n\t"
  "s_waitcnt lgkmcnt(3)\n\t"
  "v_mfma_f32_16x16x32_bf16 v[156:159], a[140:143], a[12:15], v[156:159]\n\t"
  "ds_read_b128 a[156:159], v4 offset:39936\n\t"
  "s_add_u32 s28, s28, 131072\n\t"
  "v_mfma_f32_16x16x32_bf16 v[152:155], a[140:143], a[8:11], v[152:155]\n\t"
  "v_mfma_f32_16x16x32_bf16 v[148:151], a[140:143], a[4:7], v[148:151]\n\t"
  "buffer_load_dwordx4 a[80:83], v0, s[20:23], s28 offen offset:0 sc1\n\t"
  "v_mfma_f32_16x16x32_bf16 v[144:147], a[140:143], a[0:3], v[144:147]\n\t"
  "s_waitcnt lgkmcnt(3)\n\t"
  "v_mfma_f32_16x16x32_bf16 v[160:163], a[144:147], a[0:3], v[160:163]\n\t"
  "ds_read_b128 a[128:131], v4 offset:40960\n\t"
  "v_mfma_f32_16x16x32_bf16 v[164:167], a[144:147], a[4:7], v[164:167]\n\t"
  "v_mfma_f32_16x16x32_bf16 v[168:171], a[144:147], a[8:11], v[168:171]\n\t"
  "buffer_load_dwordx4 a[84:87], v0, s[20:23], s28 offen offset:1024 sc1\n\t"
  "v_mfma_f32_16x16x32_bf16 v[172:175], a[144:147], a[12:15], v[172:175]\n\t"
  "s_waitcnt lgkmcnt(3)\n\t"
  "v_mfma_f32_16x16x32_bf16 v[188:191], a[148:151], a[12:15], v[188:191]\n\t"
  "ds_read_b128 a[132:135], v4 offset:41984\n\t"
  "v_mfma_f32_16x16x32_bf16 v[184:187], a[148:151], a[8:11], v[184:187]\n\t"
  "v_mfma_f32_16x16x32_bf16 v[180:183], a[148:151], a[4:7], v[180:183]\n\t"
  "buffer_load_dwordx4 a[88:91], v0, s[20:23], s28 offen offset:2048 sc1\n\t"
  "v_mfma_f32_16x16x32_bf16 v[176:179], a[148:151], a[0:3], v[176:179]\n\t"
  "s_waitcnt lgkmcnt(3)\n\t"
  "v_mfma_f32_16x16x32_bf16 v[192:195], a[152:155], a[0:3], v[192:195]\n\t"
  "ds_read_b128 a[136:139], v4 offset:43008\n\t"
  "v_mfma_f32_16x16x32_bf16 v[196:199], a[152:155], a[4:7], v[196:199]\n\t"
  "v_mfma_f32_16x16x32_bf16 v[200:203], a[152:155], a[8:11], v[200:203]\n\t"
  "buffer_load_dwordx4 a[92:95], v0, s[20:23], s28 offen offset:3072 sc1\n\t"
  "v_mfma_f32_16x16x32_bf16 v[204:207], a[152:155], a[12:15], v[204:207]\n\t"
  "s_waitcnt lgkmcnt(3)\n\t"
  "v_mfma_f32_16x16x32_bf16 v[220:223], a[156:159], a[12:15], v[220:223]\n\t"
  "ds_read_b128 a[140:143], v4 offset:44032\n\t"
  "s_add_u32 s28, s28, 131072\n\t"
  "v_mfma_f32_16x16x32_bf16 v[216:219], a[156:159], a[8:11], v[216:219]\n\t"
  "v_mfma_f32_16x16x32_bf16 v[212:215], a[156:159], a[4:7], v[212:215]\n\t"
  "v_mfma_f32_16x16x32_bf16 v[208:211], a[156:159], a[0:3], v[208:211]\n\t"
  "s_waitcnt lgkmcnt(3)\n\t"
  "v_mfma_f32_16x16x32_bf16 v[224:227], a[128:131], a[0:3], v[224:227]\n\t"
  "ds_read_b128 a[144:147], v4 offset:45056\n\t"
  "v_mfma_f32_16x16x32_bf16 v[228:231], a[128:131], a[4:7], v[228:231]\n\t"
  "v_mfma_f32_16x16x32_bf16 v[232:235], a[128:131], a[8:11], v[232:235]\n\t"
  "v_mfma_f32_16x16x32_bf16 v[236:239], a[128:131], a[12:15], v[236:239]\n\t"
  "s_waitcnt lgkmcnt(3)\n\t"
  "v_mfma_f32_16x16x32_bf16 v[252:255], a[132:135], a[12:15], v[252:255]\n\t"
  "ds_read_b128 a[148:151], v4 offset:46080\n\t"
  "v_mfma_f32_16x16x32_bf16 v[248:251], a[132:135], a[8:11], v[248:251]\n\t"
  "v_mfma_f32_16x16x32_bf16 v[244:247], a[132:135], a[4:7], v[244:247]\n\t"
  "v_mfma_f32_16x16x32_bf16 v[240:243], a[132:135], a[0:3], v[240:243]\n\t"
  "s_waitcnt lgkmcnt(3) vmcnt(31)\n\t"
  "v_mfma_f32_16x16x32_bf16 v[32:35], a[136:139], a[16:19], v[32:35]\n\t"
  "ds_read_b128 a[152:155], v4 offset:47104\n\t"
  "v_mfma_f32_16x16x32_bf16 v[36:39], a[136:139], a[20:23], v[36:39]\n\t"
  "v_mfma_f32_16x16x32_bf16 v[40:43], a[136:139], a[24:27], v[40:43]\n\t"
  "v_mfma_f32_16x16x32_bf16 v[44:47], a[136:139], a[28:31], v[44:47]\n\t"
  "s_waitcnt lgkmcnt(3)\n\t"
  "v_mfma_f32_16x16x32_bf16 v[60:63], a[140:143], a[28:31], v[60:63]\n\t"
  "ds_read_b128 a[156:159], v4 offset:48128\n\t"
  "v_mfma_f32_16x16x32_bf16 v[56:59], a[140:143], a[24:27], v[56:59]\n\t"
  "s_waitcnt vmcnt(25)\n\t"
  "s_mov_b32 s72, 0x20000\n\t"
  "s_not_b32 s68, s34\n\t"
  "s_mov_b32 s73, 64\n\t"
  "s_branch LPEc_%=\n\t"
  "LPEa_%=:\n\t"
  "v_not_b32 v24, s34\n\t"
  "buffer_load_dwordx2 v[24:25], v30, s[20:23], 0 offen offset:0 sc1\n\t"
  "s_mov_b32 s73, 64\n\t"
  "LPEw_%=:\n\t"
  "s_sleep 1\n\t"
  "LPEc_%=:\n\t"
  "v_cmp_eq_u32 vcc, s68, v24\n\t"
  "s_nop 1\n\t"
  "s_cmp_eq_u64 vcc, 0\n\t"
  "v_cmp_ne_u64 vcc, s[34:35], v[24:25]\n\t"
  "s_cbranch_vccz LPEb_%=\n\t"
  "s_cbranch_scc1 LPEr_%=\n\t"
  "s_sub_u32 s73, s73, 1\n\t"
  "s_cbranch_scc0 LPEw_%=\n\t"
  "s_waitcnt vmcnt(0)\n\t"
  "LPEr_%=:\n\t"
  "s_sleep 2\n\t"
  "s_sub_u32 s72, s72, 1\n\t"
  "s_cbranch_scc0 LPEa_%=\n\t"
  "LPEb_%=:\n\t"
  "v_mfma_f32_16x16x32_bf16 v[52:55], a[140:143], a[20:23], v[52:55]\n\t"
  "v_mfma_f32_16x16x32_bf16 v[48:51], a[140:143], a[16:19], v[48:51]\n\t"
  "s_waitcnt lgkmcnt(3)\n\t"
  "v_mfma_f32_16x16x32_bf16 v[64:67], a[144:147], a[16:19], v[64:67]\n\t"
  "ds_read_b128 a[128:131], v4 offset:49152\n\t"
  "v_mfma_f32_16x16x32_bf16 v[68:71], a[144:147], a[20:23], v[68:71]\n\t"
  "v_mfma_f32_16x16x32_bf16 v[72:75], a[144:147], a[24:27], v[72:75]\n\t"
  "v_mfma_f32_16x16x32_bf16 v[76:79], a[144:147], a[28:31], v[76:79]\n\t"
  "s_waitcnt lgkmcnt(3)\n\t"
  "v_mfma_f32_16x16x32_bf16 v[92:95], a[148:151], a[28:31], v[92:95]\n\t"
  "ds_read_b128 a[132:135], v4 offset:50176\n\t"
  "v_mfma_f32_16x16x32_bf16 v[88:91], a[148:151], a[24:27], v[88:91]\n\t"
  "v_mfma_f32_16x16x32_bf16 v[84:87], a[148:151], a[20:23], v[84:87]\n\t"
  "v_mfma_f32_16x16x32_bf16 v[80:83], a[148:151], a[16:19], v[80:83]\n\t"
  "s_waitcnt lgkmcnt(3)\n\t"
  "v_mfma_f32_16x16x32_bf16 v[96:99], a[152:155], a[16:19], v[96:99]\n\t"
  "ds_read_b128 a[136:139], v4 offset:51200\n\t"
  "v_mfma_f32_16x16x32_bf16 v[100:103], a[152:155], a[20:23], v[100:103]\n\t"
  "v_mfma_f32_16x16x32_bf16 v[104:107], a[152:155], a[24:27], v[104:107]\n\t"
  "v_mfma_f32_16x16x32_bf16 v[108:111], a[152:155], a[28:31], v[108:111]\n\t"
  "s_waitcnt lgkmcnt(3)\n\t"
  "v_mfma_f32_16x16x32_bf16 v[124:127], a[156:159], a[28:31], v[124:127]\n\t"
  "ds_read_b128 a[140:143], v4 offset:52224\n\t"
  "v_mfma_f32_16x16x32_bf16 v[120:123], a[156:159], a[24:27], v[120:123]\n\t"
  "v_mfma_f32_16x16x32_bf16 v[116:119], a[156:159], a[20:23], v[116:119]\n\t"
  "v_mfma_f32_16x16x32_bf16 v[112:115], a[156:159], a[16:19], v[112:115]\n\t"
  "s_waitcnt lgkmcnt(3)\n\t"
  "v_mfma_f32_16x16x32_bf16 v[128:131], a[128:131], a[16:19], v[128:131]\n\t"
  "ds_read_b128 a[144:147], v4 offset:53248\n\t"
  "v_mfma_f32_16x16x32_bf16 v[132:135], a[128:131], a[20:23], v[132:135]\n\t"
  "v_mfma_f32_16x16x32_bf16 v[136:139], a[128:131], a[24:27], v[136:139]\n\t"
  "v_mfma_f32_16x16x32_bf16 v[140:143], a[128:131], a[28:31], v[140:143]\n\t"
  "s_waitcnt lgkmcnt(3)\n\t"
  "v_mfma_f32_16x16x32_bf16 v[156:159], a[132:135], a[28:31], v[156:159]\n\t"
  "ds_read_b128 a[148:151], v4 offset:54272\n\t"
  "v_mfma_f32_16x16x32_bf16 v[152:155], a[132:135], a[24:27], v[152:155]\n\t"
  "v_mfma_f32_16x16x32_bf16 v[148:151], a[132:135], a[20:23], v[148:151]\n\t"
  "v_mfma_f32_16x16x32_bf16 v[144:147], a[132:135], a[16:19], v[144:147]\n\t"
  "s_waitcnt lgkmcnt(3)\n\t"
  "v_mfma_f32_16x16x32_bf16 v[160:163], a[136:139], a[16:19], v[160:163]\n\t"
  "ds_read_b128 a[152:155], v4 offset:55296\n\t"
  "v_mfma_f32_16x16x32_bf16 v[164:167], a[136:139], a[20:23], v[164:167]\n\t"
  "v_mfma_f32_16x16x32_bf16 v[168:171], a[136:139], a[24:27], v[168:171]\n\t"
  "v_mfma_f32_16x16x32_bf16 v[172:175], a[136:139], a[28:31], v[172:175]\n\t"
  "s_waitcnt lgkmcnt(3)\n\t"
  "v_mfma_f32_16x16x32_bf16 v[188:191], a[140:143], a[28:31], v[188:191]\n\t"
  "ds_read_b128 a[156:159], v4 offset:56320\n\t"
  "v_mfma_f32_16x16x32_bf16 v[184:187], a[140:143], a[24:27], v[184:187]\n\t"
  "s_waitcnt vmcnt(23)\n\t"
  "v_mfma_f32_16x16x32_bf16 v[180:183], a[140:143], a[20:23], v[180:183]\n\t"
  "s_barrier\n\t"
  "v_mfma_f32_16x16x32_bf16 v[176:179], a[140:143], a[16:19], v[176:179]\n\t"
  "s_waitcnt lgkmcnt(3)\n\t"
  "v_mfma_f32_16x16x32_bf16 v[192:195], a[144:147], a[16:19], v[192:195]\n\t"
  "ds_read_b128 a[128:131], v4 offset:0\n\t"
  "v_mfma_f32_16x16x32_bf16 v[196:199], a[144:147], a[20:23], v[196:199]\n\t"
  "s_add_u32 m0, s30, 28672\n\t"
  "v_mfma_f32_16x16x32_bf16 v[200:203], a[144:147], a[24:27], v[200:203]\n\t"
  "buffer_load_dwordx4 v1, s[20:23], s29 offen offset:0 sc1 lds\n\t"
  "v_mfma_f32_16x16x32_bf16 v[204:207], a[144:147], a[28:31], v[204:207]\n\t"
  "s_waitcnt lgkmcnt(3)\n\t"
  "v_mfma_f32_16x16x32_bf16 v[220:223], a[148:151], a[28:31], v[220:223]\n\t"
  "ds_read_b128 a[132:135], v4 offset:1024\n\t"
  "v_mfma_f32_16x16x32_bf16 v[216:219], a[148:151], a[24:27], v[216:219]\n\t"
  "v_mfma_f32_16x16x32_bf16 v[212:215], a[148:151], a[20:23], v[212:215]\n\t"
  "buffer_load_dwordx4 v1, s[20:23], s29 offen offset:1024 sc1 lds\n\t"
  "v_mfma_f32_16x16x32_bf16 v[208:211], a[148:151], a[16:19], v[208:211]\n\t"
  "s_waitcnt lgkmcnt(3)\n\t"
  "v_mfma_f32_16x16x32_bf16 v[224:227], a[152:155], a[16:19], v[224:227]\n\t"
  "ds_read_b128 a[136:139], v4 offset:2048\n\t"
  "v_mfma_f32_16x16x32_bf16 v[228:231], a[152:155], a[20:23], v[228:231]\n\t"
  "v_mfma_f32_16x16x32_bf16 v[232:235], a[152:155], a[24:27], v[232:235]\n\t"
  "buffer_load_dwordx4 v1, s[20:23], s29 offen offset:2048 sc1 lds\n\t"
  "v_mfma_f32_16x16x32_bf16 v[236:239], a[152:155], a[28:31], v[236:239]\n\t"
  "s_waitcnt lgkmcnt(3)\n\t"
  "v_mfma_f32_16x16x32_bf16 v[252:255], a[156:159], a[28:31], v[252:255]\n\t"
  "ds_read_b128 a[140:143], v4 offset:3072\n\t"
  "v_mfma_f32_16x16x32_bf16 v[248:251], a[156:159], a[24:27], v[248:251]\n\t"
  "v_mfma_f32_16x16x32_bf16 v[244:247], a[156:159], a[20:23], v[244:247]\n\t"
  "buffer_load_dwordx4 v1, s[20:23], s29 offen offset:3072 sc1 lds\n\t"
  "v_mfma_f32_16x16x32_bf16 v[240:243], a[156:159], a[16:19], v[240:243]\n\t"
  "s_waitcnt lgkmcnt(3) vmcnt(23)\n\t"
  "v_mfma_f32_16x16x32_bf16 v[32:35], a[128:131], a[32:35], v[32:35]\n\t"
  "ds_read_b128 a[144:147], v4 offset:4096\n\t"
  "v_mfma_f32_16x16x32_bf16 v[36:39], a[128:131], a[36:39], v[36:39]\n\t"
  "s_add_u32 m0, s30, 32768\n\t"
  "v_mfma_f32_16x16x32_bf16 v[40:43], a[128:131], a[40:43], v[40:43]\n\t"
  "buffer_load_dwordx4 v2, s[20:23], s29 offen offset:0 sc1 lds\n\t"
  "v_mfma_f32_16x16x32_bf16 v[44:47], a[128:131], a[44:47], v[44:47]\n\t"
  "s_waitcnt lgkmcnt(3)\n\t"
  "v_mfma_f32_16x16x32_bf16 v[60:63], a[132:135], a[44:47], v[60:63]\n\t"
  "ds_read_b128 a[148:151], v4 offset:5120\n\t"
  "v_mfma_f32_16x16x32_bf16 v[56:59], a[132:135], a[40:43], v[56:59]\n\t"
  "v_mfma_f32_16x16x32_bf16 v[52:55], a[132:135], a[36:39], v[52:55]\n\t"
  "buffer_load_dwordx4 v2, s[20:23], s29 offen offset:1024 sc1 lds\n\t"
  "v_mfma_f32_16x16x32_bf16 v[48:51], a[132:135], a[32:35], v[48:51]\n\t"
  "s_waitcnt lgkmcnt(3)\n\t"
  "v_mfma_f32_16x16x32_bf16 v[64:67], a[136:139], a[32:35], v[64:67]\n\t"
  "ds_read_b128 a[152:155], v4 offset:6144\n\t"
  "v_mfma_f32_16x16x32_bf16 v[68:71], a[136:139], a[36:39], v[68:71]\n\t"
  "v_mfma_f32_16x16x32_bf16 v[72:75], a[136:139], a[40:43], v[72:75]\n\t"
  "buffer_load_dwordx4 v2, s[20:23], s29 offen offset:2048 sc1 lds\n\t"
  "v_mfma_f32_16x16x32_bf16 v[76:79], a[136:139], a[44:47], v[76:79]\n\t"
  "s_waitcnt lgkmcnt(3)\n\t"
  "v_mfma_f32_16x16x32_bf16 v[92:95], a[140:143], a[44:47], v[92:95]\n\t"
  "ds_read_b128 a[156:159], v4 offset:7168\n\t"
  "s_add_u32 s29, s29, 28672\n\t"
  "v_mfma_f32_16x16x32_bf16 v[88:91], a[140:143], a[40:43], v[88:91]\n\t"
  "v_mfma_f32_16x16x32_bf16 v[84:87], a[140:143], a[36:39], v[84:87]\n\t"
  "buffer_load_dwordx4 a[96:99], v0, s[20:23], s28 offen offset:0 sc1\n\t"
  "v_mfma_f32_16x16x32_bf16 v[80:83], a[140:143], a[32:35], v[80:83]\n\t"
  "s_waitcnt lgkmcnt(3)\n\t"
  "v_mfma_f32_16x16x32_bf16 v[96:99], a[144:147], a[32:35], v[96:99]\n\t"
  "ds_read_b128 a[128:131], v4 offset:8192\n\t"
  "v_mfma_f32_16x16x32_bf16 v[100:103], a[144:147], a[36:39], v[100:103]\n\t"
  "v_mfma_f32_16x16x32_bf16 v[104:107], a[144:147], a[40:43], v[104:107]\n\t"
  "buffer_load_dwordx4 a[100:103], v0, s[20:23], s28 offen offset:1024 sc1\n\t"
  "v_mfma_f32_16x16x32_bf16 v[108:111], a[144:147], a[44:47], v[108:111]\n\t"
  "s_waitcnt lgkmcnt(3)\n\t"
  "v_mfma_f32_16x16x32_bf16 v[124:127], a[148:151], a[44:47], v[124:127]\n\t"
  "ds_read_b128 a[132:135], v4 offset:9216\n\t"
  "v_mfma_f32_16x16x32_bf16 v[120:123], a[148:151], a[40:43], v[120:123]\n\t"
  "v_mfma_f32_16x16x32_bf16 v[116:119], a[148:151], a[36:39], v[116:119]\n\t"
  "buffer_load_dwordx4 a[104:107], v0, s[20:23], s28 offen offset:2048 sc1\n\t"
  "v_mfma_f32_16x16x32_bf16 v[112:115], a[148:151], a[32:35], v[112:115]\n\t"
  "s_waitcnt lgkmcnt(3)\n\t"
  "v_mfma_f32_16x16x32_bf16 v[128:131], a[152:155], a[32:35], v[128:131]\n\t"
  "ds_read_b128 a[136:139], v4 offset:10240\n\t"
  "v_mfma_f32_16x16x32_bf16 v[132:135], a[152:155], a[36:39], v[132:135]\n\t"
  "v_mfma_f32_16x16x32_bf16 v[136:139], a[152:155], a[40:43], v[136:139]\n\t"
  "buffer_load_dwordx4 a[108:111], v0, s[20:23], s28 offen offset:3072 sc1\n\t"
  "v_mfma_f32_16x16x32_bf16 v[140:143], a[152:155], a[44:47], v[140:143]\n\t"
  "s_waitcnt lgkmcnt(3)\n\t"
  "v_mfma_f32_16x16x32_bf16 v[156:159], a[156:159], a[44:47], v[156:159]\n\t"
  "ds_read_b128 a[140:143], v4 offset:11264\n\t"
  "s_add_u32 s28, s28, 131072\n\t"
  "v_mfma_f32_16x16x32_bf16 v[152:155], a[156:159], a[40:43], v[152:155]\n\t"
  "v_mfma_f32_16x16x32_bf16 v[148:151], a[156:159], a[36:39], v[148:151]\n\t"
  "buffer_load_dwordx4 a[112:115], v0, s[20:23], s28 offen offset:0 sc1\n\t"
  "v_mfma_f32_16x16x32_bf16 v[144:147], a[156:159], a[32:35], v[144:147]\n\t"
  "s_waitcnt lgkmcnt(3)\n\t"
  "v_mfma_f32_16x16x32_bf16 v[160:163], a[128:131], a[32:35], v[160:163]\n\t"
  "ds_read_b128 a[144:147], v4 offset:12288\n\t"
  "v_mfma_f32_16x16x32_bf16 v[164:167], a[128:131], a[36:39], v[164:167]\n\t"
  "v_mfma_f32_16x16x32_bf16 v[168:171], a[128:131], a[40:43], v[168:171]\n\t"
  "buffer_load_dwordx4 a[116:119], v0, s[20:23], s28 offen offset:1024 sc1\n\t"
  "v_mfma_f32_16x16x32_bf16 v[172:175], a[128:131], a[44:47], v[172:175]\n\t"
  "s_waitcnt lgkmcnt(3)\n\t"
  "v_mfma_f32_16x16x32_bf16 v[188:191], a[132:135], a[44:47], v[188:191]\n\t"
  "ds_read_b128 a[148:151], v4 offset:13312\n\t"
  "v_mfma_f32_16x16x32_bf16 v[184:187], a[132:135], a[40:43], v[184:187]\n\t"
  "v_mfma_f32_16x16x32_bf16 v[180:183], a[132:135], a[36:39], v[180:183]\n\t"
  "buffer_load_dwordx4 a[120:123], v0, s[20:23], s28 offen offset:2048 sc1\n\t"
  "v_mfma_f32_16x16x32_bf16 v[176:179], a[132:135], a[32:35], v[176:179]\n\t"
  "s_waitcnt lgkmcnt(3)\n\t"
  "v_mfma_f32_16x16x32_bf16 v[192:195], a[136:139], a[32:35], v[192:195]\n\t"
  "ds_read_b128 a[152:155], v4 offset:14336\n\t"
  "v_mfma_f32_16x16x32_bf16 v[196:199], a[136:139], a[36:39], v[196:199]\n\t"
  "v_mfma_f32_16x16x32_bf16 v[200:203], a[136:139], a[40:43], v[200:203]\n\t"
  "buffer_load_dwordx4 a[124:127], v0, s[20:23], s28 offen offset:3072 sc1\n\t"
  "v_mfma_f32_16x16x32_bf16 v[204:207], a[136:139], a[44:47], v[204:207]\n\t"
  "s_waitcnt lgkmcnt(3)\n\t"
  "v_mfma_f32_16x16x32_bf16 v[220:223], a[140:143], a[44:47], v[220:223]\n\t"
  "ds_read_b128 a[156:159], v4 offset:15360\n\t"
  "s_add_u32 s28, s28, 131072\n\t"
  "v_mfma_f32_16x16x32_bf16 v[216:219], a[140:143], a[40:43], v[216:219]\n\t"
  "v_mfma_f32_16x16x32_bf16 v[212:215], a[140:143], a[36:39], v[212:215]\n\t"
  "global_load_dword v26, v7, s[70:71]\n\t"
  "v_mfma_f32_16x16x32_bf16 v[208:211], a[140:143], a[32:35], v[208:211]\n\t"
  "s_waitcnt lgkmcnt(3)\n\t"
  "v_mfma_f32_16x16x32_bf16 v[224:227], a[144:147], a[32:35], v[224:227]\n\t"
  "ds_read_b128 a[128:131], v4 offset:16384\n\t"
  "v_mfma_f32_16x16x32_bf16 v[228:231], a[144:147], a[36:39], v[228:231]\n\t"
  "v_mfma_f32_16x16x32_bf16 v[232:235], a[144:147], a[40:43], v[232:235]\n\t"
  "v_mfma_f32_16x16x32_bf16 v[236:239], a[144:147], a[44:47], v[236:239]\n\t"
  "s_waitcnt lgkmcnt(3)\n\t"
  "v_mfma_f32_16x16x32_bf16 v[252:255], a[148:151], a[44:47], v[252:255]\n\t"
  "ds_read_b128 a[132:135], v4 offset:17408\n\t"
  "v_mfma_f32_16x16x32_bf16 v[248:251], a[148:151], a[40:43], v[248:251]\n\t"
  "v_mfma_f32_16x16x32_bf16 v[244:247], a[148:151], a[36:39], v[244:247]\n\t"
  "v_mfma_f32_16x16x32_bf16 v[240:243], a[148:151], a[32:35], v[240:243]\n\t"
  "s_waitcnt lgkmcnt(3) vmcnt(31)\n\t"
  "v_mfma_f32_16x16x32_bf16 v[32:35], a[152:155], a[48:51], v[32:35]\n\t"
  "ds_read_b128 a[136:139], v4 offset:18432\n\t"
  "v_mfma_f32_16x16x32_bf16 v[36:39], a[152:155], a[52:55], v[36:39]\n\t"
  "v_mfma_f32_16x16x32_bf16 v[40:43], a[152:155], a[56:59], v[40:43]\n\t"
  "v_mfma_f32_16x16x32_bf16 v[44:47], a[152:155], a[60:63], v[44:47]\n\t"
  "s_waitcnt lgkmcnt(3)\n\t"
  "v_mfma_f32_16x16x32_bf16 v[60:63], a[156:159], a[60:63], v[60:63]\n\t"
  "ds_read_b128 a[140:143], v4 offset:19456\n\t"
  "v_mfma_f32_16x16x32_bf16 v[56:59], a[156:159], a[56:59], v[56:59]\n\t"
  "v_mfma_f32_16x16x32_bf16 v[52:55], a[156:159], a[52:55], v[52:55]\n\t"
  "v_mfma_f32_16x16x32_bf16 v[48:51], a[156:159], a[48:51], v[48:51]\n\t"
  "s_waitcnt lgkmcnt(3)\n\t"
  "v_mfma_f32_16x16x32_bf16 v[64:67], a[128:131], a[48:51], v[64:67]\n\t"
  "ds_read_b128 a[144:147], v4 offset:20480\n\t"
  "v_mfma_f32_16x16x32_bf16 v[68:71], a[128:131], a[52:55], v[68:71]\n\t"
  "v_mfma_f32_16x16x32_bf16 v[72:75], a[128:131], a[56:59], v[72:75]\n\t"
  "v_mfma_f32_16x16x32_bf16 v[76:79], a[128:131], a[60:63], v[76:79]\n\t"
  "s_waitcnt lgkmcnt(3)\n\t"
  "v_mfma_f32_16x16x32_bf16 v[92:95], a[132:135], a[60:63], v[92:95]\n\t"
  "ds_read_b128 a[148:151], v4 offset:21504\n\t"
  "v_mfma_f32_16x16x32_bf16 v[88:91], a[132:135], a[56:59], v[88:91]\n\t"
  "v_mfma_f32_16x16x32_bf16 v[84:87], a[132:135], a[52:55], v[84:87]\n\t"
  "v_mfma_f32_16x16x32_bf16 v[80:83], a[132:135], a[48:51], v[80:83]\n\t"
  "s_waitcnt lgkmcnt(3)\n\t"
  "v_mfma_f32_16x16x32_bf16 v[96:99], a[136:139], a[48:51], v[96:99]\n\t"
  "ds_read_b128 a[152:155], v4 offset:22528\n\t"
  "v_mfma_f32_16x16x32_bf16 v[100:103], a[136:139], a[52:55], v[100:103]\n\t"
  "v_mfma_f32_16x16x32_bf16 v[104:107], a[136:139], a[56:59], v[104:107]\n\t"
  "v_mfma_f32_16x16x32_bf16 v[108:111], a[136:139], a[60:63], v[108:111]\n\t"
  "s_waitcnt lgkmcnt(3)\n\t"
  "v_mfma_f32_16x16x32_bf16 v[124:127], a[140:143], a[60:63], v[124:127]\n\t"
  "ds_read_b128 a[156:159], v4 offset:23552\n\t"
  "v_mfma_f32_16x16x32_bf16 v[120:123], a[140:143], a[56:59], v[120:123]\n\t"
  "v_mfma_f32_16x16x32_bf16 v[116:119], a[140:143], a[52:55], v[116:119]\n\t"
  "v_mfma_f32_16x16x32_bf16 v[112:115], a[140:143], a[48:51], v[112:115]\n\t"
  "s_waitcnt lgkmcnt(3)\n\t"
  "v_mfma_f32_16x16x32_bf16 v[128:131], a[144:147], a[48:51], v[128:131]\n\t"
  "ds_read_b128 a[128:131], v4 offset:24576\n\t"
  "v_mfma_f32_16x16x32_bf16 v[132:135], a[144:147], a[52:55], v[132:135]\n\t"
  "v_mfma_f32_16x16x32_bf16 v[136:139], a[144:147], a[56:59], v[136:139]\n\t"
  "v_mfma_f32_16x16x32_bf16 v[140:143], a[144:147], a[60:63], v[140:143]\n\t"
  "s_waitcnt lgkmcnt(3)\n\t"
  "v_mfma_f32_16x16x32_bf16 v[156:159], a[148:151], a[60:63], v[156:159]\n\t"
  "ds_read_b128 a[132:135], v4 offset:25600\n\t"
  "v_mfma_f32_16x16x32_bf16 v[152:155], a[148:151], a[56:59], v[152:155]\n\t"
  "v_mfma_f32_16x16x32_bf16 v[148:151], a[148:151], a[52:55], v[148:151]\n\t"
  "v_mfma_f32_16x16x32_bf16 v[144:147], a[148:151], a[48:51], v[144:147]\n\t"
  "s_waitcnt lgkmcnt(3)\n\t"
  "v_mfma_f32_16x16x32_bf16 v[160:163], a[152:155], a[48:51], v[160:163]\n\t"
  "ds_read_b128 a[136:139], v4 offset:26624\n\t"
  "v_mfma_f32_16x16x32_bf16 v[164:167], a[152:155], a[52:55], v[164:167]\n\t"
  "v_mfma_f32_16x16x32_bf16 v[168:171], a[152:155], a[56:59], v[168:171]\n\t"
  "v_mfma_f32_16x16x32_bf16 v[172:175], a[152:155], a[60:63], v[172:175]\n\t"
  "s_waitcnt lgkmcnt(3)\n\t"
  "v_mfma_f32_16x16x32_bf16 v[188:191], a[156:159], a[60:63], v[188:191]\n\t"
  "ds_read_b128 a[140:143], v4 offset:27648\n\t"
  "v_mfma_f32_16x16x32_bf16 v[184:187], a[156:159], a[56:59], v[184:187]\n\t"
  "s_waitcnt vmcnt(24)\n\t"
  "v_mfma_f32_16x16x32_bf16 v[180:183], a[156:159], a[52:55], v[180:183]\n\t"
  "s_barrier\n\t"
  "v_mfma_f32_16x16x32_bf16 v[176:179], a[156:159], a[48:51], v[176:179]\n\t"
  "s_waitcnt lgkmcnt(3)\n\t"
  "v_mfma_f32_16x16x32_bf16 v[192:195], a[128:131], a[48:51], v[192:195]\n\t"
  "ds_read_b128 a[144:147], v3 offset:0\n\t"
  "v_mfma_f32_16x16x32_bf16 v[196:199], a[128:131], a[52:55], v[196:199]\n\t"
  "s_add_u32 m0, s30, 86016\n\t"
  "v_mfma_f32_16x16x32_bf16 v[200:203], a[128:131], a[56:59], v[200:203]\n\t"
  "buffer_load_dwordx4 v1, s[20:23], s29 offen offset:0 sc1 lds\n\t"
  "v_mfma_f32_16x16x32_bf16 v[204:207], a[128:131], a[60:63], v[204:207]\n\t"
  "s_waitcnt lgkmcnt(3)\n\t"
  "v_mfma_f32_16x16x32_bf16 v[220:223], a[132:135], a[60:63], v[220:223]\n\t"
  "ds_read_b128 a[148:151], v3 offset:1024\n\t"
  "v_mfma_f32_16x16x32_bf16 v[216:219], a[132:135], a[56:59], v[216:219]\n\t"
  "v_mfma_f32_16x16x32_bf16 v[212:215], a[132:135], a[52:55], v[212:215]\n\t"
  "buffer_load_dwordx4 v1, s[20:23], s29 offen offset:1024 sc1 lds\n\t"
  "v_mfma_f32_16x16x32_bf16 v[208:211], a[132:135], a[48:51], v[208:211]\n\t"
  "s_waitcnt lgkmcnt(3)\n\t"
  "v_mfma_f32_16x16x32_bf16 v[224:227], a[136:139], a[48:51], v[224:227]\n\t"
  "ds_read_b128 a[152:155], v3 offset:2048\n\t"
  "v_mfma_f32_16x16x32_bf16 v[228:231], a[136:139], a[52:55], v[228:231]\n\t"
  "v_mfma_f32_16x16x32_bf16 v[232:235], a[136:139], a[56:59], v[232:235]\n\t"
  "buffer_load_dwordx4 v1, s[20:23], s29 offen offset:2048 sc1 lds\n\t"
  "v_mfma_f32_16x16x32_bf16 v[236:239], a[136:139], a[60:63], v[236:239]\n\t"
  "s_waitcnt lgkmcnt(3)\n\t"
  "v_mfma_f32_16x16x32_bf16 v[252:255], a[140:143], a[60:63], v[252:255]\n\t"
  "ds_read_b128 a[156:159], v3 offset:3072\n\t"
  "v_mfma_f32_16x16x32_bf16 v[248:251], a[140:143], a[56:59], v[248:251]\n\t"
  "v_mfma_f32_16x16x32_bf16 v[244:247], a[140:143], a[52:55], v[244:247]\n\t"
  "buffer_load_dwordx4 v1, s[20:23], s29 offen offset:3072 sc1 lds\n\t"
  "v_mfma_f32_16x16x32_bf16 v[240:243], a[140:143], a[48:51], v[240:243]\n\t"
  "s_waitcnt lgkmcnt(3) vmcnt(24)\n\t"
  "v_mfma_f32_16x16x32_bf16 v[32:35], a[144:147], a[64:67], v[32:35]\n\t"
  "ds_read_b128 a[128:131], v3 offset:4096\n\t"
  "v_mfma_f32_16x16x32_bf16 v[36:39], a[144:147], a[68:71], v[36:39]\n\t"
  "s_add_u32 m0, s30, 90112\n\t"
  "v_mfma_f32_16x16x32_bf16 v[40:43], a[144:147], a[72:75], v[40:43]\n\t"
  "buffer_load_dwordx4 v2, s[20:23], s29 offen offset:0 sc1 lds\n\t"
  "v_mfma_f32_16x16x32_bf16 v[44:47], a[144:147], a[76:79], v[44:47]\n\t"
  "s_waitcnt lgkmcnt(3)\n\t"
  "v_mfma_f32_16x16x32_bf16 v[60:63], a[148:151], a[76:79], v[60:63]\n\t"
  "ds_read_b128 a[132:135], v3 offset:5120\n\t"
  "v_mfma_f32_16x16x32_bf16 v[56:59], a[148:151], a[72:75], v[56:59]\n\t"
  "v_mfma_f32_16x16x32_bf16 v[52:55], a[148:151], a[68:71], v[52:55]\n\t"
  "buffer_load_dwordx4 v2, s[20:23], s29 offen offset:1024 sc1 lds\n\t"
  "v_mfma_f32_16x16x32_bf16 v[48:51], a[148:151], a[64:67], v[48:51]\n\t"
  "s_waitcnt lgkmcnt(3)\n\t"
  "v_mfma_f32_16x16x32_bf16 v[64:67], a[152:155], a[64:67], v[64:67]\n\t"
  "ds_read_b128 a[136:139], v3 offset:6144\n\t"
  "v_mfma_f32_16x16x32_bf16 v[68:71], a[152:155], a[68:71], v[68:71]\n\t"
  "v_mfma_f32_16x16x32_bf16 v[72:75], a[152:155], a[72:75], v[72:75]\n\t"
  "buffer_load_dwordx4 v2, s[20:23], s29 offen offset:2048 sc1 lds\n\t"
  "v_mfma_f32_16x16x32_bf16 v[76:79], a[152:155], a[76:79], v[76:79]\n\t"
  "s_waitcnt lgkmcnt(3)\n\t"
  "v_mfma_f32_16x16x32_bf16 v[92:95], a[156:159], a[76:79], v[92:95]\n\t"
  "ds_read_b128 a[140:143], v3 offset:7168\n\t"
  "s_add_u32 s29, s29, 28672\n\t"
  "v_mfma_f32_16x16x32_bf16 v[88:91], a[156:159], a[72:75], v[88:91]\n\t"
  "v_mfma_f32_16x16x32_bf16 v[84:87], a[156:159], a[68:71], v[84:87]\n\t"
  "buffer_load_dwordx4 a[0:3], v0, s[20:23], s28 offen offset:0 sc1\n\t"
  "v_mfma_f32_16x16x32_bf16 v[80:83], a[156:159], a[64:67], v[80:83]\n\t"
  "s_waitcnt lgkmcnt(3)\n\t"
  "v_mfma_f32_16x16x32_bf16 v[96:99], a[128:131], a[64:67], v[96:99]\n\t"
  "ds_read_b128 a[144:147], v3 offset:8192\n\t"
  "v_mfma_f32_16x16x32_bf16 v[100:103], a[128:131], a[68:71], v[100:103]\n\t"
  "v_mfma_f32_16x16x32_bf16 v[104:107], a[128:131], a[72:75], v[104:107]\n\t"
  "buffer_load_dwordx4 a[4:7], v0, s[20:23], s28 offen offset:1024 sc1\n\t"
  "v_mfma_f32_16x16x32_bf16 v[108:111], a[128:131], a[76:79], v[108:111]\n\t"
  "s_waitcnt lgkmcnt(3)\n\t"
  "v_mfma_f32_16x16x32_bf16 v[124:127], a[132:135], a[76:79], v[124:127]\n\t"
  "ds_read_b128 a[148:151], v3 offset:9216\n\t"
  "v_mfma_f32_16x16x32_bf16 v[120:123], a[132:135], a[72:75], v[120:123]\n\t"
  "v_mfma_f32_16x16x32_bf16 v[116:119], a[132:135], a[68:71], v[116:119]\n\t"
  "buffer_load_dwordx4 a[8:11], v0, s[20:23], s28 offen offset:2048 sc1\n\t"
  "v_mfma_f32_16x16x32_bf16 v[112:115], a[132:135], a[64:67], v[112:115]\n\t"
  "s_waitcnt lgkmcnt(3)\n\t"
  "v_mfma_f32_16x16x32_bf16 v[128:131], a[136:139], a[64:67], v[128:131]\n\t"
  "ds_read_b128 a[152:155], v3 offset:10240\n\t"
  "v_mfma_f32_16x16x32_bf16 v[132:135], a[136:139], a[68:71], v[132:135]\n\t"
  "v_mfma_f32_16x16x32_bf16 v[136:139], a[136:139], a[72:75], v[136:139]\n\t"
  "buffer_load_dwordx4 a[12:15], v0, s[20:23], s28 offen offset:3072 sc1\n\t"
  "v_mfma_f32_16x16x32_bf16 v[140:143], a[136:139], a[76:79], v[140:143]\n\t"
  "s_waitcnt lgkmcnt(3)\n\t"
  "v_mfma_f32_16x16x32_bf16 v[156:159], a[140:143], a[76:79], v[156:159]\n\t"
  "ds_read_b128 a[156:159], v3 offset:11264\n\t"
  "s_add_u32 s28, s28, 131072\n\t"
  "v_mfma_f32_16x16x32_bf16 v[152:155], a[140:143], a[72:75], v[152:155]\n\t"
  "v_mfma_f32_16x16x32_bf16 v[148:151], a[140:143], a[68:71], v[148:151]\n\t"
  "buffer_load_dwordx4 a[16:19], v0, s[20:23], s28 offen offset:0 sc1\n\t"
  "v_mfma_f32_16x16x32_bf16 v[144:147], a[140:143], a[64:67], v[144:147]\n\t"
  "s_waitcnt lgkmcnt(3)\n\t"
  "v_mfma_f32_16x16x32_bf16 v[160:163], a[144:147], a[64:67], v[160:163]\n\t"
  "ds_read_b128 a[128:131], v3 offset:12288\n\t"
  "v_mfma_f32_16x16x32_bf16 v[164:167], a[144:147], a[68:71], v[164:167]\n\t"
  "v_mfma_f32_16x16x32_bf16 v[168:171], a[144:147], a[72:75], v[168:171]\n\t"
  "buffer_load_dwordx4 a[20:23], v0, s[20:23], s28 offen offset:1024 sc1\n\t"
  "v_mfma_f32_16x16x32_bf16 v[172:175], a[144:147], a[76:79], v[172:175]\n\t"
  "s_waitcnt lgkmcnt(3)\n\t"
  "v_mfma_f32_16x16x32_bf16 v[188:191], a[148:151], a[76:79], v[188:191]\n\t"
  "ds_read_b128 a[132:135], v3 offset:13312\n\t"
  "v_mfma_f32_16x16x32_bf16 v[184:187], a[148:151], a[72:75], v[184:187]\n\t"
  "v_mfma_f32_16x16x32_bf16 v[180:183], a[148:151], a[68:71], v[180:183]\n\t"
  "buffer_load_dwordx4 a[24:27], v0, s[20:23], s28 offen offset:2048 sc1\n\t"
  "v_mfma_f32_16x16x32_bf16 v[176:179], a[148:151], a[64:67], v[176:179]\n\t"
  "s_waitcnt lgkmcnt(3)\n\t"
  "v_mfma_f32_16x16x32_bf16 v[192:195], a[152:155], a[64:67], v[192:195]\n\t"
  "ds_read_b128 a[136:139], v3 offset:14336\n\t"
  "v_mfma_f32_16x16x32_bf16 v[196:199], a[152:155], a[68:71], v[196:199]\n\t"
  "v_mfma_f32_16x16x32_bf16 v[200:203], a[152:155], a[72:75], v[200:203]\n\t"
  "buffer_load_dwordx4 a[28:31], v0, s[20:23], s28 offen offset:3072 sc1\n\t"
  "v_mfma_f32_16x16x32_bf16 v[204:207], a[152:155], a[76:79], v[204:207]\n\t"
  "s_waitcnt lgkmcnt(3)\n\t"
  "v_mfma_f32_16x16x32_bf16 v[220:223], a[156:159], a[76:79], v[220:223]\n\t"
  "ds_read_b128 a[140:143], v3 offset:15360\n\t"
  "s_add_u32 s28, s28, 131072\n\t"
  "v_mfma_f32_16x16x32_bf16 v[216:219], a[156:159], a[72:75], v[216:219]\n\t"
  "v_mfma_f32_16x16x32_bf16 v[212:215], a[156:159], a[68:71], v[212:215]\n\t"
  "v_mfma_f32_16x16x32_bf16 v[208:211], a[156:159], a[64:67], v[208:211]\n\t"
  "s_waitcnt lgkmcnt(3)\n\t"
  "v_mfma_f32_16x16x32_bf16 v[224:227], a[128:131], a[64:67], v[224:227]\n\t"
  "ds_read_b128 a[144:147], v3 offset:16384\n\t"
  "v_mfma_f32_16x16x32_bf16 v[228:231], a[128:131], a[68:71], v[228:231]\n\t"
  "v_mfma_f32_16x16x32_bf16 v[232:235], a[128:131], a[72:75], v[232:235]\n\t"
  "v_mfma_f32_16x16x32_bf16 v[236:239], a[128:131], a[76:79], v[236:239]\n\t"
  "s_waitcnt lgkmcnt(3)\n\t"
  "v_mfma_f32_16x16x32_bf16 v[252:255], a[132:135], a[76:79], v[252:255]\n\t"
  "ds_read_b128 a[148:151], v3 offset:17408\n\t"
  "v_mfma_f32_16x16x32_bf16 v[248:251], a[132:135], a[72:75], v[248:251]\n\t"
  "v_mfma_f32_16x16x32_bf16 v[244:247], a[132:135], a[68:71], v[244:247]\n\t"
  "v_mfma_f32_16x16x32_bf16 v[240:243], a[132:135], a[64:67], v[240:243]\n\t"
  "s_waitcnt lgkmcnt(3) vmcnt(31)\n\t"
  "v_mfma_f32_16x16x32_bf16 v[32:35], a[136:139], a[80:83], v[32:35]\n\t"
  "ds_read_b128 a[152:155], v3 offset:18432\n\t"
  "v_mfma_f32_16x16x32_bf16 v[36:39], a[136:139], a[84:87], v[36:39]\n\t"
  "v_mfma_f32_16x16x32_bf16 v[40:43], a[136:139], a[88:91], v[40:43]\n\t"
  "v_mfma_f32_16x16x32_bf16 v[44:47], a[136:139], a[92:95], v[44:47]\n\t"
  "s_waitcnt lgkmcnt(3)\n\t"
  "v_mfma_f32_16x16x32_bf16 v[60:63], a[140:143], a[92:95], v[60:63]\n\t"
  "ds_read_b128 a[156:159], v3 offset:19456\n\t"
  "v_mfma_f32_16x16x32_bf16 v[56:59], a[140:143], a[88:91], v[56:59]\n\t"
  "v_mfma_f32_16x16x32_bf16 v[52:55], a[140:143], a[84:87], v[52:55]\n\t"
  "v_mfma_f32_16x16x32_bf16 v[48:51], a[140:143], a[80:83], v[48:51]\n\t"
  "s_waitcnt lgkmcnt(3)\n\t"
  "v_mfma_f32_16x16x32_bf16 v[64:67], a[144:147], a[80:83], v[64:67]\n\t"
  "ds_read_b128 a[128:131], v3 offset:20480\n\t"
  "v_mfma_f32_16x16x32_bf16 v[68:71], a[144:147], a[84:87], v[68:71]\n\t"
  "v_mfma_f32_16x16x32_bf16 v[72:75], a[144:147], a[88:91], v[72:75]\n\t"
  "v_mfma_f32_16x16x32_bf16 v[76:79], a[144:147], a[92:95], v[76:79]\n\t"
  "s_waitcnt lgkmcnt(3)\n\t"
  "v_mfma_f32_16x16x32_bf16 v[92:95], a[148:151], a[92:95], v[92:95]\n\t"
  "ds_read_b128 a[132:135], v3 offset:21504\n\t"
  "v_mfma_f32_16x16x32_bf16 v[88:91], a[148:151], a[88:91], v[88:91]\n\t"
  "v_mfma_f32_16x16x32_bf16 v[84:87], a[148:151], a[84:87], v[84:87]\n\t"
  "v_mfma_f32_16x16x32_bf16 v[80:83], a[148:151], a[80:83], v[80:83]\n\t"
  "s_waitcnt lgkmcnt(3)\n\t"
  "v_mfma_f32_16x16x32_bf16 v[96:99], a[152:155], a[80:83], v[96:99]\n\t"
  "ds_read_b128 a[136:139], v3 offset:22528\n\t"
  "v_mfma_f32_16x16x32_bf16 v[100:103], a[152:155], a[84:87], v[100:103]\n\t"
  "v_mfma_f32_16x16x32_bf16 v[104:107], a[152:155], a[88:91], v[104:107]\n\t"
  "v_mfma_f32_16x16x32_bf16 v[108:111], a[152:155], a[92:95], v[108:111]\n\t"
  "s_waitcnt lgkmcnt(3)\n\t"
  "v_mfma_f32_16x16x32_bf16 v[124:127], a[156:159], a[92:95], v[124:127]\n\t"
  "ds_read_b128 a[140:143], v3 offset:23552\n\t"
  "v_mfma_f32_16x16x32_bf16 v[120:123], a[156:159], a[88:91], v[120:123]\n\t"
  "v_mfma_f32_16x16x32_bf16 v[116:119], a[156:159], a[84:87], v[116:119]\n\t"
  "v_mfma_f32_16x16x32_bf16 v[112:115], a[156:159], a[80:83], v[112:115]\n\t"
  "s_waitcnt lgkmcnt(3)\n\t"
  "v_mfma_f32_16x16x32_bf16 v[128:131], a[128:131], a[80:83], v[128:131]\n\t"
  "ds_read_b128 a[144:147], v3 offset:24576\n\t"
  "v_mfma_f32_16x16x32_bf16 v[132:135], a[128:131], a[84:87], v[132:135]\n\t"
  "v_mfma_f32_16x16x32_bf16 v[136:139], a[128:131], a[88:91], v[136:139]\n\t"
  "v_mfma_f32_16x16x32_bf16 v[140:143], a[128:131], a[92:95], v[140:143]\n\t"
  "s_waitcnt lgkmcnt(3)\n\t"
  "v_mfma_f32_16x16x32_bf16 v[156:159], a[132:135], a[92:95], v[156:159]\n\t"
  "ds_read_b128 a[148:151], v3 offset:25600\n\t"
  "v_mfma_f32_16x16x32_bf16 v[152:155], a[132:135], a[88:91], v[152:155]\n\t"
  "v_mfma_f32_16x16x32_bf16 v[148:151], a[132:135], a[84:87], v[148:151]\n\t"
  "v_mfma_f32_16x16x32_bf16 v[144:147], a[132:135], a[80:83], v[144:147]\n\t"
  "s_waitcnt lgkmcnt(3)\n\t"
  "v_mfma_f32_16x16x32_bf16 v[160:163], a[136:139], a[80:83], v[160:163]\n\t"
  "ds_read_b128 a[152:155], v3 offset:26624\n\t"
  "v_mfma_f32_16x16x32_bf16 v[164:167], a[136:139], a[84:87], v[164:167]\n\t"
  "v_mfma_f32_16x16x32_bf16 v[168:171], a[136:139], a[88:91], v[168:171]\n\t"
  "v_mfma_f32_16x16x32_bf16 v[172:175], a[136:139], a[92:95], v[172:175]\n\t"
  "s_waitcnt lgkmcnt(3)\n\t"
  "v_mfma_f32_16x16x32_bf16 v[188:191], a[140:143], a[92:95], v[188:191]\n\t"
  "ds_read_b128 a[156:159], v3 offset:27648\n\t"
  "v_mfma_f32_16x16x32_bf16 v[184:187], a[140:143], a[88:91], v[184:187]\n\t"
  "s_waitcnt vmcnt(24)\n\t"
  "v_mfma_f32_16x16x32_bf16 v[180:183], a[140:143], a[84:87], v[180:183]\n\t"
  "s_barrier\n\t"
  "v_mfma_f32_16x16x32_bf16 v[176:179], a[140:143], a[80:83], v[176:179]\n\t"
  "s_waitcnt lgkmcnt(3)\n\t"
  "v_mfma_f32_16x16x32_bf16 v[192:195], a[144:147], a[80:83], v[192:195]\n\t"
  "ds_read_b128 a[128:131], v3 offset:28672\n\t"
  "v_mfma_f32_16x16x32_bf16 v[196:199], a[144:147], a[84:87], v[196:199]\n\t"
  "s_add_u32 m0, s30, 57344\n\t"
  "v_mfma_f32_16x16x32_bf16 v[200:203], a[144:147], a[88:91], v[200:203]\n\t"
  "buffer_load_dwordx4 v1, s[20:23], s29 offen offset:0 sc1 lds\n\t"
  "v_mfma_f32_16x16x32_bf16 v[204:207], a[144:147], a[92:95], v[204:207]\n\t"
  "s_waitcnt lgkmcnt(3)\n\t"
  "v_mfma_f32_16x16x32_bf16 v[220:223], a[148:151], a[92:95], v[220:223]\n\t"
  "ds_read_b128 a[132:135], v3 offset:29696\n\t"
  "v_mfma_f32_16x16x32_bf16 v[216:219], a[148:151], a[88:91], v[216:219]\n\t"
  "v_mfma_f32_16x16x32_bf16 v[212:215], a[148:151], a[84:87], v[212:215]\n\t"
  "buffer_load_dwordx4 v1, s[20:23], s29 offen offset:1024 sc1 lds\n\t"
  "v_mfma_f32_16x16x32_bf16 v[208:211], a[148:151], a[80:83], v[208:211]\n\t"
  "s_waitcnt lgkmcnt(3)\n\t"
  "v_mfma_f32_16x16x32_bf16 v[224:227], a[152:155], a[80:83], v[224:227]\n\t"
  "ds_read_b128 a[136:139], v3 offset:30720\n\t"
  "v_mfma_f32_16x16x32_bf16 v[228:231], a[152:155], a[84:87], v[228:231]\n\t"
  "v_mfma_f32_16x16x32_bf16 v[232:235], a[152:155], a[88:91], v[232:235]\n\t"
  "buffer_load_dwordx4 v1, s[20:23], s29 offen offset:2048 sc1 lds\n\t"
  "v_mfma_f32_16x16x32_bf16 v[236:239], a[152:155], a[92:95], v[236:239]\n\t"
  "s_waitcnt lgkmcnt(3)\n\t"
  "v_mfma_f32_16x16x32_bf16 v[252:255], a[156:159], a[92:95], v[252:255]\n\t"
  "ds_read_b128 a[140:143], v3 offset:31744\n\t"
  "v_mfma_f32_16x16x32_bf16 v[248:251], a[156:159], a[88:91], v[248:251]\n\t"
  "v_mfma_f32_16x16x32_bf16 v[244:247], a[156:159], a[84:87], v[244:247]\n\t"
  "buffer_load_dwordx4 v1, s[20:23], s29 offen offset:3072 sc1 lds\n\t"
  "v_mfma_f32_16x16x32_bf16 v[240:243], a[156:159], a[80:83], v[240:243]\n\t"
  "s_waitcnt lgkmcnt(3) vmcnt(24)\n\t"
  "v_mfma_f32_16x16x32_bf16 v[32:35], a[128:131], a[96:99], v[32:35]\n\t"
  "ds_read_b128 a[144:147], v3 offset:32768\n\t"
  "v_mfma_f32_16x16x32_bf16 v[36:39], a[128:131], a[100:103], v[36:39]\n\t"
  "s_add_u32 m0, s30, 61440\n\t"
  "v_mfma_f32_16x16x32_bf16 v[40:43], a[128:131], a[104:107], v[40:43]\n\t"
  "buffer_load_dwordx4 v2, s[20:23], s29 offen offset:0 sc1 lds\n\t"
  "v_mfma_f32_16x16x32_bf16 v[44:47], a[128:131], a[108:111], v[44:47]\n\t"
  "s_waitcnt lgkmcnt(3)\n\t"
  "v_mfma_f32_16x16x32_bf16 v[60:63], a[132:135], a[108:111], v[60:63]\n\t"
  "ds_read_b128 a[148:151], v3 offset:33792\n\t"
  "v_mfma_f32_16x16x32_bf16 v[56:59], a[132:135], a[104:107], v[56:59]\n\t"
  "v_mfma_f32_16x16x32_bf16 v[52:55], a[132:135], a[100:103], v[52:55]\n\t"
  "buffer_load_dwordx4 v2, s[20:23], s29 offen offset:1024 sc1 lds\n\t"
  "v_mfma_f32_16x16x32_bf16 v[48:51], a[132:135], a[96:99], v[48:51]\n\t"
  "s_waitcnt lgkmcnt(3)\n\t"
  "v_mfma_f32_16x16x32_bf16 v[64:67], a[136:139], a[96:99], v[64:67]\n\t"
  "ds_read_b128 a[152:155], v3 offset:34816\n\t"
  "v_mfma_f32_16x16x32_bf16 v[68:71], a[136:139], a[100:103], v[68:71]\n\t"
  "v_mfma_f32_16x16x32_bf16 v[72:75], a[136:139], a[104:107], v[72:75]\n\t"
  "buffer_load_dwordx4 v2, s[20:23], s29 offen offset:2048 sc1 lds\n\t"
  "v_mfma_f32_16x16x32_bf16 v[76:79], a[136:139], a[108:111], v[76:79]\n\t"
  "s_waitcnt lgkmcnt(3)\n\t"
  "v_mfma_f32_16x16x32_bf16 v[92:95], a[140:143], a[108:111], v[92:95]\n\t"
  "ds_read_b128 a[156:159], v3 offset:35840\n\t"
  "s_add_u32 s29, s29, 28672\n\t"
  "v_mfma_f32_16x16x32_bf16 v[88:91], a[140:143], a[104:107], v[88:91]\n\t"
  "v_mfma_f32_16x16x32_bf16 v[84:87], a[140:143], a[100:103], v[84:87]\n\t"
  "buffer_load_dwordx4 a[32:35], v0, s[20:23], s28 offen offset:0 sc1\n\t"
  "v_mfma_f32_16x16x32_bf16 v[80:83], a[140:143], a[96:99], v[80:83]\n\t"
  "s_waitcnt lgkmcnt(3)\n\t"
  "v_mfma_f32_16x16x32_bf16 v[96:99], a[144:147], a[96:99], v[96:99]\n\t"
  "ds_read_b128 a[128:131], v3 offset:36864\n\t"
  "v_mfma_f32_16x16x32_bf16 v[100:103], a[144:147], a[100:103], v[100:103]\n\t"
  "v_mfma_f32_16x16x32_bf16 v[104:107], a[144:147], a[104:107], v[104:107]\n\t"
  "buffer_load_dwordx4 a[36:39], v0, s[20:23], s28 offen offset:1024 sc1\n\t"
  "v_mfma_f32_16x16x32_bf16 v[108:111], a[144:147], a[108:111], v[108:111]\n\t"
  "s_waitcnt lgkmcnt(3)\n\t"
  "v_mfma_f32_16x16x32_bf16 v[124:127], a[148:151], a[108:111], v[124:127]\n\t"
  "ds_read_b128 a[132:135], v3 offset:37888\n\t"
  "v_mfma_f32_16x16x32_bf16 v[120:123], a[148:151], a[104:107], v[120:123]\n\t"
  "v_mfma_f32_16x16x32_bf16 v[116:119], a[148:151], a[100:103], v[116:119]\n\t"
  "buffer_load_dwordx4 a[40:43], v0, s[20:23], s28 offen offset:2048 sc1\n\t"
  "v_mfma_f32_16x16x32_bf16 v[112:115], a[148:151], a[96:99], v[112:115]\n\t"
  "s_waitcnt lgkmcnt(3)\n\t"
  "v_mfma_f32_16x16x32_bf16 v[128:131], a[152:155], a[96:99], v[128:131]\n\t"
  "ds_read_b128 a[136:139], v3 offset:38912\n\t"
  "v_mfma_f32_16x16x32_bf16 v[132:135], a[152:155], a[100:103], v[132:135]\n\t"
  "v_mfma_f32_16x16x32_bf16 v[136:139], a[152:155], a[104:107], v[136:139]\n\t"
  "buffer_load_dwordx4 a[44:47], v0, s[20:23], s28 offen offset:3072 sc1\n\t"
  "v_mfma_f32_16x16x32_bf16 v[140:143], a[152:155], a[108:111], v[140:143]\n\t"
  "s_waitcnt lgkmcnt(3)\n\t"
  "v_mfma_f32_16x16x32_bf16 v[156:159], a[156:159], a[108:111], v[156:159]\n\t"
  "ds_read_b128 a[140:143], v3 offset:39936\n\t"
  "s_add_u32 s28, s28, 131072\n\t"
  "v_mfma_f32_16x16x32_bf16 v[152:155], a[156:159], a[104:107], v[152:155]\n\t"
  "v_mfma_f32_16x16x32_bf16 v[148:151], a[156:159], a[100:103], v[148:151]\n\t"
  "v_mfma_f32_16x16x32_bf16 v[144:147], a[156:159], a[96:99], v[144:147]\n\t"
  "s_waitcnt lgkmcnt(3)\n\t"
  "v_mfma_f32_16x16x32_bf16 v[160:163], a[128:131], a[96:99], v[160:163]\n\t"
  "ds_read_b128 a[144:147], v3 offset:40960\n\t"
  "v_mfma_f32_16x16x32_bf16 v[164:167], a[128:131], a[100:103], v[164:167]\n\t"
  "v_mfma_f32_16x16x32_bf16 v[168:171], a[128:131], a[104:107], v[168:171]\n\t"
  "v_mfma_f32_16x16x32_bf16 v[172:175], a[128:131], a[108:111], v[172:175]\n\t"
  "s_waitcnt lgkmcnt(3)\n\t"
  "v_mfma_f32_16x16x32_bf16 v[188:191], a[132:135], a[108:111], v[188:191]\n\t"
  "ds_read_b128 a[148:151], v3 offset:41984\n\t"
  "v_mfma_f32_16x16x32_bf16 v[184:187], a[132:135], a[104:107], v[184:187]\n\t"
  "v_mfma_f32_16x16x32_bf16 v[180:183], a[132:135], a[100:103], v[180:183]\n\t"
  "v_mfma_f32_16x16x32_bf16 v[176:179], a[132:135], a[96:99], v[176:179]\n\t"
  "s_waitcnt lgkmcnt(3)\n\t"
  "v_mfma_f32_16x16x32_bf16 v[192:195], a[136:139], a[96:99], v[192:195]\n\t"
  "ds_read_b128 a[152:155], v3 offset:43008\n\t"
  "v_mfma_f32_16x16x32_bf16 v[196:199], a[136:139], a[100:103], v[196:199]\n\t"
  "v_mfma_f32_16x16x32_bf16 v[200:203], a[136:139], a[104:107], v[200:203]\n\t"
  "v_mfma_f32_16x16x32_bf16 v[204:207], a[136:139], a[108:111], v[204:207]\n\t"
  "s_waitcnt lgkmcnt(3)\n\t"
  "v_mfma_f32_16x16x32_bf16 v[220:223], a[140:143], a[108:111], v[220:223]\n\t"
  "ds_read_b128 a[156:159], v3 offset:44032\n\t"
  "v_mfma_f32_16x16x32_bf16 v[216:219], a[140:143], a[104:107], v[216:219]\n\t"
  "v_mfma_f32_16x16x32_bf16 v[212:215], a[140:143], a[100:103], v[212:215]\n\t"
  "v_mfma_f32_16x16x32_bf16 v[208:211], a[140:143], a[96:99], v[208:211]\n\t"
  "s_waitcnt lgkmcnt(3)\n\t"
  "v_mfma_f32_16x16x32_bf16 v[224:227], a[144:147], a[96:99], v[224:227]\n\t"
  "ds_read_b128 a[128:131], v3 offset:45056\n\t"
  "v_mfma_f32_16x16x32_bf16 v[228:231], a[144:147], a[100:103], v[228:231]\n\t"
  "v_mfma_f32_16x16x32_bf16 v[232:235], a[144:147], a[104:107], v[232:235]\n\t"
  "v_mfma_f32_16x16x32_bf16 v[236:239], a[144:147], a[108:111], v[236:239]\n\t"
  "s_waitcnt lgkmcnt(3)\n\t"
  "v_mfma_f32_16x16x32_bf16 v[252:255], a[148:151], a[108:111], v[252:255]\n\t"
  "ds_read_b128 a[132:135], v3 offset:46080\n\t"
  "v_mfma_f32_16x16x32_bf16 v[248:251], a[148:151], a[104:107], v[248:251]\n\t"
  "v_mfma_f32_16x16x32_bf16 v[244:247], a[148:151], a[100:103], v[244:247]\n\t"
  "v_mfma_f32_16x16x32_bf16 v[240:243], a[148:151], a[96:99], v[240:243]\n\t"
  "s_waitcnt lgkmcnt(3) vmcnt(27)\n\t"
  "v_mfma_f32_16x16x32_bf16 v[32:35], a[152:155], a[112:115], v[32:35]\n\t"
  "ds_read_b128 a[136:139], v3 offset:47104\n\t"
  "v_mfma_f32_16x16x32_bf16 v[36:39], a[152:155], a[116:119], v[36:39]\n\t"
  "v_mfma_f32_16x16x32_bf16 v[40:43], a[152:155], a[120:123], v[40:43]\n\t"
  "v_mfma_f32_16x16x32_bf16 v[44:47], a[152:155], a[124:127], v[44:47]\n\t"
  "s_waitcnt lgkmcnt(3)\n\t"
  "v_mfma_f32_16x16x32_bf16 v[60:63], a[156:159], a[124:127], v[60:63]\n\t"
  "ds_read_b128 a[140:143], v3 offset:48128\n\t"
  "v_mfma_f32_16x16x32_bf16 v[56:59], a[156:159], a[120:123], v[56:59]\n\t"
  "v_mfma_f32_16x16x32_bf16 v[52:55], a[156:159], a[116:119], v[52:55]\n\t"
  "v_mfma_f32_16x16x32_bf16 v[48:51], a[156:159], a[112:115], v[48:51]\n\t"
  "s_waitcnt lgkmcnt(3)\n\t"
  "v_mfma_f32_16x16x32_bf16 v[64:67], a[128:131], a[112:115], v[64:67]\n\t"
  "ds_read_b128 a[144:147], v3 offset:49152\n\t"
  "v_mfma_f32_16x16x32_bf16 v[68:71], a[128:131], a[116:119], v[68:71]\n\t"
  "v_mfma_f32_16x16x32_bf16 v[72:75], a[128:131], a[120:123], v[72:75]\n\t"
  "v_mfma_f32_16x16x32_bf16 v[76:79], a[128:131], a[124:127], v[76:79]\n\t"
  "s_waitcnt lgkmcnt(3)\n\t"
  "v_mfma_f32_16x16x32_bf16 v[92:95], a[132:135], a[124:127], v[92:95]\n\t"
  "ds_read_b128 a[148:151], v3 offset:50176\n\t"
  "v_mfma_f32_16x16x32_bf16 v[88:91], a[132:135], a[120:123], v[88:91]\n\t"
  "v_mfma_f32_16x16x32_bf16 v[84:87], a[132:135], a[116:119], v[84:87]\n\t"
  "v_mfma_f32_16x16x32_bf16 v[80:83], a[132:135], a[112:115], v[80:83]\n\t"
  "s_waitcnt lgkmcnt(3)\n\t"
  "v_mfma_f32_16x16x32_bf16 v[96:99], a[136:139], a[112:115], v[96:99]\n\t"
  "ds_read_b128 a[152:155], v3 offset:51200\n\t"
  "v_mfma_f32_16x16x32_bf16 v[100:103], a[136:139], a[116:119], v[100:103]\n\t"
  "v_mfma_f32_16x16x32_bf16 v[104:107], a[136:139], a[120:123], v[104:107]\n\t"
  "v_mfma_f32_16x16x32_bf16 v[108:111], a[136:139], a[124:127], v[108:111]\n\t"
  "s_waitcnt lgkmcnt(3)\n\t"
  "v_mfma_f32_16x16x32_bf16 v[124:127], a[140:143], a[124:127], v[124:127]\n\t"
  "ds_read_b128 a[156:159], v3 offset:52224\n\t"
  "v_mfma_f32_16x16x32_bf16 v[120:123], a[140:143], a[120:123], v[120:123]\n\t"
  "v_mfma_f32_16x16x32_bf16 v[116:119], a[140:143], a[116:119], v[116:119]\n\t"
  "v_mfma_f32_16x16x32_bf16 v[112:115], a[140:143], a[112:115], v[112:115]\n\t"
  "s_waitcnt lgkmcnt(3)\n\t"
  "v_mfma_f32_16x16x32_bf16 v[128:131], a[144:147], a[112:115], v[128:131]\n\t"
  "ds_read_b128 a[128:131], v3 offset:53248\n\t"
  "v_mfma_f32_16x16x32_bf16 v[132:135], a[144:147], a[116:119], v[132:135]\n\t"
  "v_mfma_f32_16x16x32_bf16 v[136:139], a[144:147], a[120:123], v[136:139]\n\t"
  "v_mfma_f32_16x16x32_bf16 v[140:143], a[144:147], a[124:127], v[140:143]\n\t"
  "s_waitcnt lgkmcnt(3)\n\t"
  "v_mfma_f32_16x16x32_bf16 v[156:159], a[148:151], a[124:127], v[156:159]\n\t"
  "ds_read_b128 a[132:135], v3 offset:54272\n\t"
  "v_mfma_f32_16x16x32_bf16 v[152:155], a[148:151], a[120:123], v[152:155]\n\t"
  "v_mfma_f32_16x16x32_bf16 v[148:151], a[148:151], a[116:119], v[148:151]\n\t"
  "v_mfma_f32_16x16x32_bf16 v[144:147], a[148:151], a[112:115], v[144:147]\n\t"
  "s_waitcnt lgkmcnt(3)\n\t"
  "v_mfma_f32_16x16x32_bf16 v[160:163], a[152:155], a[112:115], v[160:163]\n\t"
  "ds_read_b128 a[136:139], v3 offset:55296\n\t"
  "v_mfma_f32_16x16x32_bf16 v[164:167], a[152:155], a[116:119], v[164:167]\n\t"
  "v_mfma_f32_16x16x32_bf16 v[168:171], a[152:155], a[120:123], v[168:171]\n\t"
  "v_mfma_f32_16x16x32_bf16 v[172:175], a[152:155], a[124:127], v[172:175]\n\t"
  "s_waitcnt lgkmcnt(3)\n\t"
  "v_mfma_f32_16x16x32_bf16 v[188:191], a[156:159], a[124:127], v[188:191]\n\t"
  "ds_read_b128 a[140:143], v3 offset:56320\n\t"
  "v_mfma_f32_16x16x32_bf16 v[184:187], a[156:159], a[120:123], v[184:187]\n\t"
  "s_waitcnt vmcnt(19)\n\t"
  "v_mfma_f32_16x16x32_bf16 v[180:183], a[156:159], a[116:119], v[180:183]\n\t"
  "s_barrier\n\t"
  "v_mfma_f32_16x16x32_bf16 v[176:179], a[156:159], a[112:115], v[176:179]\n\t"
  "s_waitcnt lgkmcnt(3)\n\t"
  "v_mfma_f32_16x16x32_bf16 v[192:195], a[128:131], a[112:115], v[192:195]\n\t"
  "ds_read_b128 a[144:147], v4 offset:28672\n\t"
  "v_mfma_f32_16x16x32_bf16 v[196:199], a[128:131], a[116:119], v[196:199]\n\t"
  "s_add_u32 m0, s30, 0\n\t"
  "v_mfma_f32_16x16x32_bf16 v[200:203], a[128:131], a[120:123], v[200:203]\n\t"
  "buffer_load_dwordx4 v1, s[20:23], s29 offen offset:0 sc1 lds\n\t"
  "v_mfma_f32_16x16x32_bf16 v[204:207], a[128:131], a[124:127], v[204:207]\n\t"
  "s_waitcnt lgkmcnt(3)\n\t"
  "v_mfma_f32_16x16x32_bf16 v[220:223], a[132:135], a[124:127], v[220:223]\n\t"
  "ds_read_b128 a[148:151], v4 offset:29696\n\t"
  "v_mfma_f32_16x16x32_bf16 v[216:219], a[132:135], a[120:123], v[216:219]\n\t"
  "v_mfma_f32_16x16x32_bf16 v[212:215], a[132:135], a[116:119], v[212:215]\n\t"
  "buffer_load_dwordx4 v1, s[20:23], s29 offen offset:1024 sc1 lds\n\t"
  "v_mfma_f32_16x16x32_bf16 v[208:211], a[132:135], a[112:115], v[208:211]\n\t"
  "s_waitcnt lgkmcnt(3)\n\t"
  "v_mfma_f32_16x16x32_bf16 v[224:227], a[136:139], a[112:115], v[224:227]\n\t"
  "ds_read_b128 a[152:155], v4 offset:30720\n\t"
  "v_mfma_f32_16x16x32_bf16 v[228:231], a[136:139], a[116:119], v[228:231]\n\t"
  "v_mfma_f32_16x16x32_bf16 v[232:235], a[136:139], a[120:123], v[232:235]\n\t"
  "buffer_load_dwordx4 v1, s[20:23], s29 offen offset:2048 sc1 lds\n\t"
  "v_mfma_f32_16x16x32_bf16 v[236:239], a[136:139], a[124:127], v[236:239]\n\t"
  "s_waitcnt lgkmcnt(3)\n\t"
  "v_mfma_f32_16x16x32_bf16 v[252:255], a[140:143], a[124:127], v[252:255]\n\t"
  "ds_read_b128 a[156:159], v4 offset:31744\n\t"
  "v_mfma_f32_16x16x32_bf16 v[248:251], a[140:143], a[120:123], v[248:251]\n\t"
  "s_barrier\n\t"
  "v_mfma_f32_16x16x32_bf16 v[244:247], a[140:143], a[116:119], v[244:247]\n\t"
  "buffer_load_dwordx4 v1, s[20:23], s29 offen offset:3072 sc1 lds\n\t"
  "v_mfma_f32_16x16x32_bf16 v[240:243], a[140:143], a[112:115], v[240:243]\n\t"
  "s_waitcnt lgkmcnt(3) vmcnt(19)\n\t"
  "v_mfma_f32_16x16x32_bf16 v[32:35], a[144:147], a[0:3], v[32:35]\n\t"
  "ds_read_b128 a[128:131], v4 offset:32768\n\t"
  "v_mfma_f32_16x16x32_bf16 v[36:39], a[144:147], a[4:7], v[36:39]\n\t"
  "s_add_u32 m0, s30, 4096\n\t"
  "v_mfma_f32_16x16x32_bf16 v[40:43], a[144:147], a[8:11], v[40:43]\n\t"
  "buffer_load_dwordx4 v2, s[20:23], s29 offen offset:0 sc1 lds\n\t"
  "v_mfma_f32_16x16x32_bf16 v[44:47], a[144:147], a[12:15], v[44:47]\n\t"
  "s_waitcnt lgkmcnt(3)\n\t"
  "v_mfma_f32_16x16x32_bf16 v[60:63], a[148:151], a[12:15], v[60:63]\n\t"
  "ds_read_b128 a[132:135], v4 offset:33792\n\t"
  "v_mfma_f32_16x16x32_bf16 v[56:59], a[148:151], a[8:11], v[56:59]\n\t"
  "v_mfma_f32_16x16x32_bf16 v[52:55], a[148:151], a[4:7], v[52:55]\n\t"
  "buffer_load_dwordx4 v2, s[20:23], s29 offen offset:1024 sc1 lds\n\t"
  "v_mfma_f32_16x16x32_bf16 v[48:51], a[148:151], a[0:3], v[48:51]\n\t"
  "s_waitcnt lgkmcnt(3)\n\t"
  "v_mfma_f32_16x16x32_bf16 v[64:67], a[152:155], a[0:3], v[64:67]\n\t"
  "ds_read_b128 a[136:139], v4 offset:34816\n\t"
  "v_mfma_f32_16x16x32_bf16 v[68:71], a[152:155], a[4:7], v[68:71]\n\t"
  "v_mfma_f32_16x16x32_bf16 v[72:75], a[152:155], a[8:11], v[72:75]\n\t"
  "buffer_load_dwordx4 v2, s[20:23], s29 offen offset:2048 sc1 lds\n\t"
  "v_mfma_f32_16x16x32_bf16 v[76:79], a[152:155], a[12:15], v[76:79]\n\t"
  "s_waitcnt lgkmcnt(3)\n\t"
  "v_mfma_f32_16x16x32_bf16 v[92:95], a[156:159], a[12:15], v[92:95]\n\t"
  "ds_read_b128 a[140:143], v4 offset:35840\n\t"
  "s_add_u32 s29, s29, 28672\n\t"
  "v_mfma_f32_16x16x32_bf16 v[88:91], a[156:159], a[8:11], v[88:91]\n\t"
  "v_mfma_f32_16x16x32_bf16 v[84:87], a[156:159], a[4:7], v[84:87]\n\t"
  "buffer_load_dwordx4 a[48:51], v0, s[20:23], s28 offen offset:0 sc1\n\t"
  "v_mfma_f32_16x16x32_bf16 v[80:83], a[156:159], a[0:3], v[80:83]\n\t"
  "s_waitcnt lgkmcnt(3)\n\t"
  "v_mfma_f32_16x16x32_bf16 v[96:99], a[128:131], a[0:3], v[96:99]\n\t"
  "ds_read_b128 a[144:147], v4 offset:36864\n\t"
  "v_mfma_f32_16x16x32_bf16 v[100:103], a[128:131], a[4:7], v[100:103]\n\t"
  "v_mfma_f32_16x16x32_bf16 v[104:107], a[128:131], a[8:11], v[104:107]\n\t"
  "buffer_load_dwordx4 a[52:55], v0, s[20:23], s28 offen offset:1024 sc1\n\t"
  "v_mfma_f32_16x16x32_bf16 v[108:111], a[128:131], a[12:15], v[108:111]\n\t"
  "s_waitcnt lgkmcnt(3)\n\t"
  "v_mfma_f32_16x16x32_bf16 v[124:127], a[132:135], a[12:15], v[124:127]\n\t"
  "ds_read_b128 a[148:151], v4 offset:37888\n\t"
  "v_mfma_f32_16x16x32_bf16 v[120:123], a[132:135], a[8:11], v[120:123]\n\t"
  "v_mfma_f32_16x16x32_bf16 v[116:119], a[132:135], a[4:7], v[116:119]\n\t"
  "buffer_load_dwordx4 a[56:59], v0, s[20:23], s28 offen offset:2048 sc1\n\t"
  "v_mfma_f32_16x16x32_bf16 v[112:115], a[132:135], a[0:3], v[112:115]\n\t"
  "s_waitcnt lgkmcnt(3)\n\t"
  "v_mfma_f32_16x16x32_bf16 v[128:131], a[136:139], a[0:3], v[128:131]\n\t"
  "ds_read_b128 a[152:155], v4 offset:38912\n\t"
  "v_mfma_f32_16x16x32_bf16 v[132:135], a[136:139], a[4:7], v[132:135]\n\t"
  "v_mfma_f32_16x16x32_bf16 v[136:139], a[136:139], a[8:11], v[136:139]\n\t"
  "buffer_load_dwordx4 a[60:63], v0, s[20:23], s28 offen offset:3072 sc1\n\t"
  "v_mfma_f32_16x16x32_bf16 v[140:143], a[136:139], a[12:15], v[140:143]\n\t"
  "s_waitcnt lgkmcnt(3)\n\t"
  "v_mfma_f32_16x16x32_bf16 v[156:159], a[140:143], a[12:15], v[156:159]\n\t"
  "ds_read_b128 a[156:159], v4 offset:39936\n\t"
  "s_add_u32 s28, s28, 131072\n\t"
  "v_mfma_f32_16x16x32_bf16 v[152:155], a[140:143], a[8:11], v[152:155]\n\t"
  "v_mfma_f32_16x16x32_bf16 v[148:151], a[140:143], a[4:7], v[148:151]\n\t"
  "buffer_load_dwordx4 a[64:67], v0, s[20:23], s28 offen offset:0 sc1\n\t"
  "v_mfma_f32_16x16x32_bf16 v[144:147], a[140:143], a[0:3], v[144:147]\n\t"
  "s_waitcnt lgkmcnt(3)\n\t"
  "v_mfma_f32_16x16x32_bf16 v[160:163], a[144:147], a[0:3], v[160:163]\n\t"
  "ds_read_b128 a[128:131], v4 offset:40960\n\t"
  "v_mfma_f32_16x16x32_bf16 v[164:167], a[144:147], a[4:7], v[164:167]\n\t"
  "v_mfma_f32_16x16x32_bf16 v[168:171], a[144:147], a[8:11], v[168:171]\n\t"
  "buffer_load_dwordx4 a[68:71], v0, s[20:23], s28 offen offset:1024 sc1\n\t"
  "v_mfma_f32_16x16x32_bf16 v[172:175], a[144:147], a[12:15], v[172:175]\n\t"
  "s_waitcnt lgkmcnt(3)\n\t"
  "v_mfma_f32_16x16x32_bf16 v[188:191], a[148:151], a[12:15], v[188:191]\n\t"
  "ds_read_b128 a[132:135], v4 offset:41984\n\t"
  "v_mfma_f32_16x16x32_bf16 v[184:187], a[148:151], a[8:11], v[184:187]\n\t"
  "v_mfma_f32_16x16x32_bf16 v[180:183], a[148:151], a[4:7], v[180:183]\n\t"
  "buffer_load_dwordx4 a[72:75], v0, s[20:23], s28 offen offset:2048 sc1\n\t"
  "v_mfma_f32_16x16x32_bf16 v[176:179], a[148:151], a[0:3], v[176:179]\n\t"
  "s_waitcnt lgkmcnt(3)\n\t"
  "v_mfma_f32_16x16x32_bf16 v[192:195], a[152:155], a[0:3], v[192:195]\n\t"
  "ds_read_b128 a[136:139], v4 offset:43008\n\t"
  "v_mfma_f32_16x16x32_bf16 v[196:199], a[152:155], a[4:7], v[196:199]\n\t"
  "v_mfma_f32_16x16x32_bf16 v[200:203], a[152:155], a[8:11], v[200:203]\n\t"
  "buffer_load_dwordx4 a[76:79], v0, s[20:23], s28 offen offset:3072 sc1\n\t"
  "v_mfma_f32_16x16x32_bf16 v[204:207], a[152:155], a[12:15], v[204:207]\n\t"
  "s_waitcnt lgkmcnt(3)\n\t"
  "v_mfma_f32_16x16x32_bf16 v[220:223], a[156:159], a[12:15], v[220:223]\n\t"
  "ds_read_b128 a[140:143], v4 offset:44032\n\t"
  "s_add_u32 s28, s28, 131072\n\t"
  "v_mfma_f32_16x16x32_bf16 v[216:219], a[156:159], a[8:11], v[216:219]\n\t"
  "s_add_u32 m0, s30, 28672\n\t"
  "v_mfma_f32_16x16x32_bf16 v[212:215], a[156:159], a[4:7], v[212:215]\n\t"
  "buffer_load_dwordx4 v1, s[20:23], s29 offen offset:0 sc1 lds\n\t"
  "v_mfma_f32_16x16x32_bf16 v[208:211], a[156:159], a[0:3], v[208:211]\n\t"
  "s_waitcnt lgkmcnt(3)\n\t"
  "v_mfma_f32_16x16x32_bf16 v[224:227], a[128:131], a[0:3], v[224:227]\n\t"
  "ds_read_b128 a[144:147], v4 offset:45056\n\t"
  "v_mfma_f32_16x16x32_bf16 v[228:231], a[128:131], a[4:7], v[228:231]\n\t"
  "v_mfma_f32_16x16x32_bf16 v[232:235], a[128:131], a[8:11], v[232:235]\n\t"
  "buffer_load_dwordx4 v1, s[20:23], s29 offen offset:1024 sc1 lds\n\t"
  "v_mfma_f32_16x16x32_bf16 v[236:239], a[128:131], a[12:15], v[236:239]\n\t"
  "s_waitcnt lgkmcnt(3)\n\t"
  "v_mfma_f32_16x16x32_bf16 v[252:255], a[132:135], a[12:15], v[252:255]\n\t"
  "ds_read_b128 a[148:151], v4 offset:46080\n\t"
  "v_mfma_f32_16x16x32_bf16 v[248:251], a[132:135], a[8:11], v[248:251]\n\t"
  "v_mfma_f32_16x16x32_bf16 v[244:247], a[132:135], a[4:7], v[244:247]\n\t"
  "buffer_load_dwordx4 v1, s[20:23], s29 offen offset:2048 sc1 lds\n\t"
  "v_mfma_f32_16x16x32_bf16 v[240:243], a[132:135], a[0:3], v[240:243]\n\t"
  "s_waitcnt lgkmcnt(3) vmcnt(29)\n\t"
  "v_mfma_f32_16x16x32_bf16 v[32:35], a[136:139], a[16:19], v[32:35]\n\t"
  "ds_read_b128 a[152:155], v4 offset:47104\n\t"
  "v_mfma_f32_16x16x32_bf16 v[36:39], a[136:139], a[20:23], v[36:39]\n\t"
  "v_mfma_f32_16x16x32_bf16 v[40:43], a[136:139], a[24:27], v[40:43]\n\t"
  "buffer_load_dwordx4 v1, s[20:23], s29 offen offset:3072 sc1 lds\n\t"
  "v_mfma_f32_16x16x32_bf16 v[44:47], a[136:139], a[28:31], v[44:47]\n\t"
  "s_waitcnt lgkmcnt(3)\n\t"
  "v_mfma_f32_16x16x32_bf16 v[60:63], a[140:143], a[28:31], v[60:63]\n\t"
  "ds_read_b128 a[156:159], v4 offset:48128\n\t"
  "v_mfma_f32_16x16x32_bf16 v[56:59], a[140:143], a[24:27], v[56:59]\n\t"
  "s_add_u32 m0, s30, 32768\n\t"
  "v_mfma_f32_16x16x32_bf16 v[52:55], a[140:143], a[20:23], v[52:55]\n\t"
  "buffer_load_dwordx4 v2, s[20:23], s29 offen offset:0 sc1 lds\n\t"
  "v_mfma_f32_16x16x32_bf16 v[48:51], a[140:143], a[16:19], v[48:51]\n\t"
  "s_waitcnt lgkmcnt(3)\n\t"
  "v_mfma_f32_16x16x32_bf16 v[64:67], a[144:147], a[16:19], v[64:67]\n\t"
  "ds_read_b128 a[128:131], v4 offset:49152\n\t"
  "v_mfma_f32_16x16x32_bf16 v[68:71], a[144:147], a[20:23], v[68:71]\n\t"
  "v_mfma_f32_16x16x32_bf16 v[72:75], a[144:147], a[24:27], v[72:75]\n\t"
  "buffer_load_dwordx4 v2, s[20:23], s29 offen offset:1024 sc1 lds\n\t"
  "v_mfma_f32_16x16x32_bf16 v[76:79], a[144:147], a[28:31], v[76:79]\n\t"
  "s_waitcnt lgkmcnt(3)\n\t"
  "v_mfma_f32_16x16x32_bf16 v[92:95], a[148:151], a[28:31], v[92:95]\n\t"
  "ds_read_b128 a[132:135], v4 offset:50176\n\t"
  "v_mfma_f32_16x16x32_bf16 v[88:91], a[148:151], a[24:27], v[88:91]\n\t"
  "v_mfma_f32_16x16x32_bf16 v[84:87], a[148:151], a[20:23], v[84:87]\n\t"
  "buffer_load_dwordx4 v2, s[20:23], s29 offen offset:2048 sc1 lds\n\t"
  "v_mfma_f32_16x16x32_bf16 v[80:83], a[148:151], a[16:19], v[80:83]\n\t"
  "s_waitcnt lgkmcnt(3)\n\t"
  "v_mfma_f32_16x16x32_bf16 v[96:99], a[152:155], a[16:19], v[96:99]\n\t"
  "ds_read_b128 a[136:139], v4 offset:51200\n\t"
  "v_mfma_f32_16x16x32_bf16 v[100:103], a[152:155], a[20:23], v[100:103]\n\t"
  "v_mfma_f32_16x16x32_bf16 v[104:107], a[152:155], a[24:27], v[104:107]\n\t"
  "buffer_load_dwordx4 a[80:83], v0, s[20:23], s28 offen offset:0 sc1\n\t"
  "v_mfma_f32_16x16x32_bf16 v[108:111], a[152:155], a[28:31], v[108:111]\n\t"
  "s_waitcnt lgkmcnt(3)\n\t"
  "v_mfma_f32_16x16x32_bf16 v[124:127], a[156:159], a[28:31], v[124:127]\n\t"
  "ds_read_b128 a[140:143], v4 offset:52224\n\t"
  "v_mfma_f32_16x16x32_bf16 v[120:123], a[156:159], a[24:27], v[120:123]\n\t"
  "v_mfma_f32_16x16x32_bf16 v[116:119], a[156:159], a[20:23], v[116:119]\n\t"
  "buffer_load_dwordx4 a[84:87], v0, s[20:23], s28 offen offset:1024 sc1\n\t"
  "v_mfma_f32_16x16x32_bf16 v[112:115], a[156:159], a[16:19], v[112:115]\n\t"
  "s_waitcnt lgkmcnt(3)\n\t"
  "v_mfma_f32_16x16x32_bf16 v[128:131], a[128:131], a[16:19], v[128:131]\n\t"
  "ds_read_b128 a[144:147], v4 offset:53248\n\t"
  "v_mfma_f32_16x16x32_bf16 v[132:135], a[128:131], a[20:23], v[132:135]\n\t"
  "v_mfma_f32_16x16x32_bf16 v[136:139], a[128:131], a[24:27], v[136:139]\n\t"
  "buffer_load_dwordx4 a[88:91], v0, s[20:23], s28 offen offset:2048 sc1\n\t"
  "v_mfma_f32_16x16x32_bf16 v[140:143], a[128:131], a[28:31], v[140:143]\n\t"
  "s_waitcnt lgkmcnt(3)\n\t"
  "v_mfma_f32_16x16x32_bf16 v[156:159], a[132:135], a[28:31], v[156:159]\n\t"
  "ds_read_b128 a[148:151], v4 offset:54272\n\t"
  "v_mfma_f32_16x16x32_bf16 v[152:155], a[132:135], a[24:27], v[152:155]\n\t"
  "v_mfma_f32_16x16x32_bf16 v[148:151], a[132:135], a[20:23], v[148:151]\n\t"
  "buffer_load_dwordx4 a[92:95], v0, s[20:23], s28 offen offset:3072 sc1\n\t"
  "v_mfma_f32_16x16x32_bf16 v[144:147], a[132:135], a[16:19], v[144:147]\n\t"
  "s_waitcnt lgkmcnt(3)\n\t"
  "v_mfma_f32_16x16x32_bf16 v[160:163], a[136:139], a[16:19], v[160:163]\n\t"
  "ds_read_b128 a[152:155], v4 offset:55296\n\t"
  "s_add_u32 s28, s28, 131072\n\t"
  "v_mfma_f32_16x16x32_bf16 v[164:167], a[136:139], a[20:23], v[164:167]\n\t"
  "v_mfma_f32_16x16x32_bf16 v[168:171], a[136:139], a[24:27], v[168:171]\n\t"
  "buffer_load_dwordx4 a[96:99], v0, s[20:23], s28 offen offset:0 sc1\n\t"
  "v_mfma_f32_16x16x32_bf16 v[172:175], a[136:139], a[28:31], v[172:175]\n\t"
  "s_waitcnt lgkmcnt(3)\n\t"
  "v_mfma_f32_16x16x32_bf16 v[188:191], a[140:143], a[28:31], v[188:191]\n\t"
  "ds_read_b128 a[156:159], v4 offset:56320\n\t"
  "s_waitcnt vmcnt(31)\n\t"
  "v_mfma_f32_16x16x32_bf16 v[184:187], a[140:143], a[24:27], v[184:187]\n\t"
  "s_barrier\n\t"
  "v_mfma_f32_16x16x32_bf16 v[180:183], a[140:143], a[20:23], v[180:183]\n\t"
  "buffer_load_dwordx4 a[100:103], v0, s[20:23], s28 offen offset:1024 sc1\n\t"
  "v_mfma_f32_16x16x32_bf16 v[176:179], a[140:143], a[16:19], v[176:179]\n\t"
  "s_waitcnt lgkmcnt(3)\n\t"
  "v_mfma_f32_16x16x32_bf16 v[192:195], a[144:147], a[16:19], v[192:195]\n\t"
  "ds_read_b128 a[128:131], v4 offset:0\n\t"
  "v_mfma_f32_16x16x32_bf16 v[196:199], a[144:147], a[20:23], v[196:199]\n\t"
  "v_mfma_f32_16x16x32_bf16 v[200:203], a[144:147], a[24:27], v[200:203]\n\t"
  "buffer_load_dwordx4 a[104:107], v0, s[20:23], s28 offen offset:2048 sc1\n\t"
  "v_mfma_f32_16x16x32_bf16 v[204:207], a[144:147], a[28:31], v[204:207]\n\t"
  "s_waitcnt lgkmcnt(3)\n\t"
  "v_mfma_f32_16x16x32_bf16 v[220:223], a[148:151], a[28:31], v[220:223]\n\t"
  "ds_read_b128 a[132:135], v4 offset:1024\n\t"
  "v_mfma_f32_16x16x32_bf16 v[216:219], a[148:151], a[24:27], v[216:219]\n\t"
  "v_mfma_f32_16x16x32_bf16 v[212:215], a[148:151], a[20:23], v[212:215]\n\t"
  "buffer_load_dwordx4 a[108:111], v0, s[20:23], s28 offen offset:3072 sc1\n\t"
  "v_mfma_f32_16x16x32_bf16 v[208:211], a[148:151], a[16:19], v[208:211]\n\t"
  "s_waitcnt lgkmcnt(3)\n\t"
  "v_mfma_f32_16x16x32_bf16 v[224:227], a[152:155], a[16:19], v[224:227]\n\t"
  "ds_read_b128 a[136:139], v4 offset:2048\n\t"
  "s_add_u32 s28, s28, 131072\n\t"
  "v_mfma_f32_16x16x32_bf16 v[228:231], a[152:155], a[20:23], v[228:231]\n\t"
  "v_mfma_f32_16x16x32_bf16 v[232:235], a[152:155], a[24:27], v[232:235]\n\t"
  "buffer_load_dwordx4 a[112:115], v0, s[20:23], s28 offen offset:0 sc1\n\t"
  "v_mfma_f32_16x16x32_bf16 v[236:239], a[152:155], a[28:31], v[236:239]\n\t"
  "s_waitcnt lgkmcnt(3)\n\t"
  "v_mfma_f32_16x16x32_bf16 v[252:255], a[156:159], a[28:31], v[252:255]\n\t"
  "ds_read_b128 a[140:143], v4 offset:3072\n\t"
  "v_mfma_f32_16x16x32_bf16 v[248:251], a[156:159], a[24:27], v[248:251]\n\t"
  "v_mfma_f32_16x16x32_bf16 v[244:247], a[156:159], a[20:23], v[244:247]\n\t"
  "buffer_load_dwordx4 a[116:119], v0, s[20:23], s28 offen offset:1024 sc1\n\t"
  "v_mfma_f32_16x16x32_bf16 v[240:243], a[156:159], a[16:19], v[240:243]\n\t"
  "s_waitcnt lgkmcnt(3) vmcnt(32)\n\t"
  "v_mfma_f32_16x16x32_bf16 v[32:35], a[128:131], a[32:35], v[32:35]\n\t"
  "ds_read_b128 a[144:147], v4 offset:14336\n\t"
  "v_mfma_f32_16x16x32_bf16 v[36:39], a[128:131], a[36:39], v[36:39]\n\t"
  "v_mfma_f32_16x16x32_bf16 v[40:43], a[128:131], a[40:43], v[40:43]\n\t"
  "buffer_load_dwordx4 a[120:123], v0, s[20:23], s28 offen offset:2048 sc1\n\t"
  "v_mfma_f32_16x16x32_bf16 v[44:47], a[128:131], a[44:47], v[44:47]\n\t"
  "s_waitcnt lgkmcnt(3)\n\t"
  "v_mfma_f32_16x16x32_bf16 v[60:63], a[132:135], a[44:47], v[60:63]\n\t"
  "ds_read_b128 a[148:151], v4 offset:15360\n\t"
  "v_mfma_f32_16x16x32_bf16 v[56:59], a[132:135], a[40:43], v[56:59]\n\t"
  "v_mfma_f32_16x16x32_bf16 v[52:55], a[132:135], a[36:39], v[52:55]\n\t"
  "buffer_load_dwordx4 a[124:127], v0, s[20:23], s28 offen offset:3072 sc1\n\t"
  "v_mfma_f32_16x16x32_bf16 v[48:51], a[132:135], a[32:35], v[48:51]\n\t"
  "s_waitcnt lgkmcnt(3)\n\t"
  "v_mfma_f32_16x16x32_bf16 v[64:67], a[136:139], a[32:35], v[64:67]\n\t"
  "ds_read_b128 a[152:155], v4 offset:16384\n\t"
  "v_mfma_f32_16x16x32_bf16 v[68:71], a[136:139], a[36:39], v[68:71]\n\t"
  "s_add_u32 m0, s58, 0x4000\n\t"
  "v_mfma_f32_16x16x32_bf16 v[72:75], a[136:139], a[40:43], v[72:75]\n\t"
  "buffer_load_dwordx4 v14, s[20:23], s57 offen offset:0 sc1 lds\n\t"
  "v_mfma_f32_16x16x32_bf16 v[76:79], a[136:139], a[44:47], v[76:79]\n\t"
  "s_waitcnt lgkmcnt(3)\n\t"
  "v_mfma_f32_16x16x32_bf16 v[92:95], a[140:143], a[44:47], v[92:95]\n\t"
  "ds_read_b128 a[156:159], v4 offset:17408\n\t"
  "s_waitcnt vmcnt(28)\n\t"
  "v_mfma_f32_16x16x32_bf16 v[88:91], a[140:143], a[40:43], v[88:91]\n\t"
  "s_barrier\n\t"
  "v_mfma_f32_16x16x32_bf16 v[84:87], a[140:143], a[36:39], v[84:87]\n\t"
  "buffer_load_dwordx4 v15, s[20:23], s57 offen offset:1024 sc1 lds\n\t"
  "v_mfma_f32_16x16x32_bf16 v[80:83], a[140:143], a[32:35], v[80:83]\n\t"
  "s_waitcnt lgkmcnt(3) vmcnt(25)\n\t"
  "v_mfma_f32_16x16x32_bf16 v[32:35], a[144:147], a[48:51], v[32:35]\n\t"
  "ds_read_b128 a[128:131], v3 offset:0\n\t"
  "s_add_u32 s57, s57, 0x8000\n\t"
  "v_mfma_f32_16x16x32_bf16 v[36:39], a[144:147], a[52:55], v[36:39]\n\t"
  "s_add_u32 m0, s58, 0x6000\n\t"
  "v_mfma_f32_16x16x32_bf16 v[40:43], a[144:147], a[56:59], v[40:43]\n\t"
  "buffer_load_dwordx4 v14, s[20:23], s57 offen offset:0 sc1 lds\n\t"
  "v_mfma_f32_16x16x32_bf16 v[44:47], a[144:147], a[60:63], v[44:47]\n\t"
  "s_waitcnt lgkmcnt(3)\n\t"
  "v_mfma_f32_16x16x32_bf16 v[60:63], a[148:151], a[60:63], v[60:63]\n\t"
  "ds_read_b128 a[132:135], v3 offset:1024\n\t"
  "v_mfma_f32_16x16x32_bf16 v[56:59], a[148:151], a[56:59], v[56:59]\n\t"
  "v_mfma_f32_16x16x32_bf16 v[52:55], a[148:151], a[52:55], v[52:55]\n\t"
  "buffer_load_dwordx4 v15, s[20:23], s57 offen offset:1024 sc1 lds\n\t"
  "v_mfma_f32_16x16x32_bf16 v[48:51], a[148:151], a[48:51], v[48:51]\n\t"
  "s_waitcnt lgkmcnt(3)\n\t"
  "v_mfma_f32_16x16x32_bf16 v[64:67], a[152:155], a[48:51], v[64:67]\n\t"
  "ds_read_b128 a[136:139], v3 offset:2048\n\t"
  "s_add_u32 s57, s57, 0x6000\n\t"
  "v_mfma_f32_16x16x32_bf16 v[68:71], a[152:155], a[52:55], v[68:71]\n\t"
  "s_add_u32 m0, s58, 0x8000\n\t"
  "v_mfma_f32_16x16x32_bf16 v[72:75], a[152:155], a[56:59], v[72:75]\n\t"
  "buffer_load_dwordx4 v14, s[20:23], s57 offen offset:0 sc1 lds\n\t"
  "v_mfma_f32_16x16x32_bf16 v[76:79], a[152:155], a[60:63], v[76:79]\n\t"
  "s_waitcnt lgkmcnt(3)\n\t"
  "v_mfma_f32_16x16x32_bf16 v[92:95], a[156:159], a[60:63], v[92:95]\n\t"
  "ds_read_b128 a[140:143], v3 offset:3072\n\t"
  "v_mfma_f32_16x16x32_bf16 v[88:91], a[156:159], a[56:59], v[88:91]\n\t"
  "v_mfma_f32_16x16x32_bf16 v[84:87], a[156:159], a[52:55], v[84:87]\n\t"
  "buffer_load_dwordx4 v15, s[20:23], s57 offen offset:1024 sc1 lds\n\t"
  "v_mfma_f32_16x16x32_bf16 v[80:83], a[156:159], a[48:51], v[80:83]\n\t"
  "s_waitcnt lgkmcnt(3) vmcnt(25)\n\t"
  "v_mfma_f32_16x16x32_bf16 v[32:35], a[128:131], a[64:67], v[32:35]\n\t"
  "ds_read_b128 a[144:147], v3 offset:14336\n\t"
  "s_add_u32 s57, s57, 0x8000\n\t"
  "v_mfma_f32_16x16x32_bf16 v[36:39], a[128:131], a[68:71], v[36:39]\n\t"
  "s_add_u32 m0, s58, 0xa000\n\t"
  "v_mfma_f32_16x16x32_bf16 v[40:43], a[128:131], a[72:75], v[40:43]\n\t"
  "buffer_load_dwordx4 v14, s[20:23], s57 offen offset:0 sc1 lds\n\t"
  "v_mfma_f32_16x16x32_bf16 v[44:47], a[128:131], a[76:79], v[44:47]\n\t"
  "s_waitcnt lgkmcnt(3)\n\t"
  "v_mfma_f32_16x16x32_bf16 v[60:63], a[132:135], a[76:79], v[60:63]\n\t"
  "ds_read_b128 a[148:151], v3 offset:15360\n\t"
  "v_mfma_f32_16x16x32_bf16 v[56:59], a[132:135], a[72:75], v[56:59]\n\t"
  "v_mfma_f32_16x16x32_bf16 v[52:55], a[132:135], a[68:71], v[52:55]\n\t"
  "buffer_load_dwordx4 v15, s[20:23], s57 offen offset:1024 sc1 lds\n\t"
  "v_mfma_f32_16x16x32_bf16 v[48:51], a[132:135], a[64:67], v[48:51]\n\t"
  "s_waitcnt lgkmcnt(3)\n\t"
  "v_mfma_f32_16x16x32_bf16 v[64:67], a[136:139], a[64:67], v[64:67]\n\t"
  "ds_read_b128 a[152:155], v3 offset:16384\n\t"
  "s_add_u32 s57, s57, 0x6000\n\t"
  "v_mfma_f32_16x16x32_bf16 v[68:71], a[136:139], a[68:71], v[68:71]\n\t"
  "s_add_u32 m0, s58, 0xc000\n\t"
  "v_mfma_f32_16x16x32_bf16 v[72:75], a[136:139], a[72:75], v[72:75]\n\t"
  "buffer_load_dwordx4 v14, s[20:23], s57 offen offset:0 sc1 lds\n\t"
  "v_mfma_f32_16x16x32_bf16 v[76:79], a[136:139], a[76:79], v[76:79]\n\t"
  "s_waitcnt lgkmcnt(3)\n\t"
  "v_mfma_f32_16x16x32_bf16 v[92:95], a[140:143], a[76:79], v[92:95]\n\t"
  "ds_read_b128 a[156:159], v3 offset:17408\n\t"
  "s_waitcnt vmcnt(21)\n\t"
  "v_mfma_f32_16x16x32_bf16 v[88:91], a[140:143], a[72:75], v[88:91]\n\t"
  "s_barrier\n\t"
  "v_mfma_f32_16x16x32_bf16 v[84:87], a[140:143], a[68:71], v[84:87]\n\t"
  "buffer_load_dwordx4 v15, s[20:23], s57 offen offset:1024 sc1 lds\n\t"
  "v_mfma_f32_16x16x32_bf16 v[80:83], a[140:143], a[64:67], v[80:83]\n\t"
  "s_waitcnt lgkmcnt(3) vmcnt(18)\n\t"
  "v_mfma_f32_16x16x32_bf16 v[32:35], a[144:147], a[80:83], v[32:35]\n\t"
  "ds_read_b128 a[128:131], v3 offset:28672\n\t"
  "s_add_u32 s57, s57, 0x8000\n\t"
  "v_mfma_f32_16x16x32_bf16 v[36:39], a[144:147], a[84:87], v[36:39]\n\t"
  "s_add_u32 m0, s58, 0xe000\n\t"
  "v_mfma_f32_16x16x32_bf16 v[40:43], a[144:147], a[88:91], v[40:43]\n\t"
  "buffer_load_dwordx4 v14, s[20:23], s57 offen offset:0 sc1 lds\n\t"
  "v_mfma_f32_16x16x32_bf16 v[44:47], a[144:147], a[92:95], v[44:47]\n\t"
  "s_waitcnt lgkmcnt(3)\n\t"
  "v_mfma_f32_16x16x32_bf16 v[60:63], a[148:151], a[92:95], v[60:63]\n\t"
  "ds_read_b128 a[132:135], v3 offset:29696\n\t"
  "v_mfma_f32_16x16x32_bf16 v[56:59], a[148:151], a[88:91], v[56:59]\n\t"
  "v_mfma_f32_16x16x32_bf16 v[52:55], a[148:151], a[84:87], v[52:55]\n\t"
  "buffer_load_dwordx4 v15, s[20:23], s57 offen offset:1024 sc1 lds\n\t"
  "v_mfma_f32_16x16x32_bf16 v[48:51], a[148:151], a[80:83], v[48:51]\n\t"
  "s_waitcnt lgkmcnt(3)\n\t"
  "v_mfma_f32_16x16x32_bf16 v[64:67], a[152:155], a[80:83], v[64:67]\n\t"
  "ds_read_b128 a[136:139], v3 offset:30720\n\t"
  "s_add_u32 s57, s57, 0x6000\n\t"
  "v_mfma_f32_16x16x32_bf16 v[68:71], a[152:155], a[84:87], v[68:71]\n\t"
  "s_add_u32 m0, s58, 0x0\n\t"
  "v_mfma_f32_16x16x32_bf16 v[72:75], a[152:155], a[88:91], v[72:75]\n\t"
  "buffer_load_dwordx4 v14, s[20:23], s57 offen offset:0 sc1 lds\n\t"
  "v_mfma_f32_16x16x32_bf16 v[76:79], a[152:155], a[92:95], v[76:79]\n\t"
  "s_waitcnt lgkmcnt(3)\n\t"
  "v_mfma_f32_16x16x32_bf16 v[92:95], a[156:159], a[92:95], v[92:95]\n\t"
  "ds_read_b128 a[140:143], v3 offset:31744\n\t"
  "v_mfma_f32_16x16x32_bf16 v[88:91], a[156:159], a[88:91], v[88:91]\n\t"
  "v_mfma_f32_16x16x32_bf16 v[84:87], a[156:159], a[84:87], v[84:87]\n\t"
  "buffer_load_dwordx4 v15, s[20:23], s57 offen offset:1024 sc1 lds\n\t"
  "v_mfma_f32_16x16x32_bf16 v[80:83], a[156:159], a[80:83], v[80:83]\n\t"
  "s_waitcnt lgkmcnt(3) vmcnt(18)\n\t"
  "v_mfma_f32_16x16x32_bf16 v[32:35], a[128:131], a[96:99], v[32:35]\n\t"
  "ds_read_b128 a[144:147], v3 offset:43008\n\t"
  "s_add_u32 s57, s57, 0x8000\n\t"
  "v_mfma_f32_16x16x32_bf16 v[36:39], a[128:131], a[100:103], v[36:39]\n\t"
  "s_add_u32 m0, s58, 0x2000\n\t"
  "v_mfma_f32_16x16x32_bf16 v[40:43], a[128:131], a[104:107], v[40:43]\n\t"
  "buffer_load_dwordx4 v14, s[20:23], s57 offen offset:0 sc1 lds\n\t"
  "v_mfma_f32_16x16x32_bf16 v[44:47], a[128:131], a[108:111], v[44:47]\n\t"
  "s_waitcnt lgkmcnt(3)\n\t"
  "v_mfma_f32_16x16x32_bf16 v[60:63], a[132:135], a[108:111], v[60:63]\n\t"
  "ds_read_b128 a[148:151], v3 offset:44032\n\t"
  "v_mfma_f32_16x16x32_bf16 v[56:59], a[132:135], a[104:107], v[56:59]\n\t"
  "v_mfma_f32_16x16x32_bf16 v[52:55], a[132:135], a[100:103], v[52:55]\n\t"
  "v_mfma_f32_16x16x32_bf16 v[48:51], a[132:135], a[96:99], v[48:51]\n\t"
  "s_waitcnt lgkmcnt(3)\n\t"
  "v_mfma_f32_16x16x32_bf16 v[64:67], a[136:139], a[96:99], v[64:67]\n\t"
  "ds_read_b128 a[152:155], v3 offset:45056\n\t"
  "v_mfma_f32_16x16x32_bf16 v[68:71], a[136:139], a[100:103], v[68:71]\n\t"
  "v_mfma_f32_16x16x32_bf16 v[72:75], a[136:139], a[104:107], v[72:75]\n\t"
  "buffer_load_dwordx4 v15, s[20:23], s57 offen offset:1024 sc1 lds\n\t"
  "v_mfma_f32_16x16x32_bf16 v[76:79], a[136:139], a[108:111], v[76:79]\n\t"
  "s_waitcnt lgkmcnt(3)\n\t"
  "v_mfma_f32_16x16x32_bf16 v[92:95], a[140:143], a[108:111], v[92:95]\n\t"
  "ds_read_b128 a[156:159], v3 offset:46080\n\t"
  "s_add_u32 s57, s57, 0x6000\n\t"
  "v_mfma_f32_16x16x32_bf16 v[88:91], a[140:143], a[104:107], v[88:91]\n\t"
  "v_mfma_f32_16x16x32_bf16 v[84:87], a[140:143], a[100:103], v[84:87]\n\t"
  "v_mfma_f32_16x16x32_bf16 v[80:83], a[140:143], a[96:99], v[80:83]\n\t"
  "s_waitcnt lgkmcnt(3) vmcnt(16)\n\t"
  "v_mfma_f32_16x16x32_bf16 v[32:35], a[144:147], a[112:115], v[32:35]\n\t"
  "ds_read_b128 a[128:131], v4 offset:4096\n\t"
  "v_mfma_f32_16x16x32_bf16 v[36:39], a[144:147], a[116:119], v[36:39]\n\t"
  "v_mfma_f32_16x16x32_bf16 v[40:43], a[144:147], a[120:123], v[40:43]\n\t"
  "v_mfma_f32_16x16x32_bf16 v[44:47], a[144:147], a[124:127], v[44:47]\n\t"
  "s_waitcnt lgkmcnt(3)\n\t"
  "v_mfma_f32_16x16x32_bf16 v[60:63], a[148:151], a[124:127], v[60:63]\n\t"
  "ds_read_b128 a[132:135], v4 offset:5120\n\t"
  "v_mfma_f32_16x16x32_bf16 v[56:59], a[148:151], a[120:123], v[56:59]\n\t"
  "v_mfma_f32_16x16x32_bf16 v[52:55], a[148:151], a[116:119], v[52:55]\n\t"
  "v_mfma_f32_16x16x32_bf16 v[48:51], a[148:151], a[112:115], v[48:51]\n\t"
  "s_waitcnt lgkmcnt(3)\n\t"
  "v_mfma_f32_16x16x32_bf16 v[64:67], a[152:155], a[112:115], v[64:67]\n\t"
  "ds_read_b128 a[136:139], v4 offset:18432\n\t"
  "v_mfma_f32_16x16x32_bf16 v[68:71], a[152:155], a[116:119], v[68:71]\n\t"
  "v_mfma_f32_16x16x32_bf16 v[72:75], a[152:155], a[120:123], v[72:75]\n\t"
  "v_mfma_f32_16x16x32_bf16 v[76:79], a[152:155], a[124:127], v[76:79]\n\t"
  "s_waitcnt lgkmcnt(3)\n\t"
  "v_mfma_f32_16x16x32_bf16 v[92:95], a[156:159], a[124:127], v[92:95]\n\t"
  "ds_read_b128 a[140:143], v4 offset:19456\n\t"
  "v_mfma_f32_16x16x32_bf16 v[88:91], a[156:159], a[120:123], v[88:91]\n\t"
  "v_max_f32 v32, 0, v32\n\t"
  "v_max_f32 v33, 0, v33\n\t"
  "v_mfma_f32_16x16x32_bf16 v[84:87], a[156:159], a[116:119], v[84:87]\n\t"
  "v_max_f32 v34, 0, v34\n\t"
  "v_max_f32 v35, 0, v35\n\t"
  "v_mfma_f32_16x16x32_bf16 v[80:83], a[156:159], a[112:115], v[80:83]\n\t"
  "s_waitcnt lgkmcnt(3)\n\t"
  "v_mul_f32 v32, v32, v48\n\t"
  "v_mul_f32 v33, v33, v49\n\t"
  "v_mfma_f32_16x16x32_bf16 v[96:99], a[128:131], a[32:35], v[96:99]\n\t"
  "ds_read_b128 a[144:147], v3 offset:4096\n\t"
  "v_mul_f32 v34, v34, v50\n\t"
  "v_mfma_f32_16x16x32_bf16 v[100:103], a[128:131], a[36:39], v[100:103]\n\t"
  "v_mul_f32 v35, v35, v51\n\t"
  "v_cvt_pk_bf16_f32 v48, v32, v33\n\t"
  "v_mfma_f32_16x16x32_bf16 v[104:107], a[128:131], a[40:43], v[104:107]\n\t"
  "v_cvt_pk_bf16_f32 v49, v34, v35\n\t"
  "v_max_f32 v36, 0, v36\n\t"
  "v_mfma_f32_16x16x32_bf16 v[108:111], a[128:131], a[44:47], v[108:111]\n\t"
  "s_waitcnt lgkmcnt(3)\n\t"
  "v_max_f32 v37, 0, v37\n\t"
  "v_max_f32 v38, 0, v38\n\t"
  "v_mfma_f32_16x16x32_bf16 v[124:127], a[132:135], a[44:47], v[124:127]\n\t"
  "ds_read_b128 a[148:151], v3 offset:5120\n\t"
  "v_max_f32 v39, 0, v39\n\t"
  "v_mfma_f32_16x16x32_bf16 v[120:123], a[132:135], a[40:43], v[120:123]\n\t"
  "v_mul_f32 v36, v36, v52\n\t"
  "v_mul_f32 v37, v37, v53\n\t"
  "v_mfma_f32_16x16x32_bf16 v[116:119], a[132:135], a[36:39], v[116:119]\n\t"
  "v_mul_f32 v38, v38, v54\n\t"
  "v_mul_f32 v39, v39, v55\n\t"
  "v_mfma_f32_16x16x32_bf16 v[112:115], a[132:135], a[32:35], v[112:115]\n\t"
  "s_waitcnt lgkmcnt(3)\n\t"
  "v_cvt_pk_bf16_f32 v52, v36, v37\n\t"
  "v_cvt_pk_bf16_f32 v53, v38, v39\n\t"
  "v_mfma_f32_16x16x32_bf16 v[96:99], a[136:139], a[48:51], v[96:99]\n\t"
  "ds_read_b128 a[152:155], v3 offset:18432\n\t"
  "v_max_f32 v40, 0, v40\n\t"
  "v_mfma_f32_16x16x32_bf16 v[100:103], a[136:139], a[52:55], v[100:103]\n\t"
  "v_max_f32 v41, 0, v41\n\t"
  "v_max_f32 v42, 0, v42\n\t"
  "v_mfma_f32_16x16x32_bf16 v[104:107], a[136:139], a[56:59], v[104:107]\n\t"
  "v_max_f32 v43, 0, v43\n\t"
  "v_mul_f32 v40, v40, v56\n\t"
  "v_mfma_f32_16x16x32_bf16 v[108:111], a[136:139], a[60:63], v[108:111]\n\t"
  "s_waitcnt lgkmcnt(3)\n\t"
  "v_mul_f32 v41, v41, v57\n\t"
  "v_mul_f32 v42, v42, v58\n\t"
  "v_mfma_f32_16x16x32_bf16 v[124:127], a[140:143], a[60:63], v[124:127]\n\t"
  "ds_read_b128 a[156:159], v3 offset:19456\n\t"
  "v_mul_f32 v43, v43, v59\n\t"
  "v_mfma_f32_16x16x32_bf16 v[120:123], a[140:143], a[56:59], v[120:123]\n\t"
  "v_cvt_pk_bf16_f32 v56, v40, v41\n\t"
  "v_cvt_pk_bf16_f32 v57, v42, v43\n\t"
  "v_mfma_f32_16x16x32_bf16 v[116:119], a[140:143], a[52:55], v[116:119]\n\t"
  "v_max_f32 v44, 0, v44\n\t"
  "v_max_f32 v45, 0, v45\n\t"
  "v_mfma_f32_16x16x32_bf16 v[112:115], a[140:143], a[48:51], v[112:115]\n\t"
  "s_waitcnt lgkmcnt(3)\n\t"
  "v_max_f32 v46, 0, v46\n\t"
  "v_max_f32 v47, 0, v47\n\t"
  "v_mfma_f32_16x16x32_bf16 v[96:99], a[144:147], a[64:67], v[96:99]\n\t"
  "ds_read_b128 a[128:131], v3 offset:32768\n\t"
  "v_mul_f32 v44, v44, v60\n\t"
  "v_mfma_f32_16x16x32_bf16 v[100:103], a[144:147], a[68:71], v[100:103]\n\t"
  "v_mul_f32 v45, v45, v61\n\t"
  "v_mul_f32 v46, v46, v62\n\t"
  "v_mfma_f32_16x16x32_bf16 v[104:107], a[144:147], a[72:75], v[104:107]\n\t"
  "v_mul_f32 v47, v47, v63\n\t"
  "v_cvt_pk_bf16_f32 v60, v44, v45\n\t"
  "v_mfma_f32_16x16x32_bf16 v[108:111], a[144:147], a[76:79], v[108:111]\n\t"
  "s_waitcnt lgkmcnt(3)\n\t"
  "v_cvt_pk_bf16_f32 v61, v46, v47\n\t"
  "v_max_f32 v64, 0, v64\n\t"
  "v_mfma_f32_16x16x32_bf16 v[124:127], a[148:151], a[76:79], v[124:127]\n\t"
  "ds_read_b128 a[132:135], v3 offset:33792\n\t"
  "v_max_f32 v65, 0, v65\n\t"
  "v_mfma_f32_16x16x32_bf16 v[120:123], a[148:151], a[72:75], v[120:123]\n\t"
  "v_max_f32 v66, 0, v66\n\t"
  "v_max_f32 v67, 0, v67\n\t"
  "v_mfma_f32_16x16x32_bf16 v[116:119], a[148:151], a[68:71], v[116:119]\n\t"
  "v_mul_f32 v64, v64, v80\n\t"
  "v_mul_f32 v65, v65, v81\n\t"
  "v_mfma_f32_16x16x32_bf16 v[112:115], a[148:151], a[64:67], v[112:115]\n\t"
  "s_waitcnt lgkmcnt(3)\n\t"
  "v_mul_f32 v66, v66, v82\n\t"
  "v_mul_f32 v67, v67, v83\n\t"
  "v_mfma_f32_16x16x32_bf16 v[96:99], a[152:155], a[80:83], v[96:99]\n\t"
  "ds_read_b128 a[136:139], v3 offset:47104\n\t"
  "v_cvt_pk_bf16_f32 v50, v64, v65\n\t"
  "v_mfma_f32_16x16x32_bf16 v[100:103], a[152:155], a[84:87], v[100:103]\n\t"
  "v_cvt_pk_bf16_f32 v51, v66, v67\n\t"
  "v_mfma_f32_16x16x32_bf16 v[104:107], a[152:155], a[88:91], v[104:107]\n\t"
  "buffer_store_dwordx4 v[48:51], v5, s[20:23], s44 offen offset:0 sc1\n\t"
  "v_mfma_f32_16x16x32_bf16 v[108:111], a[152:155], a[92:95], v[108:111]\n\t"
  "s_waitcnt lgkmcnt(3)\n\t"
  "v_max_f32 v68, 0, v68\n\t"
  "v_max_f32 v69, 0, v69\n\t"
  "v_mfma_f32_16x16x32_bf16 v[124:127], a[156:159], a[92:95], v[124:127]\n\t"
  "ds_read_b128 a[140:143], v3 offset:48128\n\t"
  "v_max_f32 v70, 0, v70\n\t"
  "v_mfma_f32_16x16x32_bf16 v[120:123], a[156:159], a[88:91], v[120:123]\n\t"
  "v_max_f32 v71, 0, v71\n\t"
  "v_mul_f32 v68, v68, v84\n\t"
  "v_mfma_f32_16x16x32_bf16 v[116:119], a[156:159], a[84:87], v[116:119]\n\t"
  "v_mul_f32 v69, v69, v85\n\t"
  "v_mul_f32 v70, v70, v86\n\t"
  "v_mfma_f32_16x16x32_bf16 v[112:115], a[156:159], a[80:83], v[112:115]\n\t"
  "s_waitcnt lgkmcnt(3)\n\t"
  "v_mul_f32 v71, v71, v87\n\t"
  "v_cvt_pk_bf16_f32 v54, v68, v69\n\t"
  "v_mfma_f32_16x16x32_bf16 v[96:99], a[128:131], a[96:99], v[96:99]\n\t"
  "ds_read_b128 a[144:147], v4 offset:6144\n\t"
  "v_cvt_pk_bf16_f32 v55, v70, v71\n\t"
  "v_mfma_f32_16x16x32_bf16 v[100:103], a[128:131], a[100:103], v[100:103]\n\t"
  "buffer_store_dwordx4 v[52:55], v5, s[20:23], s44 offen offset:1024 sc1\n\t"
  "v_mfma_f32_16x16x32_bf16 v[104:107], a[128:131], a[104:107], v[104:107]\n\t"
  "v_max_f32 v72, 0, v72\n\t"
  "v_max_f32 v73, 0, v73\n\t"
  "v_mfma_f32_16x16x32_bf16 v[108:111], a[128:131], a[108:111], v[108:111]\n\t"
  "s_waitcnt lgkmcnt(3)\n\t"
  "v_max_f32 v74, 0, v74\n\t"
  "v_max_f32 v75, 0, v75\n\t"
  "v_mfma_f32_16x16x32_bf16 v[124:127], a[132:135], a[108:111], v[124:127]\n\t"
  "ds_read_b128 a[148:151], v4 offset:7168\n\t"
  "v_mul_f32 v72, v72, v88\n\t"
  "v_mfma_f32_16x16x32_bf16 v[120:123], a[132:135], a[104:107], v[120:123]\n\t"
  "v_mul_f32 v73, v73, v89\n\t"
  "v_mul_f32 v74, v74, v90\n\t"
  "v_mfma_f32_16x16x32_bf16 v[116:119], a[132:135], a[100:103], v[116:119]\n\t"
  "v_mul_f32 v75, v75, v91\n\t"
  "v_cvt_pk_bf16_f32 v58, v72, v73\n\t"
  "v_mfma_f32_16x16x32_bf16 v[112:115], a[132:135], a[96:99], v[112:115]\n\t"
  "s_waitcnt lgkmcnt(3)\n\t"
  "v_cvt_pk_bf16_f32 v59, v74, v75\n\t"
  "v_mfma_f32_16x16x32_bf16 v[96:99], a[136:139], a[112:115], v[96:99]\n\t"
  "ds_read_b128 a[152:155], v4 offset:20480\n\t"
  "v_mfma_f32_16x16x32_bf16 v[100:103], a[136:139], a[116:119], v[100:103]\n\t"
  "buffer_store_dwordx4 v[56:59], v5, s[20:23], s44 offen offset:2048 sc1\n\t"
  "v_mfma_f32_16x16x32_bf16 v[104:107], a[136:139], a[120:123], v[104:107]\n\t"
  "v_max_f32 v76, 0, v76\n\t"
  "v_max_f32 v77, 0, v77\n\t"
  "v_mfma_f32_16x16x32_bf16 v[108:111], a[136:139], a[124:127], v[108:111]\n\t"
  "s_waitcnt lgkmcnt(3)\n\t"
  "v_max_f32 v78, 0, v78\n\t"
  "v_max_f32 v79, 0, v79\n\t"
  "v_mfma_f32_16x16x32_bf16 v[124:127], a[140:143], a[124:127], v[124:127]\n\t"
  "ds_read_b128 a[156:159], v4 offset:21504\n\t"
  "v_mul_f32 v76, v76, v92\n\t"
  "v_mfma_f32_16x16x32_bf16 v[120:123], a[140:143], a[120:123], v[120:123]\n\t"
  "v_mul_f32 v77, v77, v93\n\t"
  "v_mul_f32 v78, v78, v94\n\t"
  "v_mfma_f32_16x16x32_bf16 v[116:119], a[140:143], a[116:119], v[116:119]\n\t"
  "v_mul_f32 v79, v79, v95\n\t"
  "v_cvt_pk_bf16_f32 v62, v76, v77\n\t"
  "v_mfma_f32_16x16x32_bf16 v[112:115], a[140:143], a[112:115], v[112:115]\n\t"
  "s_waitcnt lgkmcnt(3)\n\t"
  "v_cvt_pk_bf16_f32 v63, v78, v79\n\t"
  "v_mfma_f32_16x16x32_bf16 v[128:131], a[144:147], a[32:35], v[128:131]\n\t"
  "ds_read_b128 a[128:131], v3 offset:6144\n\t"
  "v_mfma_f32_16x16x32_bf16 v[132:135], a[144:147], a[36:39], v[132:135]\n\t"
  "buffer_store_dwordx4 v[60:63], v5, s[20:23], s44 offen offset:3072 sc1\n\t"
  "v_mfma_f32_16x16x32_bf16 v[136:139], a[144:147], a[40:43], v[136:139]\n\t"
  "s_add_u32 s44, s44, 131072\n\t"
  "v_mfma_f32_16x16x32_bf16 v[140:143], a[144:147], a[44:47], v[140:143]\n\t"
  "s_waitcnt lgkmcnt(3)\n\t"
  "v_mfma_f32_16x16x32_bf16 v[156:159], a[148:151], a[44:47], v[156:159]\n\t"
  "ds_read_b128 a[132:135], v3 offset:7168\n\t"
  "v_mfma_f32_16x16x32_bf16 v[152:155], a[148:151], a[40:43], v[152:155]\n\t"
  "v_max_f32 v96, 0, v96\n\t"
  "v_max_f32 v97, 0, v97\n\t"
  "v_mfma_f32_16x16x32_bf16 v[148:151], a[148:151], a[36:39], v[148:151]\n\t"
  "v_max_f32 v98, 0, v98\n\t"
  "v_max_f32 v99, 0, v99\n\t"
  "v_mfma_f32_16x16x32_bf16 v[144:147], a[148:151], a[32:35], v[144:147]\n\t"
  "s_waitcnt lgkmcnt(3)\n\t"
  "v_mul_f32 v96, v96, v112\n\t"
  "v_mul_f32 v97, v97, v113\n\t"
  "v_mfma_f32_16x16x32_bf16 v[128:131], a[152:155], a[48:51], v[128:131]\n\t"
  "ds_read_b128 a[136:139], v3 offset:20480\n\t"
  "v_mul_f32 v98, v98, v114\n\t"
  "v_mfma_f32_16x16x32_bf16 v[132:135], a[152:155], a[52:55], v[132:135]\n\t"
  "v_mul_f32 v99, v99, v115\n\t"
  "v_cvt_pk_bf16_f32 v112, v96, v97\n\t"
  "v_mfma_f32_16x16x32_bf16 v[136:139], a[152:155], a[56:59], v[136:139]\n\t"
  "v_cvt_pk_bf16_f32 v113, v98, v99\n\t"
  "v_max_f32 v100, 0, v100\n\t"
  "v_mfma_f32_16x16x32_bf16 v[140:143], a[152:155], a[60:63], v[140:143]\n\t"
  "s_waitcnt lgkmcnt(3)\n\t"
  "v_max_f32 v101, 0, v101\n\t"
  "v_max_f32 v102, 0, v102\n\t"
  "v_mfma_f32_16x16x32_bf16 v[156:159], a[156:159], a[60:63], v[156:159]\n\t"
  "ds_read_b128 a[140:143], v3 offset:21504\n\t"
  "v_max_f32 v103, 0, v103\n\t"
  "v_mfma_f32_16x16x32_bf16 v[152:155], a[156:159], a[56:59], v[152:155]\n\t"
  "v_mul_f32 v100, v100, v116\n\t"
  "v_mul_f32 v101, v101, v117\n\t"
  "v_mfma_f32_16x16x32_bf16 v[148:151], a[156:159], a[52:55], v[148:151]\n\t"
  "v_mul_f32 v102, v102, v118\n\t"
  "v_mul_f32 v103, v103, v119\n\t"
  "v_mfma_f32_16x16x32_bf16 v[144:147], a[156:159], a[48:51], v[144:147]\n\t"
  "s_waitcnt lgkmcnt(3)\n\t"
  "v_cvt_pk_bf16_f32 v116, v100, v101\n\t"
  "v_cvt_pk_bf16_f32 v117, v102, v103\n\t"
  "v_mfma_f32_16x16x32_bf16 v[128:131], a[128:131], a[64:67], v[128:131]\n\t"
  "ds_read_b128 a[144:147], v3 offset:34816\n\t"
  "v_max_f32 v104, 0, v104\n\t"
  "v_mfma_f32_16x16x32_bf16 v[132:135], a[128:131], a[68:71], v[132:135]\n\t"
  "v_max_f32 v105, 0, v105\n\t"
  "v_max_f32 v106, 0, v106\n\t"
  "v_mfma_f32_16x16x32_bf16 v[136:139], a[128:131], a[72:75], v[136:139]\n\t"
  "v_max_f32 v107, 0, v107\n\t"
  "v_mul_f32 v104, v104, v120\n\t"
  "v_mfma_f32_16x16x32_bf16 v[140:143], a[128:131], a[76:79], v[140:143]\n\t"
  "s_waitcnt lgkmcnt(3)\n\t"
  "v_mul_f32 v105, v105, v121\n\t"
  "v_mul_f32 v106, v106, v122\n\t"
  "v_mfma_f32_16x16x32_bf16 v[156:159], a[132:135], a[76:79], v[156:159]\n\t"
  "ds_read_b128 a[148:151], v3 offset:35840\n\t"
  "v_mul_f32 v107, v107, v123\n\t"
  "v_mfma_f32_16x16x32_bf16 v[152:155], a[132:135], a[72:75], v[152:155]\n\t"
  "v_cvt_pk_bf16_f32 v120, v104, v105\n\t"
  "v_cvt_pk_bf16_f32 v121, v106, v107\n\t"
  "v_mfma_f32_16x16x32_bf16 v[148:151], a[132:135], a[68:71], v[148:151]\n\t"
  "v_max_f32 v108, 0, v108\n\t"
  "v_max_f32 v109, 0, v109\n\t"
  "v_mfma_f32_16x16x32_bf16 v[144:147], a[132:135], a[64:67], v[144:147]\n\t"
  "s_waitcnt lgkmcnt(3)\n\t"
  "v_max_f32 v110, 0, v110\n\t"
  "v_max_f32 v111, 0, v111\n\t"
  "v_mfma_f32_16x16x32_bf16 v[128:131], a[136:139], a[80:83], v[128:131]\n\t"
  "ds_read_b128 a[152:155], v3 offset:49152\n\t"
  "v_mul_f32 v108, v108, v124\n\t"
  "v_mfma_f32_16x16x32_bf16 v[132:135], a[136:139], a[84:87], v[132:135]\n\t"
  "v_mul_f32 v109, v109, v125\n\t"
  "v_mul_f32 v110, v110, v126\n\t"
  "v_mfma_f32_16x16x32_bf16 v[136:139], a[136:139], a[88:91], v[136:139]\n\t"
  "v_mul_f32 v111, v111, v127\n\t"
  "v_cvt_pk_bf16_f32 v124, v108, v109\n\t"
  "v_mfma_f32_16x16x32_bf16 v[140:143], a[136:139], a[92:95], v[140:143]\n\t"
  "s_waitcnt lgkmcnt(3)\n\t"
  "v_cvt_pk_bf16_f32 v125, v110, v111\n\t"
  "v_mfma_f32_16x16x32_bf16 v[156:159], a[140:143], a[92:95], v[156:159]\n\t"
  "ds_read_b128 a[156:159], v3 offset:50176\n\t"
  "v_mfma_f32_16x16x32_bf16 v[152:155], a[140:143], a[88:91], v[152:155]\n\t"
  "v_mfma_f32_16x16x32_bf16 v[148:151], a[140:143], a[84:87], v[148:151]\n\t"
  "v_mfma_f32_16x16x32_bf16 v[144:147], a[140:143], a[80:83], v[144:147]\n\t"
  "s_waitcnt lgkmcnt(3)\n\t"
  "v_mfma_f32_16x16x32_bf16 v[128:131], a[144:147], a[96:99], v[128:131]\n\t"
  "ds_read_b128 a[128:131], v4 offset:8192\n\t"
  "v_mfma_f32_16x16x32_bf16 v[132:135], a[144:147], a[100:103], v[132:135]\n\t"
  "v_mfma_f32_16x16x32_bf16 v[136:139], a[144:147], a[104:107], v[136:139]\n\t"
  "v_mfma_f32_16x16x32_bf16 v[140:143], a[144:147], a[108:111], v[140:143]\n\t"
  "s_waitcnt lgkmcnt(3)\n\t"
  "v_mfma_f32_16x16x32_bf16 v[156:159], a[148:151], a[108:111], v[156:159]\n\t"
  "ds_read_b128 a[132:135], v4 offset:9216\n\t"
  "v_mfma_f32_16x16x32_bf16 v[152:155], a[148:151], a[104:107], v[152:155]\n\t"
  "v_mfma_f32_16x16x32_bf16 v[148:151], a[148:151], a[100:103], v[148:151]\n\t"
  "v_mfma_f32_16x16x32_bf16 v[144:147], a[148:151], a[96:99], v[144:147]\n\t"
  "s_waitcnt lgkmcnt(3)\n\t"
  "v_mfma_f32_16x16x32_bf16 v[128:131], a[152:155], a[112:115], v[128:131]\n\t"
  "ds_read_b128 a[136:139], v4 offset:22528\n\t"
  "v_mfma_f32_16x16x32_bf16 v[132:135], a[152:155], a[116:119], v[132:135]\n\t"
  "v_mfma_f32_16x16x32_bf16 v[136:139], a[152:155], a[120:123], v[136:139]\n\t"
  "v_mfma_f32_16x16x32_bf16 v[140:143], a[152:155], a[124:127], v[140:143]\n\t"
  "s_waitcnt lgkmcnt(3)\n\t"
  "v_mfma_f32_16x16x32_bf16 v[156:159], a[156:159], a[124:127], v[156:159]\n\t"
  "ds_read_b128 a[140:143], v4 offset:23552\n\t"
  "v_mfma_f32_16x16x32_bf16 v[152:155], a[156:159], a[120:123], v[152:155]\n\t"
  "v_mfma_f32_16x16x32_bf16 v[148:151], a[156:159], a[116:119], v[148:151]\n\t"
  "v_mfma_f32_16x16x32_bf16 v[144:147], a[156:159], a[112:115], v[144:147]\n\t"
  "s_waitcnt lgkmcnt(3)\n\t"
  "v_mfma_f32_16x16x32_bf16 v[160:163], a[128:131], a[32:35], v[160:163]\n\t"
  "ds_read_b128 a[144:147], v3 offset:8192\n\t"
  "v_mfma_f32_16x16x32_bf16 v[164:167], a[128:131], a[36:39], v[164:167]\n\t"
  "v_mfma_f32_16x16x32_bf16 v[168:171], a[128:131], a[40:43], v[168:171]\n\t"
  "v_mfma_f32_16x16x32_bf16 v[172:175], a[128:131], a[44:47], v[172:175]\n\t"
  "s_waitcnt lgkmcnt(3)\n\t"
  "v_mfma_f32_16x16x32_bf16 v[188:191], a[132:135], a[44:47], v[188:191]\n\t"
  "ds_read_b128 a[148:151], v3 offset:9216\n\t"
  "v_mfma_f32_16x16x32_bf16 v[184:187], a[132:135], a[40:43], v[184:187]\n\t"
  "v_max_f32 v128, 0, v128\n\t"
  "v_max_f32 v129, 0, v129\n\t"
  "v_mfma_f32_16x16x32_bf16 v[180:183], a[132:135], a[36:39], v[180:183]\n\t"
  "v_max_f32 v130, 0, v130\n\t"
  "v_max_f32 v131, 0, v131\n\t"
  "v_mfma_f32_16x16x32_bf16 v[176:179], a[132:135], a[32:35], v[176:179]\n\t"
  "s_waitcnt lgkmcnt(3)\n\t"
  "v_mul_f32 v128, v128, v144\n\t"
  "v_mul_f32 v129, v129, v145\n\t"
  "v_mfma_f32_16x16x32_bf16 v[160:163], a[136:139], a[48:51], v[160:163]\n\t"
  "ds_read_b128 a[152:155], v3 offset:22528\n\t"
  "v_mul_f32 v130, v130, v146\n\t"
  "v_mfma_f32_16x16x32_bf16 v[164:167], a[136:139], a[52:55], v[164:167]\n\t"
  "v_mul_f32 v131, v131, v147\n\t"
  "v_cvt_pk_bf16_f32 v114, v128, v129\n\t"
  "s_waitcnt vmcnt(0)\n\t"
  "buffer_store_dwordx2 v[16:17], v10, s[20:23], 0 offen offset:0 sc1\n\t"
  "v_mfma_f32_16x16x32_bf16 v[168:171], a[136:139], a[56:59], v[168:171]\n\t"
  "v_cvt_pk_bf16_f32 v115, v130, v131\n\t"
  "v_mfma_f32_16x16x32_bf16 v[172:175], a[136:139], a[60:63], v[172:175]\n\t"
  "s_waitcnt lgkmcnt(3)\n\t"
  "v_mfma_f32_16x16x32_bf16 v[188:191], a[140:143], a[60:63], v[188:191]\n\t"
  "ds_read_b128 a[156:159], v3 offset:23552\n\t"
  "v_mfma_f32_16x16x32_bf16 v[184:187], a[140:143], a[56:59], v[184:187]\n\t"
  "buffer_store_dwordx4 v[112:115], v5, s[20:23], s44 offen offset:0 sc1\n\t"
  "v_mfma_f32_16x16x32_bf16 v[180:183], a[140:143], a[52:55], v[180:183]\n\t"
  "v_max_f32 v132, 0, v132\n\t"
  "v_max_f32 v133, 0, v133\n\t"
  "v_mfma_f32_16x16x32_bf16 v[176:179], a[140:143], a[48:51], v[176:179]\n\t"
  "s_waitcnt lgkmcnt(3)\n\t"
  "v_max_f32 v134, 0, v134\n\t"
  "v_max_f32 v135, 0, v135\n\t"
  "v_mfma_f32_16x16x32_bf16 v[160:163], a[144:147], a[64:67], v[160:163]\n\t"
  "ds_read_b128 a[128:131], v3 offset:36864\n\t"
  "v_mul_f32 v132, v132, v148\n\t"
  "v_mfma_f32_16x16x32_bf16 v[164:167], a[144:147], a[68:71], v[164:167]\n\t"
  "v_mul_f32 v133, v133, v149\n\t"
  "v_mul_f32 v134, v134, v150\n\t"
  "v_mfma_f32_16x16x32_bf16 v[168:171], a[144:147], a[72:75], v[168:171]\n\t"
  "v_mul_f32 v135, v135, v151\n\t"
  "v_cvt_pk_bf16_f32 v118, v132, v133\n\t"
  "v_mfma_f32_16x16x32_bf16 v[172:175], a[144:147], a[76:79], v[172:175]\n\t"
  "s_waitcnt lgkmcnt(3)\n\t"
  "v_cvt_pk_bf16_f32 v119, v134, v135\n\t"
  "v_mfma_f32_16x16x32_bf16 v[188:191], a[148:151], a[76:79], v[188:191]\n\t"
  "ds_read_b128 a[132:135], v3 offset:37888\n\t"
  "v_mfma_f32_16x16x32_bf16 v[184:187], a[148:151], a[72:75], v[184:187]\n\t"
  "buffer_store_dwordx4 v[116:119], v5, s[20:23], s44 offen offset:1024 sc1\n\t"
  "v_mfma_f32_16x16x32_bf16 v[180:183], a[148:151], a[68:71], v[180:183]\n\t"
  "v_max_f32 v136, 0, v136\n\t"
  "v_max_f32 v137, 0, v137\n\t"
  "v_mfma_f32_16x16x32_bf16 v[176:179], a[148:151], a[64:67], v[176:179]\n\t"
  "s_waitcnt lgkmcnt(3)\n\t"
  "v_max_f32 v138, 0, v138\n\t"
  "v_max_f32 v139, 0, v139\n\t"
  "v_mfma_f32_16x16x32_bf16 v[160:163], a[152:155], a[80:83], v[160:163]\n\t"
  "ds_read_b128 a[136:139], v3 offset:51200\n\t"
  "v_mul_f32 v136, v136, v152\n\t"
  "v_mfma_f32_16x16x32_bf16 v[164:167], a[152:155], a[84:87], v[164:167]\n\t"
  "v_mul_f32 v137, v137, v153\n\t"
  "v_mul_f32 v138, v138, v154\n\t"
  "v_mfma_f32_16x16x32_bf16 v[168:171], a[152:155], a[88:91], v[168:171]\n\t"
  "v_mul_f32 v139, v139, v155\n\t"
  "v_cvt_pk_bf16_f32 v122, v136, v137\n\t"
  "v_mfma_f32_16x16x32_bf16 v[172:175], a[152:155], a[92:95], v[172:175]\n\t"
  "s_waitcnt lgkmcnt(3)\n\t"
  "v_cvt_pk_bf16_f32 v123, v138, v139\n\t"
  "v_mfma_f32_16x16x32_bf16 v[188:191], a[156:159], a[92:95], v[188:191]\n\t"
  "ds_read_b128 a[140:143], v3 offset:52224\n\t"
  "v_mfma_f32_16x16x32_bf16 v[184:187], a[156:159], a[88:91], v[184:187]\n\t"
  "buffer_store_dwordx4 v[120:123], v5, s[20:23], s44 offen offset:2048 sc1\n\t"
  "v_mfma_f32_16x16x32_bf16 v[180:183], a[156:159], a[84:87], v[180:183]\n\t"
  "v_max_f32 v140, 0, v140\n\t"
  "v_max_f32 v141, 0, v141\n\t"
  "v_mfma_f32_16x16x32_bf16 v[176:179], a[156:159], a[80:83], v[176:179]\n\t"
  "s_waitcnt lgkmcnt(3)\n\t"
  "v_max_f32 v142, 0, v142\n\t"
  "v_max_f32 v143, 0, v143\n\t"
  "v_mfma_f32_16x16x32_bf16 v[160:163], a[128:131], a[96:99], v[160:163]\n\t"
  "ds_read_b128 a[144:147], v4 offset:10240\n\t"
  "v_mul_f32 v140, v140, v156\n\t"
  "v_mfma_f32_16x16x32_bf16 v[164:167], a[128:131], a[100:103], v[164:167]\n\t"
  "v_mul_f32 v141, v141, v157\n\t"
  "v_mul_f32 v142, v142, v158\n\t"
  "v_mfma_f32_16x16x32_bf16 v[168:171], a[128:131], a[104:107], v[168:171]\n\t"
  "v_mul_f32 v143, v143, v159\n\t"
  "v_cvt_pk_bf16_f32 v126, v140, v141\n\t"
  "v_mfma_f32_16x16x32_bf16 v[172:175], a[128:131], a[108:111], v[172:175]\n\t"
  "s_waitcnt lgkmcnt(3)\n\t"
  "v_cvt_pk_bf16_f32 v127, v142, v143\n\t"
  "v_mfma_f32_16x16x32_bf16 v[188:191], a[132:135], a[108:111], v[188:191]\n\t"
  "ds_read_b128 a[148:151], v4 offset:11264\n\t"
  "v_mfma_f32_16x16x32_bf16 v[184:187], a[132:135], a[104:107], v[184:187]\n\t"
  "buffer_store_dwordx4 v[124:127], v5, s[20:23], s44 offen offset:3072 sc1\n\t"
  "v_mfma_f32_16x16x32_bf16 v[180:183], a[132:135], a[100:103], v[180:183]\n\t"
  "s_add_u32 s44, s44, 131072\n\t"
  "v_mfma_f32_16x16x32_bf16 v[176:179], a[132:135], a[96:99], v[176:179]\n\t"
  "s_waitcnt lgkmcnt(3)\n\t"
  "v_mfma_f32_16x16x32_bf16 v[160:163], a[136:139], a[112:115], v[160:163]\n\t"
  "ds_read_b128 a[152:155], v4 offset:24576\n\t"
  "v_mfma_f32_16x16x32_bf16 v[164:167], a[136:139], a[116:119], v[164:167]\n\t"
  "v_mfma_f32_16x16x32_bf16 v[168:171], a[136:139], a[120:123], v[168:171]\n\t"
  "v_mfma_f32_16x16x32_bf16 v[172:175], a[136:139], a[124:127], v[172:175]\n\t"
  "s_waitcnt lgkmcnt(3)\n\t"
  "v_mfma_f32_16x16x32_bf16 v[188:191], a[140:143], a[124:127], v[188:191]\n\t"
  "ds_read_b128 a[156:159], v4 offset:25600\n\t"
  "v_mfma_f32_16x16x32_bf16 v[184:187], a[140:143], a[120:123], v[184:187]\n\t"
  "v_mfma_f32_16x16x32_bf16 v[180:183], a[140:143], a[116:119], v[180:183]\n\t"
  "v_mfma_f32_16x16x32_bf16 v[176:179], a[140:143], a[112:115], v[176:179]\n\t"
  "s_waitcnt lgkmcnt(3)\n\t"
  "v_mfma_f32_16x16x32_bf16 v[192:195], a[144:147], a[32:35], v[192:195]\n\t"
  "ds_read_b128 a[128:131], v3 offset:10240\n\t"
  "v_mfma_f32_16x16x32_bf16 v[196:199], a[144:147], a[36:39], v[196:199]\n\t"
  "v_mfma_f32_16x16x32_bf16 v[200:203], a[144:147], a[40:43], v[200:203]\n\t"
  "v_mfma_f32_16x16x32_bf16 v[204:207], a[144:147], a[44:47], v[204:207]\n\t"
  "s_waitcnt lgkmcnt(3)\n\t"
  "v_mfma_f32_16x16x32_bf16 v[220:223], a[148:151], a[44:47], v[220:223]\n\t"
  "ds_read_b128 a[132:135], v3 offset:11264\n\t"
  "v_mfma_f32_16x16x32_bf16 v[216:219], a[148:151], a[40:43], v[216:219]\n\t"
  "v_max_f32 v160, 0, v160\n\t"
  "v_max_f32 v161, 0, v161\n\t"
  "v_mfma_f32_16x16x32_bf16 v[212:215], a[148:151], a[36:39], v[212:215]\n\t"
  "v_max_f32 v162, 0, v162\n\t"
  "v_max_f32 v163, 0, v163\n\t"
  "v_mfma_f32_16x16x32_bf16 v[208:211], a[148:151], a[32:35], v[208:211]\n\t"
  "s_waitcnt lgkmcnt(3)\n\t"
  "v_mul_f32 v160, v160, v176\n\t"
  "v_mul_f32 v161, v161, v177\n\t"
  "v_mfma_f32_16x16x32_bf16 v[192:195], a[152:155], a[48:51], v[192:195]\n\t"
  "ds_read_b128 a[136:139], v3 offset:24576\n\t"
  "v_mul_f32 v162, v162, v178\n\t"
  "v_mfma_f32_16x16x32_bf16 v[196:199], a[152:155], a[52:55], v[196:199]\n\t"
  "v_mul_f32 v163, v163, v179\n\t"
  "v_cvt_pk_bf16_f32 v176, v160, v161\n\t"
  "v_mfma_f32_16x16x32_bf16 v[200:203], a[152:155], a[56:59], v[200:203]\n\t"
  "v_cvt_pk_bf16_f32 v177, v162, v163\n\t"
  "v_max_f32 v164, 0, v164\n\t"
  "v_mfma_f32_16x16x32_bf16 v[204:207], a[152:155], a[60:63], v[204:207]\n\t"
  "s_waitcnt lgkmcnt(3)\n\t"
  "v_max_f32 v165, 0, v165\n\t"
  "v_max_f32 v166, 0, v166\n\t"
  "v_mfma_f32_16x16x32_bf16 v[220:223], a[156:159], a[60:63], v[220:223]\n\t"
  "ds_read_b128 a[140:143], v3 offset:25600\n\t"
  "v_max_f32 v167, 0, v167\n\t"
  "v_mfma_f32_16x16x32_bf16 v[216:219], a[156:159], a[56:59], v[216:219]\n\t"
  "v_mul_f32 v164, v164, v180\n\t"
  "v_mul_f32 v165, v165, v181\n\t"
  "v_mfma_f32_16x16x32_bf16 v[212:215], a[156:159], a[52:55], v[212:215]\n\t"
  "v_mul_f32 v166, v166, v182\n\t"
  "v_mul_f32 v167, v167, v183\n\t"
  "v_mfma_f32_16x16x32_bf16 v[208:211], a[156:159], a[48:51], v[208:211]\n\t"
  "s_waitcnt lgkmcnt(3)\n\t"
  "v_cvt_pk_bf16_f32 v180, v164, v165\n\t"
  "v_cvt_pk_bf16_f32 v181, v166, v167\n\t"
  "v_mfma_f32_16x16x32_bf16 v[192:195], a[128:131], a[64:67], v[192:195]\n\t"
  "ds_read_b128 a[144:147], v3 offset:38912\n\t"
  "v_max_f32 v168, 0, v168\n\t"
  "v_mfma_f32_16x16x32_bf16 v[196:199], a[128:131], a[68:71], v[196:199]\n\t"
  "v_max_f32 v169, 0, v169\n\t"
  "v_max_f32 v170, 0, v170\n\t"
  "v_mfma_f32_16x16x32_bf16 v[200:203], a[128:131], a[72:75], v[200:203]\n\t"
  "v_max_f32 v171, 0, v171\n\t"
  "v_mul_f32 v168, v168, v184\n\t"
  "v_mfma_f32_16x16x32_bf16 v[204:207], a[128:131], a[76:79], v[204:207]\n\t"
  "s_waitcnt lgkmcnt(3)\n\t"
  "v_mul_f32 v169, v169, v185\n\t"
  "v_mul_f32 v170, v170, v186\n\t"
  "v_mfma_f32_16x16x32_bf16 v[220:223], a[132:135], a[76:79], v[220:223]\n\t"
  "ds_read_b128 a[148:151], v3 offset:39936\n\t"
  "v_mul_f32 v171, v171, v187\n\t"
  "v_mfma_f32_16x16x32_bf16 v[216:219], a[132:135], a[72:75], v[216:219]\n\t"
  "v_cvt_pk_bf16_f32 v184, v168, v169\n\t"
  "v_cvt_pk_bf16_f32 v185, v170, v171\n\t"
  "v_mfma_f32_16x16x32_bf16 v[212:215], a[132:135], a[68:71], v[212:215]\n\t"
  "v_max_f32 v172, 0, v172\n\t"
  "v_max_f32 v173, 0, v173\n\t"
  "v_mfma_f32_16x16x32_bf16 v[208:211], a[132:135], a[64:67], v[208:211]\n\t"
  "s_waitcnt lgkmcnt(3)\n\t"
  "v_max_f32 v174, 0, v174\n\t"
  "v_max_f32 v175, 0, v175\n\t"
  "v_mfma_f32_16x16x32_bf16 v[192:195], a[136:139], a[80:83], v[192:195]\n\t"
  "ds_read_b128 a[152:155], v3 offset:53248\n\t"
  "v_mul_f32 v172, v172, v188\n\t"
  "v_mfma_f32_16x16x32_bf16 v[196:199], a[136:139], a[84:87], v[196:199]\n\t"
  "v_mul_f32 v173, v173, v189\n\t"
  "v_mul_f32 v174, v174, v190\n\t"
  "v_mfma_f32_16x16x32_bf16 v[200:203], a[136:139], a[88:91], v[200:203]\n\t"
  "v_mul_f32 v175, v175, v191\n\t"
  "v_cvt_pk_bf16_f32 v188, v172, v173\n\t"
  "v_mfma_f32_16x16x32_bf16 v[204:207], a[136:139], a[92:95], v[204:207]\n\t"
  "s_waitcnt lgkmcnt(3)\n\t"
  "v_cvt_pk_bf16_f32 v189, v174, v175\n\t"
  "v_mfma_f32_16x16x32_bf16 v[220:223], a[140:143], a[92:95], v[220:223]\n\t"
  "ds_read_b128 a[156:159], v3 offset:54272\n\t"
  "v_mfma_f32_16x16x32_bf16 v[216:219], a[140:143], a[88:91], v[216:219]\n\t"
  "v_mfma_f32_16x16x32_bf16 v[212:215], a[140:143], a[84:87], v[212:215]\n\t"
  "v_mfma_f32_16x16x32_bf16 v[208:211], a[140:143], a[80:83], v[208:211]\n\t"
  "s_waitcnt lgkmcnt(3)\n\t"
  "v_mfma_f32_16x16x32_bf16 v[192:195], a[144:147], a[96:99], v[192:195]\n\t"
  "ds_read_b128 a[128:131], v4 offset:12288\n\t"
  "v_mfma_f32_16x16x32_bf16 v[196:199], a[144:147], a[100:103], v[196:199]\n\t"
  "v_mfma_f32_16x16x32_bf16 v[200:203], a[144:147], a[104:107], v[200:203]\n\t"
  "v_mfma_f32_16x16x32_bf16 v[204:207], a[144:147], a[108:111], v[204:207]\n\t"
  "s_waitcnt lgkmcnt(3)\n\t"
  "v_mfma_f32_16x16x32_bf16 v[220:223], a[148:151], a[108:111], v[220:223]\n\t"
  "ds_read_b128 a[132:135], v4 offset:13312\n\t"
  "v_mfma_f32_16x16x32_bf16 v[216:219], a[148:151], a[104:107], v[216:219]\n\t"
  "v_mfma_f32_16x16x32_bf16 v[212:215], a[148:151], a[100:103], v[212:215]\n\t"
  "v_mfma_f32_16x16x32_bf16 v[208:211], a[148:151], a[96:99], v[208:211]\n\t"
  "s_waitcnt lgkmcnt(3)\n\t"
  "v_mfma_f32_16x16x32_bf16 v[192:195], a[152:155], a[112:115], v[192:195]\n\t"
  "ds_read_b128 a[136:139], v4 offset:26624\n\t"
  "v_mfma_f32_16x16x32_bf16 v[196:199], a[152:155], a[116:119], v[196:199]\n\t"
  "v_mfma_f32_16x16x32_bf16 v[200:203], a[152:155], a[120:123], v[200:203]\n\t"
  "v_mfma_f32_16x16x32_bf16 v[204:207], a[152:155], a[124:127], v[204:207]\n\t"
  "s_waitcnt lgkmcnt(3)\n\t"
  "v_mfma_f32_16x16x32_bf16 v[220:223], a[156:159], a[124:127], v[220:223]\n\t"
  "ds_read_b128 a[140:143], v4 offset:27648\n\t"
  "v_mfma_f32_16x16x32_bf16 v[216:219], a[156:159], a[120:123], v[216:219]\n\t"
  "v_mfma_f32_16x16x32_bf16 v[212:215], a[156:159], a[116:119], v[212:215]\n\t"
  "v_mfma_f32_16x16x32_bf16 v[208:211], a[156:159], a[112:115], v[208:211]\n\t"
  "s_waitcnt lgkmcnt(3)\n\t"
  "v_mfma_f32_16x16x32_bf16 v[224:227], a[128:131], a[32:35], v[224:227]\n\t"
  "ds_read_b128 a[144:147], v3 offset:12288\n\t"
  "v_mfma_f32_16x16x32_bf16 v[228:231], a[128:131], a[36:39], v[228:231]\n\t"
  "v_mfma_f32_16x16x32_bf16 v[232:235], a[128:131], a[40:43], v[232:235]\n\t"
  "s_waitcnt lgkmcnt(3)\n\t"
  "v_not_b32 v24, s34\n\t"
  "v_mfma_f32_16x16x32_bf16 v[236:239], a[128:131], a[44:47], v[236:239]\n\t"
  "buffer_load_dwordx2 v[24:25], v11, s[20:23], 0 offen offset:0 sc1\n\t"
  "v_mfma_f32_16x16x32_bf16 v[252:255], a[132:135], a[44:47], v[252:255]\n\t"
  "ds_read_b128 a[148:151], v3 offset:13312\n\t"
  "v_mfma_f32_16x16x32_bf16 v[248:251], a[132:135], a[40:43], v[248:251]\n\t"
  "v_max_f32 v192, 0, v192\n\t"
  "v_max_f32 v193, 0, v193\n\t"
  "v_mfma_f32_16x16x32_bf16 v[244:247], a[132:135], a[36:39], v[244:247]\n\t"
  "v_max_f32 v194, 0, v194\n\t"
  "v_max_f32 v195, 0, v195\n\t"
  "v_mfma_f32_16x16x32_bf16 v[240:243], a[132:135], a[32:35], v[240:243]\n\t"
  "s_waitcnt lgkmcnt(3)\n\t"
  "v_mul_f32 v192, v192, v208\n\t"
  "v_mul_f32 v193, v193, v209\n\t"
  "v_mfma_f32_16x16x32_bf16 v[224:227], a[136:139], a[48:51], v[224:227]\n\t"
  "ds_read_b128 a[152:155], v3 offset:26624\n\t"
  "v_mul_f32 v194, v194, v210\n\t"
  "v_mfma_f32_16x16x32_bf16 v[228:231], a[136:139], a[52:55], v[228:231]\n\t"
  "v_mul_f32 v195, v195, v211\n\t"
  "v_cvt_pk_bf16_f32 v178, v192, v193\n\t"
  "v_mfma_f32_16x16x32_bf16 v[232:235], a[136:139], a[56:59], v[232:235]\n\t"
  "v_cvt_pk_bf16_f32 v179, v194, v195\n\t"
  "v_max_f32 v196, 0, v196\n\t"
  "v_mfma_f32_16x16x32_bf16 v[236:239], a[136:139], a[60:63], v[236:239]\n\t"
  "s_waitcnt lgkmcnt(3)\n\t"
  "v_max_f32 v197, 0, v197\n\t"
  "v_max_f32 v198, 0, v198\n\t"
  "v_mfma_f32_16x16x32_bf16 v[252:255], a[140:143], a[60:63], v[252:255]\n\t"
  "ds_read_b128 a[156:159], v3 offset:27648\n\t"
  "v_max_f32 v199, 0, v199\n\t"
  "v_mfma_f32_16x16x32_bf16 v[248:251], a[140:143], a[56:59], v[248:251]\n\t"
  "v_mul_f32 v196, v196, v212\n\t"
  "v_mul_f32 v197, v197, v213\n\t"
  "v_mfma_f32_16x16x32_bf16 v[244:247], a[140:143], a[52:55], v[244:247]\n\t"
  "v_mul_f32 v198, v198, v214\n\t"
  "v_mul_f32 v199, v199, v215\n\t"
  "v_mfma_f32_16x16x32_bf16 v[240:243], a[140:143], a[48:51], v[240:243]\n\t"
  "s_waitcnt lgkmcnt(3)\n\t"
  "v_cvt_pk_bf16_f32 v182, v196, v197\n\t"
  "v_cvt_pk_bf16_f32 v183, v198, v199\n\t"
  "v_mfma_f32_16x16x32_bf16 v[224:227], a[144:147], a[64:67], v[224:227]\n\t"
  "ds_read_b128 a[128:131], v3 offset:40960\n\t"
  "v_max_f32 v200, 0, v200\n\t"
  "v_mfma_f32_16x16x32_bf16 v[228:231], a[144:147], a[68:71], v[228:231]\n\t"
  "v_max_f32 v201, 0, v201\n\t"
  "v_max_f32 v202, 0, v202\n\t"
  "v_mfma_f32_16x16x32_bf16 v[232:235], a[144:147], a[72:75], v[232:235]\n\t"
  "v_max_f32 v203, 0, v203\n\t"
  "v_mul_f32 v200, v200, v216\n\t"
  "v_mfma_f32_16x16x32_bf16 v[236:239], a[144:147], a[76:79], v[236:239]\n\t"
  "s_waitcnt lgkmcnt(3)\n\t"
  "v_mul_f32 v201, v201, v217\n\t"
  "v_mul_f32 v202, v202, v218\n\t"
  "v_mfma_f32_16x16x32_bf16 v[252:255], a[148:151], a[76:79], v[252:255]\n\t"
  "ds_read_b128 a[132:135], v3 offset:41984\n\t"
  "v_mul_f32 v203, v203, v219\n\t"
  "v_mfma_f32_16x16x32_bf16 v[248:251], a[148:151], a[72:75], v[248:251]\n\t"
  "ds_read_b128 a[136:139], v3 offset:55296\n\t"
  "v_cvt_pk_bf16_f32 v186, v200, v201\n\t"
  "v_mfma_f32_16x16x32_bf16 v[244:247], a[148:151], a[68:71], v[244:247]\n\t"
  "s_barrier\n\t"
  "v_mfma_f32_16x16x32_bf16 v[240:243], a[148:151], a[64:67], v[240:243]\n\t"
  "s_waitcnt lgkmcnt(4)\n\t"
  "v_cvt_pk_bf16_f32 v187, v202, v203\n\t"
  "v_max_f32 v204, 0, v204\n\t"
  "v_mfma_f32_16x16x32_bf16 v[224:227], a[152:155], a[80:83], v[224:227]\n\t"
  "ds_read_b128 a[140:143], v3 offset:56320\n\t"
  "ds_read_b128 a[144:147], v21 offset:16384\n\t"
  "v_max_f32 v205, 0, v205\n\t"
  "v_mfma_f32_16x16x32_bf16 v[228:231], a[152:155], a[84:87], v[228:231]\n\t"
  "v_max_f32 v206, 0, v206\n\t"
  "v_max_f32 v207, 0, v207\n\t"
  "v_mfma_f32_16x16x32_bf16 v[232:235], a[152:155], a[88:91], v[232:235]\n\t"
  "v_mul_f32 v204, v204, v220\n\t"
  "v_mul_f32 v205, v205, v221\n\t"
  "v_mfma_f32_16x16x32_bf16 v[236:239], a[152:155], a[92:95], v[236:239]\n\t"
  "s_waitcnt lgkmcnt(5)\n\t"
  "v_mul_f32 v206, v206, v222\n\t"
  "v_mul_f32 v207, v207, v223\n\t"
  "v_mfma_f32_16x16x32_bf16 v[252:255], a[156:159], a[92:95], v[252:255]\n\t"
  "ds_read_b128 a[148:151], v21 offset:18432\n\t"
  "v_cvt_pk_bf16_f32 v190, v204, v205\n\t"
  "s_mov_b32 s72, 0x20000\n\t"
  "s_not_b32 s68, s34\n\t"
  "s_mov_b32 s73, 64\n\t"
  "s_branch LPAc_%=\n\t"
  "LPAa_%=:\n\t"
  "v_not_b32 v24, s34\n\t"
  "buffer_load_dwordx2 v[24:25], v11, s[20:23], 0 offen offset:0 sc1\n\t"
  "s_mov_b32 s73, 64\n\t"
  "LPAw_%=:\n\t"
  "s_sleep 1\n\t"
  "LPAc_%=:\n\t"
  "v_cmp_eq_u32 vcc, s68, v24\n\t"
  "s_nop 1\n\t"
  "s_cmp_eq_u64 vcc, 0\n\t"
  "v_cmp_ne_u64 vcc, s[34:35], v[24:25]\n\t"
  "s_cbranch_vccz LPAb_%=\n\t"
  "s_cbranch_scc1 LPAr_%=\n\t"
  "s_sub_u32 s73, s73, 1\n\t"
  "s_cbranch_scc0 LPAw_%=\n\t"
  "s_waitcnt vmcnt(0)\n\t"
  "LPAr_%=:\n\t"
  "s_sleep 2\n\t"
  "s_sub_u32 s72, s72, 1\n\t"
  "s_cbranch_scc0 LPAa_%=\n\t"
  "LPAb_%=:\n\t"
  "v_mfma_f32_16x16x32_bf16 v[248:251], a[156:159], a[88:91], v[248:251]\n\t"
  "v_cvt_pk_bf16_f32 v191, v206, v207\n\t"
  "buffer_load_dwordx4 a[192:195], v13, s[20:23], s56 offen offset:0 sc1\n\t"
  "v_mfma_f32_16x16x32_bf16 v[244:247], a[156:159], a[84:87], v[244:247]\n\t"
  "buffer_load_dwordx4 a[196:199], v13, s[20:23], s56 offen offset:2048 sc1\n\t"
  "s_add_u32 s56, s56, 0x80000\n\t"
  "v_mfma_f32_16x16x32_bf16 v[240:243], a[156:159], a[80:83], v[240:243]\n\t"
  "s_waitcnt lgkmcnt(5)\n\t"
  "v_mfma_f32_16x16x32_bf16 v[224:227], a[128:131], a[96:99], v[224:227]\n\t"
  "ds_read_b128 a[152:155], v21 offset:20480\n\t"
  "v_mfma_f32_16x16x32_bf16 v[228:231], a[128:131], a[100:103], v[228:231]\n\t"
  "buffer_load_dwordx4 a[200:203], v13, s[20:23], s56 offen offset:0 sc1\n\t"
  "v_mfma_f32_16x16x32_bf16 v[232:235], a[128:131], a[104:107], v[232:235]\n\t"
  "buffer_load_dwordx4 a[204:207], v13, s[20:23], s56 offen offset:2048 sc1\n\t"
  "s_add_u32 s56, s56, 0x60000\n\t"
  "v_mfma_f32_16x16x32_bf16 v[236:239], a[128:131], a[108:111], v[236:239]\n\t"
  "s_waitcnt lgkmcnt(5)\n\t"
  "v_mfma_f32_16x16x32_bf16 v[252:255], a[132:135], a[108:111], v[252:255]\n\t"
  "ds_read_b128 a[156:159], v21 offset:22528\n\t"
  "v_mfma_f32_16x16x32_bf16 v[248:251], a[132:135], a[104:107], v[248:251]\n\t"
  "buffer_load_dwordx4 a[208:211], v13, s[20:23], s56 offen offset:0 sc1\n\t"
  "v_mfma_f32_16x16x32_bf16 v[244:247], a[132:135], a[100:103], v[244:247]\n\t"
  "buffer_load_dwordx4 a[212:215], v13, s[20:23], s56 offen offset:2048 sc1\n\t"
  "s_add_u32 s56, s56, 0x80000\n\t"
  "v_mfma_f32_16x16x32_bf16 v[240:243], a[132:135], a[96:99], v[240:243]\n\t"
  "s_waitcnt lgkmcnt(5)\n\t"
  "v_mfma_f32_16x16x32_bf16 v[224:227], a[136:139], a[112:115], v[224:227]\n\t"
  "ds_read_b128 a[128:131], v21 offset:17408\n\t"
  "v_mfma_f32_16x16x32_bf16 v[228:231], a[136:139], a[116:119], v[228:231]\n\t"
  "buffer_load_dwordx4 a[216:219], v13, s[20:23], s56 offen offset:0 sc1\n\t"
  "v_mfma_f32_16x16x32_bf16 v[232:235], a[136:139], a[120:123], v[232:235]\n\t"
  "buffer_load_dwordx4 a[220:223], v13, s[20:23], s56 offen offset:2048 sc1\n\t"
  "s_add_u32 s56, s56, 0x60000\n\t"
  "v_mfma_f32_16x16x32_bf16 v[236:239], a[136:139], a[124:127], v[236:239]\n\t"
  "s_waitcnt lgkmcnt(5)\n\t"
  "v_mfma_f32_16x16x32_bf16 v[252:255], a[140:143], a[124:127], v[252:255]\n\t"
  "ds_read_b128 a[132:135], v21 offset:19456\n\t"
  "v_mfma_f32_16x16x32_bf16 v[248:251], a[140:143], a[120:123], v[248:251]\n\t"
  "s_waitcnt vmcnt(9)\n\t"
  "buffer_store_dwordx2 v[16:17], v10, s[20:23], 0 offen offset:8 sc1\n\t"
  "v_mfma_f32_16x16x32_bf16 v[244:247], a[140:143], a[116:119], v[244:247]\n\t"
  "buffer_load_dwordx4 a[224:227], v13, s[20:23], s56 offen offset:0 sc1\n\t"
  "v_mfma_f32_16x16x32_bf16 v[240:243], a[140:143], a[112:115], v[240:243]\n\t"
  "buffer_load_dwordx4 a[228:231], v13, s[20:23], s56 offen offset:2048 sc1\n\t"
  "s_add_u32 s56, s56, 0x80000\n\t"
  "buffer_load_dwordx4 a[232:235], v13, s[20:23], s56 offen offset:0 sc1\n\t"
  "buffer_load_dwordx4 a[236:239], v13, s[20:23], s56 offen offset:2048 sc1\n\t"
  "s_add_u32 s56, s56, 0x60000\n\t"
  "buffer_load_dwordx4 a[240:243], v13, s[20:23], s56 offen offset:0 sc1\n\t"
  "buffer_load_dwordx4 a[244:247], v13, s[20:23], s56 offen offset:2048 sc1\n\t"
  "s_add_u32 s56, s56, 0x80000\n\t"
  "s_waitcnt lgkmcnt(4) vmcnt(13)\n\t"
  "v_mfma_f32_16x16x32_bf16 v[32:35], a[144:147], a[192:195], 0\n\t"
  "ds_read_b128 a[136:139], v21 offset:21504\n\t"
  "v_mfma_f32_16x16x32_bf16 v[36:39], a[144:147], a[196:199], 0\n\t"
  "v_mfma_f32_16x16x32_bf16 v[44:47], a[148:151], a[196:199], 0\n\t"
  "ds_read_b128 a[140:143], v21 offset:23552\n\t"
  "v_mfma_f32_16x16x32_bf16 v[40:43], a[148:151], a[192:195], 0\n\t"
  "s_waitcnt lgkmcnt(4)\n\t"
  "v_mfma_f32_16x16x32_bf16 v[48:51], a[152:155], a[192:195], 0\n\t"
  "ds_read_b128 a[144:147], v21 offset:24576\n\t"
  "v_mfma_f32_16x16x32_bf16 v[52:55], a[152:155], a[196:199], 0\n\t"
  "buffer_load_dwordx4 a[248:251], v13, s[20:23], s56 offen offset:0 sc1\n\t"
  "v_mfma_f32_16x16x32_bf16 v[60:63], a[156:159], a[196:199], 0\n\t"
  "ds_read_b128 a[148:151], v21 offset:26624\n\t"
  "v_max_f32 v224, 0, v224\n\t"
  "v_mfma_f32_16x16x32_bf16 v[56:59], a[156:159], a[192:195], 0\n\t"
  "s_waitcnt lgkmcnt(4)\n\t"
  "v_max_f32 v225, 0, v225\n\t"
  "v_max_f32 v226, 0, v226\n\t"
  "v_mfma_f32_16x16x32_bf16 v[64:67], a[128:131], a[192:195], 0\n\t"
  "ds_read_b128 a[152:155], v21 offset:28672\n\t"
  "v_max_f32 v227, 0, v227\n\t"
  "v_mfma_f32_16x16x32_bf16 v[68:71], a[128:131], a[196:199], 0\n\t"
  "v_mul_f32 v224, v224, v240\n\t"
  "v_mul_f32 v225, v225, v241\n\t"
  "v_mfma_f32_16x16x32_bf16 v[76:79], a[132:135], a[196:199], 0\n\t"
  "ds_read_b128 a[156:159], v21 offset:30720\n\t"
  "v_mul_f32 v226, v226, v242\n\t"
  "v_mfma_f32_16x16x32_bf16 v[72:75], a[132:135], a[192:195], 0\n\t"
  "s_waitcnt lgkmcnt(4)\n\t"
  "v_mul_f32 v227, v227, v243\n\t"
  "v_cvt_pk_bf16_f32 v240, v224, v225\n\t"
  "v_mfma_f32_16x16x32_bf16 v[80:83], a[136:139], a[192:195], 0\n\t"
  "ds_read_b128 a[128:131], v21 offset:25600\n\t"
  "v_cvt_pk_bf16_f32 v241, v226, v227\n\t"
  "v_mfma_f32_16x16x32_bf16 v[84:87], a[136:139], a[196:199], 0\n\t"
  "buffer_load_dwordx4 a[252:255], v13, s[20:23], s56 offen offset:2048 sc1\n\t"
  "v_mfma_f32_16x16x32_bf16 v[92:95], a[140:143], a[196:199], 0\n\t"
  "ds_read_b128 a[132:135], v21 offset:27648\n\t"
  "s_add_u32 s56, s56, 0x60000\n\t"
  "v_max_f32 v228, 0, v228\n\t"
  "v_mfma_f32_16x16x32_bf16 v[88:91], a[140:143], a[192:195], 0\n\t"
  "s_waitcnt lgkmcnt(4) vmcnt(13)\n\t"
  "v_max_f32 v229, 0, v229\n\t"
  "v_max_f32 v230, 0, v230\n\t"
  "v_mfma_f32_16x16x32_bf16 v[32:35], a[144:147], a[200:203], v[32:35]\n\t"
  "ds_read_b128 a[136:139], v21 offset:29696\n\t"
  "v_max_f32 v231, 0, v231\n\t"
  "v_mfma_f32_16x16x32_bf16 v[36:39], a[144:147], a[204:207], v[36:39]\n\t"
  "s_barrier\n\t"
  "v_mul_f32 v228, v228, v244\n\t"
  "v_mul_f32 v229, v229, v245\n\t"
  "v_mfma_f32_16x16x32_bf16 v[44:47], a[148:151], a[204:207], v[44:47]\n\t"
  "ds_read_b128 a[140:143], v21 offset:31744\n\t"
  "s_waitcnt lgkmcnt(4)\n\t"
  "v_mul_f32 v230, v230, v246\n\t"
  "v_mfma_f32_16x16x32_bf16 v[40:43], a[148:151], a[200:203], v[40:43]\n\t"
  "buffer_store_dwordx4 v[176:179], v5, s[20:23], s44 offen offset:0 sc1\n\t"
  "v_mfma_f32_16x16x32_bf16 v[48:51], a[152:155], a[200:203], v[48:51]\n\t"
  "ds_read_b128 a[144:147], v21 offset:32768\n\t"
  "v_mul_f32 v231, v231, v247\n\t"
  "v_mfma_f32_16x16x32_bf16 v[52:55], a[152:155], a[204:207], v[52:55]\n\t"
  "buffer_load_dwordx4 a[192:195], v13, s[20:23], s56 offen offset:0 sc1\n\t"
  "v_mfma_f32_16x16x32_bf16 v[60:63], a[156:159], a[204:207], v[60:63]\n\t"
  "ds_read_b128 a[148:151], v21 offset:34816\n\t"
  "s_add_u32 m0, s58, 0xffffc000\n\t"
  "s_waitcnt lgkmcnt(4)\n\t"
  "v_cvt_pk_bf16_f32 v242, v228, v229\n\t"
  "v_mfma_f32_16x16x32_bf16 v[56:59], a[156:159], a[200:203], v[56:59]\n\t"
  "buffer_load_dwordx4 v14, s[20:23], s57 offen offset:0 sc1 lds\n\t"
  "v_mfma_f32_16x16x32_bf16 v[64:67], a[128:131], a[200:203], v[64:67]\n\t"
  "ds_read_b128 a[152:155], v21 offset:36864\n\t"
  "v_cvt_pk_bf16_f32 v243, v230, v231\n\t"
  "v_mfma_f32_16x16x32_bf16 v[68:71], a[128:131], a[204:207], v[68:71]\n\t"
  "s_nop 1\n\t"
  "v_permlane16_swap_b32 v240, v242\n\t"
  "v_mfma_f32_16x16x32_bf16 v[76:79], a[132:135], a[204:207], v[76:79]\n\t"
  "ds_read_b128 a[156:159], v21 offset:38912\n\t"
  "v_permlane16_swap_b32 v241, v243\n\t"
  "v_mfma_f32_16x16x32_bf16 v[72:75], a[132:135], a[200:203], v[72:75]\n\t"
  "s_waitcnt lgkmcnt(4)\n\t"
  "v_max_f32 v232, 0, v232\n\t"
  "v_max_f32 v233, 0, v233\n\t"
  "v_mfma_f32_16x16x32_bf16 v[80:83], a[136:139], a[200:203], v[80:83]\n\t"
  "ds_read_b128 a[128:131], v21 offset:33792\n\t"
  "v_max_f32 v234, 0, v234\n\t"
  "v_mfma_f32_16x16x32_bf16 v[84:87], a[136:139], a[204:207], v[84:87]\n\t"
  "buffer_load_dwordx4 a[196:199], v13, s[20:23], s56 offen offset:2048 sc1\n\t"
  "v_mfma_f32_16x16x32_bf16 v[92:95], a[140:143], a[204:207], v[92:95]\n\t"
  "ds_read_b128 a[132:135], v21 offset:35840\n\t"
  "s_add_u32 s56, s56, 0x80000\n\t"
  "s_waitcnt lgkmcnt(4) vmcnt(15)\n\t"
  "v_max_f32 v235, 0, v235\n\t"
  "v_mfma_f32_16x16x32_bf16 v[88:91], a[140:143], a[200:203], v[88:91]\n\t"
  "buffer_load_dwordx4 v15, s[20:23], s57 offen offset:1024 sc1 lds\n\t"
  "v_mfma_f32_16x16x32_bf16 v[32:35], a[144:147], a[208:211], v[32:35]\n\t"
  "ds_read_b128 a[136:139], v21 offset:37888\n\t"
  "s_add_u32 s57, s57, 0x8000\n\t"
  "v_mul_f32 v232, v232, v248\n\t"
  "v_mfma_f32_16x16x32_bf16 v[36:39], a[144:147], a[212:215], v[36:39]\n\t"
  "v_mul_f32 v233, v233, v249\n\t"
  "v_mul_f32 v234, v234, v250\n\t"
  "v_mfma_f32_16x16x32_bf16 v[44:47], a[148:151], a[212:215], v[44:47]\n\t"
  "ds_read_b128 a[140:143], v21 offset:39936\n\t"
  "v_mul_f32 v235, v235, v251\n\t"
  "v_mfma_f32_16x16x32_bf16 v[40:43], a[148:151], a[208:211], v[40:43]\n\t"
  "s_waitcnt lgkmcnt(4)\n\t"
  "v_cvt_pk_bf16_f32 v248, v232, v233\n\t"
  "v_cvt_pk_bf16_f32 v249, v234, v235\n\t"
  "v_mfma_f32_16x16x32_bf16 v[48:51], a[152:155], a[208:211], v[48:51]\n\t"
  "ds_read_b128 a[144:147], v21 offset:40960\n\t"
  "v_max_f32 v236, 0, v236\n\t"
  "v_mfma_f32_16x16x32_bf16 v[52:55], a[152:155], a[212:215], v[52:55]\n\t"
  "buffer_load_dwordx4 a[200:203], v13, s[20:23], s56 offen offset:0 sc1\n\t"
  "v_mfma_f32_16x16x32_bf16 v[60:63], a[156:159], a[212:215], v[60:63]\n\t"
  "ds_read_b128 a[148:151], v21 offset:43008\n\t"
  "s_add_u32 m0, s58, 0xffffe000\n\t"
  "s_waitcnt lgkmcnt(4)\n\t"
  "v_max_f32 v237, 0, v237\n\t"
  "v_mfma_f32_16x16x32_bf16 v[56:59], a[156:159], a[208:211], v[56:59]\n\t"
  "buffer_load_dwordx4 v14, s[20:23], s57 offen offset:0 sc1 lds\n\t"
  "v_mfma_f32_16x16x32_bf16 v[64:67], a[128:131], a[208:211], v[64:67]\n\t"
  "ds_read_b128 a[152:155], v21 offset:45056\n\t"
  "v_max_f32 v238, 0, v238\n\t"
  "v_mfma_f32_16x16x32_bf16 v[68:71], a[128:131], a[212:215], v[68:71]\n\t"
  "v_max_f32 v239, 0, v239\n\t"
  "v_mul_f32 v236, v236, v252\n\t"
  "v_mfma_f32_16x16x32_bf16 v[76:79], a[132:135], a[212:215], v[76:79]\n\t"
  "ds_read_b128 a[156:159], v21 offset:47104\n\t"
  "v_mul_f32 v237, v237, v253\n\t"
  "v_mfma_f32_16x16x32_bf16 v[72:75], a[132:135], a[208:211], v[72:75]\n\t"
  "s_waitcnt lgkmcnt(4)\n\t"
  "v_mul_f32 v238, v238, v254\n\t"
  "v_mul_f32 v239, v239, v255\n\t"
  "v_mfma_f32_16x16x32_bf16 v[80:83], a[136:139], a[208:211], v[80:83]\n\t"
  "ds_read_b128 a[128:131], v21 offset:41984\n\t"
  "v_cvt_pk_bf16_f32 v250, v236, v237\n\t"
  "v_mfma_f32_16x16x32_bf16 v[84:87], a[136:139], a[212:215], v[84:87]\n\t"
  "buffer_load_dwordx4 a[204:207], v13, s[20:23], s56 offen offset:2048 sc1\n\t"
  "v_mfma_f32_16x16x32_bf16 v[92:95], a[140:143], a[212:215], v[92:95]\n\t"
  "ds_read_b128 a[132:135], v21 offset:44032\n\t"
  "s_add_u32 s56, s56, 0x60000\n\t"
  "s_waitcnt lgkmcnt(4) vmcnt(17)\n\t"
  "v_cvt_pk_bf16_f32 v251, v238, v239\n\t"
  "v_mfma_f32_16x16x32_bf16 v[88:91], a[140:143], a[208:211], v[88:91]\n\t"
  "buffer_load_dwordx4 v15, s[20:23], s57 offen offset:1024 sc1 lds\n\t"
  "v_mfma_f32_16x16x32_bf16 v[32:35], a[144:147], a[216:219], v[32:35]\n\t"
  "ds_read_b128 a[136:139], v21 offset:46080\n\t"
  "s_add_u32 s57, s57, 0x6000\n\t"
  "s_nop 1\n\t"
  "v_mfma_f32_16x16x32_bf16 v[36:39], a[144:147], a[220:223], v[36:39]\n\t"
  "s_barrier\n\t"
  "v_permlane16_swap_b32 v248, v250\n\t"
  "v_permlane16_swap_b32 v249, v251\n\t"
  "v_mfma_f32_16x16x32_bf16 v[44:47], a[148:151], a[220:223], v[44:47]\n\t"
  "ds_read_b128 a[140:143], v21 offset:48128\n\t"
  "s_waitcnt lgkmcnt(4)\n\t"
  "v_mfma_f32_16x16x32_bf16 v[40:43], a[148:151], a[216:219], v[40:43]\n\t"
  "buffer_store_dwordx4 v[180:183], v5, s[20:23], s44 offen offset:1024 sc1\n\t"
  "v_mfma_f32_16x16x32_bf16 v[48:51], a[152:155], a[216:219], v[48:51]\n\t"
  "ds_read_b128 a[144:147], v21 offset:49152\n\t"
  "v_mfma_f32_16x16x32_bf16 v[52:55], a[152:155], a[220:223], v[52:55]\n\t"
  "buffer_load_dwordx4 a[208:211], v13, s[20:23], s56 offen offset:0 sc1\n\t"
  "v_mfma_f32_16x16x32_bf16 v[60:63], a[156:159], a[220:223], v[60:63]\n\t"
  "ds_read_b128 a[148:151], v21 offset:51200\n\t"
  "s_add_u32 m0, s58, 0xffff8000\n\t"
  "s_waitcnt lgkmcnt(4)\n\t"
  "v_mfma_f32_16x16x32_bf16 v[56:59], a[156:159], a[216:219], v[56:59]\n\t"
  "buffer_load_dwordx4 v14, s[20:23], s57 offen offset:0 sc1 lds\n\t"
  "v_mfma_f32_16x16x32_bf16 v[64:67], a[128:131], a[216:219], v[64:67]\n\t"
  "ds_read_b128 a[152:155], v21 offset:53248\n\t"
  "v_mfma_f32_16x16x32_bf16 v[68:71], a[128:131], a[220:223], v[68:71]\n\t"
  "v_mfma_f32_16x16x32_bf16 v[76:79], a[132:135], a[220:223], v[76:79]\n\t"
  "ds_read_b128 a[156:159], v21 offset:55296\n\t"
  "v_mfma_f32_16x16x32_bf16 v[72:75], a[132:135], a[216:219], v[72:75]\n\t"
  "s_waitcnt lgkmcnt(4)\n\t"
  "v_mfma_f32_16x16x32_bf16 v[80:83], a[136:139], a[216:219], v[80:83]\n\t"
  "ds_read_b128 a[128:131], v21 offset:50176\n\t"
  "v_mfma_f32_16x16x32_bf16 v[84:87], a[136:139], a[220:223], v[84:87]\n\t"
  "buffer_load_dwordx4 a[212:215], v13, s[20:23], s56 offen offset:2048 sc1\n\t"
  "v_mfma_f32_16x16x32_bf16 v[92:95], a[140:143], a[220:223], v[92:95]\n\t"
  "ds_read_b128 a[132:135], v21 offset:52224\n\t"
  "s_add_u32 s56, s56, 0x80000\n\t"
  "s_waitcnt lgkmcnt(4) vmcnt(19)\n\t"
  "v_mfma_f32_16x16x32_bf16 v[88:91], a[140:143], a[216:219], v[88:91]\n\t"
  "buffer_load_dwordx4 v15, s[20:23], s57 offen offset:1024 sc1 lds\n\t"
  "v_mfma_f32_16x16x32_bf16 v[32:35], a[144:147], a[224:227], v[32:35]\n\t"
  "ds_read_b128 a[136:139], v21 offset:54272\n\t"
  "s_add_u32 s57, s57, 0x8000\n\t"
  "v_mfma_f32_16x16x32_bf16 v[36:39], a[144:147], a[228:231], v[36:39]\n\t"
  "v_mfma_f32_16x16x32_bf16 v[44:47], a[148:151], a[228:231], v[44:47]\n\t"
  "ds_read_b128 a[140:143], v21 offset:56320\n\t"
  "v_mfma_f32_16x16x32_bf16 v[40:43], a[148:151], a[224:227], v[40:43]\n\t"
  "s_waitcnt lgkmcnt(4)\n\t"
  "v_mfma_f32_16x16x32_bf16 v[48:51], a[152:155], a[224:227], v[48:51]\n\t"
  "ds_read_b128 a[144:147], v21 offset:57344\n\t"
  "v_mfma_f32_16x16x32_bf16 v[52:55], a[152:155], a[228:231], v[52:55]\n\t"
  "buffer_load_dwordx4 a[216:219], v13, s[20:23], s56 offen offset:0 sc1\n\t"
  "v_mfma_f32_16x16x32_bf16 v[60:63], a[156:159], a[228:231], v[60:63]\n\t"
  "ds_read_b128 a[148:151], v21 offset:59392\n\t"
  "s_add_u32 m0, s58, 0xffffa000\n\t"
  "s_waitcnt lgkmcnt(4)\n\t"
  "v_mfma_f32_16x16x32_bf16 v[56:59], a[156:159], a[224:227], v[56:59]\n\t"
  "buffer_load_dwordx4 v14, s[20:23], s57 offen offset:0 sc1 lds\n\t"
  "v_mfma_f32_16x16x32_bf16 v[64:67], a[128:131], a[224:227], v[64:67]\n\t"
  "ds_read_b128 a[152:155], v21 offset:61440\n\t"
  "v_mfma_f32_16x16x32_bf16 v[68:71], a[128:131], a[228:231], v[68:71]\n\t"
  "v_mfma_f32_16x16x32_bf16 v[76:79], a[132:135], a[228:231], v[76:79]\n\t"
  "ds_read_b128 a[156:159], v21 offset:63488\n\t"
  "v_mfma_f32_16x16x32_bf16 v[72:75], a[132:135], a[224:227], v[72:75]\n\t"
  "s_waitcnt lgkmcnt(4)\n\t"
  "v_mfma_f32_16x16x32_bf16 v[80:83], a[136:139], a[224:227], v[80:83]\n\t"
  "ds_read_b128 a[128:131], v21 offset:58368\n\t"
  "v_mfma_f32_16x16x32_bf16 v[84:87], a[136:139], a[228:231], v[84:87]\n\t"
  "buffer_load_dwordx4 a[220:223], v13, s[20:23], s56 offen offset:2048 sc1\n\t"
  "v_mfma_f32_16x16x32_bf16 v[92:95], a[140:143], a[228:231], v[92:95]\n\t"
  "ds_read_b128 a[132:135], v21 offset:60416\n\t"
  "s_add_u32 s56, s56, 0x60000\n\t"
  "s_waitcnt lgkmcnt(4) vmcnt(21)\n\t"
  "v_mfma_f32_16x16x32_bf16 v[88:91], a[140:143], a[224:227], v[88:91]\n\t"
  "buffer_load_dwordx4 v15, s[20:23], s57 offen offset:1024 sc1 lds\n\t"
  "v_mfma_f32_16x16x32_bf16 v[32:35], a[144:147], a[232:235], v[32:35]\n\t"
  "ds_read_b128 a[136:139], v21 offset:62464\n\t"
  "s_add_u32 s57, s57, 0x6000\n\t"
  "v_mfma_f32_16x16x32_bf16 v[36:39], a[144:147], a[236:239], v[36:39]\n\t"
  "s_barrier\n\t"
  "buffer_load_dwordx2 v[24:25], v11, s[20:23], 0 offen offset:8 sc1\n\t"
  "v_mfma_f32_16x16x32_bf16 v[44:47], a[148:151], a[236:239], v[44:47]\n\t"
  "ds_read_b128 a[140:143], v21 offset:64512\n\t"
  "s_waitcnt lgkmcnt(4)\n\t"
  "v_mfma_f32_16x16x32_bf16 v[40:43], a[148:151], a[232:235], v[40:43]\n\t"
  "buffer_store_dwordx4 v[184:187], v5, s[20:23], s44 offen offset:2048 sc1\n\t"
  "v_mfma_f32_16x16x32_bf16 v[48:51], a[152:155], a[232:235], v[48:51]\n\t"
  "ds_read_b128 a[144:147], v21 offset:0\n\t"
  "v_mfma_f32_16x16x32_bf16 v[52:55], a[152:155], a[236:239], v[52:55]\n\t"
  "buffer_load_dwordx4 a[224:227], v13, s[20:23], s56 offen offset:0 sc1\n\t"
  "v_mfma_f32_16x16x32_bf16 v[60:63], a[156:159], a[236:239], v[60:63]\n\t"
  "ds_read_b128 a[148:151], v21 offset:2048\n\t"
  "s_add_u32 m0, s58, 0x4000\n\t"
  "s_waitcnt lgkmcnt(4)\n\t"
  "v_mfma_f32_16x16x32_bf16 v[56:59], a[156:159], a[232:235], v[56:59]\n\t"
  "buffer_load_dwordx4 v14, s[20:23], s57 offen offset:0 sc1 lds\n\t"
  "v_mfma_f32_16x16x32_bf16 v[64:67], a[128:131], a[232:235], v[64:67]\n\t"
  "ds_read_b128 a[152:155], v21 offset:4096\n\t"
  "v_mfma_f32_16x16x32_bf16 v[68:71], a[128:131], a[236:239], v[68:71]\n\t"
  "v_mfma_f32_16x16x32_bf16 v[76:79], a[132:135], a[236:239], v[76:79]\n\t"
  "ds_read_b128 a[156:159], v21 offset:6144\n\t"
  "v_mfma_f32_16x16x32_bf16 v[72:75], a[132:135], a[232:235], v[72:75]\n\t"
  "s_waitcnt lgkmcnt(4)\n\t"
  "v_mfma_f32_16x16x32_bf16 v[80:83], a[136:139], a[232:235], v[80:83]\n\t"
  "ds_read_b128 a[128:131], v21 offset:1024\n\t"
  "v_mfma_f32_16x16x32_bf16 v[84:87], a[136:139], a[236:239], v[84:87]\n\t"
  "buffer_load_dwordx4 a[228:231], v13, s[20:23], s56 offen offset:2048 sc1\n\t"
  "v_mfma_f32_16x16x32_bf16 v[92:95], a[140:143], a[236:239], v[92:95]\n\t"
  "ds_read_b128 a[132:135], v21 offset:3072\n\t"
  "s_add_u32 s56, s56, 0x80000\n\t"
  "s_waitcnt lgkmcnt(4) vmcnt(25)\n\t"
  "v_mfma_f32_16x16x32_bf16 v[88:91], a[140:143], a[232:235], v[88:91]\n\t"
  "buffer_load_dwordx4 v15, s[20:23], s57 offen offset:1024 sc1 lds\n\t"
  "v_mfma_f32_16x16x32_bf16 v[32:35], a[144:147], a[240:243], v[32:35]\n\t"
  "ds_read_b128 a[136:139], v21 offset:5120\n\t"
  "s_add_u32 s57, s57, 0x8000\n\t"
  "v_mfma_f32_16x16x32_bf16 v[36:39], a[144:147], a[244:247], v[36:39]\n\t"
  "v_mfma_f32_16x16x32_bf16 v[44:47], a[148:151], a[244:247], v[44:47]\n\t"
  "ds_read_b128 a[140:143], v21 offset:7168\n\t"
  "v_mfma_f32_16x16x32_bf16 v[40:43], a[148:151], a[240:243], v[40:43]\n\t"
  "s_waitcnt lgkmcnt(4)\n\t"
  "v_mfma_f32_16x16x32_bf16 v[48:51], a[152:155], a[240:243], v[48:51]\n\t"
  "ds_read_b128 a[144:147], v21 offset:8192\n\t"
  "v_mfma_f32_16x16x32_bf16 v[52:55], a[152:155], a[244:247], v[52:55]\n\t"
  "buffer_load_dwordx4 a[232:235], v13, s[20:23], s56 offen offset:0 sc1\n\t"
  "v_mfma_f32_16x16x32_bf16 v[60:63], a[156:159], a[244:247], v[60:63]\n\t"
  "ds_read_b128 a[148:151], v21 offset:10240\n\t"
  "s_add_u32 m0, s58, 0x6000\n\t"
  "s_waitcnt lgkmcnt(4)\n\t"
  "v_mfma_f32_16x16x32_bf16 v[56:59], a[156:159], a[240:243], v[56:59]\n\t"
  "buffer_load_dwordx4 v14, s[20:23], s57 offen offset:0 sc1 lds\n\t"
  "v_mfma_f32_16x16x32_bf16 v[64:67], a[128:131], a[240:243], v[64:67]\n\t"
  "ds_read_b128 a[152:155], v21 offset:12288\n\t"
  "v_mfma_f32_16x16x32_bf16 v[68:71], a[128:131], a[244:247], v[68:71]\n\t"
  "v_mfma_f32_16x16x32_bf16 v[76:79], a[132:135], a[244:247], v[76:79]\n\t"
  "ds_read_b128 a[156:159], v21 offset:14336\n\t"
  "v_mfma_f32_16x16x32_bf16 v[72:75], a[132:135], a[240:243], v[72:75]\n\t"
  "s_waitcnt lgkmcnt(4)\n\t"
  "v_mfma_f32_16x16x32_bf16 v[80:83], a[136:139], a[240:243], v[80:83]\n\t"
  "ds_read_b128 a[128:131], v21 offset:9216\n\t"
  "v_mfma_f32_16x16x32_bf16 v[84:87], a[136:139], a[244:247], v[84:87]\n\t"
  "buffer_load_dwordx4 a[236:239], v13, s[20:23], s56 offen offset:2048 sc1\n\t"
  "v_mfma_f32_16x16x32_bf16 v[92:95], a[140:143], a[244:247], v[92:95]\n\t"
  "ds_read_b128 a[132:135], v21 offset:11264\n\t"
  "s_add_u32 s56, s56, 0x60000\n\t"
  "s_waitcnt lgkmcnt(4) vmcnt(27)\n\t"
  "v_mfma_f32_16x16x32_bf16 v[88:91], a[140:143], a[240:243], v[88:91]\n\t"
  "buffer_load_dwordx4 v15, s[20:23], s57 offen offset:1024 sc1 lds\n\t"
  "v_mfma_f32_16x16x32_bf16 v[32:35], a[144:147], a[248:251], v[32:35]\n\t"
  "ds_read_b128 a[136:139], v21 offset:13312\n\t"
  "s_waitcnt vmcnt(19)\n\t"
  "s_add_u32 s57, s57, 0x6000\n\t"
  "v_mfma_f32_16x16x32_bf16 v[36:39], a[144:147], a[252:255], v[36:39]\n\t"
  "s_barrier\n\t"
  "v_mfma_f32_16x16x32_bf16 v[44:47], a[148:151], a[252:255], v[44:47]\n\t"
  "ds_read_b128 a[140:143], v21 offset:15360\n\t"
  "s_waitcnt lgkmcnt(4)\n\t"
  "v_mfma_f32_16x16x32_bf16 v[40:43], a[148:151], a[248:251], v[40:43]\n\t"
  "buffer_store_dwordx4 v[188:191], v5, s[20:23], s44 offen offset:3072 sc1\n\t"
  "v_mfma_f32_16x16x32_bf16 v[48:51], a[152:155], a[248:251], v[48:51]\n\t"
  "ds_read_b128 a[144:147], v23 offset:49152\n\t"
  "v_mfma_f32_16x16x32_bf16 v[52:55], a[152:155], a[252:255], v[52:55]\n\t"
  "buffer_load_dwordx4 a[240:243], v13, s[20:23], s56 offen offset:0 sc1\n\t"
  "v_mfma_f32_16x16x32_bf16 v[60:63], a[156:159], a[252:255], v[60:63]\n\t"
  "ds_read_b128 a[148:151], v23 offset:51200\n\t"
  "s_add_u32 m0, s58, 0x8000\n\t"
  "s_waitcnt lgkmcnt(4)\n\t"
  "v_mfma_f32_16x16x32_bf16 v[56:59], a[156:159], a[248:251], v[56:59]\n\t"
  "buffer_load_dwordx4 v14, s[20:23], s57 offen offset:0 sc1 lds\n\t"
  "v_mfma_f32_16x16x32_bf16 v[64:67], a[128:131], a[248:251], v[64:67]\n\t"
  "ds_read_b128 a[152:155], v23 offset:53248\n\t"
  "v_mfma_f32_16x16x32_bf16 v[68:71], a[128:131], a[252:255], v[68:71]\n\t"
  "v_mfma_f32_16x16x32_bf16 v[76:79], a[132:135], a[252:255], v[76:79]\n\t"
  "ds_read_b128 a[156:159], v23 offset:55296\n\t"
  "v_mfma_f32_16x16x32_bf16 v[72:75], a[132:135], a[248:251], v[72:75]\n\t"
  "s_waitcnt lgkmcnt(4)\n\t"
  "v_mfma_f32_16x16x32_bf16 v[80:83], a[136:139], a[248:251], v[80:83]\n\t"
  "ds_read_b128 a[128:131], v23 offset:50176\n\t"
  "v_mfma_f32_16x16x32_bf16 v[84:87], a[136:139], a[252:255], v[84:87]\n\t"
  "buffer_load_dwordx4 a[244:247], v13, s[20:23], s56 offen offset:2048 sc1\n\t"
  "v_mfma_f32_16x16x32_bf16 v[92:95], a[140:143], a[252:255], v[92:95]\n\t"
  "ds_read_b128 a[132:135], v23 offset:52224\n\t"
  "s_add_u32 s56, s56, 0x80000\n\t"
  "s_waitcnt lgkmcnt(4)\n\t"
  "v_mfma_f32_16x16x32_bf16 v[88:91], a[140:143], a[248:251], v[88:91]\n\t"
  "buffer_load_dwordx4 v15, s[20:23], s57 offen offset:1024 sc1 lds\n\t"
  "v_mfma_f32_16x16x32_bf16 v[32:35], a[144:147], a[192:195], v[32:35]\n\t"
  "ds_read_b128 a[136:139], v23 offset:54272\n\t"
  "s_add_u32 s57, s57, 0x8000\n\t"
  "v_mfma_f32_16x16x32_bf16 v[36:39], a[144:147], a[196:199], v[36:39]\n\t"
  "v_mfma_f32_16x16x32_bf16 v[44:47], a[148:151], a[196:199], v[44:47]\n\t"
  "ds_read_b128 a[140:143], v23 offset:56320\n\t"
  "v_mfma_f32_16x16x32_bf16 v[40:43], a[148:151], a[192:195], v[40:43]\n\t"
  "s_waitcnt lgkmcnt(4)\n\t"
  "v_mfma_f32_16x16x32_bf16 v[48:51], a[152:155], a[192:195], v[48:51]\n\t"
  "ds_read_b128 a[144:147], v23 offset:57344\n\t"
  "v_mfma_f32_16x16x32_bf16 v[52:55], a[152:155], a[196:199], v[52:55]\n\t"
  "buffer_load_dwordx4 a[248:251], v13, s[20:23], s56 offen offset:0 sc1\n\t"
  "v_mfma_f32_16x16x32_bf16 v[60:63], a[156:159], a[196:199], v[60:63]\n\t"
  "ds_read_b128 a[148:151], v23 offset:59392\n\t"
  "s_add_u32 m0, s58, 0xa000\n\t"
  "s_waitcnt lgkmcnt(4)\n\t"
  "v_mfma_f32_16x16x32_bf16 v[56:59], a[156:159], a[192:195], v[56:59]\n\t"
  "buffer_load_dwordx4 v14, s[20:23], s57 offen offset:0 sc1 lds\n\t"
  "v_mfma_f32_16x16x32_bf16 v[64:67], a[128:131], a[192:195], v[64:67]\n\t"
  "ds_read_b128 a[152:155], v23 offset:61440\n\t"
  "v_mfma_f32_16x16x32_bf16 v[68:71], a[128:131], a[196:199], v[68:71]\n\t"
  "v_mfma_f32_16x16x32_bf16 v[76:79], a[132:135], a[196:199], v[76:79]\n\t"
  "ds_read_b128 a[156:159], v23 offset:63488\n\t"
  "v_mfma_f32_16x16x32_bf16 v[72:75], a[132:135], a[192:195], v[72:75]\n\t"
  "s_waitcnt lgkmcnt(4)\n\t"
  "v_mfma_f32_16x16x32_bf16 v[80:83], a[136:139], a[192:195], v[80:83]\n\t"
  "ds_read_b128 a[128:131], v23 offset:58368\n\t"
  "v_mfma_f32_16x16x32_bf16 v[84:87], a[136:139], a[196:199], v[84:87]\n\t"
  "buffer_load_dwordx4 a[252:255], v13, s[20:23], s56 offen offset:2048 sc1\n\t"
  "v_mfma_f32_16x16x32_bf16 v[92:95], a[140:143], a[196:199], v[92:95]\n\t"
  "ds_read_b128 a[132:135], v23 offset:60416\n\t"
  "s_add_u32 s56, s56, 0xff980000\n\t"
  "s_waitcnt lgkmcnt(4)\n\t"
  "v_mfma_f32_16x16x32_bf16 v[88:91], a[140:143], a[192:195], v[88:91]\n\t"
  "buffer_load_dwordx4 v15, s[20:23], s57 offen offset:1024 sc1 lds\n\t"
  "v_mfma_f32_16x16x32_bf16 v[32:35], a[144:147], a[200:203], v[32:35]\n\t"
  "ds_read_b128 a[136:139], v23 offset:62464\n\t"
  "s_waitcnt vmcnt(19)\n\t"
  "s_waitcnt vmcnt(18)\n\t"
  "s_mov_b32 s72, 0x20000\n\t"
  "s_not_b32 s68, s34\n\t"
  "s_mov_b32 s73, 64\n\t"
  "s_branch LPBc_%=\n\t"
  "LPBa_%=:\n\t"
  "v_not_b32 v24, s34\n\t"
  "buffer_load_dwordx2 v[24:25], v11, s[20:23], 0 offen offset:8 sc1\n\t"
  "s_mov_b32 s73, 64\n\t"
  "LPBw_%=:\n\t"
  "s_sleep 1\n\t"
  "LPBc_%=:\n\t"
  "v_cmp_eq_u32 vcc, s68, v24\n\t"
  "s_nop 1\n\t"
  "s_cmp_eq_u64 vcc, 0\n\t"
  "v_cmp_ne_u64 vcc, s[34:35], v[24:25]\n\t"
  "s_cbranch_vccz LPBb_%=\n\t"
  "s_cbranch_scc1 LPBr_%=\n\t"
  "s_sub_u32 s73, s73, 1\n\t"
  "s_cbranch_scc0 LPBw_%=\n\t"
  "s_waitcnt vmcnt(0)\n\t"
  "LPBr_%=:\n\t"
  "s_sleep 2\n\t"
  "s_sub_u32 s72, s72, 1\n\t"
  "s_cbranch_scc0 LPBa_%=\n\t"
  "LPBb_%=:\n\t"
  "s_add_u32 s57, s57, 0xfff98000\n\t"
  "v_mfma_f32_16x16x32_bf16 v[36:39], a[144:147], a[204:207], v[36:39]\n\t"
  "s_barrier\n\t"
  "v_mfma_f32_16x16x32_bf16 v[44:47], a[148:151], a[204:207], v[44:47]\n\t"
  "ds_read_b128 a[140:143], v23 offset:64512\n\t"
  "s_waitcnt lgkmcnt(4)\n\t"
  "v_mfma_f32_16x16x32_bf16 v[40:43], a[148:151], a[200:203], v[40:43]\n\t"
  "buffer_store_dwordx4 v[240:243], v6, s[20:23], s45 offen offset:0 sc1\n\t"
  "v_mfma_f32_16x16x32_bf16 v[48:51], a[152:155], a[200:203], v[48:51]\n\t"
  "ds_read_b128 a[144:147], v23 offset:32768\n\t"
  "v_mfma_f32_16x16x32_bf16 v[52:55], a[152:155], a[204:207], v[52:55]\n\t"
  "buffer_load_dwordx4 a[192:195], v13, s[20:23], s56 offen offset:0 sc1\n\t"
  "v_mfma_f32_16x16x32_bf16 v[60:63], a[156:159], a[204:207], v[60:63]\n\t"
  "ds_read_b128 a[148:151], v23 offset:34816\n\t"
  "s_add_u32 m0, s58, 0xc000\n\t"
  "s_waitcnt lgkmcnt(4)\n\t"
  "v_mfma_f32_16x16x32_bf16 v[56:59], a[156:159], a[200:203], v[56:59]\n\t"
  "buffer_load_dwordx4 v14, s[20:23], s57 offen offset:0 sc1 lds\n\t"
  "v_mfma_f32_16x16x32_bf16 v[64:67], a[128:131], a[200:203], v[64:67]\n\t"
  "ds_read_b128 a[152:155], v23 offset:36864\n\t"
  "v_mfma_f32_16x16x32_bf16 v[68:71], a[128:131], a[204:207], v[68:71]\n\t"
  "v_mfma_f32_16x16x32_bf16 v[76:79], a[132:135], a[204:207], v[76:79]\n\t"
  "ds_read_b128 a[156:159], v23 offset:38912\n\t"
  "v_mfma_f32_16x16x32_bf16 v[72:75], a[132:135], a[200:203], v[72:75]\n\t"
  "s_waitcnt lgkmcnt(4)\n\t"
  "v_mfma_f32_16x16x32_bf16 v[80:83], a[136:139], a[200:203], v[80:83]\n\t"
  "ds_read_b128 a[128:131], v23 offset:33792\n\t"
  "v_mfma_f32_16x16x32_bf16 v[84:87], a[136:139], a[204:207], v[84:87]\n\t"
  "buffer_load_dwordx4 a[196:199], v13, s[20:23], s56 offen offset:2048 sc1\n\t"
  "v_mfma_f32_16x16x32_bf16 v[92:95], a[140:143], a[204:207], v[92:95]\n\t"
  "ds_read_b128 a[132:135], v23 offset:35840\n\t"
  "s_add_u32 s56, s56, 0x80000\n\t"
  "s_waitcnt lgkmcnt(4)\n\t"
  "v_mfma_f32_16x16x32_bf16 v[88:91], a[140:143], a[200:203], v[88:91]\n\t"
  "buffer_load_dwordx4 v15, s[20:23], s57 offen offset:1024 sc1 lds\n\t"
  "v_mfma_f32_16x16x32_bf16 v[32:35], a[144:147], a[208:211], v[32:35]\n\t"
  "ds_read_b128 a[136:139], v23 offset:37888\n\t"
  "s_add_u32 s57, s57, 0x8000\n\t"
  "v_mfma_f32_16x16x32_bf16 v[36:39], a[144:147], a[212:215], v[36:39]\n\t"
  "v_mfma_f32_16x16x32_bf16 v[44:47], a[148:151], a[212:215], v[44:47]\n\t"
  "ds_read_b128 a[140:143], v23 offset:39936\n\t"
  "v_mfma_f32_16x16x32_bf16 v[40:43], a[148:151], a[208:211], v[40:43]\n\t"
  "s_waitcnt lgkmcnt(4)\n\t"
  "v_mfma_f32_16x16x32_bf16 v[48:51], a[152:155], a[208:211], v[48:51]\n\t"
  "ds_read_b128 a[144:147], v23 offset:40960\n\t"
  "v_mfma_f32_16x16x32_bf16 v[52:55], a[152:155], a[212:215], v[52:55]\n\t"
  "buffer_load_dwordx4 a[200:203], v13, s[20:23], s56 offen offset:0 sc1\n\t"
  "v_mfma_f32_16x16x32_bf16 v[60:63], a[156:159], a[212:215], v[60:63]\n\t"
  "ds_read_b128 a[148:151], v23 offset:43008\n\t"
  "s_add_u32 m0, s58, 0xe000\n\t"
  "s_waitcnt lgkmcnt(4)\n\t"
  "v_mfma_f32_16x16x32_bf16 v[56:59], a[156:159], a[208:211], v[56:59]\n\t"
  "buffer_load_dwordx4 v14, s[20:23], s57 offen offset:0 sc1 lds\n\t"
  "v_mfma_f32_16x16x32_bf16 v[64:67], a[128:131], a[208:211], v[64:67]\n\t"
  "ds_read_b128 a[152:155], v23 offset:45056\n\t"
  "v_mfma_f32_16x16x32_bf16 v[68:71], a[128:131], a[212:215], v[68:71]\n\t"
  "v_mfma_f32_16x16x32_bf16 v[76:79], a[132:135], a[212:215], v[76:79]\n\t"
  "ds_read_b128 a[156:159], v23 offset:47104\n\t"
  "v_mfma_f32_16x16x32_bf16 v[72:75], a[132:135], a[208:211], v[72:75]\n\t"
  "s_waitcnt lgkmcnt(4)\n\t"
  "v_mfma_f32_16x16x32_bf16 v[80:83], a[136:139], a[208:211], v[80:83]\n\t"
  "ds_read_b128 a[128:131], v23 offset:41984\n\t"
  "v_mfma_f32_16x16x32_bf16 v[84:87], a[136:139], a[212:215], v[84:87]\n\t"
  "buffer_load_dwordx4 a[204:207], v13, s[20:23], s56 offen offset:2048 sc1\n\t"
  "v_mfma_f32_16x16x32_bf16 v[92:95], a[140:143], a[212:215], v[92:95]\n\t"
  "ds_read_b128 a[132:135], v23 offset:44032\n\t"
  "s_add_u32 s56, s56, 0x60000\n\t"
  "s_waitcnt lgkmcnt(4)\n\t"
  "v_mfma_f32_16x16x32_bf16 v[88:91], a[140:143], a[208:211], v[88:91]\n\t"
  "buffer_load_dwordx4 v15, s[20:23], s57 offen offset:1024 sc1 lds\n\t"
  "v_mfma_f32_16x16x32_bf16 v[32:35], a[144:147], a[216:219], v[32:35]\n\t"
  "ds_read_b128 a[136:139], v23 offset:46080\n\t"
  "s_waitcnt vmcnt(18)\n\t"
  "s_add_u32 s57, s57, 0x6000\n\t"
  "v_mfma_f32_16x16x32_bf16 v[36:39], a[144:147], a[220:223], v[36:39]\n\t"
  "s_barrier\n\t"
  "v_mfma_f32_16x16x32_bf16 v[44:47], a[148:151], a[220:223], v[44:47]\n\t"
  "ds_read_b128 a[140:143], v23 offset:48128\n\t"
  "s_waitcnt lgkmcnt(4)\n\t"
  "v_mfma_f32_16x16x32_bf16 v[40:43], a[148:151], a[216:219], v[40:43]\n\t"
  "buffer_store_dwordx4 v[248:251], v6, s[20:23], s45 offen offset:2048 sc1\n\t"
  "v_mfma_f32_16x16x32_bf16 v[48:51], a[152:155], a[216:219], v[48:51]\n\t"
  "ds_read_b128 a[144:147], v21 offset:16384\n\t"
  "v_mfma_f32_16x16x32_bf16 v[52:55], a[152:155], a[220:223], v[52:55]\n\t"
  "buffer_load_dwordx4 a[208:211], v13, s[20:23], s56 offen offset:0 sc1\n\t"
  "v_mfma_f32_16x16x32_bf16 v[60:63], a[156:159], a[220:223], v[60:63]\n\t"
  "ds_read_b128 a[148:151], v21 offset:18432\n\t"
  "s_add_u32 m0, s58, 0x0\n\t"
  "s_waitcnt lgkmcnt(4)\n\t"
  "v_mfma_f32_16x16x32_bf16 v[56:59], a[156:159], a[216:219], v[56:59]\n\t"
  "buffer_load_dwordx4 v14, s[20:23], s57 offen offset:0 sc1 lds\n\t"
  "v_mfma_f32_16x16x32_bf16 v[64:67], a[128:131], a[216:219], v[64:67]\n\t"
  "ds_read_b128 a[152:155], v21 offset:20480\n\t"
  "v_mfma_f32_16x16x32_bf16 v[68:71], a[128:131], a[220:223], v[68:71]\n\t"
  "v_mfma_f32_16x16x32_bf16 v[76:79], a[132:135], a[220:223], v[76:79]\n\t"
  "ds_read_b128 a[156:159], v21 offset:22528\n\t"
  "v_mfma_f32_16x16x32_bf16 v[72:75], a[132:135], a[216:219], v[72:75]\n\t"
  "s_waitcnt lgkmcnt(4)\n\t"
  "v_mfma_f32_16x16x32_bf16 v[80:83], a[136:139], a[216:219], v[80:83]\n\t"
  "ds_read_b128 a[128:131], v21 offset:17408\n\t"
  "v_mfma_f32_16x16x32_bf16 v[84:87], a[136:139], a[220:223], v[84:87]\n\t"
  "buffer_load_dwordx4 a[212:215], v13, s[20:23], s56 offen offset:2048 sc1\n\t"
  "v_mfma_f32_16x16x32_bf16 v[92:95], a[140:143], a[220:223], v[92:95]\n\t"
  "ds_read_b128 a[132:135], v21 offset:19456\n\t"
  "s_add_u32 s56, s56, 0x80000\n\t"
  "s_waitcnt lgkmcnt(4)\n\t"
  "v_mfma_f32_16x16x32_bf16 v[88:91], a[140:143], a[216:219], v[88:91]\n\t"
  "buffer_load_dwordx4 v15, s[20:23], s57 offen offset:1024 sc1 lds\n\t"
  "v_mfma_f32_16x16x32_bf16 v[32:35], a[144:147], a[224:227], v[32:35]\n\t"
  "ds_read_b128 a[136:139], v21 offset:21504\n\t"
  "s_add_u32 s57, s57, 0x8000\n\t"
  "v_mfma_f32_16x16x32_bf16 v[36:39], a[144:147], a[228:231], v[36:39]\n\t"
  "v_mfma_f32_16x16x32_bf16 v[44:47], a[148:151], a[228:231], v[44:47]\n\t"
  "ds_read_b128 a[140:143], v21 offset:23552\n\t"
  "v_mfma_f32_16x16x32_bf16 v[40:43], a[148:151], a[224:227], v[40:43]\n\t"
  "s_waitcnt lgkmcnt(4)\n\t"
  "v_mfma_f32_16x16x32_bf16 v[48:51], a[152:155], a[224:227], v[48:51]\n\t"
  "ds_read_b128 a[144:147], v21 offset:24576\n\t"
  "v_mfma_f32_16x16x32_bf16 v[52:55], a[152:155], a[228:231], v[52:55]\n\t"
  "buffer_load_dwordx4 a[216:219], v13, s[20:23], s56 offen offset:0 sc1\n\t"
  "v_mfma_f32_16x16x32_bf16 v[60:63], a[156:159], a[228:231], v[60:63]\n\t"
  "ds_read_b128 a[148:151], v21 offset:26624\n\t"
  "s_add_u32 m0, s58, 0x2000\n\t"
  "s_waitcnt lgkmcnt(4)\n\t"
  "v_mfma_f32_16x16x32_bf16 v[56:59], a[156:159], a[224:227], v[56:59]\n\t"
  "buffer_load_dwordx4 v14, s[20:23], s57 offen offset:0 sc1 lds\n\t"
  "v_mfma_f32_16x16x32_bf16 v[64:67], a[128:131], a[224:227], v[64:67]\n\t"
  "ds_read_b128 a[152:155], v21 offset:28672\n\t"
  "v_mfma_f32_16x16x32_bf16 v[68:71], a[128:131], a[228:231], v[68:71]\n\t"
  "v_mfma_f32_16x16x32_bf16 v[76:79], a[132:135], a[228:231], v[76:79]\n\t"
  "ds_read_b128 a[156:159], v21 offset:30720\n\t"
  "v_mfma_f32_16x16x32_bf16 v[72:75], a[132:135], a[224:227], v[72:75]\n\t"
  "s_waitcnt lgkmcnt(4)\n\t"
  "v_mfma_f32_16x16x32_bf16 v[80:83], a[136:139], a[224:227], v[80:83]\n\t"
  "ds_read_b128 a[128:131], v21 offset:25600\n\t"
  "v_mfma_f32_16x16x32_bf16 v[84:87], a[136:139], a[228:231], v[84:87]\n\t"
  "buffer_load_dwordx4 a[220:223], v13, s[20:23], s56 offen offset:2048 sc1\n\t"
  "v_mfma_f32_16x16x32_bf16 v[92:95], a[140:143], a[228:231], v[92:95]\n\t"
  "ds_read_b128 a[132:135], v21 offset:27648\n\t"
  "s_add_u32 s56, s56, 0x60000\n\t"
  "s_waitcnt lgkmcnt(4)\n\t"
  "v_mfma_f32_16x16x32_bf16 v[88:91], a[140:143], a[224:227], v[88:91]\n\t"
  "buffer_load_dwordx4 v15, s[20:23], s57 offen offset:1024 sc1 lds\n\t"
  "v_mfma_f32_16x16x32_bf16 v[32:35], a[144:147], a[232:235], v[32:35]\n\t"
  "ds_read_b128 a[136:139], v21 offset:29696\n\t"
  "s_waitcnt vmcnt(18)\n\t"
  "s_add_u32 s57, s57, 0x6000\n\t"
  "v_mfma_f32_16x16x32_bf16 v[36:39], a[144:147], a[236:239], v[36:39]\n\t"
  "s_barrier\n\t"
  "v_mfma_f32_16x16x32_bf16 v[44:47], a[148:151], a[236:239], v[44:47]\n\t"
  "ds_read_b128 a[140:143], v21 offset:31744\n\t"
  "v_mfma_f32_16x16x32_bf16 v[40:43], a[148:151], a[232:235], v[40:43]\n\t"
  "s_waitcnt lgkmcnt(4)\n\t"
  "v_mfma_f32_16x16x32_bf16 v[48:51], a[152:155], a[232:235], v[48:51]\n\t"
  "ds_read_b128 a[144:147], v21 offset:32768\n\t"
  "v_mfma_f32_16x16x32_bf16 v[52:55], a[152:155], a[236:239], v[52:55]\n\t"
  "buffer_load_dwordx4 a[224:227], v13, s[20:23], s56 offen offset:0 sc1\n\t"
  "v_mfma_f32_16x16x32_bf16 v[60:63], a[156:159], a[236:239], v[60:63]\n\t"
  "ds_read_b128 a[148:151], v21 offset:34816\n\t"
  "s_add_u32 m0, s58, 0xffffc000\n\t"
  "s_waitcnt lgkmcnt(4)\n\t"
  "v_mfma_f32_16x16x32_bf16 v[56:59], a[156:159], a[232:235], v[56:59]\n\t"
  "buffer_load_dwordx4 v14, s[20:23], s57 offen offset:0 sc1 lds\n\t"
  "v_mfma_f32_16x16x32_bf16 v[64:67], a[128:131], a[232:235], v[64:67]\n\t"
  "ds_read_b128 a[152:155], v21 offset:36864\n\t"
  "v_mfma_f32_16x16x32_bf16 v[68:71], a[128:131], a[236:239], v[68:71]\n\t"
  "v_mfma_f32_16x16x32_bf16 v[76:79], a[132:135], a[236:239], v[76:79]\n\t"
  "ds_read_b128 a[156:159], v21 offset:38912\n\t"
  "v_mfma_f32_16x16x32_bf16 v[72:75], a[132:135], a[232:235], v[72:75]\n\t"
  "s_waitcnt lgkmcnt(4)\n\t"
  "v_mfma_f32_16x16x32_bf16 v[80:83], a[136:139], a[232:235], v[80:83]\n\t"
  "ds_read_b128 a[128:131], v21 offset:33792\n\t"
  "v_mfma_f32_16x16x32_bf16 v[84:87], a[136:139], a[236:239], v[84:87]\n\t"
  "buffer_load_dwordx4 a[228:231], v13, s[20:23], s56 offen offset:2048 sc1\n\t"
  "v_mfma_f32_16x16x32_bf16 v[92:95], a[140:143], a[236:239], v[92:95]\n\t"
  "ds_read_b128 a[132:135], v21 offset:35840\n\t"
  "s_add_u32 s56, s56, 0x80000\n\t"
  "s_waitcnt lgkmcnt(4)\n\t"
  "v_mfma_f32_16x16x32_bf16 v[88:91], a[140:143], a[232:235], v[88:91]\n\t"
  "buffer_load_dwordx4 v15, s[20:23], s57 offen offset:1024 sc1 lds\n\t"
  "v_mfma_f32_16x16x32_bf16 v[32:35], a[144:147], a[240:243], v[32:35]\n\t"
  "ds_read_b128 a[136:139], v21 offset:37888\n\t"
  "s_add_u32 s57, s57, 0x8000\n\t"
  "v_mfma_f32_16x16x32_bf16 v[36:39], a[144:147], a[244:247], v[36:39]\n\t"
  "v_mfma_f32_16x16x32_bf16 v[44:47], a[148:151], a[244:247], v[44:47]\n\t"
  "ds_read_b128 a[140:143], v21 offset:39936\n\t"
  "v_mfma_f32_16x16x32_bf16 v[40:43], a[148:151], a[240:243], v[40:43]\n\t"
  "s_waitcnt lgkmcnt(4)\n\t"
  "v_mfma_f32_16x16x32_bf16 v[48:51], a[152:155], a[240:243], v[48:51]\n\t"
  "ds_read_b128 a[144:147], v21 offset:40960\n\t"
  "v_mfma_f32_16x16x32_bf16 v[52:55], a[152:155], a[244:247], v[52:55]\n\t"
  "buffer_load_dwordx4 a[232:235], v13, s[20:23], s56 offen offset:0 sc1\n\t"
  "v_mfma_f32_16x16x32_bf16 v[60:63], a[156:159], a[244:247], v[60:63]\n\t"
  "ds_read_b128 a[148:151], v21 offset:43008\n\t"
  "s_add_u32 m0, s58, 0xffffe000\n\t"
  "s_waitcnt lgkmcnt(4)\n\t"
  "v_mfma_f32_16x16x32_bf16 v[56:59], a[156:159], a[240:243], v[56:59]\n\t"
  "buffer_load_dwordx4 v14, s[20:23], s57 offen offset:0 sc1 lds\n\t"
  "v_mfma_f32_16x16x32_bf16 v[64:67], a[128:131], a[240:243], v[64:67]\n\t"
  "ds_read_b128 a[152:155], v21 offset:45056\n\t"
  "v_mfma_f32_16x16x32_bf16 v[68:71], a[128:131], a[244:247], v[68:71]\n\t"
  "v_mfma_f32_16x16x32_bf16 v[76:79], a[132:135], a[244:247], v[76:79]\n\t"
  "ds_read_b128 a[156:159], v21 offset:47104\n\t"
  "v_mfma_f32_16x16x32_bf16 v[72:75], a[132:135], a[240:243], v[72:75]\n\t"
  "s_waitcnt lgkmcnt(4)\n\t"
  "v_mfma_f32_16x16x32_bf16 v[80:83], a[136:139], a[240:243], v[80:83]\n\t"
  "ds_read_b128 a[128:131], v21 offset:41984\n\t"
  "v_mfma_f32_16x16x32_bf16 v[84:87], a[136:139], a[244:247], v[84:87]\n\t"
  "buffer_load_dwordx4 a[236:239], v13, s[20:23], s56 offen offset:2048 sc1\n\t"
  "v_mfma_f32_16x16x32_bf16 v[92:95], a[140:143], a[244:247], v[92:95]\n\t"
  "ds_read_b128 a[132:135], v21 offset:44032\n\t"
  "s_add_u32 s56, s56, 0x60000\n\t"
  "s_waitcnt lgkmcnt(4)\n\t"
  "v_mfma_f32_16x16x32_bf16 v[88:91], a[140:143], a[240:243], v[88:91]\n\t"
  "buffer_load_dwordx4 v15, s[20:23], s57 offen offset:1024 sc1 lds\n\t"
  "v_mfma_f32_16x16x32_bf16 v[32:35], a[144:147], a[248:251], v[32:35]\n\t"
  "ds_read_b128 a[136:139], v21 offset:46080\n\t"
  "s_waitcnt vmcnt(17)\n\t"
  "s_add_u32 s57, s57, 0x6000\n\t"
  "v_mfma_f32_16x16x32_bf16 v[36:39], a[144:147], a[252:255], v[36:39]\n\t"
  "s_barrier\n\t"
  "v_mfma_f32_16x16x32_bf16 v[44:47], a[148:151], a[252:255], v[44:47]\n\t"
  "ds_read_b128 a[140:143], v21 offset:48128\n\t"
  "v_mfma_f32_16x16x32_bf16 v[40:43], a[148:151], a[248:251], v[40:43]\n\t"
  "s_waitcnt lgkmcnt(4)\n\t"
  "v_mfma_f32_16x16x32_bf16 v[48:51], a[152:155], a[248:251], v[48:51]\n\t"
  "ds_read_b128 a[144:147], v21 offset:49152\n\t"
  "v_mfma_f32_16x16x32_bf16 v[52:55], a[152:155], a[252:255], v[52:55]\n\t"
  "buffer_load_dwordx4 a[240:243], v13, s[20:23], s56 offen offset:0 sc1\n\t"
  "v_mfma_f32_16x16x32_bf16 v[60:63], a[156:159], a[252:255], v[60:63]\n\t"
  "ds_read_b128 a[148:151], v21 offset:51200\n\t"
  "s_add_u32 m0, s58, 0xffff8000\n\t"
  "s_waitcnt lgkmcnt(4)\n\t"
  "v_mfma_f32_16x16x32_bf16 v[56:59], a[156:159], a[248:251], v[56:59]\n\t"
  "buffer_load_dwordx4 v14, s[20:23], s57 offen offset:0 sc1 lds\n\t"
  "v_mfma_f32_16x16x32_bf16 v[64:67], a[128:131], a[248:251], v[64:67]\n\t"
  "ds_read_b128 a[152:155], v21 offset:53248\n\t"
  "v_mfma_f32_16x16x32_bf16 v[68:71], a[128:131], a[252:255], v[68:71]\n\t"
  "v_mfma_f32_16x16x32_bf16 v[76:79], a[132:135], a[252:255], v[76:79]\n\t"
  "ds_read_b128 a[156:159], v21 offset:55296\n\t"
  "v_mfma_f32_16x16x32_bf16 v[72:75], a[132:135], a[248:251], v[72:75]\n\t"
  "s_waitcnt lgkmcnt(4)\n\t"
  "v_mfma_f32_16x16x32_bf16 v[80:83], a[136:139], a[248:251], v[80:83]\n\t"
  "ds_read_b128 a[128:131], v21 offset:50176\n\t"
  "v_mfma_f32_16x16x32_bf16 v[84:87], a[136:139], a[252:255], v[84:87]\n\t"
  "buffer_load_dwordx4 a[244:247], v13, s[20:23], s56 offen offset:2048 sc1\n\t"
  "v_mfma_f32_16x16x32_bf16 v[92:95], a[140:143], a[252:255], v[92:95]\n\t"
  "ds_read_b128 a[132:135], v21 offset:52224\n\t"
  "s_add_u32 s56, s56, 0x80000\n\t"
  "s_waitcnt lgkmcnt(4)\n\t"
  "v_mfma_f32_16x16x32_bf16 v[88:91], a[140:143], a[248:251], v[88:91]\n\t"
  "buffer_load_dwordx4 v15, s[20:23], s57 offen offset:1024 sc1 lds\n\t"
  "v_mfma_f32_16x16x32_bf16 v[32:35], a[144:147], a[192:195], v[32:35]\n\t"
  "ds_read_b128 a[136:139], v21 offset:54272\n\t"
  "s_add_u32 s57, s57, 0x8000\n\t"
  "v_mfma_f32_16x16x32_bf16 v[36:39], a[144:147], a[196:199], v[36:39]\n\t"
  "v_mfma_f32_16x16x32_bf16 v[44:47], a[148:151], a[196:199], v[44:47]\n\t"
  "ds_read_b128 a[140:143], v21 offset:56320\n\t"
  "v_mfma_f32_16x16x32_bf16 v[40:43], a[148:151], a[192:195], v[40:43]\n\t"
  "s_waitcnt lgkmcnt(4)\n\t"
  "v_mfma_f32_16x16x32_bf16 v[48:51], a[152:155], a[192:195], v[48:51]\n\t"
  "ds_read_b128 a[144:147], v21 offset:57344\n\t"
  "v_mfma_f32_16x16x32_bf16 v[52:55], a[152:155], a[196:199], v[52:55]\n\t"
  "buffer_load_dwordx4 a[248:251], v13, s[20:23], s56 offen offset:0 sc1\n\t"
  "v_mfma_f32_16x16x32_bf16 v[60:63], a[156:159], a[196:199], v[60:63]\n\t"
  "ds_read_b128 a[148:151], v21 offset:59392\n\t"
  "s_add_u32 m0, s58, 0xffffa000\n\t"
  "s_waitcnt vmcnt(21)\n\t"
  "buffer_store_dwordx2 v[16:17], v10, s[20:23], 0 offen offset:16 sc1\n\t"
  "v_mfma_f32_16x16x32_bf16 v[56:59], a[156:159], a[192:195], v[56:59]\n\t"
  "buffer_load_dwordx4 v14, s[20:23], s57 offen offset:0 sc1 lds\n\t"
  "s_waitcnt lgkmcnt(4)\n\t"
  "v_mfma_f32_16x16x32_bf16 v[64:67], a[128:131], a[192:195], v[64:67]\n\t"
  "ds_read_b128 a[152:155], v21 offset:61440\n\t"
  "v_mfma_f32_16x16x32_bf16 v[68:71], a[128:131], a[196:199], v[68:71]\n\t"
  "v_mfma_f32_16x16x32_bf16 v[76:79], a[132:135], a[196:199], v[76:79]\n\t"
  "ds_read_b128 a[156:159], v21 offset:63488\n\t"
  "v_mfma_f32_16x16x32_bf16 v[72:75], a[132:135], a[192:195], v[72:75]\n\t"
  "s_waitcnt lgkmcnt(4)\n\t"
  "v_mfma_f32_16x16x32_bf16 v[80:83], a[136:139], a[192:195], v[80:83]\n\t"
  "ds_read_b128 a[128:131], v21 offset:58368\n\t"
  "v_mfma_f32_16x16x32_bf16 v[84:87], a[136:139], a[196:199], v[84:87]\n\t"
  "buffer_load_dwordx4 a[252:255], v13, s[20:23], s56 offen offset:2048 sc1\n\t"
  "v_mfma_f32_16x16x32_bf16 v[92:95], a[140:143], a[196:199], v[92:95]\n\t"
  "ds_read_b128 a[132:135], v21 offset:60416\n\t"
  "s_add_u32 s56, s56, 0x60000\n\t"
  "s_waitcnt lgkmcnt(4)\n\t"
  "v_mfma_f32_16x16x32_bf16 v[88:91], a[140:143], a[192:195], v[88:91]\n\t"
  "buffer_load_dwordx4 v15, s[20:23], s57 offen offset:1024 sc1 lds\n\t"
  "v_mfma_f32_16x16x32_bf16 v[32:35], a[144:147], a[200:203], v[32:35]\n\t"
  "ds_read_b128 a[136:139], v21 offset:62464\n\t"
  "s_waitcnt vmcnt(17)\n\t"
  "s_add_u32 s57, s57, 0x6000\n\t"
  "v_mfma_f32_16x16x32_bf16 v[36:39], a[144:147], a[204:207], v[36:39]\n\t"
  "s_barrier\n\t"
  "v_mfma_f32_16x16x32_bf16 v[44:47], a[148:151], a[204:207], v[44:47]\n\t"
  "ds_read_b128 a[140:143], v21 offset:64512\n\t"
  "v_mfma_f32_16x16x32_bf16 v[40:43], a[148:151], a[200:203], v[40:43]\n\t"
  "s_waitcnt lgkmcnt(4)\n\t"
  "v_mfma_f32_16x16x32_bf16 v[48:51], a[152:155], a[200:203], v[48:51]\n\t"
  "ds_read_b128 a[144:147], v21 offset:0\n\t"
  "v_mfma_f32_16x16x32_bf16 v[52:55], a[152:155], a[204:207], v[52:55]\n\t"
  "buffer_load_dwordx4 a[192:195], v13, s[20:23], s56 offen offset:0 sc1\n\t"
  "v_mfma_f32_16x16x32_bf16 v[60:63], a[156:159], a[204:207], v[60:63]\n\t"
  "ds_read_b128 a[148:151], v21 offset:2048\n\t"
  "s_add_u32 m0, s58, 0x4000\n\t"
  "s_waitcnt lgkmcnt(4)\n\t"
  "v_mfma_f32_16x16x32_bf16 v[56:59], a[156:159], a[200:203], v[56:59]\n\t"
  "buffer_load_dwordx4 v14, s[20:23], s57 offen offset:0 sc1 lds\n\t"
  "v_mfma_f32_16x16x32_bf16 v[64:67], a[128:131], a[200:203], v[64:67]\n\t"
  "ds_read_b128 a[152:155], v21 offset:4096\n\t"
  "v_mfma_f32_16x16x32_bf16 v[68:71], a[128:131], a[204:207], v[68:71]\n\t"
  "v_mfma_f32_16x16x32_bf16 v[76:79], a[132:135], a[204:207], v[76:79]\n\t"
  "ds_read_b128 a[156:159], v21 offset:6144\n\t"
  "v_mfma_f32_16x16x32_bf16 v[72:75], a[132:135], a[200:203], v[72:75]\n\t"
  "s_waitcnt lgkmcnt(4)\n\t"
  "v_mfma_f32_16x16x32_bf16 v[80:83], a[136:139], a[200:203], v[80:83]\n\t"
  "ds_read_b128 a[128:131], v21 offset:1024\n\t"
  "v_mfma_f32_16x16x32_bf16 v[84:87], a[136:139], a[204:207], v[84:87]\n\t"
  "buffer_load_dwordx4 a[196:199], v13, s[20:23], s56 offen offset:2048 sc1\n\t"
  "v_mfma_f32_16x16x32_bf16 v[92:95], a[140:143], a[204:207], v[92:95]\n\t"
  "ds_read_b128 a[132:135], v21 offset:3072\n\t"
  "s_add_u32 s56, s56, 0x80000\n\t"
  "s_waitcnt lgkmcnt(4)\n\t"
  "v_mfma_f32_16x16x32_bf16 v[88:91], a[140:143], a[200:203], v[88:91]\n\t"
  "buffer_load_dwordx4 v15, s[20:23], s57 offen offset:1024 sc1 lds\n\t"
  "v_mfma_f32_16x16x32_bf16 v[32:35], a[144:147], a[208:211], v[32:35]\n\t"
  "ds_read_b128 a[136:139], v21 offset:5120\n\t"
  "s_add_u32 s57, s57, 0x8000\n\t"
  "v_mfma_f32_16x16x32_bf16 v[36:39], a[144:147], a[212:215], v[36:39]\n\t"
  "v_mfma_f32_16x16x32_bf16 v[44:47], a[148:151], a[212:215], v[44:47]\n\t"
  "ds_read_b128 a[140:143], v21 offset:7168\n\t"
  "v_mfma_f32_16x16x32_bf16 v[40:43], a[148:151], a[208:211], v[40:43]\n\t"
  "s_waitcnt lgkmcnt(4)\n\t"
  "v_mfma_f32_16x16x32_bf16 v[48:51], a[152:155], a[208:211], v[48:51]\n\t"
  "ds_read_b128 a[144:147], v21 offset:8192\n\t"
  "v_mfma_f32_16x16x32_bf16 v[52:55], a[152:155], a[212:215], v[52:55]\n\t"
  "buffer_load_dwordx4 a[200:203], v13, s[20:23], s56 offen offset:0 sc1\n\t"
  "v_mfma_f32_16x16x32_bf16 v[60:63], a[156:159], a[212:215], v[60:63]\n\t"
  "ds_read_b128 a[148:151], v21 offset:10240\n\t"
  "s_add_u32 m0, s58, 0x6000\n\t"
  "s_waitcnt lgkmcnt(4)\n\t"
  "v_mfma_f32_16x16x32_bf16 v[56:59], a[156:159], a[208:211], v[56:59]\n\t"
  "buffer_load_dwordx4 v14, s[20:23], s57 offen offset:0 sc1 lds\n\t"
  "v_mfma_f32_16x16x32_bf16 v[64:67], a[128:131], a[208:211], v[64:67]\n\t"
  "ds_read_b128 a[152:155], v21 offset:12288\n\t"
  "v_mfma_f32_16x16x32_bf16 v[68:71], a[128:131], a[212:215], v[68:71]\n\t"
  "v_mfma_f32_16x16x32_bf16 v[76:79], a[132:135], a[212:215], v[76:79]\n\t"
  "ds_read_b128 a[156:159], v21 offset:14336\n\t"
  "v_mfma_f32_16x16x32_bf16 v[72:75], a[132:135], a[208:211], v[72:75]\n\t"
  "s_waitcnt lgkmcnt(4)\n\t"
  "v_mfma_f32_16x16x32_bf16 v[80:83], a[136:139], a[208:211], v[80:83]\n\t"
  "ds_read_b128 a[128:131], v21 offset:9216\n\t"
  "v_mfma_f32_16x16x32_bf16 v[84:87], a[136:139], a[212:215], v[84:87]\n\t"
  "buffer_load_dwordx4 a[204:207], v13, s[20:23], s56 offen offset:2048 sc1\n\t"
  "v_mfma_f32_16x16x32_bf16 v[92:95], a[140:143], a[212:215], v[92:95]\n\t"
  "ds_read_b128 a[132:135], v21 offset:11264\n\t"
  "s_add_u32 s56, s56, 0x60000\n\t"
  "s_waitcnt lgkmcnt(4)\n\t"
  "v_mfma_f32_16x16x32_bf16 v[88:91], a[140:143], a[208:211], v[88:91]\n\t"
  "buffer_load_dwordx4 v15, s[20:23], s57 offen offset:1024 sc1 lds\n\t"
  "v_mfma_f32_16x16x32_bf16 v[32:35], a[144:147], a[216:219], v[32:35]\n\t"
  "ds_read_b128 a[136:139], v21 offset:13312\n\t"
  "s_waitcnt vmcnt(17)\n\t"
  "s_add_u32 s57, s57, 0x6000\n\t"
  "v_mfma_f32_16x16x32_bf16 v[36:39], a[144:147], a[220:223], v[36:39]\n\t"
  "s_barrier\n\t"
  "v_mfma_f32_16x16x32_bf16 v[44:47], a[148:151], a[220:223], v[44:47]\n\t"
  "ds_read_b128 a[140:143], v21 offset:15360\n\t"
  "v_mfma_f32_16x16x32_bf16 v[40:43], a[148:151], a[216:219], v[40:43]\n\t"
  "s_waitcnt lgkmcnt(4)\n\t"
  "v_mfma_f32_16x16x32_bf16 v[48:51], a[152:155], a[216:219], v[48:51]\n\t"
  "ds_read_b128 a[144:147], v23 offset:49152\n\t"
  "v_mfma_f32_16x16x32_bf16 v[52:55], a[152:155], a[220:223], v[52:55]\n\t"
  "buffer_load_dwordx4 a[208:211], v13, s[20:23], s56 offen offset:0 sc1\n\t"
  "v_mfma_f32_16x16x32_bf16 v[60:63], a[156:159], a[220:223], v[60:63]\n\t"
  "ds_read_b128 a[148:151], v23 offset:51200\n\t"
  "s_add_u32 m0, s58, 0x8000\n\t"
  "s_waitcnt lgkmcnt(4)\n\t"
  "v_mfma_f32_16x16x32_bf16 v[56:59], a[156:159], a[216:219], v[56:59]\n\t"
  "buffer_load_dwordx4 v14, s[20:23], s57 offen offset:0 sc1 lds\n\t"
  "v_mfma_f32_16x16x32_bf16 v[64:67], a[128:131], a[216:219], v[64:67]\n\t"
  "ds_read_b128 a[152:155], v23 offset:53248\n\t"
  "v_mfma_f32_16x16x32_bf16 v[68:71], a[128:131], a[220:223], v[68:71]\n\t"
  "v_mfma_f32_16x16x32_bf16 v[76:79], a[132:135], a[220:223], v[76:79]\n\t"
  "ds_read_b128 a[156:159], v23 offset:55296\n\t"
  "v_mfma_f32_16x16x32_bf16 v[72:75], a[132:135], a[216:219], v[72:75]\n\t"
  "s_waitcnt lgkmcnt(4)\n\t"
  "v_mfma_f32_16x16x32_bf16 v[80:83], a[136:139], a[216:219], v[80:83]\n\t"
  "ds_read_b128 a[128:131], v23 offset:50176\n\t"
  "v_mfma_f32_16x16x32_bf16 v[84:87], a[136:139], a[220:223], v[84:87]\n\t"
  "buffer_load_dwordx4 a[212:215], v13, s[20:23], s56 offen offset:2048 sc1\n\t"
  "v_mfma_f32_16x16x32_bf16 v[92:95], a[140:143], a[220:223], v[92:95]\n\t"
  "ds_read_b128 a[132:135], v23 offset:52224\n\t"
  "s_add_u32 s56, s56, 0x80000\n\t"
  "s_waitcnt lgkmcnt(4)\n\t"
  "v_mfma_f32_16x16x32_bf16 v[88:91], a[140:143], a[216:219], v[88:91]\n\t"
  "buffer_load_dwordx4 v15, s[20:23], s57 offen offset:1024 sc1 lds\n\t"
  "v_mfma_f32_16x16x32_bf16 v[32:35], a[144:147], a[224:227], v[32:35]\n\t"
  "ds_read_b128 a[136:139], v23 offset:54272\n\t"
  "s_add_u32 s57, s57, 0x8000\n\t"
  "v_mfma_f32_16x16x32_bf16 v[36:39], a[144:147], a[228:231], v[36:39]\n\t"
  "global_load_dword v26, v7, s[70:71]\n\t"
  "v_mfma_f32_16x16x32_bf16 v[44:47], a[148:151], a[228:231], v[44:47]\n\t"
  "ds_read_b128 a[140:143], v23 offset:56320\n\t"
  "v_mfma_f32_16x16x32_bf16 v[40:43], a[148:151], a[224:227], v[40:43]\n\t"
  "s_waitcnt lgkmcnt(4)\n\t"
  "v_mfma_f32_16x16x32_bf16 v[48:51], a[152:155], a[224:227], v[48:51]\n\t"
  "ds_read_b128 a[144:147], v23 offset:57344\n\t"
  "v_mfma_f32_16x16x32_bf16 v[52:55], a[152:155], a[228:231], v[52:55]\n\t"
  "buffer_load_dwordx4 a[216:219], v13, s[20:23], s56 offen offset:0 sc1\n\t"
  "v_mfma_f32_16x16x32_bf16 v[60:63], a[156:159], a[228:231], v[60:63]\n\t"
  "ds_read_b128 a[148:151], v23 offset:59392\n\t"
  "s_add_u32 m0, s58, 0xa000\n\t"
  "s_waitcnt lgkmcnt(4)\n\t"
  "v_mfma_f32_16x16x32_bf16 v[56:59], a[156:159], a[224:227], v[56:59]\n\t"
  "buffer_load_dwordx4 v14, s[20:23], s57 offen offset:0 sc1 lds\n\t"
  "v_mfma_f32_16x16x32_bf16 v[64:67], a[128:131], a[224:227], v[64:67]\n\t"
  "ds_read_b128 a[152:155], v23 offset:61440\n\t"
  "v_mfma_f32_16x16x32_bf16 v[68:71], a[128:131], a[228:231], v[68:71]\n\t"
  "v_mfma_f32_16x16x32_bf16 v[76:79], a[132:135], a[228:231], v[76:79]\n\t"
  "ds_read_b128 a[156:159], v23 offset:63488\n\t"
  "v_mfma_f32_16x16x32_bf16 v[72:75], a[132:135], a[224:227], v[72:75]\n\t"
  "s_waitcnt lgkmcnt(4)\n\t"
  "v_mfma_f32_16x16x32_bf16 v[80:83], a[136:139], a[224:227], v[80:83]\n\t"
  "ds_read_b128 a[128:131], v23 offset:58368\n\t"
  "v_mfma_f32_16x16x32_bf16 v[84:87], a[136:139], a[228:231], v[84:87]\n\t"
  "buffer_load_dwordx4 a[220:223], v13, s[20:23], s56 offen offset:2048 sc1\n\t"
  "v_mfma_f32_16x16x32_bf16 v[92:95], a[140:143], a[228:231], v[92:95]\n\t"
  "ds_read_b128 a[132:135], v23 offset:60416\n\t"
  "s_add_u32 s56, s56, 0x60000\n\t"
  "s_waitcnt lgkmcnt(4)\n\t"
  "v_mfma_f32_16x16x32_bf16 v[88:91], a[140:143], a[224:227], v[88:91]\n\t"
  "buffer_load_dwordx4 v15, s[20:23], s57 offen offset:1024 sc1 lds\n\t"
  "v_mfma_f32_16x16x32_bf16 v[32:35], a[144:147], a[232:235], v[32:35]\n\t"
  "ds_read_b128 a[136:139], v23 offset:62464\n\t"
  "s_waitcnt vmcnt(17)\n\t"
  "s_add_u32 s57, s57, 0x6000\n\t"
  "v_mfma_f32_16x16x32_bf16 v[36:39], a[144:147], a[236:239], v[36:39]\n\t"
  "s_barrier\n\t"
  "buffer_load_dwordx2 v[24:25], v12, s[20:23], 0 offen offset:16 sc1\n\t"
  "v_mfma_f32_16x16x32_bf16 v[44:47], a[148:151], a[236:239], v[44:47]\n\t"
  "ds_read_b128 a[140:143], v23 offset:64512\n\t"
  "v_mfma_f32_16x16x32_bf16 v[40:43], a[148:151], a[232:235], v[40:43]\n\t"
  "s_waitcnt lgkmcnt(4)\n\t"
  "v_mfma_f32_16x16x32_bf16 v[48:51], a[152:155], a[232:235], v[48:51]\n\t"
  "ds_read_b128 a[144:147], v23 offset:32768\n\t"
  "v_mfma_f32_16x16x32_bf16 v[52:55], a[152:155], a[236:239], v[52:55]\n\t"
  "buffer_load_dwordx4 a[224:227], v13, s[20:23], s56 offen offset:0 sc1\n\t"
  "v_mfma_f32_16x16x32_bf16 v[60:63], a[156:159], a[236:239], v[60:63]\n\t"
  "ds_read_b128 a[148:151], v23 offset:34816\n\t"
  "s_add_u32 m0, s58, 0xc000\n\t"
  "s_waitcnt lgkmcnt(4)\n\t"
  "v_mfma_f32_16x16x32_bf16 v[56:59], a[156:159], a[232:235], v[56:59]\n\t"
  "buffer_load_dwordx4 v14, s[20:23], s57 offen offset:0 sc1 lds\n\t"
  "v_mfma_f32_16x16x32_bf16 v[64:67], a[128:131], a[232:235], v[64:67]\n\t"
  "ds_read_b128 a[152:155], v23 offset:36864\n\t"
  "v_mfma_f32_16x16x32_bf16 v[68:71], a[128:131], a[236:239], v[68:71]\n\t"
  "v_mfma_f32_16x16x32_bf16 v[76:79], a[132:135], a[236:239], v[76:79]\n\t"
  "ds_read_b128 a[156:159], v23 offset:38912\n\t"
  "v_mfma_f32_16x16x32_bf16 v[72:75], a[132:135], a[232:235], v[72:75]\n\t"
  "s_waitcnt lgkmcnt(4)\n\t"
  "v_mfma_f32_16x16x32_bf16 v[80:83], a[136:139], a[232:235], v[80:83]\n\t"
  "ds_read_b128 a[128:131], v23 offset:33792\n\t"
  "v_mfma_f32_16x16x32_bf16 v[84:87], a[136:139], a[236:239], v[84:87]\n\t"
  "buffer_load_dwordx4 a[228:231], v13, s[20:23], s56 offen offset:2048 sc1\n\t"
  "v_mfma_f32_16x16x32_bf16 v[92:95], a[140:143], a[236:239], v[92:95]\n\t"
  "ds_read_b128 a[132:135], v23 offset:35840\n\t"
  "s_add_u32 s56, s56, 0x80000\n\t"
  "s_waitcnt lgkmcnt(4)\n\t"
  "v_mfma_f32_16x16x32_bf16 v[88:91], a[140:143], a[232:235], v[88:91]\n\t"
  "buffer_load_dwordx4 v15, s[20:23], s57 offen offset:1024 sc1 lds\n\t"
  "v_mfma_f32_16x16x32_bf16 v[32:35], a[144:147], a[240:243], v[32:35]\n\t"
  "ds_read_b128 a[136:139], v23 offset:37888\n\t"
  "s_add_u32 s57, s57, 0x8000\n\t"
  "v_mfma_f32_16x16x32_bf16 v[36:39], a[144:147], a[244:247], v[36:39]\n\t"
  "v_mfma_f32_16x16x32_bf16 v[44:47], a[148:151], a[244:247], v[44:47]\n\t"
  "ds_read_b128 a[140:143], v23 offset:39936\n\t"
  "v_mfma_f32_16x16x32_bf16 v[40:43], a[148:151], a[240:243], v[40:43]\n\t"
  "s_waitcnt lgkmcnt(4)\n\t"
  "v_mfma_f32_16x16x32_bf16 v[48:51], a[152:155], a[240:243], v[48:51]\n\t"
  "ds_read_b128 a[144:147], v23 offset:40960\n\t"
  "v_mfma_f32_16x16x32_bf16 v[52:55], a[152:155], a[244:247], v[52:55]\n\t"
  "buffer_load_dwordx4 a[232:235], v13, s[20:23], s56 offen offset:0 sc1\n\t"
  "v_mfma_f32_16x16x32_bf16 v[60:63], a[156:159], a[244:247], v[60:63]\n\t"
  "ds_read_b128 a[148:151], v23 offset:43008\n\t"
  "s_add_u32 m0, s58, 0xe000\n\t"
  "s_waitcnt lgkmcnt(4)\n\t"
  "v_mfma_f32_16x16x32_bf16 v[56:59], a[156:159], a[240:243], v[56:59]\n\t"
  "buffer_load_dwordx4 v14, s[20:23], s57 offen offset:0 sc1 lds\n\t"
  "v_mfma_f32_16x16x32_bf16 v[64:67], a[128:131], a[240:243], v[64:67]\n\t"
  "ds_read_b128 a[152:155], v23 offset:45056\n\t"
  "v_mfma_f32_16x16x32_bf16 v[68:71], a[128:131], a[244:247], v[68:71]\n\t"
  "v_mfma_f32_16x16x32_bf16 v[76:79], a[132:135], a[244:247], v[76:79]\n\t"
  "ds_read_b128 a[156:159], v23 offset:47104\n\t"
  "v_mfma_f32_16x16x32_bf16 v[72:75], a[132:135], a[240:243], v[72:75]\n\t"
  "s_waitcnt lgkmcnt(4)\n\t"
  "v_mfma_f32_16x16x32_bf16 v[80:83], a[136:139], a[240:243], v[80:83]\n\t"
  "ds_read_b128 a[128:131], v23 offset:41984\n\t"
  "v_mfma_f32_16x16x32_bf16 v[84:87], a[136:139], a[244:247], v[84:87]\n\t"
  "buffer_load_dwordx4 a[236:239], v13, s[20:23], s56 offen offset:2048 sc1\n\t"
  "v_mfma_f32_16x16x32_bf16 v[92:95], a[140:143], a[244:247], v[92:95]\n\t"
  "ds_read_b128 a[132:135], v23 offset:44032\n\t"
  "s_add_u32 s56, s56, 0x60000\n\t"
  "s_waitcnt lgkmcnt(4)\n\t"
  "v_mfma_f32_16x16x32_bf16 v[88:91], a[140:143], a[240:243], v[88:91]\n\t"
  "buffer_load_dwordx4 v15, s[20:23], s57 offen offset:1024 sc1 lds\n\t"
  "v_mfma_f32_16x16x32_bf16 v[32:35], a[144:147], a[248:251], v[32:35]\n\t"
  "ds_read_b128 a[136:139], v23 offset:46080\n\t"
  "s_waitcnt vmcnt(18)\n\t"
  "s_add_u32 s57, s57, 0x6000\n\t"
  "v_mfma_f32_16x16x32_bf16 v[36:39], a[144:147], a[252:255], v[36:39]\n\t"
  "s_barrier\n\t"
  "v_mfma_f32_16x16x32_bf16 v[44:47], a[148:151], a[252:255], v[44:47]\n\t"
  "ds_read_b128 a[140:143], v23 offset:48128\n\t"
  "v_mfma_f32_16x16x32_bf16 v[40:43], a[148:151], a[248:251], v[40:43]\n\t"
  "s_waitcnt lgkmcnt(4)\n\t"
  "v_mfma_f32_16x16x32_bf16 v[48:51], a[152:155], a[248:251], v[48:51]\n\t"
  "ds_read_b128 a[144:147], v21 offset:16384\n\t"
  "v_mfma_f32_16x16x32_bf16 v[52:55], a[152:155], a[252:255], v[52:55]\n\t"
  "buffer_load_dwordx4 a[240:243], v13, s[20:23], s56 offen offset:0 sc1\n\t"
  "v_mfma_f32_16x16x32_bf16 v[60:63], a[156:159], a[252:255], v[60:63]\n\t"
  "ds_read_b128 a[148:151], v21 offset:18432\n\t"
  "s_add_u32 m0, s58, 0x0\n\t"
  "s_waitcnt lgkmcnt(4)\n\t"
  "v_mfma_f32_16x16x32_bf16 v[56:59], a[156:159], a[248:251], v[56:59]\n\t"
  "buffer_load_dwordx4 v14, s[20:23], s57 offen offset:0 sc1 lds\n\t"
  "v_mfma_f32_16x16x32_bf16 v[64:67], a[128:131], a[248:251], v[64:67]\n\t"
  "ds_read_b128 a[152:155], v21 offset:20480\n\t"
  "v_mfma_f32_16x16x32_bf16 v[68:71], a[128:131], a[252:255], v[68:71]\n\t"
  "v_mfma_f32_16x16x32_bf16 v[76:79], a[132:135], a[252:255], v[76:79]\n\t"
  "ds_read_b128 a[156:159], v21 offset:22528\n\t"
  "v_mfma_f32_16x16x32_bf16 v[72:75], a[132:135], a[248:251], v[72:75]\n\t"
  "s_waitcnt lgkmcnt(4)\n\t"
  "v_mfma_f32_16x16x32_bf16 v[80:83], a[136:139], a[248:251], v[80:83]\n\t"
  "ds_read_b128 a[128:131], v21 offset:17408\n\t"
  "v_mfma_f32_16x16x32_bf16 v[84:87], a[136:139], a[252:255], v[84:87]\n\t"
  "buffer_load_dwordx4 a[244:247], v13, s[20:23], s56 offen offset:2048 sc1\n\t"
  "v_mfma_f32_16x16x32_bf16 v[92:95], a[140:143], a[252:255], v[92:95]\n\t"
  "ds_read_b128 a[132:135], v21 offset:19456\n\t"
  "s_add_u32 s56, s56, 0x80000\n\t"
  "s_waitcnt lgkmcnt(4)\n\t"
  "v_mfma_f32_16x16x32_bf16 v[88:91], a[140:143], a[248:251], v[88:91]\n\t"
  "buffer_load_dwordx4 v15, s[20:23], s57 offen offset:1024 sc1 lds\n\t"
  "v_mfma_f32_16x16x32_bf16 v[32:35], a[144:147], a[192:195], v[32:35]\n\t"
  "ds_read_b128 a[136:139], v21 offset:21504\n\t"
  "s_add_u32 s57, s57, 0x8000\n\t"
  "v_mfma_f32_16x16x32_bf16 v[36:39], a[144:147], a[196:199], v[36:39]\n\t"
  "v_mfma_f32_16x16x32_bf16 v[44:47], a[148:151], a[196:199], v[44:47]\n\t"
  "ds_read_b128 a[140:143], v21 offset:23552\n\t"
  "v_mfma_f32_16x16x32_bf16 v[40:43], a[148:151], a[192:195], v[40:43]\n\t"
  "s_waitcnt lgkmcnt(4)\n\t"
  "v_mfma_f32_16x16x32_bf16 v[48:51], a[152:155], a[192:195], v[48:51]\n\t"
  "ds_read_b128 a[144:147], v21 offset:24576\n\t"
  "v_mfma_f32_16x16x32_bf16 v[52:55], a[152:155], a[196:199], v[52:55]\n\t"
  "buffer_load_dwordx4 a[248:251], v13, s[20:23], s56 offen offset:0 sc1\n\t"
  "v_mfma_f32_16x16x32_bf16 v[60:63], a[156:159], a[196:199], v[60:63]\n\t"
  "ds_read_b128 a[148:151], v21 offset:26624\n\t"
  "s_add_u32 m0, s58, 0x2000\n\t"
  "s_waitcnt lgkmcnt(4)\n\t"
  "v_mfma_f32_16x16x32_bf16 v[56:59], a[156:159], a[192:195], v[56:59]\n\t"
  "buffer_load_dwordx4 v14, s[20:23], s57 offen offset:0 sc1 lds\n\t"
  "v_mfma_f32_16x16x32_bf16 v[64:67], a[128:131], a[192:195], v[64:67]\n\t"
  "ds_read_b128 a[152:155], v21 offset:28672\n\t"
  "v_mfma_f32_16x16x32_bf16 v[68:71], a[128:131], a[196:199], v[68:71]\n\t"
  "v_mfma_f32_16x16x32_bf16 v[76:79], a[132:135], a[196:199], v[76:79]\n\t"
  "ds_read_b128 a[156:159], v21 offset:30720\n\t"
  "v_mfma_f32_16x16x32_bf16 v[72:75], a[132:135], a[192:195], v[72:75]\n\t"
  "s_waitcnt lgkmcnt(4)\n\t"
  "v_mfma_f32_16x16x32_bf16 v[80:83], a[136:139], a[192:195], v[80:83]\n\t"
  "ds_read_b128 a[128:131], v21 offset:25600\n\t"
  "v_mfma_f32_16x16x32_bf16 v[84:87], a[136:139], a[196:199], v[84:87]\n\t"
  "buffer_load_dwordx4 a[252:255], v13, s[20:23], s56 offen offset:2048 sc1\n\t"
  "v_mfma_f32_16x16x32_bf16 v[92:95], a[140:143], a[196:199], v[92:95]\n\t"
  "ds_read_b128 a[132:135], v21 offset:27648\n\t"
  "s_add_u32 s56, s56, 0xff980000\n\t"
  "s_waitcnt lgkmcnt(4)\n\t"
  "v_mfma_f32_16x16x32_bf16 v[88:91], a[140:143], a[192:195], v[88:91]\n\t"
  "buffer_load_dwordx4 v15, s[20:23], s57 offen offset:1024 sc1 lds\n\t"
  "v_mfma_f32_16x16x32_bf16 v[32:35], a[144:147], a[200:203], v[32:35]\n\t"
  "ds_read_b128 a[136:139], v21 offset:29696\n\t"
  "s_waitcnt vmcnt(17)\n\t"
  "s_waitcnt vmcnt(16)\n\t"
  "s_mov_b32 s72, 0x20000\n\t"
  "s_not_b32 s68, s34\n\t"
  "s_mov_b32 s73, 64\n\t"
  "s_branch LPCc_%=\n\t"
  "LPCa_%=:\n\t"
  "v_not_b32 v24, s34\n\t"
  "buffer_load_dwordx2 v[24:25], v12, s[20:23], 0 offen offset:16 sc1\n\t"
  "s_mov_b32 s73, 64\n\t"
  "LPCw_%=:\n\t"
  "s_sleep 1\n\t"
  "LPCc_%=:\n\t"
  "v_cmp_eq_u32 vcc, s68, v24\n\t"
  "s_nop 1\n\t"
  "s_cmp_eq_u64 vcc, 0\n\t"
  "v_cmp_ne_u64 vcc, s[34:35], v[24:25]\n\t"
  "s_cbranch_vccz LPCb_%=\n\t"
  "s_cbranch_scc1 LPCr_%=\n\t"
  "s_sub_u32 s73, s73, 1\n\t"
  "s_cbranch_scc0 LPCw_%=\n\t"
  "s_waitcnt vmcnt(0)\n\t"
  "LPCr_%=:\n\t"
  "s_sleep 2\n\t"
  "s_sub_u32 s72, s72, 1\n\t"
  "s_cbranch_scc0 LPCa_%=\n\t"
  "LPCb_%=:\n\t"
  "s_add_u32 s57, s57, 0xfff98000\n\t"
  "v_mfma_f32_16x16x32_bf16 v[36:39], a[144:147], a[204:207], v[36:39]\n\t"
  "s_barrier\n\t"
  "v_mfma_f32_16x16x32_bf16 v[44:47], a[148:151], a[204:207], v[44:47]\n\t"
  "ds_read_b128 a[140:143], v21 offset:31744\n\t"
  "v_mfma_f32_16x16x32_bf16 v[40:43], a[148:151], a[200:203], v[40:43]\n\t"
  "s_waitcnt lgkmcnt(4)\n\t"
  "v_mfma_f32_16x16x32_bf16 v[48:51], a[152:155], a[200:203], v[48:51]\n\t"
  "ds_read_b128 a[144:147], v21 offset:32768\n\t"
  "v_mfma_f32_16x16x32_bf16 v[52:55], a[152:155], a[204:207], v[52:55]\n\t"
  "buffer_load_dwordx4 a[192:195], v13, s[20:23], s56 offen offset:0 sc1\n\t"
  "v_mfma_f32_16x16x32_bf16 v[60:63], a[156:159], a[204:207], v[60:63]\n\t"
  "ds_read_b128 a[148:151], v21 offset:34816\n\t"
  "s_add_u32 m0, s58, 0xffffc000\n\t"
  "s_waitcnt lgkmcnt(4)\n\t"
  "v_mfma_f32_16x16x32_bf16 v[56:59], a[156:159], a[200:203], v[56:59]\n\t"
  "buffer_load_dwordx4 v14, s[20:23], s57 offen offset:0 sc1 lds\n\t"
  "v_mfma_f32_16x16x32_bf16 v[64:67], a[128:131], a[200:203], v[64:67]\n\t"
  "ds_read_b128 a[152:155], v21 offset:36864\n\t"
  "v_mfma_f32_16x16x32_bf16 v[68:71], a[128:131], a[204:207], v[68:71]\n\t"
  "v_mfma_f32_16x16x32_bf16 v[76:79], a[132:135], a[204:207], v[76:79]\n\t"
  "ds_read_b128 a[156:159], v21 offset:38912\n\t"
  "v_mfma_f32_16x16x32_bf16 v[72:75], a[132:135], a[200:203], v[72:75]\n\t"
  "s_waitcnt lgkmcnt(4)\n\t"
  "v_mfma_f32_16x16x32_bf16 v[80:83], a[136:139], a[200:203], v[80:83]\n\t"
  "ds_read_b128 a[128:131], v21 offset:33792\n\t"
  "v_mfma_f32_16x16x32_bf16 v[84:87], a[136:139], a[204:207], v[84:87]\n\t"
  "buffer_load_dwordx4 a[196:199], v13, s[20:23], s56 offen offset:2048 sc1\n\t"
  "v_mfma_f32_16x16x32_bf16 v[92:95], a[140:143], a[204:207], v[92:95]\n\t"
  "ds_read_b128 a[132:135], v21 offset:35840\n\t"
  "s_add_u32 s56, s56, 0x80000\n\t"
  "s_waitcnt lgkmcnt(4)\n\t"
  "v_mfma_f32_16x16x32_bf16 v[88:91], a[140:143], a[200:203], v[88:91]\n\t"
  "buffer_load_dwordx4 v15, s[20:23], s57 offen offset:1024 sc1 lds\n\t"
  "v_mfma_f32_16x16x32_bf16 v[32:35], a[144:147], a[208:211], v[32:35]\n\t"
  "ds_read_b128 a[136:139], v21 offset:37888\n\t"
  "s_add_u32 s57, s57, 0x8000\n\t"
  "v_mfma_f32_16x16x32_bf16 v[36:39], a[144:147], a[212:215], v[36:39]\n\t"
  "v_mfma_f32_16x16x32_bf16 v[44:47], a[148:151], a[212:215], v[44:47]\n\t"
  "ds_read_b128 a[140:143], v21 offset:39936\n\t"
  "v_mfma_f32_16x16x32_bf16 v[40:43], a[148:151], a[208:211], v[40:43]\n\t"
  "s_waitcnt lgkmcnt(4)\n\t"
  "v_mfma_f32_16x16x32_bf16 v[48:51], a[152:155], a[208:211], v[48:51]\n\t"
  "ds_read_b128 a[144:147], v21 offset:40960\n\t"
  "v_mfma_f32_16x16x32_bf16 v[52:55], a[152:155], a[212:215], v[52:55]\n\t"
  "buffer_load_dwordx4 a[200:203], v13, s[20:23], s56 offen offset:0 sc1\n\t"
  "v_mfma_f32_16x16x32_bf16 v[60:63], a[156:159], a[212:215], v[60:63]\n\t"
  "ds_read_b128 a[148:151], v21 offset:43008\n\t"
  "s_add_u32 m0, s58, 0xffffe000\n\t"
  "s_waitcnt lgkmcnt(4)\n\t"
  "v_mfma_f32_16x16x32_bf16 v[56:59], a[156:159], a[208:211], v[56:59]\n\t"
  "buffer_load_dwordx4 v14, s[20:23], s57 offen offset:0 sc1 lds\n\t"
  "v_mfma_f32_16x16x32_bf16 v[64:67], a[128:131], a[208:211], v[64:67]\n\t"
  "ds_read_b128 a[152:155], v21 offset:45056\n\t"
  "v_mfma_f32_16x16x32_bf16 v[68:71], a[128:131], a[212:215], v[68:71]\n\t"
  "v_mfma_f32_16x16x32_bf16 v[76:79], a[132:135], a[212:215], v[76:79]\n\t"
  "ds_read_b128 a[156:159], v21 offset:47104\n\t"
  "v_mfma_f32_16x16x32_bf16 v[72:75], a[132:135], a[208:211], v[72:75]\n\t"
  "s_waitcnt lgkmcnt(4)\n\t"
  "v_mfma_f32_16x16x32_bf16 v[80:83], a[136:139], a[208:211], v[80:83]\n\t"
  "ds_read_b128 a[128:131], v21 offset:41984\n\t"
  "v_mfma_f32_16x16x32_bf16 v[84:87], a[136:139], a[212:215], v[84:87]\n\t"
  "buffer_load_dwordx4 a[204:207], v13, s[20:23], s56 offen offset:2048 sc1\n\t"
  "v_mfma_f32_16x16x32_bf16 v[92:95], a[140:143], a[212:215], v[92:95]\n\t"
  "ds_read_b128 a[132:135], v21 offset:44032\n\t"
  "s_add_u32 s56, s56, 0x60000\n\t"
  "s_waitcnt lgkmcnt(4)\n\t"
  "v_mfma_f32_16x16x32_bf16 v[88:91], a[140:143], a[208:211], v[88:91]\n\t"
  "buffer_load_dwordx4 v15, s[20:23], s57 offen offset:1024 sc1 lds\n\t"
  "v_mfma_f32_16x16x32_bf16 v[32:35], a[144:147], a[216:219], v[32:35]\n\t"
  "ds_read_b128 a[136:139], v21 offset:46080\n\t"
  "s_waitcnt vmcnt(16)\n\t"
  "s_add_u32 s57, s57, 0x6000\n\t"
  "v_mfma_f32_16x16x32_bf16 v[36:39], a[144:147], a[220:223], v[36:39]\n\t"
  "s_barrier\n\t"
  "v_mfma_f32_16x16x32_bf16 v[44:47], a[148:151], a[220:223], v[44:47]\n\t"
  "ds_read_b128 a[140:143], v21 offset:48128\n\t"
  "v_mfma_f32_16x16x32_bf16 v[40:43], a[148:151], a[216:219], v[40:43]\n\t"
  "s_waitcnt lgkmcnt(4)\n\t"
  "v_mfma_f32_16x16x32_bf16 v[48:51], a[152:155], a[216:219], v[48:51]\n\t"
  "ds_read_b128 a[144:147], v21 offset:49152\n\t"
  "v_mfma_f32_16x16x32_bf16 v[52:55], a[152:155], a[220:223], v[52:55]\n\t"
  "buffer_load_dwordx4 a[208:211], v13, s[20:23], s56 offen offset:0 sc1\n\t"
  "v_mfma_f32_16x16x32_bf16 v[60:63], a[156:159], a[220:223], v[60:63]\n\t"
  "ds_read_b128 a[148:151], v21 offset:51200\n\t"
  "s_add_u32 m0, s58, 0xffff8000\n\t"
  "s_waitcnt lgkmcnt(4)\n\t"
  "v_mfma_f32_16x16x32_bf16 v[56:59], a[156:159], a[216:219], v[56:59]\n\t"
  "buffer_load_dwordx4 v14, s[20:23], s57 offen offset:0 sc1 lds\n\t"
  "v_mfma_f32_16x16x32_bf16 v[64:67], a[128:131], a[216:219], v[64:67]\n\t"
  "ds_read_b128 a[152:155], v21 offset:53248\n\t"
  "v_mfma_f32_16x16x32_bf16 v[68:71], a[128:131], a[220:223], v[68:71]\n\t"
  "v_mfma_f32_16x16x32_bf16 v[76:79], a[132:135], a[220:223], v[76:79]\n\t"
  "ds_read_b128 a[156:159], v21 offset:55296\n\t"
  "v_mfma_f32_16x16x32_bf16 v[72:75], a[132:135], a[216:219], v[72:75]\n\t"
  "s_waitcnt lgkmcnt(4)\n\t"
  "v_mfma_f32_16x16x32_bf16 v[80:83], a[136:139], a[216:219], v[80:83]\n\t"
  "ds_read_b128 a[128:131], v21 offset:50176\n\t"
  "v_mfma_f32_16x16x32_bf16 v[84:87], a[136:139], a[220:223], v[84:87]\n\t"
  "buffer_load_dwordx4 a[212:215], v13, s[20:23], s56 offen offset:2048 sc1\n\t"
  "v_mfma_f32_16x16x32_bf16 v[92:95], a[140:143], a[220:223], v[92:95]\n\t"
  "ds_read_b128 a[132:135], v21 offset:52224\n\t"
  "s_add_u32 s56, s56, 0x80000\n\t"
  "s_waitcnt lgkmcnt(4)\n\t"
  "v_mfma_f32_16x16x32_bf16 v[88:91], a[140:143], a[216:219], v[88:91]\n\t"
  "buffer_load_dwordx4 v15, s[20:23], s57 offen offset:1024 sc1 lds\n\t"
  "v_mfma_f32_16x16x32_bf16 v[32:35], a[144:147], a[224:227], v[32:35]\n\t"
  "ds_read_b128 a[136:139], v21 offset:54272\n\t"
  "s_add_u32 s57, s57, 0x8000\n\t"
  "v_mfma_f32_16x16x32_bf16 v[36:39], a[144:147], a[228:231], v[36:39]\n\t"
  "v_mfma_f32_16x16x32_bf16 v[44:47], a[148:151], a[228:231], v[44:47]\n\t"
  "ds_read_b128 a[140:143], v21 offset:56320\n\t"
  "v_mfma_f32_16x16x32_bf16 v[40:43], a[148:151], a[224:227], v[40:43]\n\t"
  "s_waitcnt lgkmcnt(4)\n\t"
  "v_mfma_f32_16x16x32_bf16 v[48:51], a[152:155], a[224:227], v[48:51]\n\t"
  "ds_read_b128 a[144:147], v21 offset:57344\n\t"
  "v_mfma_f32_16x16x32_bf16 v[52:55], a[152:155], a[228:231], v[52:55]\n\t"
  "buffer_load_dwordx4 a[216:219], v13, s[20:23], s56 offen offset:0 sc1\n\t"
  "v_mfma_f32_16x16x32_bf16 v[60:63], a[156:159], a[228:231], v[60:63]\n\t"
  "ds_read_b128 a[148:151], v21 offset:59392\n\t"
  "s_add_u32 m0, s58, 0xffffa000\n\t"
  "s_waitcnt lgkmcnt(4)\n\t"
  "v_mfma_f32_16x16x32_bf16 v[56:59], a[156:159], a[224:227], v[56:59]\n\t"
  "buffer_load_dwordx4 v14, s[20:23], s57 offen offset:0 sc1 lds\n\t"
  "v_mfma_f32_16x16x32_bf16 v[64:67], a[128:131], a[224:227], v[64:67]\n\t"
  "ds_read_b128 a[152:155], v21 offset:61440\n\t"
  "v_mfma_f32_16x16x32_bf16 v[68:71], a[128:131], a[228:231], v[68:71]\n\t"
  "v_mfma_f32_16x16x32_bf16 v[76:79], a[132:135], a[228:231], v[76:79]\n\t"
  "ds_read_b128 a[156:159], v21 offset:63488\n\t"
  "v_mfma_f32_16x16x32_bf16 v[72:75], a[132:135], a[224:227], v[72:75]\n\t"
  "s_waitcnt lgkmcnt(4)\n\t"
  "v_mfma_f32_16x16x32_bf16 v[80:83], a[136:139], a[224:227], v[80:83]\n\t"
  "ds_read_b128 a[128:131], v21 offset:58368\n\t"
  "v_mfma_f32_16x16x32_bf16 v[84:87], a[136:139], a[228:231], v[84:87]\n\t"
  "buffer_load_dwordx4 a[220:223], v13, s[20:23], s56 offen offset:2048 sc1\n\t"
  "v_mfma_f32_16x16x32_bf16 v[92:95], a[140:143], a[228:231], v[92:95]\n\t"
  "ds_read_b128 a[132:135], v21 offset:60416\n\t"
  "s_add_u32 s56, s56, 0x60000\n\t"
  "s_waitcnt lgkmcnt(4)\n\t"
  "v_mfma_f32_16x16x32_bf16 v[88:91], a[140:143], a[224:227], v[88:91]\n\t"
  "buffer_load_dwordx4 v15, s[20:23], s57 offen offset:1024 sc1 lds\n\t"
  "v_mfma_f32_16x16x32_bf16 v[32:35], a[144:147], a[232:235], v[32:35]\n\t"
  "ds_read_b128 a[136:139], v21 offset:62464\n\t"
  "s_waitcnt vmcnt(16)\n\t"
  "s_add_u32 s57, s57, 0x6000\n\t"
  "v_mfma_f32_16x16x32_bf16 v[36:39], a[144:147], a[236:239], v[36:39]\n\t"
  "s_barrier\n\t"
  "v_mfma_f32_16x16x32_bf16 v[44:47], a[148:151], a[236:239], v[44:47]\n\t"
  "ds_read_b128 a[140:143], v21 offset:64512\n\t"
  "v_mfma_f32_16x16x32_bf16 v[40:43], a[148:151], a[232:235], v[40:43]\n\t"
  "s_waitcnt lgkmcnt(4)\n\t"
  "v_mfma_f32_16x16x32_bf16 v[48:51], a[152:155], a[232:235], v[48:51]\n\t"
  "ds_read_b128 a[144:147], v21 offset:0\n\t"
  "v_mfma_f32_16x16x32_bf16 v[52:55], a[152:155], a[236:239], v[52:55]\n\t"
  "buffer_load_dwordx4 a[224:227], v13, s[20:23], s56 offen offset:0 sc1\n\t"
  "v_mfma_f32_16x16x32_bf16 v[60:63], a[156:159], a[236:239], v[60:63]\n\t"
  "ds_read_b128 a[148:151], v21 offset:2048\n\t"
  "s_add_u32 m0, s58, 0x4000\n\t"
  "s_waitcnt lgkmcnt(4)\n\t"
  "v_mfma_f32_16x16x32_bf16 v[56:59], a[156:159], a[232:235], v[56:59]\n\t"
  "buffer_load_dwordx4 v14, s[20:23], s57 offen offset:0 sc1 lds\n\t"
  "v_mfma_f32_16x16x32_bf16 v[64:67], a[128:131], a[232:235], v[64:67]\n\t"
  "ds_read_b128 a[152:155], v21 offset:4096\n\t"
  "v_mfma_f32_16x16x32_bf16 v[68:71], a[128:131], a[236:239], v[68:71]\n\t"
  "v_mfma_f32_16x16x32_bf16 v[76:79], a[132:135], a[236:239], v[76:79]\n\t"
  "ds_read_b128 a[156:159], v21 offset:6144\n\t"
  "v_mfma_f32_16x16x32_bf16 v[72:75], a[132:135], a[232:235], v[72:75]\n\t"
  "s_waitcnt lgkmcnt(4)\n\t"
  "v_mfma_f32_16x16x32_bf16 v[80:83], a[136:139], a[232:235], v[80:83]\n\t"
  "ds_read_b128 a[128:131], v21 offset:1024\n\t"
  "v_mfma_f32_16x16x32_bf16 v[84:87], a[136:139], a[236:239], v[84:87]\n\t"
  "buffer_load_dwordx4 a[228:231], v13, s[20:23], s56 offen offset:2048 sc1\n\t"
  "v_mfma_f32_16x16x32_bf16 v[92:95], a[140:143], a[236:239], v[92:95]\n\t"
  "ds_read_b128 a[132:135], v21 offset:3072\n\t"
  "s_add_u32 s56, s56, 0x80000\n\t"
  "s_waitcnt lgkmcnt(4)\n\t"
  "v_mfma_f32_16x16x32_bf16 v[88:91], a[140:143], a[232:235], v[88:91]\n\t"
  "buffer_load_dwordx4 v15, s[20:23], s57 offen offset:1024 sc1 lds\n\t"
  "v_mfma_f32_16x16x32_bf16 v[32:35], a[144:147], a[240:243], v[32:35]\n\t"
  "ds_read_b128 a[136:139], v21 offset:5120\n\t"
  "s_add_u32 s57, s57, 0x8000\n\t"
  "v_mfma_f32_16x16x32_bf16 v[36:39], a[144:147], a[244:247], v[36:39]\n\t"
  "v_mfma_f32_16x16x32_bf16 v[44:47], a[148:151], a[244:247], v[44:47]\n\t"
  "ds_read_b128 a[140:143], v21 offset:7168\n\t"
  "v_mfma_f32_16x16x32_bf16 v[40:43], a[148:151], a[240:243], v[40:43]\n\t"
  "s_waitcnt lgkmcnt(4)\n\t"
  "v_mfma_f32_16x16x32_bf16 v[48:51], a[152:155], a[240:243], v[48:51]\n\t"
  "ds_read_b128 a[144:147], v21 offset:8192\n\t"
  "v_mfma_f32_16x16x32_bf16 v[52:55], a[152:155], a[244:247], v[52:55]\n\t"
  "buffer_load_dwordx4 a[232:235], v13, s[20:23], s56 offen offset:0 sc1\n\t"
  "v_mfma_f32_16x16x32_bf16 v[60:63], a[156:159], a[244:247], v[60:63]\n\t"
  "ds_read_b128 a[148:151], v21 offset:10240\n\t"
  "s_add_u32 m0, s58, 0x6000\n\t"
  "s_waitcnt lgkmcnt(4)\n\t"
  "v_mfma_f32_16x16x32_bf16 v[56:59], a[156:159], a[240:243], v[56:59]\n\t"
  "buffer_load_dwordx4 v14, s[20:23], s57 offen offset:0 sc1 lds\n\t"
  "v_mfma_f32_16x16x32_bf16 v[64:67], a[128:131], a[240:243], v[64:67]\n\t"
  "ds_read_b128 a[152:155], v21 offset:12288\n\t"
  "v_mfma_f32_16x16x32_bf16 v[68:71], a[128:131], a[244:247], v[68:71]\n\t"
  "v_mfma_f32_16x16x32_bf16 v[76:79], a[132:135], a[244:247], v[76:79]\n\t"
  "ds_read_b128 a[156:159], v21 offset:14336\n\t"
  "v_mfma_f32_16x16x32_bf16 v[72:75], a[132:135], a[240:243], v[72:75]\n\t"
  "s_waitcnt lgkmcnt(4)\n\t"
  "v_mfma_f32_16x16x32_bf16 v[80:83], a[136:139], a[240:243], v[80:83]\n\t"
  "ds_read_b128 a[128:131], v21 offset:9216\n\t"
  "v_mfma_f32_16x16x32_bf16 v[84:87], a[136:139], a[244:247], v[84:87]\n\t"
  "buffer_load_dwordx4 a[236:239], v13, s[20:23], s56 offen offset:2048 sc1\n\t"
  "v_mfma_f32_16x16x32_bf16 v[92:95], a[140:143], a[244:247], v[92:95]\n\t"
  "ds_read_b128 a[132:135], v21 offset:11264\n\t"
  "s_add_u32 s56, s56, 0x60000\n\t"
  "s_waitcnt lgkmcnt(4)\n\t"
  "v_mfma_f32_16x16x32_bf16 v[88:91], a[140:143], a[240:243], v[88:91]\n\t"
  "buffer_load_dwordx4 v15, s[20:23], s57 offen offset:1024 sc1 lds\n\t"
  "v_mfma_f32_16x16x32_bf16 v[32:35], a[144:147], a[248:251], v[32:35]\n\t"
  "ds_read_b128 a[136:139], v21 offset:13312\n\t"
  "s_waitcnt vmcnt(16)\n\t"
  "s_add_u32 s57, s57, 0x6000\n\t"
  "v_mfma_f32_16x16x32_bf16 v[36:39], a[144:147], a[252:255], v[36:39]\n\t"
  "s_barrier\n\t"
  "v_mfma_f32_16x16x32_bf16 v[44:47], a[148:151], a[252:255], v[44:47]\n\t"
  "ds_read_b128 a[140:143], v21 offset:15360\n\t"
  "v_mfma_f32_16x16x32_bf16 v[40:43], a[148:151], a[248:251], v[40:43]\n\t"
  "s_waitcnt lgkmcnt(4)\n\t"
  "v_mfma_f32_16x16x32_bf16 v[48:51], a[152:155], a[248:251], v[48:51]\n\t"
  "ds_read_b128 a[144:147], v23 offset:49152\n\t"
  "v_mfma_f32_16x16x32_bf16 v[52:55], a[152:155], a[252:255], v[52:55]\n\t"
  "buffer_load_dwordx4 a[240:243], v13, s[20:23], s56 offen offset:0 sc1\n\t"
  "v_mfma_f32_16x16x32_bf16 v[60:63], a[156:159], a[252:255], v[60:63]\n\t"
  "ds_read_b128 a[148:151], v23 offset:51200\n\t"
  "s_add_u32 m0, s58, 0x8000\n\t"
  "s_waitcnt lgkmcnt(4)\n\t"
  "v_mfma_f32_16x16x32_bf16 v[56:59], a[156:159], a[248:251], v[56:59]\n\t"
  "buffer_load_dwordx4 v14, s[20:23], s57 offen offset:0 sc1 lds\n\t"
  "v_mfma_f32_16x16x32_bf16 v[64:67], a[128:131], a[248:251], v[64:67]\n\t"
  "ds_read_b128 a[152:155], v23 offset:53248\n\t"
  "v_mfma_f32_16x16x32_bf16 v[68:71], a[128:131], a[252:255], v[68:71]\n\t"
  "v_mfma_f32_16x16x32_bf16 v[76:79], a[132:135], a[252:255], v[76:79]\n\t"
  "ds_read_b128 a[156:159], v23 offset:55296\n\t"
  "v_mfma_f32_16x16x32_bf16 v[72:75], a[132:135], a[248:251], v[72:75]\n\t"
  "s_waitcnt lgkmcnt(4)\n\t"
  "v_mfma_f32_16x16x32_bf16 v[80:83], a[136:139], a[248:251], v[80:83]\n\t"
  "ds_read_b128 a[128:131], v23 offset:50176\n\t"
  "v_mfma_f32_16x16x32_bf16 v[84:87], a[136:139], a[252:255], v[84:87]\n\t"
  "buffer_load_dwordx4 a[244:247], v13, s[20:23], s56 offen offset:2048 sc1\n\t"
  "v_mfma_f32_16x16x32_bf16 v[92:95], a[140:143], a[252:255], v[92:95]\n\t"
  "ds_read_b128 a[132:135], v23 offset:52224\n\t"
  "s_add_u32 s56, s56, 0x80000\n\t"
  "s_waitcnt lgkmcnt(4)\n\t"
  "v_mfma_f32_16x16x32_bf16 v[88:91], a[140:143], a[248:251], v[88:91]\n\t"
  "buffer_load_dwordx4 v15, s[20:23], s57 offen offset:1024 sc1 lds\n\t"
  "v_mfma_f32_16x16x32_bf16 v[32:35], a[144:147], a[192:195], v[32:35]\n\t"
  "ds_read_b128 a[136:139], v23 offset:54272\n\t"
  "s_add_u32 s57, s57, 0x8000\n\t"
  "v_mfma_f32_16x16x32_bf16 v[36:39], a[144:147], a[196:199], v[36:39]\n\t"
  "v_mfma_f32_16x16x32_bf16 v[44:47], a[148:151], a[196:199], v[44:47]\n\t"
  "ds_read_b128 a[140:143], v23 offset:56320\n\t"
  "v_mfma_f32_16x16x32_bf16 v[40:43], a[148:151], a[192:195], v[40:43]\n\t"
  "s_waitcnt lgkmcnt(4)\n\t"
  "v_mfma_f32_16x16x32_bf16 v[48:51], a[152:155], a[192:195], v[48:51]\n\t"
  "ds_read_b128 a[144:147], v23 offset:57344\n\t"
  "v_mfma_f32_16x16x32_bf16 v[52:55], a[152:155], a[196:199], v[52:55]\n\t"
  "buffer_load_dwordx4 a[248:251], v13, s[20:23], s56 offen offset:0 sc1\n\t"
  "v_mfma_f32_16x16x32_bf16 v[60:63], a[156:159], a[196:199], v[60:63]\n\t"
  "ds_read_b128 a[148:151], v23 offset:59392\n\t"
  "s_add_u32 m0, s58, 0xa000\n\t"
  "s_waitcnt lgkmcnt(4)\n\t"
  "v_mfma_f32_16x16x32_bf16 v[56:59], a[156:159], a[192:195], v[56:59]\n\t"
  "buffer_load_dwordx4 v14, s[20:23], s57 offen offset:0 sc1 lds\n\t"
  "v_mfma_f32_16x16x32_bf16 v[64:67], a[128:131], a[192:195], v[64:67]\n\t"
  "ds_read_b128 a[152:155], v23 offset:61440\n\t"
  "v_mfma_f32_16x16x32_bf16 v[68:71], a[128:131], a[196:199], v[68:71]\n\t"
  "v_mfma_f32_16x16x32_bf16 v[76:79], a[132:135], a[196:199], v[76:79]\n\t"
  "ds_read_b128 a[156:159], v23 offset:63488\n\t"
  "v_mfma_f32_16x16x32_bf16 v[72:75], a[132:135], a[192:195], v[72:75]\n\t"
  "s_waitcnt lgkmcnt(4)\n\t"
  "v_mfma_f32_16x16x32_bf16 v[80:83], a[136:139], a[192:195], v[80:83]\n\t"
  "ds_read_b128 a[128:131], v23 offset:58368\n\t"
  "v_mfma_f32_16x16x32_bf16 v[84:87], a[136:139], a[196:199], v[84:87]\n\t"
  "buffer_load_dwordx4 a[252:255], v13, s[20:23], s56 offen offset:2048 sc1\n\t"
  "v_mfma_f32_16x16x32_bf16 v[92:95], a[140:143], a[196:199], v[92:95]\n\t"
  "ds_read_b128 a[132:135], v23 offset:60416\n\t"
  "s_add_u32 s56, s56, 0x60000\n\t"
  "s_waitcnt lgkmcnt(4)\n\t"
  "v_mfma_f32_16x16x32_bf16 v[88:91], a[140:143], a[192:195], v[88:91]\n\t"
  "buffer_load_dwordx4 v15, s[20:23], s57 offen offset:1024 sc1 lds\n\t"
  "v_mfma_f32_16x16x32_bf16 v[32:35], a[144:147], a[200:203], v[32:35]\n\t"
  "ds_read_b128 a[136:139], v23 offset:62464\n\t"
  "s_waitcnt vmcnt(16)\n\t"
  "s_add_u32 s57, s57, 0x6000\n\t"
  "v_mfma_f32_16x16x32_bf16 v[36:39], a[144:147], a[204:207], v[36:39]\n\t"
  "s_barrier\n\t"
  "v_mfma_f32_16x16x32_bf16 v[44:47], a[148:151], a[204:207], v[44:47]\n\t"
  "ds_read_b128 a[140:143], v23 offset:64512\n\t"
  "v_mfma_f32_16x16x32_bf16 v[40:43], a[148:151], a[200:203], v[40:43]\n\t"
  "s_waitcnt lgkmcnt(4)\n\t"
  "v_mfma_f32_16x16x32_bf16 v[48:51], a[152:155], a[200:203], v[48:51]\n\t"
  "ds_read_b128 a[144:147], v23 offset:32768\n\t"
  "v_mfma_f32_16x16x32_bf16 v[52:55], a[152:155], a[204:207], v[52:55]\n\t"
  "buffer_load_dwordx4 a[192:195], v13, s[20:23], s56 offen offset:0 sc1\n\t"
  "v_mfma_f32_16x16x32_bf16 v[60:63], a[156:159], a[204:207], v[60:63]\n\t"
  "ds_read_b128 a[148:151], v23 offset:34816\n\t"
  "s_add_u32 m0, s58, 0xc000\n\t"
  "s_waitcnt lgkmcnt(4)\n\t"
  "v_mfma_f32_16x16x32_bf16 v[56:59], a[156:159], a[200:203], v[56:59]\n\t"
  "buffer_load_dwordx4 v14, s[20:23], s57 offen offset:0 sc1 lds\n\t"
  "v_mfma_f32_16x16x32_bf16 v[64:67], a[128:131], a[200:203], v[64:67]\n\t"
  "ds_read_b128 a[152:155], v23 offset:36864\n\t"
  "v_mfma_f32_16x16x32_bf16 v[68:71], a[128:131], a[204:207], v[68:71]\n\t"
  "v_mfma_f32_16x16x32_bf16 v[76:79], a[132:135], a[204:207], v[76:79]\n\t"
  "ds_read_b128 a[156:159], v23 offset:38912\n\t"
  "v_mfma_f32_16x16x32_bf16 v[72:75], a[132:135], a[200:203], v[72:75]\n\t"
  "s_waitcnt lgkmcnt(4)\n\t"
  "v_mfma_f32_16x16x32_bf16 v[80:83], a[136:139], a[200:203], v[80:83]\n\t"
  "ds_read_b128 a[128:131], v23 offset:33792\n\t"
  "v_mfma_f32_16x16x32_bf16 v[84:87], a[136:139], a[204:207], v[84:87]\n\t"
  "buffer_load_dwordx4 a[196:199], v13, s[20:23], s56 offen offset:2048 sc1\n\t"
  "v_mfma_f32_16x16x32_bf16 v[92:95], a[140:143], a[204:207], v[92:95]\n\t"
  "ds_read_b128 a[132:135], v23 offset:35840\n\t"
  "s_add_u32 s56, s56, 0x80000\n\t"
  "s_waitcnt lgkmcnt(4)\n\t"
  "v_mfma_f32_16x16x32_bf16 v[88:91], a[140:143], a[200:203], v[88:91]\n\t"
  "buffer_load_dwordx4 v15, s[20:23], s57 offen offset:1024 sc1 lds\n\t"
  "v_mfma_f32_16x16x32_bf16 v[32:35], a[144:147], a[208:211], v[32:35]\n\t"
  "ds_read_b128 a[136:139], v23 offset:37888\n\t"
  "s_add_u32 s57, s57, 0x8000\n\t"
  "v_mfma_f32_16x16x32_bf16 v[36:39], a[144:147], a[212:215], v[36:39]\n\t"
  "buffer_load_dword v128, v20, s[20:23], 0 offen offset:0 sc1\n\t"
  "v_mfma_f32_16x16x32_bf16 v[44:47], a[148:151], a[212:215], v[44:47]\n\t"
  "ds_read_b128 a[140:143], v23 offset:39936\n\t"
  "v_mfma_f32_16x16x32_bf16 v[40:43], a[148:151], a[208:211], v[40:43]\n\t"
  "s_waitcnt lgkmcnt(4)\n\t"
  "v_mfma_f32_16x16x32_bf16 v[48:51], a[152:155], a[208:211], v[48:51]\n\t"
  "ds_read_b128 a[144:147], v23 offset:40960\n\t"
  "v_mfma_f32_16x16x32_bf16 v[52:55], a[152:155], a[212:215], v[52:55]\n\t"
  "buffer_load_dword v129, v20, s[20:23], 0 offen offset:64 sc1\n\t"
  "v_mfma_f32_16x16x32_bf16 v[60:63], a[156:159], a[212:215], v[60:63]\n\t"
  "ds_read_b128 a[148:151], v23 offset:43008\n\t"
  "s_waitcnt lgkmcnt(4)\n\t"
  "v_mfma_f32_16x16x32_bf16 v[56:59], a[156:159], a[208:211], v[56:59]\n\t"
  "buffer_load_dwordx4 a[200:203], v13, s[20:23], s56 offen offset:0 sc1\n\t"
  "v_mfma_f32_16x16x32_bf16 v[64:67], a[128:131], a[208:211], v[64:67]\n\t"
  "ds_read_b128 a[152:155], v23 offset:45056\n\t"
  "s_add_u32 m0, s58, 0xe000\n\t"
  "v_mfma_f32_16x16x32_bf16 v[68:71], a[128:131], a[212:215], v[68:71]\n\t"
  "buffer_load_dwordx4 v14, s[20:23], s57 offen offset:0 sc1 lds\n\t"
  "v_mfma_f32_16x16x32_bf16 v[76:79], a[132:135], a[212:215], v[76:79]\n\t"
  "ds_read_b128 a[156:159], v23 offset:47104\n\t"
  "s_waitcnt lgkmcnt(4)\n\t"
  "v_mfma_f32_16x16x32_bf16 v[72:75], a[132:135], a[208:211], v[72:75]\n\t"
  "buffer_load_dword v130, v20, s[20:23], 0 offen offset:128 sc1\n\t"
  "v_mfma_f32_16x16x32_bf16 v[80:83], a[136:139], a[208:211], v[80:83]\n\t"
  "ds_read_b128 a[128:131], v23 offset:41984\n\t"
  "v_mfma_f32_16x16x32_bf16 v[84:87], a[136:139], a[212:215], v[84:87]\n\t"
  "buffer_load_dword v131, v20, s[20:23], 0 offen offset:192 sc1\n\t"
  "v_mfma_f32_16x16x32_bf16 v[92:95], a[140:143], a[212:215], v[92:95]\n\t"
  "ds_read_b128 a[132:135], v23 offset:44032\n\t"
  "s_waitcnt lgkmcnt(4)\n\t"
  "v_mfma_f32_16x16x32_bf16 v[88:91], a[140:143], a[208:211], v[88:91]\n\t"
  "buffer_load_dwordx4 a[204:207], v13, s[20:23], s56 offen offset:2048 sc1\n\t"
  "v_mfma_f32_16x16x32_bf16 v[32:35], a[144:147], a[216:219], v[32:35]\n\t"
  "ds_read_b128 a[136:139], v23 offset:46080\n\t"
  "s_waitcnt vmcnt(19)\n\t"
  "s_add_u32 s56, s56, 0x60000\n\t"
  "v_mfma_f32_16x16x32_bf16 v[36:39], a[144:147], a[220:223], v[36:39]\n\t"
  "s_barrier\n\t"
  "buffer_load_dwordx4 v15, s[20:23], s57 offen offset:1024 sc1 lds\n\t"
  "v_mfma_f32_16x16x32_bf16 v[44:47], a[148:151], a[220:223], v[44:47]\n\t"
  "ds_read_b128 a[140:143], v23 offset:48128\n\t"
  "s_add_u32 s57, s57, 0x6000\n\t"
  "s_waitcnt lgkmcnt(4)\n\t"
  "v_mfma_f32_16x16x32_bf16 v[40:43], a[148:151], a[216:219], v[40:43]\n\t"
  "buffer_load_dword v132, v20, s[20:23], 0 offen offset:2048 sc1\n\t"
  "v_mfma_f32_16x16x32_bf16 v[48:51], a[152:155], a[216:219], v[48:51]\n\t"
  "ds_read_b128 a[144:147], v21 offset:16384\n\t"
  "v_mfma_f32_16x16x32_bf16 v[52:55], a[152:155], a[220:223], v[52:55]\n\t"
  "buffer_load_dword v133, v20, s[20:23], 0 offen offset:2112 sc1\n\t"
  "v_mfma_f32_16x16x32_bf16 v[60:63], a[156:159], a[220:223], v[60:63]\n\t"
  "ds_read_b128 a[148:151], v21 offset:18432\n\t"
  "s_waitcnt lgkmcnt(4)\n\t"
  "v_mfma_f32_16x16x32_bf16 v[56:59], a[156:159], a[216:219], v[56:59]\n\t"
  "buffer_load_dwordx4 a[208:211], v13, s[20:23], s56 offen offset:0 sc1\n\t"
  "v_mfma_f32_16x16x32_bf16 v[64:67], a[128:131], a[216:219], v[64:67]\n\t"
  "ds_read_b128 a[152:155], v21 offset:20480\n\t"
  "s_add_u32 m0, s58, 0x0\n\t"
  "v_mfma_f32_16x16x32_bf16 v[68:71], a[128:131], a[220:223], v[68:71]\n\t"
  "buffer_load_dwordx4 v14, s[20:23], s57 offen offset:0 sc1 lds\n\t"
  "v_mfma_f32_16x16x32_bf16 v[76:79], a[132:135], a[220:223], v[76:79]\n\t"
  "ds_read_b128 a[156:159], v21 offset:22528\n\t"
  "s_waitcnt lgkmcnt(4)\n\t"
  "v_mfma_f32_16x16x32_bf16 v[72:75], a[132:135], a[216:219], v[72:75]\n\t"
  "buffer_load_dword v134, v20, s[20:23], 0 offen offset:2176 sc1\n\t"
  "v_mfma_f32_16x16x32_bf16 v[80:83], a[136:139], a[216:219], v[80:83]\n\t"
  "ds_read_b128 a[128:131], v21 offset:17408\n\t"
  "v_mfma_f32_16x16x32_bf16 v[84:87], a[136:139], a[220:223], v[84:87]\n\t"
  "buffer_load_dword v135, v20, s[20:23], 0 offen offset:2240 sc1\n\t"
  "v_mfma_f32_16x16x32_bf16 v[92:95], a[140:143], a[220:223], v[92:95]\n\t"
  "ds_read_b128 a[132:135], v21 offset:19456\n\t"
  "s_waitcnt lgkmcnt(4)\n\t"
  "v_mfma_f32_16x16x32_bf16 v[88:91], a[140:143], a[216:219], v[88:91]\n\t"
  "buffer_load_dwordx4 a[212:215], v13, s[20:23], s56 offen offset:2048 sc1\n\t"
  "v_mfma_f32_16x16x32_bf16 v[32:35], a[144:147], a[224:227], v[32:35]\n\t"
  "ds_read_b128 a[136:139], v21 offset:21504\n\t"
  "s_add_u32 s56, s56, 0x80000\n\t"
  "v_mfma_f32_16x16x32_bf16 v[36:39], a[144:147], a[228:231], v[36:39]\n\t"
  "buffer_load_dwordx4 v15, s[20:23], s57 offen offset:1024 sc1 lds\n\t"
  "v_mfma_f32_16x16x32_bf16 v[44:47], a[148:151], a[228:231], v[44:47]\n\t"
  "ds_read_b128 a[140:143], v21 offset:23552\n\t"
  "s_add_u32 s57, s57, 0x8000\n\t"
  "v_mfma_f32_16x16x32_bf16 v[40:43], a[148:151], a[224:227], v[40:43]\n\t"
  "s_waitcnt lgkmcnt(4)\n\t"
  "v_mfma_f32_16x16x32_bf16 v[48:51], a[152:155], a[224:227], v[48:51]\n\t"
  "ds_read_b128 a[144:147], v21 offset:24576\n\t"
  "v_mfma_f32_16x16x32_bf16 v[52:55], a[152:155], a[228:231], v[52:55]\n\t"
  "buffer_load_dwordx4 a[216:219], v13, s[20:23], s56 offen offset:0 sc1\n\t"
  "v_mfma_f32_16x16x32_bf16 v[60:63], a[156:159], a[228:231], v[60:63]\n\t"
  "ds_read_b128 a[148:151], v21 offset:26624\n\t"
  "s_add_u32 m0, s58, 0x2000\n\t"
  "s_waitcnt lgkmcnt(4)\n\t"
  "v_mfma_f32_16x16x32_bf16 v[56:59], a[156:159], a[224:227], v[56:59]\n\t"
  "buffer_load_dwordx4 v14, s[20:23], s57 offen offset:0 sc1 lds\n\t"
  "v_mfma_f32_16x16x32_bf16 v[64:67], a[128:131], a[224:227], v[64:67]\n\t"
  "ds_read_b128 a[152:155], v21 offset:28672\n\t"
  "v_mfma_f32_16x16x32_bf16 v[68:71], a[128:131], a[228:231], v[68:71]\n\t"
  "v_mfma_f32_16x16x32_bf16 v[76:79], a[132:135], a[228:231], v[76:79]\n\t"
  "ds_read_b128 a[156:159], v21 offset:30720\n\t"
  "v_mfma_f32_16x16x32_bf16 v[72:75], a[132:135], a[224:227], v[72:75]\n\t"
  "s_waitcnt lgkmcnt(4)\n\t"
  "v_mfma_f32_16x16x32_bf16 v[80:83], a[136:139], a[224:227], v[80:83]\n\t"
  "ds_read_b128 a[128:131], v21 offset:25600\n\t"
  "v_mfma_f32_16x16x32_bf16 v[84:87], a[136:139], a[228:231], v[84:87]\n\t"
  "buffer_load_dwordx4 a[220:223], v13, s[20:23], s56 offen offset:2048 sc1\n\t"
  "v_mfma_f32_16x16x32_bf16 v[92:95], a[140:143], a[228:231], v[92:95]\n\t"
  "ds_read_b128 a[132:135], v21 offset:27648\n\t"
  "s_add_u32 s56, s56, 0x60000\n\t"
  "s_waitcnt lgkmcnt(4)\n\t"
  "v_mfma_f32_16x16x32_bf16 v[88:91], a[140:143], a[224:227], v[88:91]\n\t"
  "buffer_load_dwordx4 v15, s[20:23], s57 offen offset:1024 sc1 lds\n\t"
  "v_mfma_f32_16x16x32_bf16 v[32:35], a[144:147], a[232:235], v[32:35]\n\t"
  "ds_read_b128 a[136:139], v21 offset:29696\n\t"
  "s_waitcnt vmcnt(24)\n\t"
  "s_add_u32 s57, s57, 0x6000\n\t"
  "v_mfma_f32_16x16x32_bf16 v[36:39], a[144:147], a[236:239], v[36:39]\n\t"
  "s_barrier\n\t"
  "v_mfma_f32_16x16x32_bf16 v[44:47], a[148:151], a[236:239], v[44:47]\n\t"
  "ds_read_b128 a[140:143], v21 offset:31744\n\t"
  "v_mfma_f32_16x16x32_bf16 v[40:43], a[148:151], a[232:235], v[40:43]\n\t"
  "s_waitcnt lgkmcnt(4)\n\t"
  "v_mfma_f32_16x16x32_bf16 v[48:51], a[152:155], a[232:235], v[48:51]\n\t"
  "ds_read_b128 a[144:147], v21 offset:32768\n\t"
  "v_mfma_f32_16x16x32_bf16 v[52:55], a[152:155], a[236:239], v[52:55]\n\t"
  "buffer_load_dwordx4 a[224:227], v13, s[20:23], s56 offen offset:0 sc1\n\t"
  "v_mfma_f32_16x16x32_bf16 v[60:63], a[156:159], a[236:239], v[60:63]\n\t"
  "ds_read_b128 a[148:151], v21 offset:34816\n\t"
  "s_add_u32 m0, s58, 0xffffc000\n\t"
  "s_waitcnt lgkmcnt(4)\n\t"
  "v_mfma_f32_16x16x32_bf16 v[56:59], a[156:159], a[232:235], v[56:59]\n\t"
  "buffer_load_dwordx4 v14, s[20:23], s57 offen offset:0 sc1 lds\n\t"
  "v_mfma_f32_16x16x32_bf16 v[64:67], a[128:131], a[232:235], v[64:67]\n\t"
  "ds_read_b128 a[152:155], v21 offset:36864\n\t"
  "v_mfma_f32_16x16x32_bf16 v[68:71], a[128:131], a[236:239], v[68:71]\n\t"
  "v_mfma_f32_16x16x32_bf16 v[76:79], a[132:135], a[236:239], v[76:79]\n\t"
  "ds_read_b128 a[156:159], v21 offset:38912\n\t"
  "v_mfma_f32_16x16x32_bf16 v[72:75], a[132:135], a[232:235], v[72:75]\n\t"
  "s_waitcnt lgkmcnt(4)\n\t"
  "v_mfma_f32_16x16x32_bf16 v[80:83], a[136:139], a[232:235], v[80:83]\n\t"
  "ds_read_b128 a[128:131], v21 offset:33792\n\t"
  "v_mfma_f32_16x16x32_bf16 v[84:87], a[136:139], a[236:239], v[84:87]\n\t"
  "buffer_load_dwordx4 a[228:231], v13, s[20:23], s56 offen offset:2048 sc1\n\t"
  "v_mfma_f32_16x16x32_bf16 v[92:95], a[140:143], a[236:239], v[92:95]\n\t"
  "ds_read_b128 a[132:135], v21 offset:35840\n\t"
  "s_add_u32 s56, s56, 0x80000\n\t"
  "s_waitcnt lgkmcnt(4)\n\t"
  "v_mfma_f32_16x16x32_bf16 v[88:91], a[140:143], a[232:235], v[88:91]\n\t"
  "buffer_load_dwordx4 v15, s[20:23], s57 offen offset:1024 sc1 lds\n\t"
  "v_mfma_f32_16x16x32_bf16 v[32:35], a[144:147], a[240:243], v[32:35]\n\t"
  "ds_read_b128 a[136:139], v21 offset:37888\n\t"
  "s_add_u32 s57, s57, 0x8000\n\t"
  "v_mfma_f32_16x16x32_bf16 v[36:39], a[144:147], a[244:247], v[36:39]\n\t"
  "v_mfma_f32_16x16x32_bf16 v[44:47], a[148:151], a[244:247], v[44:47]\n\t"
  "ds_read_b128 a[140:143], v21 offset:39936\n\t"
  "v_mfma_f32_16x16x32_bf16 v[40:43], a[148:151], a[240:243], v[40:43]\n\t"
  "s_waitcnt lgkmcnt(4)\n\t"
  "v_mfma_f32_16x16x32_bf16 v[48:51], a[152:155], a[240:243], v[48:51]\n\t"
  "ds_read_b128 a[144:147], v21 offset:40960\n\t"
  "v_mfma_f32_16x16x32_bf16 v[52:55], a[152:155], a[244:247], v[52:55]\n\t"
  "buffer_load_dwordx4 a[232:235], v13, s[20:23], s56 offen offset:0 sc1\n\t"
  "v_mfma_f32_16x16x32_bf16 v[60:63], a[156:159], a[244:247], v[60:63]\n\t"
  "ds_read_b128 a[148:151], v21 offset:43008\n\t"
  "s_add_u32 m0, s58, 0xffffe000\n\t"
  "s_waitcnt lgkmcnt(4)\n\t"
  "v_mfma_f32_16x16x32_bf16 v[56:59], a[156:159], a[240:243], v[56:59]\n\t"
  "buffer_load_dwordx4 v14, s[20:23], s57 offen offset:0 sc1 lds\n\t"
  "v_mfma_f32_16x16x32_bf16 v[64:67], a[128:131], a[240:243], v[64:67]\n\t"
  "ds_read_b128 a[152:155], v21 offset:45056\n\t"
  "v_mfma_f32_16x16x32_bf16 v[68:71], a[128:131], a[244:247], v[68:71]\n\t"
  "v_mfma_f32_16x16x32_bf16 v[76:79], a[132:135], a[244:247], v[76:79]\n\t"
  "ds_read_b128 a[156:159], v21 offset:47104\n\t"
  "v_mfma_f32_16x16x32_bf16 v[72:75], a[132:135], a[240:243], v[72:75]\n\t"
  "s_waitcnt lgkmcnt(4)\n\t"
  "v_mfma_f32_16x16x32_bf16 v[80:83], a[136:139], a[240:243], v[80:83]\n\t"
  "ds_read_b128 a[128:131], v21 offset:41984\n\t"
  "v_mfma_f32_16x16x32_bf16 v[84:87], a[136:139], a[244:247], v[84:87]\n\t"
  "buffer_load_dwordx4 a[236:239], v13, s[20:23], s56 offen offset:2048 sc1\n\t"
  "v_mfma_f32_16x16x32_bf16 v[92:95], a[140:143], a[244:247], v[92:95]\n\t"
  "ds_read_b128 a[132:135], v21 offset:44032\n\t"
  "s_add_u32 s56, s56, 0x60000\n\t"
  "s_waitcnt lgkmcnt(4)\n\t"
  "v_mfma_f32_16x16x32_bf16 v[88:91], a[140:143], a[240:243], v[88:91]\n\t"
  "buffer_load_dwordx4 v15, s[20:23], s57 offen offset:1024 sc1 lds\n\t"
  "v_mfma_f32_16x16x32_bf16 v[32:35], a[144:147], a[248:251], v[32:35]\n\t"
  "ds_read_b128 a[136:139], v21 offset:46080\n\t"
  "s_waitcnt vmcnt(20)\n\t"
  "s_add_u32 s57, s57, 0x6000\n\t"
  "v_mfma_f32_16x16x32_bf16 v[36:39], a[144:147], a[252:255], v[36:39]\n\t"
  "s_barrier\n\t"
  "v_mfma_f32_16x16x32_bf16 v[44:47], a[148:151], a[252:255], v[44:47]\n\t"
  "ds_read_b128 a[140:143], v21 offset:48128\n\t"
  "v_mfma_f32_16x16x32_bf16 v[40:43], a[148:151], a[248:251], v[40:43]\n\t"
  "s_waitcnt lgkmcnt(4)\n\t"
  "v_mfma_f32_16x16x32_bf16 v[48:51], a[152:155], a[248:251], v[48:51]\n\t"
  "ds_read_b128 a[144:147], v21 offset:49152\n\t"
  "v_mfma_f32_16x16x32_bf16 v[52:55], a[152:155], a[252:255], v[52:55]\n\t"
  "buffer_load_dwordx4 a[240:243], v13, s[20:23], s56 offen offset:0 sc1\n\t"
  "v_mfma_f32_16x16x32_bf16 v[60:63], a[156:159], a[252:255], v[60:63]\n\t"
  "ds_read_b128 a[148:151], v21 offset:51200\n\t"
  "s_add_u32 m0, s58, 0xffff8000\n\t"
  "s_waitcnt lgkmcnt(4)\n\t"
  "v_mfma_f32_16x16x32_bf16 v[56:59], a[156:159], a[248:251], v[56:59]\n\t"
  "buffer_load_dwordx4 v14, s[20:23], s57 offen offset:0 sc1 lds\n\t"
  "v_mfma_f32_16x16x32_bf16 v[64:67], a[128:131], a[248:251], v[64:67]\n\t"
  "ds_read_b128 a[152:155], v21 offset:53248\n\t"
  "v_mfma_f32_16x16x32_bf16 v[68:71], a[128:131], a[252:255], v[68:71]\n\t"
  "v_mfma_f32_16x16x32_bf16 v[76:79], a[132:135], a[252:255], v[76:79]\n\t"
  "ds_read_b128 a[156:159], v21 offset:55296\n\t"
  "v_mfma_f32_16x16x32_bf16 v[72:75], a[132:135], a[248:251], v[72:75]\n\t"
  "s_waitcnt lgkmcnt(4)\n\t"
  "v_mfma_f32_16x16x32_bf16 v[80:83], a[136:139], a[248:251], v[80:83]\n\t"
  "ds_read_b128 a[128:131], v21 offset:50176\n\t"
  "v_mfma_f32_16x16x32_bf16 v[84:87], a[136:139], a[252:255], v[84:87]\n\t"
  "buffer_load_dwordx4 a[244:247], v13, s[20:23], s56 offen offset:2048 sc1\n\t"
  "v_mfma_f32_16x16x32_bf16 v[92:95], a[140:143], a[252:255], v[92:95]\n\t"
  "ds_read_b128 a[132:135], v21 offset:52224\n\t"
  "s_add_u32 s56, s56, 0x80000\n\t"
  "s_waitcnt lgkmcnt(4)\n\t"
  "v_mfma_f32_16x16x32_bf16 v[88:91], a[140:143], a[248:251], v[88:91]\n\t"
  "buffer_load_dwordx4 v15, s[20:23], s57 offen offset:1024 sc1 lds\n\t"
  "v_mfma_f32_16x16x32_bf16 v[32:35], a[144:147], a[192:195], v[32:35]\n\t"
  "ds_read_b128 a[136:139], v21 offset:54272\n\t"
  "s_add_u32 s57, s57, 0x8000\n\t"
  "s_waitcnt vmcnt(18)\n\t"
  "v_add_f32 v128, v128, v129\n\t"
  "v_mfma_f32_16x16x32_bf16 v[36:39], a[144:147], a[196:199], v[36:39]\n\t"
  "v_add_f32 v132, v132, v133\n\t"
  "v_add_f32 v130, v130, v131\n\t"
  "v_mfma_f32_16x16x32_bf16 v[44:47], a[148:151], a[196:199], v[44:47]\n\t"
  "ds_read_b128 a[140:143], v21 offset:56320\n\t"
  "v_add_f32 v134, v134, v135\n\t"
  "v_mfma_f32_16x16x32_bf16 v[40:43], a[148:151], a[192:195], v[40:43]\n\t"
  "s_waitcnt lgkmcnt(4)\n\t"
  "v_add_f32 v128, v128, v130\n\t"
  "v_add_f32 v132, v132, v134\n\t"
  "v_mfma_f32_16x16x32_bf16 v[48:51], a[152:155], a[192:195], v[48:51]\n\t"
  "ds_read_b128 a[144:147], v21 offset:57344\n\t"
  "v_mov_b32 v129, v128\n\t"
  "v_mfma_f32_16x16x32_bf16 v[52:55], a[152:155], a[196:199], v[52:55]\n\t"
  "buffer_load_dwordx4 a[248:251], v13, s[20:23], s56 offen offset:0 sc1\n\t"
  "v_mfma_f32_16x16x32_bf16 v[60:63], a[156:159], a[196:199], v[60:63]\n\t"
  "ds_read_b128 a[148:151], v21 offset:59392\n\t"
  "s_add_u32 m0, s58, 0xffffa000\n\t"
  "s_waitcnt lgkmcnt(4)\n\t"
  "v_mov_b32 v133, v132\n\t"
  "v_mfma_f32_16x16x32_bf16 v[56:59], a[156:159], a[192:195], v[56:59]\n\t"
  "buffer_load_dwordx4 v14, s[20:23], s57 offen offset:0 sc1 lds\n\t"
  "v_mfma_f32_16x16x32_bf16 v[64:67], a[128:131], a[192:195], v[64:67]\n\t"
  "ds_read_b128 a[152:155], v21 offset:61440\n\t"
  "s_nop 1\n\t"
  "v_mfma_f32_16x16x32_bf16 v[68:71], a[128:131], a[196:199], v[68:71]\n\t"
  "v_permlane16_swap_b32 v128, v129\n\t"
  "v_permlane16_swap_b32 v132, v133\n\t"
  "v_mfma_f32_16x16x32_bf16 v[76:79], a[132:135], a[196:199], v[76:79]\n\t"
  "ds_read_b128 a[156:159], v21 offset:63488\n\t"
  "s_nop 1\n\t"
  "v_mfma_f32_16x16x32_bf16 v[72:75], a[132:135], a[192:195], v[72:75]\n\t"
  "s_waitcnt lgkmcnt(4)\n\t"
  "v_add_f32 v128, v128, v129\n\t"
  "v_add_f32 v132, v132, v133\n\t"
  "v_mfma_f32_16x16x32_bf16 v[80:83], a[136:139], a[192:195], v[80:83]\n\t"
  "ds_read_b128 a[128:131], v21 offset:58368\n\t"
  "v_mov_b32 v129, v128\n\t"
  "v_mfma_f32_16x16x32_bf16 v[84:87], a[136:139], a[196:199], v[84:87]\n\t"
  "buffer_load_dwordx4 a[252:255], v13, s[20:23], s56 offen offset:2048 sc1\n\t"
  "v_mfma_f32_16x16x32_bf16 v[92:95], a[140:143], a[196:199], v[92:95]\n\t"
  "ds_read_b128 a[132:135], v21 offset:60416\n\t"
  "s_add_u32 s56, s56, s60\n\t"
  "s_waitcnt lgkmcnt(4)\n\t"
  "v_mov_b32 v133, v132\n\t"
  "v_mfma_f32_16x16x32_bf16 v[88:91], a[140:143], a[192:195], v[88:91]\n\t"
  "buffer_load_dwordx4 v15, s[20:23], s57 offen offset:1024 sc1 lds\n\t"
  "v_mfma_f32_16x16x32_bf16 v[32:35], a[144:147], a[200:203], v[32:35]\n\t"
  "ds_read_b128 a[136:139], v21 offset:62464\n\t"
  "s_waitcnt vmcnt(16)\n\t"
  "s_add_u32 s57, s57, s61\n\t"
  "s_nop 1\n\t"
  "v_mfma_f32_16x16x32_bf16 v[36:39], a[144:147], a[204:207], v[36:39]\n\t"
  "s_barrier\n\t"
  "v_permlane32_swap_b32 v128, v129\n\t"
  "v_permlane32_swap_b32 v132, v133\n\t"
  "v_mfma_f32_16x16x32_bf16 v[44:47], a[148:151], a[204:207], v[44:47]\n\t"
  "ds_read_b128 a[140:143], v21 offset:64512\n\t"
  "s_nop 1\n\t"
  "v_mfma_f32_16x16x32_bf16 v[40:43], a[148:151], a[200:203], v[40:43]\n\t"
  "s_waitcnt lgkmcnt(4)\n\t"
  "v_add_f32 v128, v128, v129\n\t"
  "v_add_f32 v132, v132, v133\n\t"
  "v_mfma_f32_16x16x32_bf16 v[48:51], a[152:155], a[200:203], v[48:51]\n\t"
  "ds_read_b128 a[144:147], v21 offset:0\n\t"
  "v_mov_b32 v129, 0x3727c5ac\n\t"
  "v_mfma_f32_16x16x32_bf16 v[52:55], a[152:155], a[204:207], v[52:55]\n\t"
  "buffer_load_dwordx4 a[192:195], v13, s[20:23], s56 offen offset:0 sc1\n\t"
  "v_mfma_f32_16x16x32_bf16 v[60:63], a[156:159], a[204:207], v[60:63]\n\t"
  "ds_read_b128 a[148:151], v21 offset:2048\n\t"
  "v_mov_b32 v133, 0x3727c5ac\n\t"
  "v_mfma_f32_16x16x32_bf16 v[56:59], a[156:159], a[200:203], v[56:59]\n\t"
  "s_waitcnt lgkmcnt(4)\n\t"
  "v_fmac_f32 v129, 0x3a800000, v128\n\t"
  "v_fmac_f32 v133, 0x3a800000, v132\n\t"
  "v_mfma_f32_16x16x32_bf16 v[64:67], a[128:131], a[200:203], v[64:67]\n\t"
  "ds_read_b128 a[152:155], v21 offset:4096\n\t"
  "s_add_u32 m0, s58, 0x4000\n\t"
  "v_rcp_f32 v128, v129\n\t"
  "v_mfma_f32_16x16x32_bf16 v[68:71], a[128:131], a[204:207], v[68:71]\n\t"
  "buffer_load_dwordx4 v14, s[20:23], s57 offen offset:0 sc1 lds\n\t"
  "v_mfma_f32_16x16x32_bf16 v[76:79], a[132:135], a[204:207], v[76:79]\n\t"
  "ds_read_b128 a[156:159], v21 offset:6144\n\t"
  "s_add_u32 s57, s57, 0xe000\n\t"
  "v_rcp_f32 v132, v133\n\t"
  "v_mfma_f32_16x16x32_bf16 v[72:75], a[132:135], a[200:203], v[72:75]\n\t"
  "s_waitcnt lgkmcnt(4)\n\t"
  "v_accvgpr_read_b32 v130, a176\n\t"
  "v_accvgpr_read_b32 v134, a177\n\t"
  "v_mfma_f32_16x16x32_bf16 v[80:83], a[136:139], a[200:203], v[80:83]\n\t"
  "ds_read_b128 a[128:131], v21 offset:1024\n\t"
  "v_accvgpr_read_b32 v137, a160\n\t"
  "v_mfma_f32_16x16x32_bf16 v[84:87], a[136:139], a[204:207], v[84:87]\n\t"
  "buffer_load_dwordx4 a[196:199], v13, s[20:23], s56 offen offset:2048 sc1\n\t"
  "v_mfma_f32_16x16x32_bf16 v[92:95], a[140:143], a[204:207], v[92:95]\n\t"
  "ds_read_b128 a[132:135], v21 offset:3072\n\t"
  "s_add_u32 s56, s56, 0xe0000\n\t"
  "v_lshlrev_b32 v136, 16, v137\n\t"
  "v_mfma_f32_16x16x32_bf16 v[88:91], a[140:143], a[200:203], v[88:91]\n\t"
  "s_waitcnt lgkmcnt(4)\n\t"
  "v_and_b32 v137, 0xffff0000, v137\n\t"
  "v_add_f32 v136, v136, v130\n\t"
  "v_mfma_f32_16x16x32_bf16 v[32:35], a[144:147], a[208:211], v[32:35]\n\t"
  "ds_read_b128 a[136:139], v21 offset:5120\n\t"
  "v_add_f32 v137, v137, v130\n\t"
  "v_mfma_f32_16x16x32_bf16 v[36:39], a[144:147], a[212:215], v[36:39]\n\t"
  "global_load_dword v26, v7, s[70:71]\n\t"
  "v_mfma_f32_16x16x32_bf16 v[44:47], a[148:151], a[212:215], v[44:47]\n\t"
  "ds_read_b128 a[140:143], v21 offset:7168\n\t"
  "v_accvgpr_read_b32 v139, a161\n\t"
  "v_mfma_f32_16x16x32_bf16 v[40:43], a[148:151], a[208:211], v[40:43]\n\t"
  "s_waitcnt lgkmcnt(4)\n\t"
  "v_lshlrev_b32 v138, 16, v139\n\t"
  "v_and_b32 v139, 0xffff0000, v139\n\t"
  "v_mfma_f32_16x16x32_bf16 v[48:51], a[152:155], a[208:211], v[48:51]\n\t"
  "ds_read_b128 a[144:147], v21 offset:8192\n\t"
  "v_add_f32 v138, v138, v130\n\t"
  "v_mfma_f32_16x16x32_bf16 v[52:55], a[152:155], a[212:215], v[52:55]\n\t"
  "buffer_load_dwordx4 a[200:203], v13, s[20:23], s56 offen offset:0 sc1\n\t"
  "v_mfma_f32_16x16x32_bf16 v[60:63], a[156:159], a[212:215], v[60:63]\n\t"
  "ds_read_b128 a[148:151], v21 offset:10240\n\t"
  "v_add_f32 v139, v139, v130\n\t"
  "v_mfma_f32_16x16x32_bf16 v[56:59], a[156:159], a[208:211], v[56:59]\n\t"
  "s_waitcnt lgkmcnt(4)\n\t"
  "v_accvgpr_read_b32 v141, a162\n\t"
  "v_lshlrev_b32 v140, 16, v141\n\t"
  "v_mfma_f32_16x16x32_bf16 v[64:67], a[128:131], a[208:211], v[64:67]\n\t"
  "ds_read_b128 a[152:155], v21 offset:12288\n\t"
  "s_add_u32 m0, s58, 0x6000\n\t"
  "v_and_b32 v141, 0xffff0000, v141\n\t"
  "v_mfma_f32_16x16x32_bf16 v[68:71], a[128:131], a[212:215], v[68:71]\n\t"
  "buffer_load_dwordx4 v14, s[20:23], s57 offen offset:0 sc1 lds\n\t"
  "v_mfma_f32_16x16x32_bf16 v[76:79], a[132:135], a[212:215], v[76:79]\n\t"
  "ds_read_b128 a[156:159], v21 offset:14336\n\t"
  "s_add_u32 s57, s57, 0xe000\n\t"
  "v_add_f32 v140, v140, v130\n\t"
  "v_mfma_f32_16x16x32_bf16 v[72:75], a[132:135], a[208:211], v[72:75]\n\t"
  "s_waitcnt lgkmcnt(4)\n\t"
  "v_add_f32 v141, v141, v130\n\t"
  "v_accvgpr_read_b32 v143, a163\n\t"
  "v_mfma_f32_16x16x32_bf16 v[80:83], a[136:139], a[208:211], v[80:83]\n\t"
  "ds_read_b128 a[128:131], v21 offset:9216\n\t"
  "v_lshlrev_b32 v142, 16, v143\n\t"
  "v_mfma_f32_16x16x32_bf16 v[84:87], a[136:139], a[212:215], v[84:87]\n\t"
  "buffer_load_dwordx4 a[204:207], v13, s[20:23], s56 offen offset:2048 sc1\n\t"
  "v_mfma_f32_16x16x32_bf16 v[92:95], a[140:143], a[212:215], v[92:95]\n\t"
  "ds_read_b128 a[132:135], v21 offset:11264\n\t"
  "s_add_u32 s56, s56, 0xe0000\n\t"
  "v_and_b32 v143, 0xffff0000, v143\n\t"
  "v_mfma_f32_16x16x32_bf16 v[88:91], a[140:143], a[208:211], v[88:91]\n\t"
  "s_waitcnt lgkmcnt(4)\n\t"
  "v_add_f32 v142, v142, v130\n\t"
  "v_add_f32 v143, v143, v130\n\t"
  "v_mfma_f32_16x16x32_bf16 v[32:35], a[144:147], a[216:219], v[32:35]\n\t"
  "ds_read_b128 a[136:139], v21 offset:13312\n\t"
  "s_waitcnt vmcnt(15)\n\t"
  "v_accvgpr_read_b32 v145, a164\n\t"
  "v_mfma_f32_16x16x32_bf16 v[36:39], a[144:147], a[220:223], v[36:39]\n\t"
  "s_barrier\n\t"
  "v_lshlrev_b32 v144, 16, v145\n\t"
  "v_and_b32 v145, 0xffff0000, v145\n\t"
  "v_mfma_f32_16x16x32_bf16 v[44:47], a[148:151], a[220:223], v[44:47]\n\t"
  "ds_read_b128 a[140:143], v21 offset:15360\n\t"
  "v_add_f32 v144, v144, v130\n\t"
  "v_mfma_f32_16x16x32_bf16 v[40:43], a[148:151], a[216:219], v[40:43]\n\t"
  "s_waitcnt lgkmcnt(4)\n\t"
  "v_add_f32 v145, v145, v130\n\t"
  "v_accvgpr_read_b32 v147, a165\n\t"
  "v_mfma_f32_16x16x32_bf16 v[48:51], a[152:155], a[216:219], v[48:51]\n\t"
  "ds_read_b128 a[144:147], v23 offset:49152\n\t"
  "v_lshlrev_b32 v146, 16, v147\n\t"
  "v_mfma_f32_16x16x32_bf16 v[52:55], a[152:155], a[220:223], v[52:55]\n\t"
  "buffer_load_dwordx4 a[208:211], v13, s[20:23], s56 offen offset:0 sc1\n\t"
  "v_mfma_f32_16x16x32_bf16 v[60:63], a[156:159], a[220:223], v[60:63]\n\t"
  "ds_read_b128 a[148:151], v23 offset:51200\n\t"
  "v_and_b32 v147, 0xffff0000, v147\n\t"
  "v_mfma_f32_16x16x32_bf16 v[56:59], a[156:159], a[216:219], v[56:59]\n\t"
  "s_waitcnt lgkmcnt(4)\n\t"
  "v_add_f32 v146, v146, v130\n\t"
  "v_add_f32 v147, v147, v130\n\t"
  "v_mfma_f32_16x16x32_bf16 v[64:67], a[128:131], a[216:219], v[64:67]\n\t"
  "ds_read_b128 a[152:155], v23 offset:53248\n\t"
  "s_add_u32 m0, s58, 0x8000\n\t"
  "v_accvgpr_read_b32 v149, a166\n\t"
  "v_mfma_f32_16x16x32_bf16 v[68:71], a[128:131], a[220:223], v[68:71]\n\t"
  "buffer_load_dwordx4 v14, s[20:23], s57 offen offset:0 sc1 lds\n\t"
  "v_mfma_f32_16x16x32_bf16 v[76:79], a[132:135], a[220:223], v[76:79]\n\t"
  "ds_read_b128 a[156:159], v23 offset:55296\n\t"
  "s_add_u32 s57, s57, 0xe000\n\t"
  "v_lshlrev_b32 v148, 16, v149\n\t"
  "v_mfma_f32_16x16x32_bf16 v[72:75], a[132:135], a[216:219], v[72:75]\n\t"
  "s_waitcnt lgkmcnt(4)\n\t"
  "v_and_b32 v149, 0xffff0000, v149\n\t"
  "v_add_f32 v148, v148, v130\n\t"
  "v_mfma_f32_16x16x32_bf16 v[80:83], a[136:139], a[216:219], v[80:83]\n\t"
  "ds_read_b128 a[128:131], v23 offset:50176\n\t"
  "v_add_f32 v149, v149, v130\n\t"
  "v_mfma_f32_16x16x32_bf16 v[84:87], a[136:139], a[220:223], v[84:87]\n\t"
  "buffer_load_dwordx4 a[212:215], v13, s[20:23], s56 offen offset:2048 sc1\n\t"
  "v_mfma_f32_16x16x32_bf16 v[92:95], a[140:143], a[220:223], v[92:95]\n\t"
  "ds_read_b128 a[132:135], v23 offset:52224\n\t"
  "s_add_u32 s56, s56, 0xe0000\n\t"
  "v_accvgpr_read_b32 v151, a167\n\t"
  "v_mfma_f32_16x16x32_bf16 v[88:91], a[140:143], a[216:219], v[88:91]\n\t"
  "s_waitcnt lgkmcnt(4)\n\t"
  "v_lshlrev_b32 v150, 16, v151\n\t"
  "v_and_b32 v151, 0xffff0000, v151\n\t"
  "v_mfma_f32_16x16x32_bf16 v[32:35], a[144:147], a[224:227], v[32:35]\n\t"
  "ds_read_b128 a[136:139], v23 offset:54272\n\t"
  "v_add_f32 v150, v150, v130\n\t"
  "v_mfma_f32_16x16x32_bf16 v[36:39], a[144:147], a[228:231], v[36:39]\n\t"
  "v_add_f32 v151, v151, v130\n\t"
  "v_accvgpr_read_b32 v153, a168\n\t"
  "v_mfma_f32_16x16x32_bf16 v[44:47], a[148:151], a[228:231], v[44:47]\n\t"
  "ds_read_b128 a[140:143], v23 offset:56320\n\t"
  "v_lshlrev_b32 v152, 16, v153\n\t"
  "v_mfma_f32_16x16x32_bf16 v[40:43], a[148:151], a[224:227], v[40:43]\n\t"
  "s_waitcnt lgkmcnt(4)\n\t"
  "v_and_b32 v153, 0xffff0000, v153\n\t"
  "v_add_f32 v152, v152, v134\n\t"
  "v_mfma_f32_16x16x32_bf16 v[48:51], a[152:155], a[224:227], v[48:51]\n\t"
  "ds_read_b128 a[144:147], v23 offset:57344\n\t"
  "v_add_f32 v153, v153, v134\n\t"
  "v_mfma_f32_16x16x32_bf16 v[52:55], a[152:155], a[228:231], v[52:55]\n\t"
  "buffer_load_dwordx4 a[216:219], v13, s[20:23], s56 offen offset:0 sc1\n\t"
  "v_mfma_f32_16x16x32_bf16 v[60:63], a[156:159], a[228:231], v[60:63]\n\t"
  "ds_read_b128 a[148:151], v23 offset:59392\n\t"
  "v_accvgpr_read_b32 v155, a169\n\t"
  "v_mfma_f32_16x16x32_bf16 v[56:59], a[156:159], a[224:227], v[56:59]\n\t"
  "s_waitcnt lgkmcnt(4)\n\t"
  "v_lshlrev_b32 v154, 16, v155\n\t"
  "v_and_b32 v155, 0xffff0000, v155\n\t"
  "v_mfma_f32_16x16x32_bf16 v[64:67], a[128:131], a[224:227], v[64:67]\n\t"
  "ds_read_b128 a[152:155], v23 offset:61440\n\t"
  "s_add_u32 m0, s58, 0xa000\n\t"
  "v_add_f32 v154, v154, v134\n\t"
  "v_mfma_f32_16x16x32_bf16 v[68:71], a[128:131], a[228:231], v[68:71]\n\t"
  "buffer_load_dwordx4 v14, s[20:23], s57 offen offset:0 sc1 lds\n\t"
  "v_mfma_f32_16x16x32_bf16 v[76:79], a[132:135], a[228:231], v[76:79]\n\t"
  "ds_read_b128 a[156:159], v23 offset:63488\n\t"
  "s_add_u32 s57, s57, 0xe000\n\t"
  "v_add_f32 v155, v155, v134\n\t"
  "v_mfma_f32_16x16x32_bf16 v[72:75], a[132:135], a[224:227], v[72:75]\n\t"
  "s_waitcnt lgkmcnt(4)\n\t"
  "v_accvgpr_read_b32 v157, a170\n\t"
  "v_lshlrev_b32 v156, 16, v157\n\t"
  "v_mfma_f32_16x16x32_bf16 v[80:83], a[136:139], a[224:227], v[80:83]\n\t"
  "ds_read_b128 a[128:131], v23 offset:58368\n\t"
  "v_and_b32 v157, 0xffff0000, v157\n\t"
  "v_mfma_f32_16x16x32_bf16 v[84:87], a[136:139], a[228:231], v[84:87]\n\t"
  "buffer_load_dwordx4 a[220:223], v13, s[20:23], s56 offen offset:2048 sc1\n\t"
  "v_mfma_f32_16x16x32_bf16 v[92:95], a[140:143], a[228:231], v[92:95]\n\t"
  "ds_read_b128 a[132:135], v23 offset:60416\n\t"
  "s_add_u32 s56, s56, 0xe0000\n\t"
  "v_add_f32 v156, v156, v134\n\t"
  "v_mfma_f32_16x16x32_bf16 v[88:91], a[140:143], a[224:227], v[88:91]\n\t"
  "s_waitcnt lgkmcnt(4)\n\t"
  "v_add_f32 v157, v157, v134\n\t"
  "v_accvgpr_read_b32 v159, a171\n\t"
  "v_mfma_f32_16x16x32_bf16 v[32:35], a[144:147], a[232:235], v[32:35]\n\t"
  "ds_read_b128 a[136:139], v23 offset:62464\n\t"
  "s_waitcnt vmcnt(13)\n\t"
  "v_lshlrev_b32 v158, 16, v159\n\t"
  "v_mfma_f32_16x16x32_bf16 v[36:39], a[144:147], a[236:239], v[36:39]\n\t"
  "s_barrier\n\t"
  "v_and_b32 v159, 0xffff0000, v159\n\t"
  "v_add_f32 v158, v158, v134\n\t"
  "v_mfma_f32_16x16x32_bf16 v[44:47], a[148:151], a[236:239], v[44:47]\n\t"
  "ds_read_b128 a[140:143], v23 offset:64512\n\t"
  "v_add_f32 v159, v159, v134\n\t"
  "v_mfma_f32_16x16x32_bf16 v[40:43], a[148:151], a[232:235], v[40:43]\n\t"
  "s_waitcnt lgkmcnt(4)\n\t"
  "v_accvgpr_read_b32 v161, a172\n\t"
  "v_lshlrev_b32 v160, 16, v161\n\t"
  "v_mfma_f32_16x16x32_bf16 v[48:51], a[152:155], a[232:235], v[48:51]\n\t"
  "ds_read_b128 a[144:147], v23 offset:32768\n\t"
  "v_and_b32 v161, 0xffff0000, v161\n\t"
  "v_mfma_f32_16x16x32_bf16 v[52:55], a[152:155], a[236:239], v[52:55]\n\t"
  "buffer_load_dwordx4 a[224:227], v13, s[20:23], s56 offen offset:0 sc1\n\t"
  "v_mfma_f32_16x16x32_bf16 v[60:63], a[156:159], a[236:239], v[60:63]\n\t"
  "ds_read_b128 a[148:151], v23 offset:34816\n\t"
  "v_add_f32 v160, v160, v134\n\t"
  "v_mfma_f32_16x16x32_bf16 v[56:59], a[156:159], a[232:235], v[56:59]\n\t"
  "s_waitcnt lgkmcnt(4)\n\t"
  "v_add_f32 v161, v161, v134\n\t"
  "v_accvgpr_read_b32 v163, a173\n\t"
  "v_mfma_f32_16x16x32_bf16 v[64:67], a[128:131], a[232:235], v[64:67]\n\t"
  "ds_read_b128 a[152:155], v23 offset:36864\n\t"
  "s_add_u32 m0, s58, 0xc000\n\t"
  "v_lshlrev_b32 v162, 16, v163\n\t"
  "v_mfma_f32_16x16x32_bf16 v[68:71], a[128:131], a[236:239], v[68:71]\n\t"
  "buffer_load_dwordx4 v14, s[20:23], s57 offen offset:0 sc1 lds\n\t"
  "v_mfma_f32_16x16x32_bf16 v[76:79], a[132:135], a[236:239], v[76:79]\n\t"
  "ds_read_b128 a[156:159], v23 offset:38912\n\t"
  "s_add_u32 s57, s57, 0xe000\n\t"
  "v_and_b32 v163, 0xffff0000, v163\n\t"
  "v_mfma_f32_16x16x32_bf16 v[72:75], a[132:135], a[232:235], v[72:75]\n\t"
  "s_waitcnt lgkmcnt(4)\n\t"
  "v_add_f32 v162, v162, v134\n\t"
  "v_add_f32 v163, v163, v134\n\t"
  "v_mfma_f32_16x16x32_bf16 v[80:83], a[136:139], a[232:235], v[80:83]\n\t"
  "ds_read_b128 a[128:131], v23 offset:33792\n\t"
  "v_accvgpr_read_b32 v165, a174\n\t"
  "v_mfma_f32_16x16x32_bf16 v[84:87], a[136:139], a[236:239], v[84:87]\n\t"
  "buffer_load_dwordx4 a[228:231], v13, s[20:23], s56 offen offset:2048 sc1\n\t"
  "v_mfma_f32_16x16x32_bf16 v[92:95], a[140:143], a[236:239], v[92:95]\n\t"
  "ds_read_b128 a[132:135], v23 offset:35840\n\t"
  "s_add_u32 s56, s56, 0xe0000\n\t"
  "v_lshlrev_b32 v164, 16, v165\n\t"
  "v_mfma_f32_16x16x32_bf16 v[88:91], a[140:143], a[232:235], v[88:91]\n\t"
  "s_waitcnt lgkmcnt(4)\n\t"
  "v_and_b32 v165, 0xffff0000, v165\n\t"
  "v_add_f32 v164, v164, v134\n\t"
  "v_mfma_f32_16x16x32_bf16 v[32:35], a[144:147], a[240:243], v[32:35]\n\t"
  "ds_read_b128 a[136:139], v23 offset:37888\n\t"
  "v_add_f32 v165, v165, v134\n\t"
  "v_mfma_f32_16x16x32_bf16 v[36:39], a[144:147], a[244:247], v[36:39]\n\t"
  "v_accvgpr_read_b32 v167, a175\n\t"
  "v_lshlrev_b32 v166, 16, v167\n\t"
  "v_mfma_f32_16x16x32_bf16 v[44:47], a[148:151], a[244:247], v[44:47]\n\t"
  "ds_read_b128 a[140:143], v23 offset:39936\n\t"
  "v_and_b32 v167, 0xffff0000, v167\n\t"
  "v_mfma_f32_16x16x32_bf16 v[40:43], a[148:151], a[240:243], v[40:43]\n\t"
  "s_waitcnt lgkmcnt(4)\n\t"
  "v_add_f32 v166, v166, v134\n\t"
  "v_add_f32 v167, v167, v134\n\t"
  "v_mfma_f32_16x16x32_bf16 v[48:51], a[152:155], a[240:243], v[48:51]\n\t"
  "ds_read_b128 a[144:147], v23 offset:41984\n\t"
  "v_mfma_f32_16x16x32_bf16 v[52:55], a[152:155], a[244:247], v[52:55]\n\t"
  "buffer_load_dwordx4 a[232:235], v13, s[20:23], s56 offen offset:0 sc1\n\t"
  "v_mfma_f32_16x16x32_bf16 v[60:63], a[156:159], a[244:247], v[60:63]\n\t"
  "ds_read_b128 a[148:151], v23 offset:44032\n\t"
  "v_mfma_f32_16x16x32_bf16 v[56:59], a[156:159], a[240:243], v[56:59]\n\t"
  "s_waitcnt lgkmcnt(4)\n\t"
  "v_mfma_f32_16x16x32_bf16 v[64:67], a[128:131], a[240:243], v[64:67]\n\t"
  "ds_read_b128 a[152:155], v23 offset:46080\n\t"
  "s_add_u32 m0, s58, 0xe000\n\t"
  "v_mfma_f32_16x16x32_bf16 v[68:71], a[128:131], a[244:247], v[68:71]\n\t"
  "buffer_load_dwordx4 v14, s[20:23], s57 offen offset:0 sc1 lds\n\t"
  "v_mfma_f32_16x16x32_bf16 v[76:79], a[132:135], a[244:247], v[76:79]\n\t"
  "ds_read_b128 a[156:159], v23 offset:48128\n\t"
  "s_add_u32 s57, s57, 0xe000\n\t"
  "v_mfma_f32_16x16x32_bf16 v[72:75], a[132:135], a[240:243], v[72:75]\n\t"
  "s_waitcnt lgkmcnt(4)\n\t"
  "v_mfma_f32_16x16x32_bf16 v[80:83], a[136:139], a[240:243], v[80:83]\n\t"
  "ds_read_b128 a[128:131], v23 offset:40960\n\t"
  "v_mfma_f32_16x16x32_bf16 v[84:87], a[136:139], a[244:247], v[84:87]\n\t"
  "buffer_load_dwordx4 a[236:239], v13, s[20:23], s56 offen offset:2048 sc1\n\t"
  "v_mfma_f32_16x16x32_bf16 v[92:95], a[140:143], a[244:247], v[92:95]\n\t"
  "ds_read_b128 a[132:135], v23 offset:43008\n\t"
  "s_add_u32 s56, s56, 0xe0000\n\t"
  "v_mfma_f32_16x16x32_bf16 v[88:91], a[140:143], a[240:243], v[88:91]\n\t"
  "s_waitcnt lgkmcnt(4)\n\t"
  "v_mfma_f32_16x16x32_bf16 v[64:67], a[144:147], a[248:251], v[64:67]\n\t"
  "ds_read_b128 a[136:139], v23 offset:45056\n\t"
  "s_waitcnt vmcnt(13)\n\t"
  "v_mfma_f32_16x16x32_bf16 v[68:71], a[144:147], a[252:255], v[68:71]\n\t"
  "s_barrier\n\t"
  "v_mfma_f32_16x16x32_bf16 v[76:79], a[148:151], a[252:255], v[76:79]\n\t"
  "ds_read_b128 a[140:143], v23 offset:47104\n\t"
  "v_mfma_f32_16x16x32_bf16 v[72:75], a[148:151], a[248:251], v[72:75]\n\t"
  "s_waitcnt lgkmcnt(4)\n\t"
  "v_mfma_f32_16x16x32_bf16 v[80:83], a[152:155], a[248:251], v[80:83]\n\t"
  "ds_read_b128 a[144:147], v21 offset:16384\n\t"
  "v_mfma_f32_16x16x32_bf16 v[84:87], a[152:155], a[252:255], v[84:87]\n\t"
  "buffer_load_dwordx4 a[240:243], v13, s[20:23], s56 offen offset:0 sc1\n\t"
  "v_mfma_f32_16x16x32_bf16 v[92:95], a[156:159], a[252:255], v[92:95]\n\t"
  "ds_read_b128 a[148:151], v21 offset:18432\n\t"
  "s_add_u32 m0, s58, 0x0\n\t"
  "s_waitcnt lgkmcnt(4)\n\t"
  "v_mfma_f32_16x16x32_bf16 v[88:91], a[156:159], a[248:251], v[88:91]\n\t"
  "buffer_load_dwordx4 v14, s[20:23], s57 offen offset:0 sc1 lds\n\t"
  "v_mfma_f32_16x16x32_bf16 v[32:35], a[128:131], a[248:251], v[32:35]\n\t"
  "ds_read_b128 a[152:155], v21 offset:20480\n\t"
  "s_add_u32 s57, s57, 0xe000\n\t"
  "v_mfma_f32_16x16x32_bf16 v[36:39], a[128:131], a[252:255], v[36:39]\n\t"
  "v_mfma_f32_16x16x32_bf16 v[44:47], a[132:135], a[252:255], v[44:47]\n\t"
  "ds_read_b128 a[156:159], v21 offset:22528\n\t"
  "v_mfma_f32_16x16x32_bf16 v[40:43], a[132:135], a[248:251], v[40:43]\n\t"
  "s_waitcnt lgkmcnt(4)\n\t"
  "v_mfma_f32_16x16x32_bf16 v[48:51], a[136:139], a[248:251], v[48:51]\n\t"
  "ds_read_b128 a[128:131], v21 offset:24576\n\t"
  "v_cvt_pk_bf16_f32 v96, v64, v65\n\t"
  "v_cvt_pk_bf16_f32 v97, v66, v67\n\t"
  "v_mfma_f32_16x16x32_bf16 v[52:55], a[136:139], a[252:255], v[52:55]\n\t"
  "buffer_load_dwordx4 a[244:247], v13, s[20:23], s56 offen offset:2048 sc1\n\t"
  "v_cvt_pk_bf16_f32 v98, v72, v73\n\t"
  "v_cvt_pk_bf16_f32 v99, v74, v75\n\t"
  "v_mfma_f32_16x16x32_bf16 v[60:63], a[140:143], a[252:255], v[60:63]\n\t"
  "ds_read_b128 a[132:135], v21 offset:26624\n\t"
  "s_add_u32 s56, s56, 0xe0000\n\t"
  "s_add_u32 m0, s58, 0x2000\n\t"
  "v_min_u32 v96, -2, v96\n\t"
  "v_min_u32 v97, -2, v97\n\t"
  "s_waitcnt lgkmcnt(4)\n\t"
  "v_mfma_f32_16x16x32_bf16 v[56:59], a[140:143], a[248:251], v[56:59]\n\t"
  "buffer_load_dwordx4 v14, s[20:23], s57 offen offset:0 sc1 lds\n\t"
  "v_min_u32 v98, -2, v98\n\t"
  "v_min_u32 v99, -2, v99\n\t"
  "v_mfma_f32_16x16x32_bf16 v[32:35], a[144:147], a[192:195], v[32:35]\n\t"
  "ds_read_b128 a[136:139], v21 offset:28672\n\t"
  "s_waitcnt vmcnt(11)\n\t"
  "s_add_u32 s57, s57, 0xe000\n\t"
  "buffer_store_dwordx4 v[96:99], v18, s[20:23], 0 offen offset:0 sc1\n\t"
  "v_cvt_pk_bf16_f32 v100, v80, v81\n\t"
  "v_mfma_f32_16x16x32_bf16 v[36:39], a[144:147], a[196:199], v[36:39]\n\t"
  "s_barrier\n\t"
  "v_cvt_pk_bf16_f32 v101, v82, v83\n\t"
  "v_cvt_pk_bf16_f32 v102, v88, v89\n\t"
  "v_mfma_f32_16x16x32_bf16 v[44:47], a[148:151], a[196:199], v[44:47]\n\t"
  "ds_read_b128 a[140:143], v21 offset:30720\n\t"
  "s_waitcnt lgkmcnt(4)\n\t"
  "v_cvt_pk_bf16_f32 v103, v90, v91\n\t"
  "v_min_u32 v100, -2, v100\n\t"
  "v_mfma_f32_16x16x32_bf16 v[40:43], a[148:151], a[192:195], v[40:43]\n\t"
  "buffer_load_dwordx4 a[248:251], v13, s[20:23], s56 offen offset:0 sc1\n\t"
  "v_min_u32 v101, -2, v101\n\t"
  "v_min_u32 v102, -2, v102\n\t"
  "v_mfma_f32_16x16x32_bf16 v[48:51], a[152:155], a[192:195], v[48:51]\n\t"
  "ds_read_b128 a[144:147], v21 offset:32768\n\t"
  "v_min_u32 v103, -2, v103\n\t"
  "buffer_store_dwordx4 v[100:103], v18, s[20:23], 0 offen offset:1024 sc1\n\t"
  "v_mfma_f32_16x16x32_bf16 v[52:55], a[152:155], a[196:199], v[52:55]\n\t"
  "v_cvt_pk_bf16_f32 v104, v68, v69\n\t"
  "v_cvt_pk_bf16_f32 v105, v70, v71\n\t"
  "v_mfma_f32_16x16x32_bf16 v[60:63], a[156:159], a[196:199], v[60:63]\n\t"
  "ds_read_b128 a[148:151], v21 offset:34816\n\t"
  "s_waitcnt lgkmcnt(4)\n\t"
  "v_cvt_pk_bf16_f32 v106, v76, v77\n\t"
  "v_cvt_pk_bf16_f32 v107, v78, v79\n\t"
  "v_mfma_f32_16x16x32_bf16 v[56:59], a[156:159], a[192:195], v[56:59]\n\t"
  "buffer_load_dwordx4 a[252:255], v13, s[20:23], s56 offen offset:2048 sc1\n\t"
  "v_min_u32 v104, -2, v104\n\t"
  "v_min_u32 v105, -2, v105\n\t"
  "v_mfma_f32_16x16x32_bf16 v[32:35], a[128:131], a[200:203], v[32:35]\n\t"
  "ds_read_b128 a[152:155], v21 offset:36864\n\t"
  "s_add_u32 s56, s56, 0xe0000\n\t"
  "s_add_u32 m0, s58, 0xffffc000\n\t"
  "v_min_u32 v106, -2, v106\n\t"
  "v_min_u32 v107, -2, v107\n\t"
  "v_mfma_f32_16x16x32_bf16 v[36:39], a[128:131], a[204:207], v[36:39]\n\t"
  "buffer_load_dwordx4 v14, s[20:23], s57 offen offset:0 sc1 lds\n\t"
  "buffer_store_dwordx4 v[104:107], v18, s[20:23], 0 offen offset:2048 sc1\n\t"
  "v_cvt_pk_bf16_f32 v108, v84, v85\n\t"
  "v_mfma_f32_16x16x32_bf16 v[44:47], a[132:135], a[204:207], v[44:47]\n\t"
  "ds_read_b128 a[156:159], v21 offset:38912\n\t"
  "s_add_u32 s57, s57, 0xe000\n\t"
  "s_waitcnt lgkmcnt(4)\n\t"
  "v_cvt_pk_bf16_f32 v109, v86, v87\n\t"
  "v_cvt_pk_bf16_f32 v110, v92, v93\n\t"
  "v_mfma_f32_16x16x32_bf16 v[40:43], a[132:135], a[200:203], v[40:43]\n\t"
  "buffer_load_dwordx4 a[192:195], v13, s[20:23], s56 offen offset:0 sc1\n\t"
  "v_cvt_pk_bf16_f32 v111, v94, v95\n\t"
  "v_min_u32 v108, -2, v108\n\t"
  "v_mfma_f32_16x16x32_bf16 v[48:51], a[136:139], a[200:203], v[48:51]\n\t"
  "ds_read_b128 a[128:131], v21 offset:40960\n\t"
  "v_min_u32 v109, -2, v109\n\t"
  "v_min_u32 v110, -2, v110\n\t"
  "v_mfma_f32_16x16x32_bf16 v[52:55], a[136:139], a[204:207], v[52:55]\n\t"
  "v_min_u32 v111, -2, v111\n\t"
  "buffer_store_dwordx4 v[108:111], v18, s[20:23], 0 offen offset:3072 sc1\n\t"
  "v_mfma_f32_16x16x32_bf16 v[60:63], a[140:143], a[204:207], v[60:63]\n\t"
  "ds_read_b128 a[132:135], v21 offset:43008\n\t"
  "s_add_u32 m0, s58, 0xffffe000\n\t"
  "s_waitcnt lgkmcnt(4)\n\t"
  "v_mfma_f32_16x16x32_bf16 v[56:59], a[140:143], a[200:203], v[56:59]\n\t"
  "buffer_load_dwordx4 v14, s[20:23], s57 offen offset:0 sc1 lds\n\t"
  "v_mfma_f32_16x16x32_bf16 v[32:35], a[144:147], a[208:211], v[32:35]\n\t"
  "ds_read_b128 a[136:139], v21 offset:45056\n\t"
  "s_waitcnt vmcnt(14)\n\t"
  "s_add_u32 s57, s57, 0xe000\n\t"
  "v_mfma_f32_16x16x32_bf16 v[36:39], a[144:147], a[212:215], v[36:39]\n\t"
  "s_barrier\n\t"
  "buffer_load_dwordx4 a[196:199], v13, s[20:23], s56 offen offset:2048 sc1\n\t"
  "v_mfma_f32_16x16x32_bf16 v[44:47], a[148:151], a[212:215], v[44:47]\n\t"
  "ds_read_b128 a[140:143], v21 offset:47104\n\t"
  "s_add_u32 s56, s56, 0xe0000\n\t"
  "s_waitcnt lgkmcnt(4)\n\t"
  "v_mfma_f32_16x16x32_bf16 v[40:43], a[148:151], a[208:211], v[40:43]\n\t"
  "buffer_load_dwordx4 a[200:203], v13, s[20:23], s56 offen offset:0 sc1\n\t"
  "v_mfma_f32_16x16x32_bf16 v[48:51], a[152:155], a[208:211], v[48:51]\n\t"
  "ds_read_b128 a[144:147], v21 offset:49152\n\t"
  "v_mfma_f32_16x16x32_bf16 v[52:55], a[152:155], a[212:215], v[52:55]\n\t"
  "v_mfma_f32_16x16x32_bf16 v[60:63], a[156:159], a[212:215], v[60:63]\n\t"
  "ds_read_b128 a[148:151], v21 offset:51200\n\t"
  "s_waitcnt lgkmcnt(4)\n\t"
  "v_mfma_f32_16x16x32_bf16 v[56:59], a[156:159], a[208:211], v[56:59]\n\t"
  "buffer_load_dwordx4 a[204:207], v13, s[20:23], s56 offen offset:2048 sc1\n\t"
  "v_mfma_f32_16x16x32_bf16 v[32:35], a[128:131], a[216:219], v[32:35]\n\t"
  "ds_read_b128 a[152:155], v21 offset:53248\n\t"
  "s_add_u32 s56, s56, 0xe0000\n\t"
  "s_add_u32 m0, s58, 0xffff8000\n\t"
  "v_mfma_f32_16x16x32_bf16 v[36:39], a[128:131], a[220:223], v[36:39]\n\t"
  "buffer_load_dwordx4 v14, s[20:23], s57 offen offset:0 sc1 lds\n\t"
  "v_mfma_f32_16x16x32_bf16 v[44:47], a[132:135], a[220:223], v[44:47]\n\t"
  "ds_read_b128 a[156:159], v21 offset:55296\n\t"
  "s_add_u32 s57, s57, 0xe000\n\t"
  "s_waitcnt lgkmcnt(4)\n\t"
  "v_mfma_f32_16x16x32_bf16 v[40:43], a[132:135], a[216:219], v[40:43]\n\t"
  "buffer_load_dwordx4 a[208:211], v13, s[20:23], s56 offen offset:0 sc1\n\t"
  "v_mfma_f32_16x16x32_bf16 v[48:51], a[136:139], a[216:219], v[48:51]\n\t"
  "ds_read_b128 a[128:131], v21 offset:57344\n\t"
  "v_mfma_f32_16x16x32_bf16 v[52:55], a[136:139], a[220:223], v[52:55]\n\t"
  "v_mfma_f32_16x16x32_bf16 v[60:63], a[140:143], a[220:223], v[60:63]\n\t"
  "ds_read_b128 a[132:135], v21 offset:59392\n\t"
  "s_add_u32 m0, s58, 0xffffa000\n\t"
  "s_waitcnt lgkmcnt(4)\n\t"
  "v_mfma_f32_16x16x32_bf16 v[56:59], a[140:143], a[216:219], v[56:59]\n\t"
  "buffer_load_dwordx4 v14, s[20:23], s57 offen offset:0 sc1 lds\n\t"
  "v_mfma_f32_16x16x32_bf16 v[32:35], a[144:147], a[224:227], v[32:35]\n\t"
  "ds_read_b128 a[136:139], v21 offset:61440\n\t"
  "s_waitcnt vmcnt(15)\n\t"
  "s_add_u32 s57, s57, 0xe000\n\t"
  "v_mfma_f32_16x16x32_bf16 v[36:39], a[144:147], a[228:231], v[36:39]\n\t"
  "s_barrier\n\t"
  "buffer_load_dwordx4 a[212:215], v13, s[20:23], s56 offen offset:2048 sc1\n\t"
  "v_mfma_f32_16x16x32_bf16 v[44:47], a[148:151], a[228:231], v[44:47]\n\t"
  "ds_read_b128 a[140:143], v21 offset:63488\n\t"
  "s_add_u32 s56, s56, 0xe0000\n\t"
  "s_waitcnt lgkmcnt(4)\n\t"
  "v_mfma_f32_16x16x32_bf16 v[40:43], a[148:151], a[224:227], v[40:43]\n\t"
  "buffer_load_dwordx4 a[216:219], v13, s[20:23], s56 offen offset:0 sc1\n\t"
  "v_mfma_f32_16x16x32_bf16 v[48:51], a[152:155], a[224:227], v[48:51]\n\t"
  "ds_read_b128 a[144:147], v21 offset:0\n\t"
  "v_mfma_f32_16x16x32_bf16 v[52:55], a[152:155], a[228:231], v[52:55]\n\t"
  "v_mfma_f32_16x16x32_bf16 v[60:63], a[156:159], a[228:231], v[60:63]\n\t"
  "ds_read_b128 a[148:151], v21 offset:2048\n\t"
  "s_waitcnt lgkmcnt(4)\n\t"
  "v_mfma_f32_16x16x32_bf16 v[56:59], a[156:159], a[224:227], v[56:59]\n\t"
  "buffer_load_dwordx4 a[220:223], v13, s[20:23], s56 offen offset:2048 sc1\n\t"
  "v_mfma_f32_16x16x32_bf16 v[32:35], a[128:131], a[232:235], v[32:35]\n\t"
  "ds_read_b128 a[152:155], v21 offset:4096\n\t"
  "s_add_u32 s56, s56, 0xe0000\n\t"
  "s_add_u32 m0, s58, 0x4000\n\t"
  "v_mfma_f32_16x16x32_bf16 v[36:39], a[128:131], a[236:239], v[36:39]\n\t"
  "buffer_load_dwordx4 v14, s[20:23], s57 offen offset:0 sc1 lds\n\t"
  "v_mfma_f32_16x16x32_bf16 v[44:47], a[132:135], a[236:239], v[44:47]\n\t"
  "ds_read_b128 a[156:159], v21 offset:6144\n\t"
  "s_add_u32 s57, s57, 0xe000\n\t"
  "s_waitcnt lgkmcnt(4)\n\t"
  "v_mfma_f32_16x16x32_bf16 v[40:43], a[132:135], a[232:235], v[40:43]\n\t"
  "buffer_load_dwordx4 a[224:227], v13, s[20:23], s56 offen offset:0 sc1\n\t"
  "v_mfma_f32_16x16x32_bf16 v[48:51], a[136:139], a[232:235], v[48:51]\n\t"
  "ds_read_b128 a[128:131], v21 offset:8192\n\t"
  "v_mfma_f32_16x16x32_bf16 v[52:55], a[136:139], a[236:239], v[52:55]\n\t"
  "v_mfma_f32_16x16x32_bf16 v[60:63], a[140:143], a[236:239], v[60:63]\n\t"
  "ds_read_b128 a[132:135], v21 offset:10240\n\t"
  "s_add_u32 m0, s58, 0x6000\n\t"
  "s_waitcnt lgkmcnt(4)\n\t"
  "v_mfma_f32_16x16x32_bf16 v[56:59], a[140:143], a[232:235], v[56:59]\n\t"
  "buffer_load_dwordx4 v14, s[20:23], s57 offen offset:0 sc1 lds\n\t"
  "v_mfma_f32_16x16x32_bf16 v[32:35], a[144:147], a[240:243], v[32:35]\n\t"
  "ds_read_b128 a[136:139], v21 offset:12288\n\t"
  "s_waitcnt vmcnt(12)\n\t"
  "s_add_u32 s57, s57, 0xe000\n\t"
  "v_mfma_f32_16x16x32_bf16 v[36:39], a[144:147], a[244:247], v[36:39]\n\t"
  "s_barrier\n\t"
  "buffer_load_dwordx4 a[228:231], v13, s[20:23], s56 offen offset:2048 sc1\n\t"
  "v_mfma_f32_16x16x32_bf16 v[44:47], a[148:151], a[244:247], v[44:47]\n\t"
  "ds_read_b128 a[140:143], v21 offset:14336\n\t"
  "s_add_u32 s56, s56, 0xe0000\n\t"
  "s_waitcnt lgkmcnt(4)\n\t"
  "v_mfma_f32_16x16x32_bf16 v[40:43], a[148:151], a[240:243], v[40:43]\n\t"
  "buffer_load_dwordx4 a[232:235], v13, s[20:23], s56 offen offset:0 sc1\n\t"
  "v_mfma_f32_16x16x32_bf16 v[48:51], a[152:155], a[240:243], v[48:51]\n\t"
  "ds_read_b128 a[144:147], v23 offset:49152\n\t"
  "v_mfma_f32_16x16x32_bf16 v[52:55], a[152:155], a[244:247], v[52:55]\n\t"
  "v_mfma_f32_16x16x32_bf16 v[60:63], a[156:159], a[244:247], v[60:63]\n\t"
  "ds_read_b128 a[148:151], v23 offset:51200\n\t"
  "s_waitcnt lgkmcnt(4)\n\t"
  "v_mfma_f32_16x16x32_bf16 v[56:59], a[156:159], a[240:243], v[56:59]\n\t"
  "buffer_load_dwordx4 a[236:239], v13, s[20:23], s56 offen offset:2048 sc1\n\t"
  "v_mfma_f32_16x16x32_bf16 v[32:35], a[128:131], a[248:251], v[32:35]\n\t"
  "ds_read_b128 a[152:155], v23 offset:53248\n\t"
  "s_add_u32 s56, s56, 0xe0000\n\t"
  "s_add_u32 m0, s58, 0x8000\n\t"
  "v_mfma_f32_16x16x32_bf16 v[36:39], a[128:131], a[252:255], v[36:39]\n\t"
  "buffer_load_dwordx4 v14, s[20:23], s57 offen offset:0 sc1 lds\n\t"
  "v_mfma_f32_16x16x32_bf16 v[44:47], a[132:135], a[252:255], v[44:47]\n\t"
  "ds_read_b128 a[156:159], v23 offset:55296\n\t"
  "s_add_u32 s57, s57, 0xe000\n\t"
  "s_waitcnt lgkmcnt(4)\n\t"
  "v_mfma_f32_16x16x32_bf16 v[40:43], a[132:135], a[248:251], v[40:43]\n\t"
  "buffer_load_dwordx4 a[240:243], v13, s[20:23], s56 offen offset:0 sc1\n\t"
  "v_mfma_f32_16x16x32_bf16 v[48:51], a[136:139], a[248:251], v[48:51]\n\t"
  "ds_read_b128 a[128:131], v23 offset:57344\n\t"
  "v_mfma_f32_16x16x32_bf16 v[52:55], a[136:139], a[252:255], v[52:55]\n\t"
  "v_mfma_f32_16x16x32_bf16 v[60:63], a[140:143], a[252:255], v[60:63]\n\t"
  "ds_read_b128 a[132:135], v23 offset:59392\n\t"
  "s_add_u32 m0, s58, 0xa000\n\t"
  "s_waitcnt lgkmcnt(4) vmcnt(16)\n\t"
  "v_mfma_f32_16x16x32_bf16 v[56:59], a[140:143], a[248:251], v[56:59]\n\t"
  "buffer_load_dwordx4 v14, s[20:23], s57 offen offset:0 sc1 lds\n\t"
  "v_mfma_f32_16x16x32_bf16 v[32:35], a[144:147], a[192:195], v[32:35]\n\t"
  "ds_read_b128 a[136:139], v23 offset:61440\n\t"
  "s_waitcnt vmcnt(12)\n\t"
  "v_mfma_f32_16x16x32_bf16 v[36:39], a[144:147], a[196:199], v[36:39]\n\t"
  "s_barrier\n\t"
  "buffer_load_dwordx4 a[244:247], v13, s[20:23], s56 offen offset:2048 sc1\n\t"
  "v_mfma_f32_16x16x32_bf16 v[44:47], a[148:151], a[196:199], v[44:47]\n\t"
  "ds_read_b128 a[140:143], v23 offset:63488\n\t"
  "s_add_u32 s56, s56, 0xe0000\n\t"
  "s_waitcnt lgkmcnt(4)\n\t"
  "v_mfma_f32_16x16x32_bf16 v[40:43], a[148:151], a[192:195], v[40:43]\n\t"
  "buffer_load_dwordx4 a[248:251], v13, s[20:23], s56 offen offset:0 sc1\n\t"
  "v_mfma_f32_16x16x32_bf16 v[48:51], a[152:155], a[192:195], v[48:51]\n\t"
  "ds_read_b128 a[144:147], v23 offset:32768\n\t"
  "v_mfma_f32_16x16x32_bf16 v[52:55], a[152:155], a[196:199], v[52:55]\n\t"
  "v_mfma_f32_16x16x32_bf16 v[60:63], a[156:159], a[196:199], v[60:63]\n\t"
  "ds_read_b128 a[148:151], v23 offset:34816\n\t"
  "s_waitcnt lgkmcnt(4)\n\t"
  "v_mfma_f32_16x16x32_bf16 v[56:59], a[156:159], a[192:195], v[56:59]\n\t"
  "buffer_load_dwordx4 a[252:255], v13, s[20:23], s56 offen offset:2048 sc1\n\t"
  "v_mfma_f32_16x16x32_bf16 v[32:35], a[128:131], a[200:203], v[32:35]\n\t"
  "ds_read_b128 a[152:155], v23 offset:36864\n\t"
  "v_mfma_f32_16x16x32_bf16 v[36:39], a[128:131], a[204:207], v[36:39]\n\t"
  "v_mfma_f32_16x16x32_bf16 v[44:47], a[132:135], a[204:207], v[44:47]\n\t"
  "ds_read_b128 a[156:159], v23 offset:38912\n\t"
  "v_mfma_f32_16x16x32_bf16 v[40:43], a[132:135], a[200:203], v[40:43]\n\t"
  "s_waitcnt lgkmcnt(4)\n\t"
  "v_mfma_f32_16x16x32_bf16 v[48:51], a[136:139], a[200:203], v[48:51]\n\t"
  "ds_read_b128 a[128:131], v23 offset:40960\n\t"
  "v_mfma_f32_16x16x32_bf16 v[52:55], a[136:139], a[204:207], v[52:55]\n\t"
  "v_mfma_f32_16x16x32_bf16 v[60:63], a[140:143], a[204:207], v[60:63]\n\t"
  "ds_read_b128 a[132:135], v23 offset:43008\n\t"
  "v_mfma_f32_16x16x32_bf16 v[56:59], a[140:143], a[200:203], v[56:59]\n\t"
  "s_waitcnt lgkmcnt(4) vmcnt(14)\n\t"
  "v_mfma_f32_16x16x32_bf16 v[32:35], a[144:147], a[208:211], v[32:35]\n\t"
  "ds_read_b128 a[136:139], v23 offset:45056\n\t"
  "s_waitcnt vmcnt(9)\n\t"
  "v_mfma_f32_16x16x32_bf16 v[36:39], a[144:147], a[212:215], v[36:39]\n\t"
  "s_barrier\n\t"
  "buffer_load_dwordx4 v[112:115], v19, s[20:23], 0 offen offset:0 sc1\n\t"
  "v_mfma_f32_16x16x32_bf16 v[44:47], a[148:151], a[212:215], v[44:47]\n\t"
  "ds_read_b128 a[140:143], v23 offset:47104\n\t"
  "s_waitcnt lgkmcnt(4)\n\t"
  "v_mfma_f32_16x16x32_bf16 v[40:43], a[148:151], a[208:211], v[40:43]\n\t"
  "buffer_load_dwordx4 v[116:119], v19, s[20:23], 0 offen offset:1024 sc1\n\t"
  "v_mfma_f32_16x16x32_bf16 v[48:51], a[152:155], a[208:211], v[48:51]\n\t"
  "ds_read_b128 a[144:147], v21 offset:16384\n\t"
  "v_mfma_f32_16x16x32_bf16 v[52:55], a[152:155], a[212:215], v[52:55]\n\t"
  "buffer_load_dwordx4 v[120:123], v19, s[20:23], 0 offen offset:2048 sc1\n\t"
  "v_mfma_f32_16x16x32_bf16 v[60:63], a[156:159], a[212:215], v[60:63]\n\t"
  "ds_read_b128 a[148:151], v21 offset:18432\n\t"
  "s_waitcnt lgkmcnt(4)\n\t"
  "v_mfma_f32_16x16x32_bf16 v[56:59], a[156:159], a[208:211], v[56:59]\n\t"
  "buffer_load_dwordx4 v[124:127], v19, s[20:23], 0 offen offset:3072 sc1\n\t"
  "v_mfma_f32_16x16x32_bf16 v[32:35], a[128:131], a[216:219], v[32:35]\n\t"
  "ds_read_b128 a[152:155], v21 offset:20480\n\t"
  "v_mfma_f32_16x16x32_bf16 v[36:39], a[128:131], a[220:223], v[36:39]\n\t"
  "v_mfma_f32_16x16x32_bf16 v[44:47], a[132:135], a[220:223], v[44:47]\n\t"
  "ds_read_b128 a[156:159], v21 offset:22528\n\t"
  "v_mfma_f32_16x16x32_bf16 v[40:43], a[132:135], a[216:219], v[40:43]\n\t"
  "s_waitcnt lgkmcnt(4)\n\t"
  "v_mfma_f32_16x16x32_bf16 v[48:51], a[136:139], a[216:219], v[48:51]\n\t"
  "ds_read_b128 a[128:131], v21 offset:24576\n\t"
  "v_mfma_f32_16x16x32_bf16 v[52:55], a[136:139], a[220:223], v[52:55]\n\t"
  "v_mfma_f32_16x16x32_bf16 v[60:63], a[140:143], a[220:223], v[60:63]\n\t"
  "ds_read_b128 a[132:135], v21 offset:26624\n\t"
  "v_mfma_f32_16x16x32_bf16 v[56:59], a[140:143], a[216:219], v[56:59]\n\t"
  "s_waitcnt lgkmcnt(4) vmcnt(12)\n\t"
  "v_mfma_f32_16x16x32_bf16 v[32:35], a[144:147], a[224:227], v[32:35]\n\t"
  "ds_read_b128 a[136:139], v21 offset:28672\n\t"
  "s_waitcnt vmcnt(7)\n\t"
  "v_mfma_f32_16x16x32_bf16 v[36:39], a[144:147], a[228:231], v[36:39]\n\t"
  "s_barrier\n\t"
  "v_mfma_f32_16x16x32_bf16 v[44:47], a[148:151], a[228:231], v[44:47]\n\t"
  "ds_read_b128 a[140:143], v21 offset:30720\n\t"
  "v_mfma_f32_16x16x32_bf16 v[40:43], a[148:151], a[224:227], v[40:43]\n\t"
  "s_waitcnt lgkmcnt(4)\n\t"
  "v_mfma_f32_16x16x32_bf16 v[48:51], a[152:155], a[224:227], v[48:51]\n\t"
  "ds_read_b128 a[144:147], v21 offset:32768\n\t"
  "v_mfma_f32_16x16x32_bf16 v[52:55], a[152:155], a[228:231], v[52:55]\n\t"
  "v_mfma_f32_16x16x32_bf16 v[60:63], a[156:159], a[228:231], v[60:63]\n\t"
  "ds_read_b128 a[148:151], v21 offset:34816\n\t"
  "v_mfma_f32_16x16x32_bf16 v[56:59], a[156:159], a[224:227], v[56:59]\n\t"
  "s_waitcnt lgkmcnt(4)\n\t"
  "v_mfma_f32_16x16x32_bf16 v[32:35], a[128:131], a[232:235], v[32:35]\n\t"
  "ds_read_b128 a[152:155], v21 offset:36864\n\t"
  "v_mfma_f32_16x16x32_bf16 v[36:39], a[128:131], a[236:239], v[36:39]\n\t"
  "v_mfma_f32_16x16x32_bf16 v[44:47], a[132:135], a[236:239], v[44:47]\n\t"
  "ds_read_b128 a[156:159], v21 offset:38912\n\t"
  "v_mfma_f32_16x16x32_bf16 v[40:43], a[132:135], a[232:235], v[40:43]\n\t"
  "s_waitcnt lgkmcnt(4)\n\t"
  "v_mfma_f32_16x16x32_bf16 v[48:51], a[136:139], a[232:235], v[48:51]\n\t"
  "ds_read_b128 a[128:131], v21 offset:40960\n\t"
  "v_mfma_f32_16x16x32_bf16 v[52:55], a[136:139], a[236:239], v[52:55]\n\t"
  "v_mfma_f32_16x16x32_bf16 v[60:63], a[140:143], a[236:239], v[60:63]\n\t"
  "ds_read_b128 a[132:135], v21 offset:43008\n\t"
  "v_mfma_f32_16x16x32_bf16 v[56:59], a[140:143], a[232:235], v[56:59]\n\t"
  "s_waitcnt lgkmcnt(4) vmcnt(6)\n\t"
  "v_mfma_f32_16x16x32_bf16 v[32:35], a[144:147], a[240:243], v[32:35]\n\t"
  "ds_read_b128 a[136:139], v21 offset:45056\n\t"
  "s_waitcnt vmcnt(0)\n\t"
  "s_mov_b32 s72, 0x20000\n\t"
  "LXa_%=:\n\t"
  "v_max3_u32 v168, v112, v113, v114\n\t"
  "v_max3_u32 v168, v168, v115, v116\n\t"
  "v_max3_u32 v168, v168, v117, v118\n\t"
  "v_max3_u32 v168, v168, v119, v120\n\t"
  "v_max3_u32 v168, v168, v121, v122\n\t"
  "v_max3_u32 v168, v168, v123, v124\n\t"
  "v_max3_u32 v168, v168, v125, v126\n\t"
  "v_max_u32 v168, v168, v127\n\t"
  "v_cmp_eq_u32 vcc, -1, v168\n\t"
  "s_cbranch_vccz LXb_%=\n\t"
  "buffer_load_dwordx4 v[112:115], v19, s[20:23], 0 offen offset:0 sc1\n\t"
  "buffer_load_dwordx4 v[116:119], v19, s[20:23], 0 offen offset:1024 sc1\n\t"
  "buffer_load_dwordx4 v[120:123], v19, s[20:23], 0 offen offset:2048 sc1\n\t"
  "buffer_load_dwordx4 v[124:127], v19, s[20:23], 0 offen offset:3072 sc1\n\t"
  "s_waitcnt vmcnt(0)\n\t"
  "s_sub_u32 s72, s72, 1\n\t"
  "s_cbranch_scc0 LXa_%=\n\t"
  "LXb_%=:\n\t"
  "v_lshlrev_b32 v168, 16, v112\n\t"
  "v_and_b32 v112, 0xffff0000, v112\n\t"
  "v_fmac_f32 v136, v128, v168\n\t"
  "v_mfma_f32_16x16x32_bf16 v[36:39], a[144:147], a[244:247], v[36:39]\n\t"
  "v_fmac_f32 v137, v128, v112\n\t"
  "v_lshlrev_b32 v168, 16, v113\n\t"
  "v_and_b32 v113, 0xffff0000, v113\n\t"
  "v_mfma_f32_16x16x32_bf16 v[44:47], a[148:151], a[244:247], v[44:47]\n\t"
  "ds_read_b128 a[140:143], v21 offset:47104\n\t"
  "v_fmac_f32 v138, v128, v168\n\t"
  "v_fmac_f32 v139, v128, v113\n\t"
  "v_lshlrev_b32 v168, 16, v114\n\t"
  "v_mfma_f32_16x16x32_bf16 v[40:43], a[148:151], a[240:243], v[40:43]\n\t"
  "s_waitcnt lgkmcnt(4)\n\t"
  "v_and_b32 v114, 0xffff0000, v114\n\t"
  "v_fmac_f32 v140, v128, v168\n\t"
  "v_fmac_f32 v141, v128, v114\n\t"
  "v_mfma_f32_16x16x32_bf16 v[48:51], a[152:155], a[240:243], v[48:51]\n\t"
  "v_lshlrev_b32 v168, 16, v115\n\t"
  "v_and_b32 v115, 0xffff0000, v115\n\t"
  "v_fmac_f32 v142, v128, v168\n\t"
  "v_mfma_f32_16x16x32_bf16 v[52:55], a[152:155], a[244:247], v[52:55]\n\t"
  "v_fmac_f32 v143, v128, v115\n\t"
  "v_lshlrev_b32 v168, 16, v116\n\t"
  "v_and_b32 v116, 0xffff0000, v116\n\t"
  "v_mfma_f32_16x16x32_bf16 v[60:63], a[156:159], a[244:247], v[60:63]\n\t"
  "v_fmac_f32 v144, v128, v168\n\t"
  "v_fmac_f32 v145, v128, v116\n\t"
  "v_lshlrev_b32 v168, 16, v117\n\t"
  "v_mfma_f32_16x16x32_bf16 v[56:59], a[156:159], a[240:243], v[56:59]\n\t"
  "s_waitcnt lgkmcnt(2)\n\t"
  "v_and_b32 v117, 0xffff0000, v117\n\t"
  "v_fmac_f32 v146, v128, v168\n\t"
  "v_fmac_f32 v147, v128, v117\n\t"
  "v_mfma_f32_16x16x32_bf16 v[32:35], a[128:131], a[248:251], v[32:35]\n\t"
  "v_lshlrev_b32 v168, 16, v118\n\t"
  "v_and_b32 v118, 0xffff0000, v118\n\t"
  "v_fmac_f32 v148, v128, v168\n\t"
  "v_mfma_f32_16x16x32_bf16 v[36:39], a[128:131], a[252:255], v[36:39]\n\t"
  "v_fmac_f32 v149, v128, v118\n\t"
  "v_lshlrev_b32 v168, 16, v119\n\t"
  "v_and_b32 v119, 0xffff0000, v119\n\t"
  "v_mfma_f32_16x16x32_bf16 v[44:47], a[132:135], a[252:255], v[44:47]\n\t"
  "v_fmac_f32 v150, v128, v168\n\t"
  "v_fmac_f32 v151, v128, v119\n\t"
  "v_lshlrev_b32 v168, 16, v120\n\t"
  "v_mfma_f32_16x16x32_bf16 v[40:43], a[132:135], a[248:251], v[40:43]\n\t"
  "s_waitcnt lgkmcnt(0)\n\t"
  "v_and_b32 v120, 0xffff0000, v120\n\t"
  "v_fmac_f32 v152, v132, v168\n\t"
  "v_fmac_f32 v153, v132, v120\n\t"
  "v_mfma_f32_16x16x32_bf16 v[48:51], a[136:139], a[248:251], v[48:51]\n\t"
  "v_lshlrev_b32 v168, 16, v121\n\t"
  "v_and_b32 v121, 0xffff0000, v121\n\t"
  "v_fmac_f32 v154, v132, v168\n\t"
  "v_mfma_f32_16x16x32_bf16 v[52:55], a[136:139], a[252:255], v[52:55]\n\t"
  "v_fmac_f32 v155, v132, v121\n\t"
  "v_lshlrev_b32 v168, 16, v122\n\t"
  "v_and_b32 v122, 0xffff0000, v122\n\t"
  "v_mfma_f32_16x16x32_bf16 v[60:63], a[140:143], a[252:255], v[60:63]\n\t"
  "v_fmac_f32 v156, v132, v168\n\t"
  "v_fmac_f32 v157, v132, v122\n\t"
  "v_lshlrev_b32 v168, 16, v123\n\t"
  "v_mfma_f32_16x16x32_bf16 v[56:59], a[140:143], a[248:251], v[56:59]\n\t"
  "v_and_b32 v123, 0xffff0000, v123\n\t"
  "v_fmac_f32 v158, v132, v168\n\t"
  "v_fmac_f32 v159, v132, v123\n\t"
  "v_lshlrev_b32 v168, 16, v124\n\t"
  "v_and_b32 v124, 0xffff0000, v124\n\t"
  "v_fmac_f32 v160, v132, v168\n\t"
  "v_fmac_f32 v161, v132, v124\n\t"
  "v_lshlrev_b32 v168, 16, v125\n\t"
  "v_and_b32 v125, 0xffff0000, v125\n\t"
  "v_fmac_f32 v162, v132, v168\n\t"
  "v_fmac_f32 v163, v132, v125\n\t"
  "v_lshlrev_b32 v168, 16, v126\n\t"
  "v_and_b32 v126, 0xffff0000, v126\n\t"
  "v_fmac_f32 v164, v132, v168\n\t"
  "v_fmac_f32 v165, v132, v126\n\t"
  "v_lshlrev_b32 v168, 16, v127\n\t"
  "v_and_b32 v127, 0xffff0000, v127\n\t"
  "v_fmac_f32 v166, v132, v168\n\t"
  "v_fmac_f32 v167, v132, v127\n\t"
  "s_nop 6\n\t"
  "s_add_u32 s56, s62, 0x10000\n\t"
  "s_addc_u32 s57, s63, 0\n\t"
  "v_pk_fma_f32 v[32:33], v[32:33], v[128:129], v[136:137] op_sel_hi:[1,0,1]\n\t"
  "v_pk_fma_f32 v[34:35], v[34:35], v[128:129], v[138:139] op_sel_hi:[1,0,1]\n\t"
  "v_pk_fma_f32 v[40:41], v[40:41], v[128:129], v[140:141] op_sel_hi:[1,0,1]\n\t"
  "v_pk_fma_f32 v[42:43], v[42:43], v[128:129], v[142:143] op_sel_hi:[1,0,1]\n\t"
  "v_cvt_pk_bf16_f32 v96, v32, v33\n\t"
  "v_cvt_pk_bf16_f32 v98, v40, v41\n\t"
  "v_cvt_pk_bf16_f32 v97, v34, v35\n\t"
  "v_cvt_pk_bf16_f32 v99, v42, v43\n\t"
  "s_nop 0\n\t"
  "v_permlane16_swap_b32 v96, v98\n\t"
  "v_permlane16_swap_b32 v97, v99\n\t"
  "global_store_dwordx4 v22, v[96:99], s[62:63] offset:0\n\t"
  "v_pk_fma_f32 v[48:49], v[48:49], v[128:129], v[144:145] op_sel_hi:[1,0,1]\n\t"
  "v_pk_fma_f32 v[50:51], v[50:51], v[128:129], v[146:147] op_sel_hi:[1,0,1]\n\t"
  "v_pk_fma_f32 v[56:57], v[56:57], v[128:129], v[148:149] op_sel_hi:[1,0,1]\n\t"
  "v_pk_fma_f32 v[58:59], v[58:59], v[128:129], v[150:151] op_sel_hi:[1,0,1]\n\t"
  "v_cvt_pk_bf16_f32 v100, v48, v49\n\t"
  "v_cvt_pk_bf16_f32 v102, v56, v57\n\t"
  "v_cvt_pk_bf16_f32 v101, v50, v51\n\t"
  "v_cvt_pk_bf16_f32 v103, v58, v59\n\t"
  "s_nop 0\n\t"
  "v_permlane16_swap_b32 v100, v102\n\t"
  "v_permlane16_swap_b32 v101, v103\n\t"
  "global_store_dwordx4 v22, v[100:103], s[62:63] offset:64\n\t"
  "v_pk_fma_f32 v[36:37], v[36:37], v[132:133], v[152:153] op_sel_hi:[1,0,1]\n\t"
  "v_pk_fma_f32 v[38:39], v[38:39], v[132:133], v[154:155] op_sel_hi:[1,0,1]\n\t"
  "v_pk_fma_f32 v[44:45], v[44:45], v[132:133], v[156:157] op_sel_hi:[1,0,1]\n\t"
  "v_pk_fma_f32 v[46:47], v[46:47], v[132:133], v[158:159] op_sel_hi:[1,0,1]\n\t"
  "v_cvt_pk_bf16_f32 v104, v36, v37\n\t"
  "v_cvt_pk_bf16_f32 v106, v44, v45\n\t"
  "v_cvt_pk_bf16_f32 v105, v38, v39\n\t"
  "v_cvt_pk_bf16_f32 v107, v46, v47\n\t"
  "s_nop 0\n\t"
  "v_permlane16_swap_b32 v104, v106\n\t"
  "v_permlane16_swap_b32 v105, v107\n\t"
  "global_store_dwordx4 v22, v[104:107], s[56:57] offset:0\n\t"
  "v_pk_fma_f32 v[52:53], v[52:53], v[132:133], v[160:161] op_sel_hi:[1,0,1]\n\t"
  "v_pk_fma_f32 v[54:55], v[54:55], v[132:133], v[162:163] op_sel_hi:[1,0,1]\n\t"
  "v_pk_fma_f32 v[60:61], v[60:61], v[132:133], v[164:165] op_sel_hi:[1,0,1]\n\t"
  "v_pk_fma_f32 v[62:63], v[62:63], v[132:133], v[166:167] op_sel_hi:[1,0,1]\n\t"
  "v_cvt_pk_bf16_f32 v108, v52, v53\n\t"
  "v_cvt_pk_bf16_f32 v110, v60, v61\n\t"
  "v_cvt_pk_bf16_f32 v109, v54, v55\n\t"
  "v_cvt_pk_bf16_f32 v111, v62, v63\n\t"
  "s_nop 0\n\t"
  "v_permlane16_swap_b32 v108, v110\n\t"
  "v_permlane16_swap_b32 v109, v111\n\t"
  "global_store_dwordx4 v22, v[108:111], s[56:57] offset:64\n\t"
     : "+{v[32:47]}"(ia), "+{s[44:45]}"(so), "+{s[56:59]}"(sd), "+{s38}"(so8), "+{s37}"(late),
       "+{v[200:215]}"(ib), "+{v[196:199]}"(b1), "+{v[252:255]}"(b2), "+{v[216:217]}"(xm), "+{v[222:223]}"(wc), "+{v[224:239]}"(wv), "+{v[246:247]}"(wx), "+{v[248:249]}"(we)
     : "{s[20:23]}"(sws), "{v[16:23]}"(fw), "{s[62:63]}"(out), "{s30}"(m7), "{s[60:61]}"(sj), "{s[70:71]}"(ca), "{v[0:15]}"(fv), "{s[34:35]}"(tag), "{v27}"(mv), "{v30}"(dp)
     : "memory", "scc", "vcc", "s28", "s29", "s39", "s41", "s42", "s68", "s72", "s73",
       "v24","v25","v26","v48","v49","v50","v51","v52","v53","v54","v55","v56","v57","v58","v59","v60","v61","v62","v63","v64","v65","v66","v67","v68","v69","v70","v71","v72","v73","v74","v75","v76","v77","v78","v79","v80","v81","v82","v83","v84","v85","v86","v87","v88","v89","v90","v91","v92","v93","v94","v95","v96","v97","v98","v99","v100","v101","v102","v103","v104","v105","v106","v107","v108","v109","v110","v111","v112","v113","v114","v115","v116","v117","v118","v119","v120","v121","v122","v123","v124","v125","v126","v127","v128","v129","v130","v131","v132","v133","v134","v135","v136","v137","v138","v139","v140","v141","v142","v143","v144","v145","v146","v147","v148","v149","v150","v151","v152","v153","v154","v155","v156","v157","v158","v159","v160","v161","v162","v163","v164","v165","v166","v167","v168","v169","v170","v171","v172","v173","v174","v175","v176","v177","v178","v179","v180","v181","v182","v183","v184","v185","v186","v187","v188","v189","v190","v191","v192","v193","v194","v195","v218","v219","v220","v221","v240","v241","v242","v243","v244","v245","v250","v251","a0","a1","a2","a3","a4","a5","a6","a7","a8","a9","a10","a11","a12","a13","a14","a15","a16","a17","a18","a19","a20","a21","a22","a23","a24","a25","a26","a27","a28","a29","a30","a31","a32","a33","a34","a35","a36","a37","a38","a39","a40","a41","a42","a43","a44","a45","a46","a47","a48","a49","a50","a51","a52","a53","a54","a55","a56","a57","a58","a59","a60","a61","a62","a63","a64","a65","a66","a67","a68","a69","a70","a71","a72","a73","a74","a75","a76","a77","a78","a79","a80","a81","a82","a83","a84","a85","a86","a87","a88","a89","a90","a91","a92","a93","a94","a95","a96","a97","a98","a99","a100","a101","a102","a103","a104","a105","a106","a107","a108","a109","a110","a111","a112","a113","a114","a115","a116","a117","a118","a119","a120","a121","a122","a123","a124","a125","a126","a127","a128","a129","a130","a131","a132","a133","a134","a135","a136","a137","a138","a139","a140","a141","a142","a143","a144","a145","a146","a147","a148","a149","a150","a151","a152","a153","a154","a155","a156","a157","a158","a159","a160","a161","a162","a163","a164","a165","a166","a167","a168","a169","a170","a171","a172","a173","a174","a175","a176","a177","a178","a179","a180","a181","a182","a183","a184","a185","a186","a187","a188","a189","a190","a191","a192","a193","a194","a195","a196","a197","a198","a199","a200","a201","a202","a203","a204","a205","a206","a207","a208","a209","a210","a211","a212","a213","a214","a215","a216","a217","a218","a219","a220","a221","a222","a223","a224","a225","a226","a227","a228","a229","a230","a231","a232","a233","a234","a235","a236","a237","a238","a239","a240","a241","a242","a243","a244","a245","a246","a247","a248","a249","a250","a251","a252","a253","a254","a255");
    if (bid == 0 && wid == 0 && lane_id() == 0) *(unsigned long long*)(ws + WS_EP) = tag;
    asm volatile("ktext_end:");
  STAMP_END(5);
  }
}

extern "C" void kernel_launch(void* const* d_in, const int* in_sizes, int n_in, void* d_out, int out_size, void* d_ws, size_t ws_size, hipStream_t stream) {
  const u16* x = (const u16*)d_in[0]; const u16* wqkv = (const u16*)d_in[1]; const u16* wo = (const u16*)d_in[2];
  const u16* wg = (const u16*)d_in[4]; const u16* wu = (const u16*)d_in[5]; const u16* wd = (const u16*)d_in[6];
  char* ws = (char*)d_ws;
  (void)hipFuncSetAttribute((const void*)k_main, hipFuncAttributeMaxDynamicSharedMemorySize, 163840);
  k_main<<<256, 256, 163840, stream>>>(ws, x, wqkv, wo, wg, wu, wd, (u16*)d_out);
}
```
